```python
import torch

def _softmax(x, axis=-1):
    return torch.softmax(x, dim=axis)
def _gelu(x):
    return torch.nn.functional.gelu(x, approximate="tanh")
def _silu(x):
    return torch.nn.functional.silu(x)
def _sigmoid(x):
    return torch.sigmoid(x)
def _layernorm(x, axis=-1, eps=1e-5):
    m = x.mean(dim=axis, keepdim=True)
    v = x.var(dim=axis, keepdim=True, unbiased=False)
    return (x - m) / torch.sqrt(v + eps)
def _scatter_add(tgt, idx, src, axis=0):
    return tgt.clone().index_add_(axis, idx, src)
def _segment_sum(src, seg, n):
    out = torch.zeros((n,) + src.shape[1:], dtype=src.dtype)
    return out.index_add_(0, seg, src)
def _pad(x, pw):
    flat = tuple(v for pair in reversed(pw) for v in pair)
    return torch.nn.functional.pad(x, flat)

kernel_name = 'recurrent_scan__sliding_block_cached__moe_arch_mod__prompt'

def setup_inputs():

    _w = (lambda a: (lambda t: t.to(torch.bfloat16) if t.is_floating_point() else t)(torch.as_tensor(a)))
    def _A_setup():
        B, Sq, D = 1, 2048, 1024
        Hq, Hkv, Sk, Dh = 32, 4, 2048, 128
        assert Hq % Hkv == 0, (Hq, Hkv)
        return {
            "x": _w((lambda *s: torch.randn(*s, dtype=torch.bfloat16) * 0.05)(B, Sq, D)),
            "k_cache": _w((lambda *s: torch.randn(*s, dtype=torch.bfloat16) * 0.8)(B, Hkv, Sk, Dh)),
            "v_cache": _w((lambda *s: torch.randn(*s, dtype=torch.bfloat16) * 0.2)(B, Hkv, Sk, Dh)),
            "wq": _w((lambda *s: torch.randn(*s, dtype=torch.bfloat16) * 0.8)(D, Hq * Dh) * (D ** -0.5)),
            "wo": _w((lambda *s: torch.randn(*s, dtype=torch.bfloat16) * 0.2)(Hq * Dh, D) * ((Hq * Dh) ** -0.5)),
        }
    _A_in = _A_setup()

    def _B_setup():
        B, S, D, F, C = 1, 2048, 1024, 3584, 1
        return {
            "x": _w((lambda *s: torch.randn(*s, dtype=torch.bfloat16) * 0.05)(B, S, D)),
            "w_r": _w((lambda *s: torch.randn(*s, dtype=torch.bfloat16) * 0.2)(D)),
            "w_gate": _w((lambda *s: torch.randn(*s, dtype=torch.bfloat16) * 0.1)(D, F)),
            "w_up": _w((lambda *s: torch.randn(*s, dtype=torch.bfloat16) * 0.1)(D, F)),
            "w_down": _w((lambda *s: torch.randn(*s, dtype=torch.bfloat16) * 0.1)(F, D)),
        }
    _B_in = _B_setup()

    return {
        "A_x": _A_in["x"],
        "A_k_cache": _A_in["k_cache"],
        "A_v_cache": _A_in["v_cache"],
        "A_wq": _A_in["wq"],
        "A_wo": _A_in["wo"],
        "B_x": _B_in["x"],
        "B_w_r": _B_in["w_r"],
        "B_w_gate": _B_in["w_gate"],
        "B_w_up": _B_in["w_up"],
        "B_w_down": _B_in["w_down"],
    }

def reference(A_x, A_k_cache, A_v_cache, A_wq, A_wo, B_x, B_w_r, B_w_gate, B_w_up, B_w_down):
    def _A_ref(x, k_cache, v_cache, wq, wo):
        B, Sq, D = x.shape
        Hq, Dh = 32, 128
        Hkv, Sk = k_cache.shape[1], k_cache.shape[2]
        W = min(4096, max(256, Sk // 4))
        rep = Hq // Hkv
        q = (lambda x, *d: x.permute(*d))((x @ wq).reshape(B, Sq, Hq, Dh), 0, 2, 1, 3)
        k = (lambda x, n, axis: torch.repeat_interleave(x, n, dim=axis))(k_cache[:, :, Sk - W:, :], rep, axis=1)
        v = (lambda x, n, axis: torch.repeat_interleave(x, n, dim=axis))(v_cache[:, :, Sk - W:, :], rep, axis=1)
        scores = torch.einsum("bhqd,bhkd->bhqk", q, k) * (Dh ** -0.5)
        p = _softmax(scores, axis=-1)
        o = torch.einsum("bhqk,bhkd->bhqd", p, v)
        return (lambda x, *d: x.permute(*d))(o, 0, 2, 1, 3).reshape(B, Sq, Hq * Dh) @ wo

    def _B_ref(x, w_r, w_gate, w_up, w_down):
        B, S, D = x.shape
        C = 1
        r = x @ w_r
        idx = torch.argsort(-r, axis=-1)[:, :C]
        idx_e = (lambda x, n, axis: torch.repeat_interleave(x, n, dim=axis))(idx.reshape(B, C, 1), D, axis=2)
        x_sel = (lambda x, i, axis: torch.take_along_dim(x, i, dim=axis))(x, idx_e, axis=1)
        g = _silu(x_sel @ w_gate)
        u = x_sel @ w_up
        y = (g * u) @ w_down
        r_sel = _sigmoid((lambda x, i, axis: torch.take_along_dim(x, i, dim=axis))(r, idx, axis=1))
        y = y * r_sel.reshape(B, C, 1)
        b_off = (torch.arange(B) * S).reshape(B, 1)
        flat_idx = (idx + b_off).reshape(B * C)
        zero = (x * 0.0).reshape(B * S, D)
        upd = _scatter_add(zero, flat_idx, y.reshape(B * C, D), axis=0)
        return x + upd.reshape(B, S, D)

    x = A_x
    xn = _layernorm(x, axis=-1)
    s = _A_ref(xn, A_k_cache, A_v_cache, A_wq, A_wo)
    g = _silu(_B_ref(xn, B_w_r, B_w_gate, B_w_up, B_w_down))
    return x + s * g
```

```cpp
#include <hip/hip_runtime.h>
#include <stdint.h>
#include <type_traits>

typedef unsigned short u16;
typedef short bf16x8 __attribute__((ext_vector_type(8)));
typedef short s16x4 __attribute__((ext_vector_type(4)));
typedef float f32x16 __attribute__((ext_vector_type(16)));
typedef float f32x4 __attribute__((ext_vector_type(4)));
typedef float f32x2 __attribute__((ext_vector_type(2)));
typedef unsigned u32x4 __attribute__((ext_vector_type(4)));
typedef unsigned u32x2 __attribute__((ext_vector_type(2)));
typedef __bf16 bf2_t __attribute__((ext_vector_type(2)));
#define LAS __attribute__((address_space(3)))
#define DI __device__ __forceinline__
#define AI __attribute__((always_inline))
#define SBAR() __builtin_amdgcn_sched_barrier(0)
#define PIN(x) asm volatile("" : "+v"(x))
#define MFMA(a, b, c) __builtin_amdgcn_mfma_f32_32x32x16_bf16((a), (b), (c), 0, 0, 0)

constexpr int SEQ = 2048, DM = 1024, HQ = 32, DH = 128, SK = 2048, WIN = 512, FF = 3584;
constexpr size_t WS_XN = 0;
constexpr size_t WS_KV = 4u << 20;
constexpr size_t WS_ST = 5u << 20;
constexpr size_t WS_MX = WS_ST + 16384;
constexpr size_t WS_YP = WS_ST + 65536;
constexpr size_t WS_O = 8u << 20;

template <int I, int N, class F> DI void sfor_impl(F&& f) {
  if constexpr (I < N) { f(std::integral_constant<int, I>{}); sfor_impl<I + 1, N>(f); }
}
template <int N, class F> DI void sfor(F&& f) { sfor_impl<0, N>(f); }

DI unsigned pk2(float a, float b) { f32x2 v = {a, b}; return __builtin_bit_cast(unsigned, __builtin_convertvector(v, bf2_t)); }
DI float rbf(float a) { return (float)(__bf16)a; }
DI float bflo(unsigned u) { return __uint_as_float(u << 16); }
DI float bfhi(unsigned u) { return __uint_as_float(u & 0xffff0000u); }
DI float silu_f(float v) { return v / (1.f + __expf(-v)); }
DI float swap32(float v) {
  auto rr = __builtin_amdgcn_permlane32_swap(__float_as_uint(v), __float_as_uint(v), false, false);
  return (threadIdx.x & 32) ? __uint_as_float(rr[0]) : __uint_as_float(rr[1]);
}

template <int OFF> DI void glds(unsigned ldsaddr, unsigned voff, const void* sbase) {
  asm volatile("s_mov_b32 m0, %0\n\ts_nop 0\n\tglobal_load_lds_dwordx4 %1, %2 offset:%3" ::"s"(ldsaddr), "v"(voff), "s"(sbase), "i"(OFF) : "memory");
}
template <int OFF, class T> DI void gld16(T& dst, unsigned voff, const void* sbase) {
  asm volatile("global_load_dwordx4 %0, %1, %2 offset:%3" : "=v"(dst) : "v"(voff), "s"(sbase), "i"(OFF) : "memory");
}
template <int OFF, class T> DI void gld8(T& dst, unsigned voff, const void* sbase) {
  asm volatile("global_load_dwordx2 %0, %1, %2 offset:%3" : "=v"(dst) : "v"(voff), "s"(sbase), "i"(OFF) : "memory");
}
template <int N, class T> DI void waitv(T& x) { asm volatile("s_waitcnt vmcnt(%1)" : "+v"(x) : "n"(N) : "memory"); }
template <int N> DI void waitbar() { asm volatile("s_waitcnt vmcnt(%0)\n\ts_barrier" ::"n"(N) : "memory"); }
template <int N> DI void waitbar_l() { asm volatile("s_waitcnt vmcnt(%0) lgkmcnt(0)\n\ts_barrier" ::"n"(N) : "memory"); }
typedef short v4i16_t __attribute__((ext_vector_type(4)));
DI s16x4 trd(LAS const char* p) { return __builtin_amdgcn_ds_read_tr16_b64_v4i16((LAS v4i16_t*)p); }
struct LBase {
  unsigned lo, hi;
  DI void init(unsigned a) { lo = a; hi = a + 65536; PIN(lo); PIN(hi); }
  template <int OFF> DI LAS const char* at() const { return (LAS const char*)(uintptr_t)(OFF < 65536 ? lo : hi) + (OFF & 65535); }
};
DI bf16x8 cat8(s16x4 lo, s16x4 hi) { return __builtin_shufflevector(lo, hi, 0, 1, 2, 3, 4, 5, 6, 7); }
DI unsigned wave_max_u32(unsigned v) {
  for (int o = 1; o < 64; o <<= 1) { unsigned t = __shfl_xor(v, o); v = v > t ? v : t; }
  return __builtin_amdgcn_readfirstlane(v);
}

__global__ __launch_bounds__(512) void k1_prep(const u16* __restrict__ x, const u16* __restrict__ kc, const u16* __restrict__ vc,
                                               const u16* __restrict__ wr, char* __restrict__ ws) {
  __shared__ float red[3][8][8];
  const int t = threadIdx.x, w = t >> 6, l = t & 63, r8 = l & 7, kk = l >> 3, b = blockIdx.x;
  const int row = 8 * b + r8, ks = 8 * w + kk;
  const u32x4 a0 = *(const u32x4*)(x + row * DM + ks * 16), a1 = *(const u32x4*)(x + row * DM + ks * 16 + 8);
  const u32x4 w0 = *(const u32x4*)(wr + ks * 16), w1 = *(const u32x4*)(wr + ks * 16 + 8);
  if (t < 256) {
    const int c = b * 128 + (t & 127), kvh = c >> 13, tile = (c >> 10) & 7, fr = (c >> 6) & 15, ln = c & 63, r = ln & 31, h = ln >> 5;
    u32x4 o;
    int slot;
    if (t < 128) {
      const u16* p = kc + ((size_t)kvh * SK + (SK - WIN) + tile * 64 + (fr >> 3) * 32 + r) * DH + (fr & 7) * 16 + 4 * h;
      const u32x2 lo = *(const u32x2*)p, hi = *(const u32x2*)(p + 8);
      o = (u32x4){lo[0], lo[1], hi[0], hi[1]};
      slot = tile == 0 ? 0 : 2 * tile - 1;
    } else {
      const u16* p = vc + ((size_t)kvh * SK + (SK - WIN) + tile * 64 + (fr & 3) * 16 + 4 * h) * DH + (fr >> 2) * 32 + r;
      unsigned e[8];
#pragma unroll
      for (int j = 0; j < 8; ++j) e[j] = p[(8 * (j >> 2) + (j & 3)) * DH];
      o = (u32x4){e[0] | (e[1] << 16), e[2] | (e[3] << 16), e[4] | (e[5] << 16), e[6] | (e[7] << 16)};
      slot = tile == 7 ? 15 : 2 * tile + 2;
    }
    *(u32x4*)(ws + WS_KV + ((size_t)(kvh * 16 + slot) * 16 + fr) * 1024 + ln * 16) = o;
  }
  float v[16];
#pragma unroll
  for (int j = 0; j < 4; ++j) { v[2 * j] = bflo(a0[j]); v[2 * j + 1] = bfhi(a0[j]); v[8 + 2 * j] = bflo(a1[j]); v[9 + 2 * j] = bfhi(a1[j]); }
  float s = 0.f;
#pragma unroll
  for (int j = 0; j < 16; ++j) s += v[j];
  s += __shfl_xor(s, 8); s += __shfl_xor(s, 16); s += __shfl_xor(s, 32);
  if (l < 8) red[0][w][l] = s;
  __syncthreads();
  float mean = 0.f;
#pragma unroll
  for (int j = 0; j < 8; ++j) mean += red[0][j][r8];
  mean *= (1.f / DM);
  float q = 0.f;
#pragma unroll
  for (int j = 0; j < 16; ++j) { const float d = v[j] - mean; q += d * d; }
  q += __shfl_xor(q, 8); q += __shfl_xor(q, 16); q += __shfl_xor(q, 32);
  if (l < 8) red[1][w][l] = q;
  __syncthreads();
  float var = 0.f;
#pragma unroll
  for (int j = 0; j < 8; ++j) var += red[1][j][r8];
  var *= (1.f / DM);
  const float mb = rbf(mean), den = rbf(sqrtf(rbf(rbf(var) + 1e-5f)));
  float rr = 0.f;
#pragma unroll
  for (int j = 0; j < 16; ++j) {
    v[j] = rbf(rbf(v[j] - mb) / den);
    const unsigned ww = j < 8 ? w0[j >> 1] : w1[(j - 8) >> 1];
    rr += v[j] * ((j & 1) ? bfhi(ww) : bflo(ww));
  }
  {
    char* dst = ws + WS_XN + ((size_t)((row >> 5) * 64 + ks) * 64 + (row & 31)) * 16;
    *(u32x4*)dst = (u32x4){pk2(v[0], v[1]), pk2(v[2], v[3]), pk2(v[8], v[9]), pk2(v[10], v[11])};
    *(u32x4*)(dst + 512) = (u32x4){pk2(v[4], v[5]), pk2(v[6], v[7]), pk2(v[12], v[13]), pk2(v[14], v[15])};
  }
  rr += __shfl_xor(rr, 8); rr += __shfl_xor(rr, 16); rr += __shfl_xor(rr, 32);
  if (l < 8) red[2][w][l] = rr;
  __syncthreads();
  if (t < 64) {
    float rs = 0.f;
#pragma unroll
    for (int j = 0; j < 8; ++j) rs += red[2][j][r8];
    const unsigned bits = pk2(rs, 0.f) & 0xffffu;
    const unsigned ord = (bits & 0x8000u) ? (~bits & 0xffffu) : (bits | 0x8000u);
    unsigned key = (ord << 16) | (unsigned)(SEQ - 1 - row);
    for (int o = 1; o < 8; o <<= 1) { const unsigned u = __shfl_xor(key, o); key = key > u ? key : u; }
    if (t == 0) *(unsigned*)(ws + WS_MX + b * 4) = key;
    if (t < 8) *(f32x2*)(ws + WS_ST + row * 8) = (f32x2){mb, den};
  }
}

namespace k2 {
constexpr int RING = 4 * 32768, RED1 = RING, RED2 = RING + 1024, LDS = RED2 + 16384;
constexpr int XD = 8;
constexpr float THR = 8.f;
constexpr float QSCALE = 0.08838834764831845f * 1.4426950408889634f;
constexpr int extras(int g) {
  const int u = g >> 3, i = g & 7;
  int n = 0;
  if (u >= 1 && i < 4) n += 1;
  if (u <= 2 && i >= 4) n += 1;
  if (g == 11) n += 1;
  return n;
}
constexpr int ops(int g) { return (g + XD < 64 ? 1 : 0) + extras(g); }
constexpr int ops_sum(int a, int b) { int n = 0; for (int s = a; s <= b; ++s) n += ops(s); return n; }
constexpr int xwait(int g) { return g >= XD ? ops(g - XD) - 1 + ops_sum(g - XD + 1, g - 1) : (XD - 1 - g) + 13 + ops_sum(0, g - 1); }
constexpr int gwait(int v, int g) { return v <= 3 ? 4 * (3 - v) + ops_sum(0, g) : ops_sum(8 * (v - 3) + 4, g); }
}

__global__ __launch_bounds__(512, 2) void k2_attn(const u16* __restrict__ wq, const u16* __restrict__ wgate, const u16* __restrict__ wup,
                                                  const u16* __restrict__ wdown, char* __restrict__ ws) {
  using namespace k2;
  __shared__ __attribute__((aligned(16))) char lds_raw[LDS];
  LAS char* const lds = (LAS char*)lds_raw;
  const int t = threadIdx.x, l = t & 63, bid = blockIdx.x;
  const int wid = __builtin_amdgcn_readfirstlane(t >> 6);
  const int xcd = bid & 7, within = bid >> 3, head = xcd * 4 + (within & 3), rb = within >> 2;
  const int fj = xcd * 28 + (within < 28 ? within : within - 28);
  const unsigned lds0 = (unsigned)(uintptr_t)lds;

  const char* const wq_b = (const char*)wq + head * 256;
  const unsigned wq_v = (unsigned)(((8 * wid + ((l & 31) >> 2)) * 4096 + (l >> 5) * 32 + (l & 3) * 8) * 2);
  const char* const kv_b = ws + WS_KV + (size_t)(head >> 3) * 262144;
  const unsigned kv_v = (unsigned)(wid * 4096 + l * 16);
  const char* const xn_b = ws + WS_XN + (size_t)rb * 8 * 65536 + 4096;
  const unsigned xn_v = (unsigned)(wid * 65536 + l * 16);
  const unsigned ldsw = lds0 + wid * 2048, ldsk = lds0 + wid * 4096;

  auto GP = [&](auto V, auto P) AI {
    constexpr int v = V, p = P;
    if constexpr (v < 8) {
      const char* sb = wq_b + (size_t)(v * 128 + (p >> 1) * 64) * 8192;
      if constexpr (p & 1) glds<128>(ldsw + (v & 3) * 32768 + (p >> 1) * 16384 + 1024 - 128, wq_v, sb);
      else glds<0>(ldsw + (v & 3) * 32768 + (p >> 1) * 16384, wq_v, sb);
    } else {
      glds<p * 1024>(ldsk + (v & 3) * 32768, kv_v, kv_b + (size_t)(v - 8) * 32768);
    }
  };
  auto G = [&](auto V) AI { sfor<4>([&](auto P) AI { GP(V, P); }); };
  bf16x8 xf[XD];
  auto X = [&](auto Gi) AI { constexpr int g = Gi; gld16<(g & 7) * 1024 - 4096>(xf[g % XD], xn_v, xn_b + (size_t)(g >> 3) * 8192); };

  u32x4 fg[4], fu[4], fd[4], fm;
  u32x2 fx;
  const unsigned fgu_v = (unsigned)(((t >> 1) * 4 * FF + (t & 1) * 8) * 2);
  const unsigned fd_v = (unsigned)(((t >> 7) * 4 * DM + (t & 127) * 8) * 2);

  G(std::integral_constant<int, 0>{});
  sfor<XD>([&](auto I) AI { X(I); });
  gld16<0>(fm, (unsigned)(l * 16), ws + WS_MX);
  G(std::integral_constant<int, 1>{});
  G(std::integral_constant<int, 2>{});
  G(std::integral_constant<int, 3>{});

  f32x16 acc[4];
#pragma unroll
  for (int d = 0; d < 4; ++d) acc[d] = (f32x16){};
  LBase trb, frb;
  trb.init(lds0 + (4 * (l >> 5) + ((l & 15) >> 2)) * 64 + ((l >> 4) & 1) * 32 + (l & 3) * 8);
  frb.init(lds0 + l * 16);
  bf16x8 wf[2][4];
  auto WRD = [&](auto Gi) AI {
    constexpr int g = Gi, u = g >> 3, i = g & 7, base = (u & 3) * 32768 + (i >> 2) * 16384 + (i & 3) * 4096;
    sfor<4>([&](auto Dd) AI { constexpr int d = Dd; wf[g & 1][d] = cat8(trd(trb.at<base + d * 512>()), trd(trb.at<base + 2048 + d * 512>())); });
  };
  bf16x8 fr[4];
  auto FRD = [&](auto Ni) AI { constexpr int n = Ni; if constexpr (n < 256) fr[n & 3] = *(LAS const bf16x8*)(frb.at<((n >> 5) & 3) * 32768 + (n & 31) * 1024>()); };

  waitbar<XD + 13>();
  WRD(std::integral_constant<int, 0>{});
  SBAR();
  sfor<64>([&](auto Gi) AI {
    constexpr int g = Gi, u = g >> 3, i = g & 7;
    if constexpr (g + 1 < 64) WRD(std::integral_constant<int, g + 1>{});
    else { FRD(std::integral_constant<int, 0>{}); FRD(std::integral_constant<int, 1>{}); FRD(std::integral_constant<int, 2>{}); }
    SBAR();
    waitv<xwait(g)>(xf[g % XD]);
    sfor<4>([&](auto Dd) AI { constexpr int d = Dd; acc[d] = MFMA(wf[g & 1][d], xf[g % XD], acc[d]); });
    SBAR();
    if constexpr (g + XD < 64) X(std::integral_constant<int, g + XD>{});
    if constexpr (u >= 1 && i < 4) GP(std::integral_constant<int, u + 3>{}, std::integral_constant<int, i>{});
    if constexpr (u == 0 && i >= 4) gld16<0>(fg[i - 4], fgu_v, (const char*)wgate + fj * 32 + (i - 4) * (FF * 2));
    if constexpr (u == 1 && i >= 4) gld16<0>(fu[i - 4], fgu_v, (const char*)wup + fj * 32 + (i - 4) * (FF * 2));
    if constexpr (u == 2 && i >= 4) gld16<(i - 4 - 2) * 2048>(fd[i - 4], fd_v, (const char*)wdown + (size_t)fj * 16 * DM * 2 + 4096);
    if constexpr (g == 11) {
      PIN(fm);
      unsigned key = fm[0] > fm[1] ? fm[0] : fm[1], k2_ = fm[2] > fm[3] ? fm[2] : fm[3];
      key = wave_max_u32(key > k2_ ? key : k2_);
      const int idx = SEQ - 1 - (int)(key & 0xffffu), kq = t >> 1;
      const unsigned xo = (unsigned)((((idx >> 5) * 64 + (kq >> 2)) * 64 + (idx & 31) + 32 * (kq & 1)) * 16 + ((kq >> 1) & 1) * 8);
      gld8<0>(fx, xo, ws + WS_XN);
    }
    if constexpr (g == 26) {
      PIN(fx);
      float pg[8], pu[8];
      const float xs[4] = {bflo(fx[0]), bfhi(fx[0]), bflo(fx[1]), bfhi(fx[1])};
#pragma unroll
      for (int c = 0; c < 8; ++c) { pg[c] = 0.f; pu[c] = 0.f; }
#pragma unroll
      for (int r = 0; r < 4; ++r) {
        PIN(fg[r]); PIN(fu[r]);
#pragma unroll
        for (int c = 0; c < 4; ++c) {
          pg[2 * c] += xs[r] * bflo(fg[r][c]); pg[2 * c + 1] += xs[r] * bfhi(fg[r][c]);
          pu[2 * c] += xs[r] * bflo(fu[r][c]); pu[2 * c + 1] += xs[r] * bfhi(fu[r][c]);
        }
      }
      float a8[8], a4[4], a2[2], a1;
      const bool b5 = l & 32, b4 = l & 16, b3 = l & 8, b2 = l & 4;
#pragma unroll
      for (int c = 0; c < 8; ++c) a8[c] = (b5 ? pu[c] : pg[c]) + __shfl_xor(b5 ? pg[c] : pu[c], 32);
#pragma unroll
      for (int c = 0; c < 4; ++c) a4[c] = (b4 ? a8[4 + c] : a8[c]) + __shfl_xor(b4 ? a8[c] : a8[4 + c], 16);
#pragma unroll
      for (int c = 0; c < 2; ++c) a2[c] = (b3 ? a4[2 + c] : a4[c]) + __shfl_xor(b3 ? a4[c] : a4[2 + c], 8);
      a1 = (b2 ? a2[1] : a2[0]) + __shfl_xor(b2 ? a2[0] : a2[1], 4);
      a1 += __shfl_xor(a1, 2);
      if (!(l & 2)) *(LAS float*)(lds + RED1 + (wid * 32 + (l >> 5) * 16 + (l & 1) * 8 + ((l >> 2) & 7)) * 4) = a1;
    }
    if constexpr (g == 34) {
      f32x4 g4 = {}, u4 = {};
#pragma unroll
      for (int w = 0; w < 8; ++w) {
        g4 += *(LAS const f32x4*)(lds + RED1 + (w * 32 + (t >> 7) * 4) * 4);
        u4 += *(LAS const f32x4*)(lds + RED1 + (w * 32 + 16 + (t >> 7) * 4) * 4);
      }
      float y[8];
#pragma unroll
      for (int c = 0; c < 8; ++c) y[c] = 0.f;
#pragma unroll
      for (int r = 0; r < 4; ++r) {
        PIN(fd[r]);
        const float hh = rbf(rbf(silu_f(rbf(g4[r]))) * rbf(u4[r]));
#pragma unroll
        for (int c = 0; c < 4; ++c) { y[2 * c] += hh * bflo(fd[r][c]); y[2 * c + 1] += hh * bfhi(fd[r][c]); }
      }
      LAS f32x4* dst = (LAS f32x4*)(lds + RED2 + ((t >> 7) * 1024 + (t & 127) * 8) * 4);
      dst[0] = (f32x4){y[0], y[1], y[2], y[3]};
      dst[1] = (f32x4){y[4], y[5], y[6], y[7]};
    }
    if constexpr (g == 42) {
      f32x2 y = {};
#pragma unroll
      for (int r = 0; r < 4; ++r) y += *(LAS const f32x2*)(lds + RED2 + (r * 1024 + t * 2) * 4);
      *(f32x2*)(ws + WS_YP + ((size_t)(within < 28 ? fj : 224 + xcd * 4 + within - 28) * 1024 + t * 2) * 4) = y;
    }
    SBAR();
    if constexpr (g == 3) waitbar<gwait(1, 3)>();
    if constexpr (i == 7) {
      if constexpr (u == 3 || u == 4) waitbar_l<gwait(u + 2, g)>();
      else waitbar<gwait(u + 2, g)>();
    }
  });

  bf16x8 qf[8];
  sfor<8>([&](auto Ii) AI {
    constexpr int ds = Ii, d = ds >> 1, s = ds & 1;
    u32x4 p;
#pragma unroll
    for (int q = 0; q < 4; ++q) p[q] = pk2(acc[d][8 * s + 2 * q] * QSCALE, acc[d][8 * s + 2 * q + 1] * QSCALE);
    qf[ds] = __builtin_bit_cast(bf16x8, p);
  });

  f32x16 o[4], sc[2][2], negm;
#pragma unroll
  for (int d = 0; d < 4; ++d) o[d] = (f32x16){};
  u32x4 pw[4];
  float lsum = 0.f, fpend = 1.f;
  bool resc = false;
#define MX3(a, b, c) __builtin_fmaxf(__builtin_fmaxf((a), (b)), (c))
  auto max16 = [&](const f32x16& p, float m) AI {
    float a = MX3(m, p[0], p[1]), b2 = MX3(p[2], p[3], p[4]);
    a = MX3(a, p[5], p[6]); b2 = MX3(b2, p[7], p[8]); a = MX3(a, p[9], p[10]); b2 = MX3(b2, p[11], p[12]);
    a = MX3(a, p[13], p[14]); return MX3(a, b2, p[15]);
  };
  G(std::integral_constant<int, 11>{});
  sfor<16>([&](auto Ni) AI {
    constexpr int n = Ni, kb = n >> 3, ds = n & 7;
    FRD(std::integral_constant<int, n + 3>{});
    if constexpr (ds == 0) sc[0][kb] = MFMA(fr[n & 3], qf[0], (f32x16){});
    else sc[0][kb] = MFMA(fr[n & 3], qf[ds], sc[0][kb]);
    SBAR();
  });
  {
    float m = max16(sc[0][1], max16(sc[0][0], sc[0][0][0]));
    m = __builtin_fmaxf(m, swap32(m));
#pragma unroll
    for (int r = 0; r < 16; ++r) negm[r] = -m;
    PIN(negm);
#pragma unroll
    for (int r = 0; r < 16; ++r) { sc[0][0][r] = __builtin_amdgcn_exp2f(sc[0][0][r] - m); sc[0][1][r] = __builtin_amdgcn_exp2f(sc[0][1][r] - m); }
  }
  auto QK = [&](auto Ti, auto N0) AI {
    constexpr int T = Ti, n0 = N0, c = T & 1, pv = c ^ 1;
    float mx = 0.f;
    sfor<16>([&](auto Ii) AI {
      constexpr int i = Ii, n = n0 + i, kb = i >> 3, ds = i & 7;
      FRD(std::integral_constant<int, n + 3>{});
      if constexpr (ds == 0) sc[c][kb] = MFMA(fr[n & 3], qf[0], negm);
      else sc[c][kb] = MFMA(fr[n & 3], qf[ds], sc[c][kb]);
      lsum += sc[pv][kb][2 * ds]; lsum += sc[pv][kb][2 * ds + 1];
      pw[i >> 2][i & 3] = pk2(sc[pv][kb][2 * ds], sc[pv][kb][2 * ds + 1]);
      PIN(lsum); PIN(pw[i >> 2]);
      if constexpr (i == 12) { mx = max16(sc[c][0], sc[c][0][0]); PIN(mx); }
      SBAR();
    });
    mx = max16(sc[c][1], mx);
    if (__builtin_expect(__any(mx > THR), 0)) {
      const float dl = __builtin_fmaxf(__builtin_fmaxf(mx, swap32(mx)), 0.f);
      fpend = __builtin_amdgcn_exp2f(-dl);
      lsum *= fpend;
#pragma unroll
      for (int r = 0; r < 16; ++r) { sc[c][0][r] -= dl; sc[c][1][r] -= dl; negm[r] -= dl; }
      resc = true;
    }
    SBAR();
  };
  auto PV = [&](auto Ti, auto N0, auto EXPS) AI {
    constexpr int T = Ti, n0 = N0, c = (T + 1) & 1;
    sfor<16>([&](auto Ii) AI {
      constexpr int i = Ii, n = n0 + i, db = i >> 2, ks = i & 3;
      FRD(std::integral_constant<int, n + 3>{});
      o[db] = MFMA(fr[n & 3], __builtin_bit_cast(bf16x8, pw[ks]), o[db]);
      if constexpr (EXPS) {
        sc[c][i >> 3][2 * (i & 7)] = __builtin_amdgcn_exp2f(sc[c][i >> 3][2 * (i & 7)]);
        sc[c][i >> 3][2 * (i & 7) + 1] = __builtin_amdgcn_exp2f(sc[c][i >> 3][2 * (i & 7) + 1]);
        PIN(sc[c][i >> 3]);
      }
      SBAR();
    });
    if (__builtin_expect(resc, 0)) {
#pragma unroll
      for (int d = 0; d < 4; ++d) o[d] *= fpend;
      resc = false;
    }
    SBAR();
  };
  QK(std::integral_constant<int, 1>{}, std::integral_constant<int, 16>{});
  waitbar<4>();
  sfor<6>([&](auto Ti) AI {
    constexpr int T = Ti, u = 9 + T;
    if constexpr (u + 3 <= 15) G(std::integral_constant<int, u + 3>{});
    PV(Ti, std::integral_constant<int, 32 + 32 * T>{}, std::true_type{});
    QK(std::integral_constant<int, T + 2>{}, std::integral_constant<int, 48 + 32 * T>{});
    if constexpr (u + 2 <= 15) waitbar<(u + 3 <= 15) ? 4 : 0>();
  });
  PV(std::integral_constant<int, 6>{}, std::integral_constant<int, 224>{}, std::true_type{});
#pragma unroll
  for (int i = 0; i < 16; ++i) {
    lsum += sc[1][i >> 3][2 * (i & 7)]; lsum += sc[1][i >> 3][2 * (i & 7) + 1];
    pw[i >> 2][i & 3] = pk2(sc[1][i >> 3][2 * (i & 7)], sc[1][i >> 3][2 * (i & 7) + 1]);
  }
  PV(std::integral_constant<int, 7>{}, std::integral_constant<int, 240>{}, std::false_type{});
  lsum += swap32(lsum);
  const float inv = 1.f / lsum;
  char* const od = ws + WS_O + ((size_t)((rb * 8 + wid) * 256 + head * 8) * 64 + l) * 16;
#pragma unroll
  for (int i = 0; i < 8; ++i) {
    const int d = i >> 1, s = i & 1;
    u32x4 p;
#pragma unroll
    for (int q = 0; q < 4; ++q) p[q] = pk2(o[d][8 * s + 2 * q] * inv, o[d][8 * s + 2 * q + 1] * inv);
    *(u32x4*)(od + i * 1024) = p;
  }
}

namespace k3 {
constexpr int RING = 4 * 32768, PITCH = 68, YS = RING, LDS = RING + 32 * 64 * 4 + 256;
constexpr int XD = 16;
constexpr int ops(int g) { const int u = g >> 3, i = g & 7; return (g + XD < 128 ? 1 : 0) + ((u >= 1 && u + 3 <= 15 && i < 4) ? 1 : 0); }
constexpr int ops_sum(int a, int b) { int n = 0; for (int s = a; s <= b; ++s) n += ops(s); return n; }
constexpr int xwait(int g) {
  return g >= XD ? ops(g - XD) - 1 + ops_sum(g - XD + 1, g - 1) : (g < 8 ? (7 - g) + 20 : (15 - g) + 8) + ops_sum(0, g - 1);
}
constexpr int gwait(int v, int g) { return v <= 3 ? (v == 1 ? 16 : 4 * (3 - v)) + ops_sum(0, g) : ops_sum(8 * (v - 3) + 4, g); }
}

__global__ __launch_bounds__(512, 2) void k3_out(const u16* __restrict__ x, const u16* __restrict__ wo, const char* __restrict__ ws,
                                                 u16* __restrict__ out) {
  using namespace k3;
  __shared__ __attribute__((aligned(16))) char lds_raw[LDS];
  LAS char* const lds = (LAS char*)lds_raw;
  const int t = threadIdx.x, l = t & 63, bid = blockIdx.x;
  const int wid = __builtin_amdgcn_readfirstlane(t >> 6);
  const int xcd = bid & 7, within = bid >> 3, mb = 4 * (xcd >> 1) + (within >> 3), nb = 8 * (xcd & 1) + (within & 7);
  const int rbw = wid & 3, kg = wid >> 2;
  const unsigned lds0 = (unsigned)(uintptr_t)lds;

  const char* const wo_b = (const char*)wo + nb * 128;
  const unsigned wo_v = (unsigned)(((32 * wid + ((l & 31) >> 2)) * DM + (l >> 5) * 32 + (l & 3) * 8) * 2);
  const unsigned ldsw = lds0 + wid * 4096;
  auto GP = [&](auto V, auto P) AI {
    constexpr int v = V, p = P;
    glds<0>(ldsw + (v & 3) * 32768 + p * 1024, wo_v, wo_b + (size_t)(v * 256 + p * 8) * (DM * 2));
  };
  auto G = [&](auto V) AI { sfor<4>([&](auto P) AI { GP(V, P); }); };
  const char* const o_b = ws + WS_O + (size_t)mb * 4 * 262144 + 4096;
  const unsigned o_v = (unsigned)(rbw * 262144 + kg * 8192 + l * 16);
  bf16x8 xf[XD];
  auto X = [&](auto Gi) AI { constexpr int g = Gi; gld16<(g & 7) * 1024 - 4096>(xf[g % XD], o_v, o_b + (size_t)(g >> 3) * 16384); };

  const int erow = mb * 128 + (t >> 2), ecol = nb * 64 + (t & 3) * 16;
  u32x4 fm, xe0, xe1;
  f32x2 st;
  gld16<0>(fm, (unsigned)(l * 16), ws + WS_MX);
  gld16<0>(xe0, (unsigned)((erow * DM + ecol) * 2), x);
  gld16<16>(xe1, (unsigned)((erow * DM + ecol) * 2), x);
  gld8<0>(st, (unsigned)(erow * 8), ws + WS_ST);
  G(std::integral_constant<int, 0>{});
  sfor<8>([&](auto I) AI { X(I); });
  G(std::integral_constant<int, 1>{});
  sfor<8>([&](auto I) AI { X(std::integral_constant<int, 8 + I>{}); });
  G(std::integral_constant<int, 2>{});
  G(std::integral_constant<int, 3>{});

  f32x16 acc[2] = {};
  LBase trb;
  trb.init(lds0 + kg * 16384 + (4 * (l >> 5) + ((l & 15) >> 2)) * 64 + ((l >> 4) & 1) * 32 + (l & 3) * 8);
  bf16x8 wf[2][2];
  auto WRD = [&](auto Gi) AI {
    constexpr int g = Gi, u = g >> 3, i = g & 7, base = (u & 3) * 32768 + i * 2048;
    sfor<2>([&](auto Dd) AI { constexpr int d = Dd; wf[g & 1][d] = cat8(trd(trb.at<base + d * 512>()), trd(trb.at<base + 1024 + d * 512>())); });
  };
  f32x4 yp[7];
  unsigned key = 0;
  bool owner = false;

  waitbar<28>();
  WRD(std::integral_constant<int, 0>{});
  SBAR();
  sfor<128>([&](auto Gi) AI {
    constexpr int g = Gi, u = g >> 3, i = g & 7;
    if constexpr (g + 1 < 128) WRD(std::integral_constant<int, g + 1>{});
    SBAR();
    waitv<xwait(g)>(xf[g % XD]);
    acc[0] = MFMA(wf[g & 1][0], xf[g % XD], acc[0]);
    acc[1] = MFMA(wf[g & 1][1], xf[g % XD], acc[1]);
    SBAR();
    if constexpr (g + XD < 128) X(std::integral_constant<int, g + XD>{});
    if constexpr (u >= 1 && u + 3 <= 15 && i < 4) GP(std::integral_constant<int, u + 3>{}, std::integral_constant<int, i>{});
    if constexpr (g == 5) {
      PIN(fm);
      unsigned ka = fm[0] > fm[1] ? fm[0] : fm[1], kb = fm[2] > fm[3] ? fm[2] : fm[3];
      key = wave_max_u32(ka > kb ? ka : kb);
      owner = ((SEQ - 1 - (int)(key & 0xffffu)) >> 7) == mb;
      if (owner) {
        const unsigned yv = (unsigned)(((t >> 4) * 1024 + nb * 64 + (t & 15) * 4) * 4);
        sfor<7>([&](auto I) AI { constexpr int q = I; gld16<0>(yp[q], yv, ws + WS_YP + (size_t)q * 32 * 4096); });
      }
    }
    SBAR();
    if constexpr (g == 3) waitbar<gwait(1, 3)>();
    if constexpr (i == 7 && u + 2 <= 15) waitbar<gwait(u + 2, g)>();
  });
  waitbar<0>();

  {
    LAS char* dst = lds + ((kg * 128 + rbw * 32 + (l & 31)) * PITCH + 4 * (l >> 5)) * 4;
#pragma unroll
    for (int d = 0; d < 2; ++d)
#pragma unroll
      for (int q = 0; q < 4; ++q)
        *(LAS f32x4*)(dst + (d * 32 + q * 8) * 4) = (f32x4){acc[d][4 * q], acc[d][4 * q + 1], acc[d][4 * q + 2], acc[d][4 * q + 3]};
  }
  if (owner) {
    f32x4 s = {};
#pragma unroll
    for (int q = 0; q < 7; ++q) { PIN(yp[q]); s += yp[q]; }
    *(LAS f32x4*)(lds + YS + ((t >> 4) * 64 + (t & 15) * 4) * 4) = s;
  }
  __syncthreads();
  PIN(xe0); PIN(xe1); PIN(st);
  const int idx = SEQ - 1 - (int)(key & 0xffffu);
  float s[16], ya[16];
  {
    LAS const char* src = lds + ((t >> 2) * PITCH + (t & 3) * 16) * 4;
#pragma unroll
    for (int q = 0; q < 4; ++q) {
      const f32x4 a = *(LAS const f32x4*)(src + q * 16), b = *(LAS const f32x4*)(src + 128 * PITCH * 4 + q * 16);
#pragma unroll
      for (int j = 0; j < 4; ++j) { s[4 * q + j] = a[j] + b[j]; ya[4 * q + j] = 0.f; }
    }
  }
  const bool sel = erow == idx;
  if (sel) {
    const unsigned ord = key >> 16, bits = (ord & 0x8000u) ? (ord & 0x7fffu) : (~ord & 0xffffu);
    const float rv = bflo(bits), sg = rbf(1.f / (1.f + __expf(-rv)));
#pragma unroll
    for (int q = 0; q < 4; ++q) {
      f32x4 a = {};
      for (int p = 0; p < 32; ++p) a += *(LAS const f32x4*)(lds + YS + (p * 64 + (t & 3) * 16 + q * 4) * 4);
#pragma unroll
      for (int j = 0; j < 4; ++j) ya[4 * q + j] = rbf(rbf(a[j]) * sg);
    }
  }
  float res[16];
#pragma unroll
  for (int j = 0; j < 16; ++j) {
    const unsigned xw = j < 8 ? xe0[j >> 1] : xe1[(j - 8) >> 1];
    const float xv = (j & 1) ? bfhi(xw) : bflo(xw);
    float xn = rbf(rbf(xv - st[0]) / st[1]);
    if (sel) xn = rbf(xn + ya[j]);
    res[j] = xv + rbf(rbf(s[j]) * rbf(silu_f(xn)));
  }
  u16* op = out + (size_t)erow * DM + ecol;
  *(u32x4*)op = (u32x4){pk2(res[0], res[1]), pk2(res[2], res[3]), pk2(res[4], res[5]), pk2(res[6], res[7])};
  *(u32x4*)(op + 8) = (u32x4){pk2(res[8], res[9]), pk2(res[10], res[11]), pk2(res[12], res[13]), pk2(res[14], res[15])};
}

extern "C" void kernel_launch(void* const* d_in, const int* in_sizes, int n_in, void* d_out, int out_size, void* d_ws, size_t ws_size,
                              hipStream_t stream) {
  const u16* x = (const u16*)d_in[0];
  const u16* kc = (const u16*)d_in[1];
  const u16* vc = (const u16*)d_in[2];
  const u16* wq = (const u16*)d_in[3];
  const u16* wo = (const u16*)d_in[4];
  const u16* wr = (const u16*)d_in[6];
  const u16* wg = (const u16*)d_in[7];
  const u16* wu = (const u16*)d_in[8];
  const u16* wd = (const u16*)d_in[9];
  char* ws = (char*)d_ws;
  k1_prep<<<256, 512, 0, stream>>>(x, kc, vc, wr, ws);
  k2_attn<<<256, 512, 0, stream>>>(wq, wg, wu, wd, ws);
  k3_out<<<256, 512, 0, stream>>>(x, wo, ws, (u16*)d_out);
}
```

```cpp
#include <hip/hip_runtime.h>
#include <stdint.h>
#include <type_traits>

typedef unsigned short u16;
typedef short bf16x8 __attribute__((ext_vector_type(8)));
typedef short s16x4 __attribute__((ext_vector_type(4)));
typedef float f32x4 __attribute__((ext_vector_type(4)));
typedef float f32x2 __attribute__((ext_vector_type(2)));
typedef unsigned u32x4 __attribute__((ext_vector_type(4)));
typedef unsigned u32x2 __attribute__((ext_vector_type(2)));
typedef __bf16 bf2_t __attribute__((ext_vector_type(2)));
#define LAS __attribute__((address_space(3)))
#define DI __device__ __forceinline__
#define AI __attribute__((always_inline))
#define SBAR() __builtin_amdgcn_sched_barrier(0)
#define PIN(x) asm volatile("" : "+v"(x))
#define MFMA(a, b, c) __builtin_amdgcn_mfma_f32_16x16x32_bf16((a), (b), (c), 0, 0, 0)
#define MFMA16(a, b, c) __builtin_amdgcn_mfma_f32_16x16x16bf16_1k((a), (b), (c), 0, 0, 0)
#define IC(n) std::integral_constant<int, (n)>{}

constexpr int SEQ = 2048, DM = 1024, DH = 128, SK = 2048, WIN = 512, FF = 3584;
constexpr size_t WS_XN = 0;
constexpr size_t WS_KV = 4u << 20;
constexpr size_t WS_ST = 5u << 20;
constexpr size_t WS_MX = WS_ST + 16384;
constexpr size_t WS_YP = WS_ST + 65536;
constexpr size_t WS_O = 8u << 20;

template <int I, int N, class F> DI void sfor_impl(F&& f) {
  if constexpr (I < N) { f(std::integral_constant<int, I>{}); sfor_impl<I + 1, N>(f); }
}
template <int N, class F> DI void sfor(F&& f) { sfor_impl<0, N>(f); }

DI unsigned pk2(float a, float b) { f32x2 v = {a, b}; return __builtin_bit_cast(unsigned, __builtin_convertvector(v, bf2_t)); }
DI float rbf(float a) { return (float)(__bf16)a; }
DI float bflo(unsigned u) { return __uint_as_float(u << 16); }
DI float bfhi(unsigned u) { return __uint_as_float(u & 0xffff0000u); }
DI float sigmoid_f(float v) { return __builtin_amdgcn_rcpf(1.f + __builtin_amdgcn_exp2f(v * -1.4426950408889634f)); }
DI float silu_f(float v) { return v * sigmoid_f(v); }
template <int CTRL> DI float dppf(float x) { return __builtin_bit_cast(float, __builtin_amdgcn_mov_dpp(__builtin_bit_cast(int, x), CTRL, 0xf, 0xf, true)); }
template <int CTRL> DI unsigned dppu(unsigned x) { return (unsigned)__builtin_amdgcn_mov_dpp((int)x, CTRL, 0xf, 0xf, true); }
constexpr int XOR1 = 0xB1, XOR2 = 0x4E, XOR7 = 0x141, XOR8 = 0x128;
DI float xg_sum(float x) {
  auto s = __builtin_amdgcn_permlane16_swap(__float_as_uint(x), __float_as_uint(x), false, false);
  x = __uint_as_float(s[0]) + __uint_as_float(s[1]);
  auto t = __builtin_amdgcn_permlane32_swap(__float_as_uint(x), __float_as_uint(x), false, false);
  return __uint_as_float(t[0]) + __uint_as_float(t[1]);
}
DI float xg_max(float x) {
  auto s = __builtin_amdgcn_permlane16_swap(__float_as_uint(x), __float_as_uint(x), false, false);
  x = __builtin_fmaxf(__uint_as_float(s[0]), __uint_as_float(s[1]));
  auto t = __builtin_amdgcn_permlane32_swap(__float_as_uint(x), __float_as_uint(x), false, false);
  return __builtin_fmaxf(__uint_as_float(t[0]), __uint_as_float(t[1]));
}
DI float wave_sum(float v) { v += dppf<XOR1>(v); v += dppf<XOR2>(v); v += dppf<XOR7>(v); v += dppf<XOR8>(v); return xg_sum(v); }
DI unsigned umax(unsigned a, unsigned b) { return a > b ? a : b; }
DI unsigned wave_max_u32(unsigned v) {
  v = umax(v, dppu<XOR1>(v)); v = umax(v, dppu<XOR2>(v)); v = umax(v, dppu<XOR7>(v)); v = umax(v, dppu<XOR8>(v));
  auto s = __builtin_amdgcn_permlane16_swap(v, v, false, false); v = umax(s[0], s[1]);
  auto t = __builtin_amdgcn_permlane32_swap(v, v, false, false); v = umax(t[0], t[1]);
  return __builtin_amdgcn_readfirstlane(v);
}

template <int OFF> DI void glds(unsigned ldsaddr, unsigned voff, const void* sbase) {
  asm volatile("s_mov_b32 m0, %0\n\ts_nop 0\n\tglobal_load_lds_dwordx4 %1, %2 offset:%3" ::"s"(ldsaddr), "v"(voff), "s"(sbase), "i"(OFF) : "memory");
}
DI void glds64(unsigned ldsaddr, const void* vaddr) {
  asm volatile("s_mov_b32 m0, %0\n\ts_nop 0\n\tglobal_load_lds_dwordx4 %1, off" ::"s"(ldsaddr), "v"(vaddr) : "memory");
}
template <int OFF, class T> DI void gld16(T& dst, unsigned voff, const void* sbase) {
  asm volatile("global_load_dwordx4 %0, %1, %2 offset:%3" : "=v"(dst) : "v"(voff), "s"(sbase), "i"(OFF) : "memory");
}
template <int OFF, class T> DI void gld8(T& dst, unsigned voff, const void* sbase) {
  asm volatile("global_load_dwordx2 %0, %1, %2 offset:%3" : "=v"(dst) : "v"(voff), "s"(sbase), "i"(OFF) : "memory");
}
template <int N> DI void waitv() { static_assert(N >= 0, "count"); asm volatile("s_waitcnt vmcnt(%0)" ::"n"(N > 63 ? 63 : N) : "memory"); }
template <int N, class T> DI void waitv(T& x) { static_assert(N >= 0, "count"); asm volatile("s_waitcnt vmcnt(%1)" : "+v"(x) : "n"(N > 63 ? 63 : N) : "memory"); }
template <int N, class T> DI void waitv(T& x, T& y) { static_assert(N >= 0, "count"); asm volatile("s_waitcnt vmcnt(%2)" : "+v"(x), "+v"(y) : "n"(N > 63 ? 63 : N) : "memory"); }
template <int N> DI void waitbar() { static_assert(N >= 0, "count"); asm volatile("s_waitcnt vmcnt(%0)\n\ts_barrier" ::"n"(N > 63 ? 63 : N) : "memory"); }
template <int N> DI void waitbar_l() { static_assert(N >= 0, "count"); asm volatile("s_waitcnt vmcnt(%0) lgkmcnt(0)\n\ts_barrier" ::"n"(N > 63 ? 63 : N) : "memory"); }
typedef short v4i16_t __attribute__((ext_vector_type(4)));
DI s16x4 trd(LAS const char* p) { return __builtin_amdgcn_ds_read_tr16_b64_v4i16((LAS v4i16_t*)p); }
struct LBase {
  unsigned lo, hi;
  DI void init(unsigned a) { lo = a; hi = a + 65536; PIN(lo); PIN(hi); }
  template <int OFF> DI LAS const char* at() const { return (LAS const char*)(uintptr_t)(OFF < 65536 ? lo : hi) + (OFF & 65535); }
};
DI bf16x8 cat8(s16x4 lo, s16x4 hi) { return __builtin_shufflevector(lo, hi, 0, 1, 2, 3, 4, 5, 6, 7); }

enum { OX, OG, OF, OM, OS, OP, OE };
struct Sched {
  int n; short kind[320], idx[320], slot[320];
  constexpr Sched() : n(0), kind{}, idx{}, slot{} {}
  constexpr void add(int s, int k, int i) { kind[n] = (short)k; idx[n] = (short)i; slot[n] = (short)s; ++n; }
  constexpr int younger(int k, int i, int s) const {
    int p = -1;
    for (int q = 0; q < n; ++q) if (kind[q] == k && idx[q] == i) p = q;
    if (p < 0 || slot[p] > s) return -1;
    int c = 0;
    for (int q = p + 1; q < n; ++q) if (slot[q] <= s) ++c;
    return c;
  }
  constexpr int first(int s) const { int q = 0; while (q < n && slot[q] < s) ++q; return q; }
  constexpr int count(int s) const { int c = 0; for (int q = 0; q < n; ++q) if (slot[q] == s) ++c; return c; }
};

#define STAMP_DECL
#define STAMP(i)
#define STAMP_OUT(kid, n)
__global__ __launch_bounds__(768) void k1_prep(const u16* __restrict__ x, const u16* __restrict__ kc, const u16* __restrict__ vc,
                                               const u16* __restrict__ wr, const u16* __restrict__ wq, char* __restrict__ ws) {
  __shared__ __attribute__((aligned(16))) char stage[16384 + 32];
  const int t = threadIdx.x, w = t >> 6, l = t & 63, b = blockIdx.x;
  if (t >= 512) {
    const int tt = t - 512;
    const int ch = b * 128 + (tt & 127), kvh = ch >> 13, tile = (ch >> 10) & 7, fr = (ch >> 6) & 15, ln = ch & 63, c = ln & 15, g = ln >> 4;
    u32x4 o;
    int slot;
    if (tt < 128) {
      const u16* p = kc + ((size_t)kvh * SK + (SK - WIN) + tile * 64 + (fr >> 2) * 16 + c) * DH + (fr & 3) * 32 + 4 * g;
      const u32x2 lo = *(const u32x2*)p, hi = *(const u32x2*)(p + 16);
      o = (u32x4){lo[0], lo[1], hi[0], hi[1]};
      slot = tile == 0 ? 0 : 2 * tile - 1;
    } else {
      const int dt = fr >> 1, d = 32 * (dt >> 1) + 8 * (c >> 2) + 4 * (dt & 1) + (c & 3);
      const u16* p = vc + ((size_t)kvh * SK + (SK - WIN) + tile * 64 + (fr & 1) * 32 + 4 * g) * DH + d;
      unsigned e[8];
#pragma unroll
      for (int j = 0; j < 8; ++j) e[j] = p[(16 * (j >> 2) + (j & 3)) * DH];
      o = (u32x4){e[0] | (e[1] << 16), e[2] | (e[3] << 16), e[4] | (e[5] << 16), e[6] | (e[7] << 16)};
      slot = tile == 7 ? 15 : 2 * tile + 2;
    }
    {
      const unsigned po = (unsigned)(((b >> 3) * 32 + (tt >> 6)) * 8192 + (b & 7) * 1024 + l * 16);
      u32x4 dummy;
      sfor<8>([&](auto I) AI { gld16<0>(dummy, po, (const char*)wq + (size_t)I * 4 * 8192); });
    }
    *(u32x4*)(ws + WS_KV + ((size_t)(kvh * 16 + slot) * 16 + fr) * 1024 + ln * 16) = o;
    return;
  }
  const int row = 8 * b + w;
  const u32x4 a0 = *(const u32x4*)(x + row * DM + l * 8), a1 = *(const u32x4*)(x + row * DM + 512 + l * 8);
  const u32x4 w0 = *(const u32x4*)(wr + l * 8), w1 = *(const u32x4*)(wr + 512 + l * 8);
  float v[16];
#pragma unroll
  for (int j = 0; j < 4; ++j) { v[2 * j] = bflo(a0[j]); v[2 * j + 1] = bfhi(a0[j]); v[8 + 2 * j] = bflo(a1[j]); v[9 + 2 * j] = bfhi(a1[j]); }
  float s = 0.f;
#pragma unroll
  for (int j = 0; j < 16; ++j) s += v[j];
  const float mean = wave_sum(s) * (1.f / DM);
  float q = 0.f;
#pragma unroll
  for (int j = 0; j < 16; ++j) { const float d = v[j] - mean; q += d * d; }
  const float var = wave_sum(q) * (1.f / DM);
  const float mb = rbf(mean), rden = 1.f / rbf(sqrtf(rbf(rbf(var) + 1e-5f)));
  float rr = 0.f;
  u32x4 o0, o1;
#pragma unroll
  for (int j = 0; j < 8; ++j) {
    const unsigned dd = pk2(v[2 * j] - mb, v[2 * j + 1] - mb);
    const unsigned nn = pk2(bflo(dd) * rden, bfhi(dd) * rden);
    const unsigned ww = j < 4 ? w0[j] : w1[j - 4];
    rr += bflo(nn) * bflo(ww); rr += bfhi(nn) * bfhi(ww);
    if (j < 4) o0[j] = nn; else o1[j - 4] = nn;
  }
  *(u32x4*)(stage + (l * 8 + (w ^ (l & 7))) * 16) = o0;
  *(u32x4*)(stage + 8192 + (l * 8 + (w ^ (l & 7))) * 16) = o1;
  rr = wave_sum(rr);
  if (l == 0) {
    const unsigned bits = pk2(rr, 0.f) & 0xffffu;
    const unsigned ord = (bits & 0x8000u) ? (~bits & 0xffffu) : (bits | 0x8000u);
    *(unsigned*)(stage + 16384 + w * 4) = (ord << 16) | (unsigned)(SEQ - 1 - row);
    *(f32x2*)(ws + WS_ST + row * 8) = (f32x2){mb, rden};
  }
  __syncthreads();
#pragma unroll
  for (int hh = 0; hh < 2; ++hh) {
    const int ch = hh * 64 + (t >> 3), r = 8 * b + ((t & 7) ^ (ch & 7));
    const u32x4 o = *(const u32x4*)(stage + hh * 8192 + t * 16);
    *(u32x4*)(ws + WS_XN + ((size_t)(((r >> 5) * 32 + (ch >> 2)) * 2 + ((r >> 4) & 1)) * 64 + (ch & 3) * 16 + (r & 15)) * 16) = o;
  }
  if (t == 0) {
    const u32x4 ka = *(const u32x4*)(stage + 16384), kb = *(const u32x4*)(stage + 16400);
    *(unsigned*)(ws + WS_MX + b * 4) = umax(umax(umax(ka[0], ka[1]), umax(ka[2], ka[3])), umax(umax(kb[0], kb[1]), umax(kb[2], kb[3])));
  }
}

namespace k2 {
constexpr int RING = 4 * 32768, FRING = RING, RED1 = FRING + 8 * 3072, HSC = RED1 + 1024, DUMMY = HSC + 512, LDS = DUMMY + 1024;
constexpr int XD = 16;
constexpr float THR = 8.f;
constexpr float QSCALE = 0.08838834764831845f * 1.4426950408889634f;
constexpr Sched make_sched() {
  Sched s;
  s.add(-1, OG, 0); s.add(-1, OG, 1);
  for (int i = 0; i < 4; ++i) s.add(-1, OX, i);
  s.add(-1, OG, 2); s.add(-1, OG, 3);
  for (int i = 4; i < 8; ++i) s.add(-1, OX, i);
  s.add(-1, OM, 0);
  for (int t = 0; t < 128; ++t) {
    if (t == 1 || t == 2) { s.add(t, OX, 6 + 2 * t); s.add(t, OX, 7 + 2 * t); }
    if (t == 4 || t == 5) { s.add(t, OX, 4 + 2 * t); s.add(t, OX, 5 + 2 * t); }
    for (int p = 0; p < 24; ++p) { const int a = 6 + p, b2 = 2 * p + 1; if ((a > b2 ? a : b2) == t) { s.add(t, OX, 16 + 2 * p); s.add(t, OX, 17 + 2 * p); } }
    if (t == 0) { s.add(t, OG, 4); s.add(t, OG, 5); }
    if (t == 3) { s.add(t, OG, 6); s.add(t, OG, 7); }
    if (t >= 6 && t <= 9) s.add(t, OG, 8 + t - 6);
    if (t >= 11 && t <= 14) s.add(t, OG, 12 + t - 11);
    for (int v = 4; v < 16; ++v) for (int p = 0; p < 4; ++p) if (8 * (v - 3) + 8 + 2 * p == t) s.add(t, OG, 4 * v + p);
    if ((t & 7) == 5 && (t >> 3) <= 10) s.add(t, OF, t >> 3);
    if (t == 8 * 12 + 5) s.add(t, OF, 11);
    if (t == 10) for (int i = 0; i < 4; ++i) s.add(t, OS, i);
    if (t >= 112 && t < 116) s.add(t, OP, t - 112);
  }
  return s;
}
constexpr Sched SC = make_sched();
}

__global__ __launch_bounds__(512, 2) void k2_attn(const u16* __restrict__ wq, const u16* __restrict__ wgate, const u16* __restrict__ wup,
                                                  const u16* __restrict__ wdown, const u16* __restrict__ wo, char* __restrict__ ws) {
  using namespace k2;
  __shared__ __attribute__((aligned(16))) char lds_raw[LDS];
  LAS char* const lds = (LAS char*)lds_raw;
  const int t = threadIdx.x, l = t & 63, bid = blockIdx.x, c = l & 15, g = l >> 4;
  const int wid = __builtin_amdgcn_readfirstlane(t >> 6);
  const int xcd = bid & 7, within = bid >> 3, head = xcd * 4 + (within & 3), rb = within >> 2;
  const int fj = xcd * 28 + (within < 28 ? within : within - 28);
  const unsigned lds0 = (unsigned)(uintptr_t)lds;
  STAMP_DECL
  STAMP(0);

  const char* const wq_b = (const char*)wq + head * 256;
  const unsigned wq_v = (unsigned)(((8 * wid + ((l & 31) >> 2)) * 4096 + (l >> 5) * 32 + ((l & 3) ^ (2 * (wid & 1))) * 8) * 2);
  const char* const kv_b = ws + WS_KV + (size_t)(head >> 3) * 262144;
  const unsigned kv_v = (unsigned)(wid * 4096 + l * 16);
  const char* const xn_b = ws + WS_XN + (size_t)rb * 8 * 65536 + 4096;
  const unsigned xn_v = (unsigned)(wid * 65536 + l * 16);
  const unsigned ldsw = lds0 + wid * 2048, ldsk = lds0 + wid * 4096, ldsf = lds0 + FRING + wid * 3072;
  const int frho = l >> 2, fch = (l & 3) ^ (frho & 4 ? 2 : 0);
  const char* fgu = (const char*)(fch & 2 ? wup : wgate) + ((size_t)(128 * wid + 32 * (frho >> 2) + (frho & 3)) * FF + fj * 16 + (fch & 1) * 8) * 2;
  const unsigned fd_v = (unsigned)((frho * DM + wid * 128 + fch * 8) * 2);
  bf16x8 xf[XD];
  u32x4 fm, fx[4];
  unsigned fx_v = 0;

  auto issue = [&](auto Sl) AI {
    constexpr int sl = Sl, q0 = SC.first(sl), nq = SC.count(sl);
    sfor<nq>([&](auto Qi) AI {
      constexpr int k = SC.kind[q0 + Qi], i = SC.idx[q0 + Qi];
      if constexpr (k == OX) gld16<(i & 7) * 1024 - 4096>(xf[i % XD], xn_v, xn_b + (size_t)(i >> 3) * 8192);
      if constexpr (k == OG) {
        constexpr int v = i >> 2, p = i & 3;
        if constexpr (v < 8) {
          const char* sb = wq_b + (size_t)(v * 128 + (p >> 1) * 64) * 8192;
          if constexpr (p & 1) glds<128>(ldsw + (v & 3) * 32768 + (p >> 1) * 16384 + 1024 - 128, wq_v, sb);
          else glds<0>(ldsw + (v & 3) * 32768 + (p >> 1) * 16384, wq_v, sb);
        } else {
          glds<p * 1024>(ldsk + (v & 3) * 32768, kv_v, kv_b + (size_t)(v - 8) * 32768);
        }
      }
      if constexpr (k == OF) {
        if constexpr (i < 8) glds64(ldsf + (i % 3) * 1024, fgu + (size_t)i * 4 * FF * 2);
        else glds<0>(ldsf + (i % 3) * 1024, fd_v, (const char*)wdown + (size_t)fj * 16 * DM * 2 + (i - 8) * 64);
      }
      if constexpr (k == OM) gld16<0>(fm, (unsigned)(l * 16), ws + WS_MX);
      if constexpr (k == OS) gld16<i * 256>(fx[i], fx_v, ws + WS_XN);
      if constexpr (k == OP) glds<0>(lds0 + DUMMY, kv_v, (const char*)wo + (size_t)bid * 32768 + i * 1024);
    });
  };

  LBase trb[2], frb;
  trb[0].init(lds0 + g * 2048 + (c >> 2) * 64 + (g & 1) * 32 + (c & 3) * 8);
  trb[1].init(lds0 + g * 2048 + (c >> 2) * 64 + ((g & 1) ^ 1) * 32 + (c & 3) * 8);
  frb.init(lds0 + l * 16);
  unsigned ftr[2];
  ftr[0] = ldsf + (4 * g + (c >> 2)) * 64 + (g & 1) * 32 + (c & 3) * 8;
  ftr[1] = ftr[0] ^ 32;
  f32x4 fa[2] = {};
  s16x4 hfrag;

  auto side = [&](auto Sl) AI {
    constexpr int sl = Sl, u = sl >> 3, i = sl & 7;
    if constexpr (sl == 9) {
      waitv<SC.younger(OM, 0, sl - 1)>(fm);
      const unsigned key = wave_max_u32(umax(umax(fm[0], fm[1]), umax(fm[2], fm[3])));
      const int idx = SEQ - 1 - (int)(key & 0xffffu);
      fx_v = (unsigned)(((((idx >> 5) * 32 + 4 * wid + g) * 2 + ((idx >> 4) & 1)) * 64 + (idx & 15)) * 16);
    }
    if constexpr (i == 1 && u >= 3 && u <= 10) {
      constexpr int p = u - 3;
      if constexpr (p == 0) { waitv<SC.younger(OS, 3, sl - 1)>(fx[0], fx[1]); PIN(fx[2]); PIN(fx[3]); }
      waitv<SC.younger(OF, p, sl - 1)>();
      const s16x4 bg = trd((LAS const char*)(uintptr_t)ftr[0] + (p % 3) * 1024), bu = trd((LAS const char*)(uintptr_t)ftr[1] + (p % 3) * 1024);
      const u32x2 xa = {fx[p >> 1][2 * (p & 1)], fx[p >> 1][2 * (p & 1) + 1]};
      fa[0] = MFMA16(__builtin_bit_cast(s16x4, xa), bg, fa[0]);
      fa[1] = MFMA16(__builtin_bit_cast(s16x4, xa), bu, fa[1]);
    }
    if constexpr (sl == 8 * 10 + 4) {
      if (l < 32) *(LAS float*)(lds + RED1 + (wid * 32 + l) * 4) = g ? fa[1][0] : fa[0][0];
    }
    if constexpr (sl == 8 * 11 + 1) {
      float gs = 0.f, us = 0.f;
#pragma unroll
      for (int w = 0; w < 2; ++w) { gs += *(LAS const float*)(lds + RED1 + ((2 * g + w) * 32 + c) * 4); us += *(LAS const float*)(lds + RED1 + ((2 * g + w) * 32 + 16 + c) * 4); }
      gs = xg_sum(gs); us = xg_sum(us);
      const float hh = rbf(silu_f(rbf(gs))) * rbf(us);
      *(LAS u16*)(lds + HSC + wid * 64 + c * 2) = (u16)pk2(hh, hh);
    }
    if constexpr (sl == 8 * 11 + 3) { hfrag = *(LAS const s16x4*)(lds + HSC + wid * 64 + g * 8); PIN(hfrag); }
    if constexpr (sl == 8 * 12 + 1 || sl == 8 * 12 + 3 || sl == 8 * 13 + 1 || sl == 8 * 15 + 1) {
      constexpr int p = sl == 8 * 12 + 1 ? 8 : sl == 8 * 12 + 3 ? 9 : sl == 8 * 13 + 1 ? 10 : 11;
      waitv<SC.younger(OF, p, sl - 1)>();
      const s16x4 b0 = trd((LAS const char*)(uintptr_t)ftr[0] + (p % 3) * 1024), b1 = trd((LAS const char*)(uintptr_t)ftr[1] + (p % 3) * 1024);
      const f32x4 y0 = MFMA16(hfrag, b0, (f32x4){}), y1 = MFMA16(hfrag, b1, (f32x4){});
      if (l < 32) *(float*)(ws + WS_YP + ((size_t)(within < 28 ? fj : 224 + xcd * 4 + within - 28) * 1024 + wid * 128 + (p - 8) * 32 + l) * 4) = g ? y1[0] : y0[0];
    }
  };
  auto endslot = [&](auto Sl) AI {
    constexpr int sl = Sl, u = sl >> 3, i = sl & 7;
    side(Sl);
    issue(Sl);
    SBAR();
    if constexpr (sl == 2) waitbar<SC.younger(OG, 3, sl)>();
    if constexpr (i == 6 && u < 15) {
      if constexpr (u == 10) waitbar_l<SC.younger(OG, 4 * u + 7, sl)>();
      else waitbar<SC.younger(OG, 4 * u + 7, sl)>();
      STAMP(2 + u);
    }
  };

  issue(IC(-1));
  f32x4 acc[8][2];
#pragma unroll
  for (int d = 0; d < 8; ++d) { acc[d][0] = (f32x4){}; acc[d][1] = (f32x4){}; }
  bf16x8 wf[2][4];
  auto WRD = [&](auto Si) AI {
    constexpr int s = Si, base = ((s >> 3) & 3) * 32768 + ((s >> 1) & 3) * 8192 + (s & 1) * 1024;
    sfor<4>([&](auto Dd) AI {
      constexpr int d = Dd, o = base + (d >> 1) * 512;
      wf[s & 1][d] = cat8(trd(trb[d & 1].template at<o>()), trd(trb[d & 1].template at<o + 256>()));
    });
  };
  bf16x8 fr[4];
  auto FRD = [&](auto Ni) AI { constexpr int n = Ni; if constexpr (n < 256) fr[n & 3] = *(LAS const bf16x8*)(frb.at<((n >> 5) & 3) * 32768 + (n & 31) * 1024>()); };

  waitbar<SC.younger(OG, 1, -1)>();
  STAMP(1);
  WRD(IC(0));
  SBAR();
  sfor<64>([&](auto Si) AI {
    constexpr int s = Si, x0 = (s & ~1) % XD, x1 = (s | 1) % XD;
    if constexpr (s + 1 < 64) WRD(IC(s + 1));
    else { FRD(IC(0)); FRD(IC(1)); FRD(IC(2)); }
    SBAR();
    if constexpr (!(s & 1)) waitv<SC.younger(OX, s + 1, s - 1)>(xf[x0], xf[x1]);
    sfor<4>([&](auto Dd) AI {
      constexpr int d = Dd, dt = (s & 1) * 4 + d;
      acc[dt][0] = MFMA(wf[s & 1][d], xf[x0], acc[dt][0]);
      acc[dt][1] = MFMA(wf[s & 1][d], xf[x1], acc[dt][1]);
    });
    SBAR();
    endslot(Si);
  });

  bf16x8 qf[4][2];
#pragma unroll
  for (int ds = 0; ds < 4; ++ds)
#pragma unroll
    for (int qb = 0; qb < 2; ++qb) {
      const f32x4 a = acc[2 * ds][qb] * QSCALE, b = acc[2 * ds + 1][qb] * QSCALE;
      qf[ds][qb] = __builtin_bit_cast(bf16x8, (u32x4){pk2(a[0], a[1]), pk2(a[2], a[3]), pk2(b[0], b[1]), pk2(b[2], b[3])});
    }

  f32x4 o[8][2], sc[2][4][2], negm[2];
#pragma unroll
  for (int d = 0; d < 8; ++d) { o[d][0] = (f32x4){}; o[d][1] = (f32x4){}; }
  u32x4 pw[2][2];
  float lsum[2] = {0.f, 0.f}, fpend[2] = {1.f, 1.f};
  bool resc = false;
#define MX3(a, b, c) __builtin_fmaxf(__builtin_fmaxf((a), (b)), (c))
#define EXP2(x) __builtin_amdgcn_exp2f(x)
  auto max8 = [&](float m, const f32x4& a, const f32x4& b) AI { return MX3(MX3(m, a[0], a[1]), MX3(a[2], a[3], b[0]), MX3(b[1], b[2], b[3])); };
  auto check = [&](auto Ci, float mx) AI {
    constexpr int cc = Ci;
    if (__builtin_expect(__any(mx > THR), 0)) {
#pragma unroll
      for (int qb = 0; qb < 2; ++qb) {
        const float dl = xg_max(max8(max8(0.f, sc[cc][0][qb], sc[cc][1][qb]), sc[cc][2][qb], sc[cc][3][qb]));
        fpend[qb] = EXP2(-dl);
        lsum[qb] *= fpend[qb];
#pragma unroll
        for (int kt = 0; kt < 4; ++kt) sc[cc][kt][qb] -= dl;
        negm[qb] -= dl;
      }
      resc = true;
    }
    SBAR();
  };
  {
    float mx = 0.f;
    sfor<16>([&](auto Ni) AI {
      constexpr int n = Ni, kt = n >> 2, ds = n & 3;
      FRD(IC(n + 3));
      sfor<2>([&](auto Qb) AI {
        constexpr int qb = Qb;
        if constexpr (ds == 0) sc[0][kt][qb] = MFMA(fr[n & 3], qf[0][qb], kt < 2 ? (f32x4){} : negm[qb]);
        else sc[0][kt][qb] = MFMA(fr[n & 3], qf[ds][qb], sc[0][kt][qb]);
      });
      if constexpr (n == 5 || n == 6) {
        constexpr int qb = n - 5;
        const float m = -xg_max(__builtin_fmaxf(MX3(sc[0][0][qb][0], sc[0][0][qb][1], sc[0][0][qb][2]), sc[0][0][qb][3]));
        negm[qb] = (f32x4){m, m, m, m};
        PIN(negm[qb]);
      }
      if constexpr (n >= 9 && n <= 12) { constexpr int k2_ = (n - 9) >> 1, qb = (n - 9) & 1; sc[0][k2_][qb] += negm[qb]; PIN(sc[0][k2_][qb]); }
      if constexpr (n == 13) { mx = max8(mx, sc[0][1][0], sc[0][1][1]); PIN(mx); }
      if constexpr (n == 14) { mx = max8(mx, sc[0][2][0], sc[0][2][1]); PIN(mx); }
      SBAR();
      if constexpr (ds == 3) endslot(IC(64 + kt));
    });
    mx = max8(mx, sc[0][3][0], sc[0][3][1]);
    check(IC(0), mx);
    resc = false;
  }
  auto QK = [&](auto Ti, auto N0, auto FIRST) AI {
    constexpr int T = Ti, n0 = N0, cc = T & 1, pv = cc ^ 1;
    float mx = 0.f;
    sfor<16>([&](auto Ii) AI {
      constexpr int i = Ii, n = n0 + i, kt = i >> 2, ds = i & 3, pq = (i >> 1) & 1, pr = 2 * (i & 1);
      FRD(IC(n + 3));
      sfor<2>([&](auto Qb) AI {
        constexpr int qb = Qb;
        if constexpr (ds == 0) sc[cc][kt][qb] = MFMA(fr[n & 3], qf[0][qb], negm[qb]);
        else sc[cc][kt][qb] = MFMA(fr[n & 3], qf[ds][qb], sc[cc][kt][qb]);
      });
      float e0 = sc[pv][kt][pq][pr], e1 = sc[pv][kt][pq][pr + 1];
      if constexpr (FIRST) { e0 = EXP2(e0); e1 = EXP2(e1); }
      lsum[pq] += e0; lsum[pq] += e1;
      pw[kt >> 1][pq][(kt & 1) * 2 + (i & 1)] = pk2(e0, e1);
      PIN(lsum[pq]); PIN(pw[kt >> 1][pq]);
      if constexpr (ds == 2 && kt >= 1) { mx = max8(mx, sc[cc][kt - 1][0], sc[cc][kt - 1][1]); PIN(mx); }
      SBAR();
      if constexpr (ds == 3) endslot(IC(64 + (n >> 2)));
    });
    mx = max8(mx, sc[cc][3][0], sc[cc][3][1]);
    check(IC(cc), mx);
  };
  auto PV = [&](auto Ti, auto N0, auto EXPS) AI {
    constexpr int T = Ti, n0 = N0, cc = (T + 1) & 1;
    sfor<16>([&](auto Ii) AI {
      constexpr int i = Ii, n = n0 + i, dt = i >> 1, ks = i & 1, kt = i >> 2, pq = (i >> 1) & 1, pr = 2 * (i & 1);
      FRD(IC(n + 3));
      o[dt][0] = MFMA(fr[n & 3], __builtin_bit_cast(bf16x8, pw[ks][0]), o[dt][0]);
      o[dt][1] = MFMA(fr[n & 3], __builtin_bit_cast(bf16x8, pw[ks][1]), o[dt][1]);
      if constexpr (EXPS) {
        sc[cc][kt][pq][pr] = EXP2(sc[cc][kt][pq][pr]);
        sc[cc][kt][pq][pr + 1] = EXP2(sc[cc][kt][pq][pr + 1]);
        PIN(sc[cc][kt][pq]);
      }
      SBAR();
      if constexpr ((i & 3) == 3) endslot(IC(64 + (n >> 2)));
    });
    if (__builtin_expect(resc, 0)) {
#pragma unroll
      for (int d = 0; d < 8; ++d) { o[d][0] *= fpend[0]; o[d][1] *= fpend[1]; }
      resc = false;
    }
    SBAR();
  };
  QK(IC(1), IC(16), std::true_type{});
  sfor<6>([&](auto Ti) AI {
    constexpr int T = Ti;
    PV(Ti, IC(32 + 32 * T), std::true_type{});
    QK(IC(T + 2), IC(48 + 32 * T), std::false_type{});
  });
  PV(IC(6), IC(224), std::true_type{});
#pragma unroll
  for (int i = 0; i < 16; ++i) {
    const int kt = i >> 2, pq = (i >> 1) & 1, pr = 2 * (i & 1);
    lsum[pq] += sc[1][kt][pq][pr]; lsum[pq] += sc[1][kt][pq][pr + 1];
    pw[kt >> 1][pq][(kt & 1) * 2 + (i & 1)] = pk2(sc[1][kt][pq][pr], sc[1][kt][pq][pr + 1]);
  }
  PV(IC(7), IC(240), std::false_type{});
  STAMP(17);
  char* const od = ws + WS_O + ((size_t)((rb * 8 + wid) * 128 + head * 4) * 128 + l) * 16;
#pragma unroll
  for (int qb = 0; qb < 2; ++qb) {
    const float inv = __builtin_amdgcn_rcpf(xg_sum(lsum[qb]));
#pragma unroll
    for (int os = 0; os < 4; ++os) {
      const f32x4 a = o[2 * os][qb] * inv, b = o[2 * os + 1][qb] * inv;
      *(u32x4*)(od + (os * 2 + qb) * 1024) = (u32x4){pk2(a[0], a[1]), pk2(a[2], a[3]), pk2(b[0], b[1]), pk2(b[2], b[3])};
    }
  }
  STAMP(18);
  STAMP_OUT(0, 19);
}

namespace k3 {
constexpr int RING = 4 * 32768, PITCH = 68, YS = RING, LDS = RING + 32 * 64 * 4;
constexpr int XD = 16;
constexpr Sched make_sched() {
  Sched s;
  s.add(-1, OE, 0); s.add(-1, OE, 1); s.add(-1, OE, 2); s.add(-1, OM, 0);
  for (int i = 0; i < 4; ++i) s.add(-1, OG, i);
  for (int i = 0; i < 8; ++i) s.add(-1, OX, i);
  for (int i = 4; i < 8; ++i) s.add(-1, OG, i);
  for (int i = 8; i < XD; ++i) s.add(-1, OX, i);
  for (int t = 0; t < 64; ++t) {
    if (t < 4) { s.add(t, OG, 8 + 2 * t); s.add(t, OG, 9 + 2 * t); }
    if (2 * t + XD < 128) { s.add(t, OX, 2 * t + XD); s.add(t, OX, 2 * t + 1 + XD); }
    for (int v = 4; v < 16; ++v) for (int p = 0; p < 4; ++p) if (4 * (v - 3) + 3 + p == t) s.add(t, OG, 4 * v + p);
  }
  return s;
}
constexpr Sched SC = make_sched();
}

__global__ __launch_bounds__(512, 2) void k3_out(const u16* __restrict__ x, const u16* __restrict__ wo, const char* __restrict__ ws,
                                                 u16* __restrict__ out) {
  using namespace k3;
  __shared__ __attribute__((aligned(16))) char lds_raw[LDS];
  LAS char* const lds = (LAS char*)lds_raw;
  const int t = threadIdx.x, l = t & 63, bid = blockIdx.x, c = l & 15, g = l >> 4;
  const int wid = __builtin_amdgcn_readfirstlane(t >> 6);
  const int xcd = bid & 7, within = bid >> 3, mb = 4 * (xcd >> 1) + (within >> 3), nb = 8 * (xcd & 1) + (within & 7);
  const int rbw = wid & 3, kg = wid >> 2;
  const unsigned lds0 = (unsigned)(uintptr_t)lds;
  STAMP_DECL
  STAMP(0);

  const char* const wo_b = (const char*)wo + nb * 128;
  const unsigned wo_v0 = (unsigned)(((32 * wid + ((l & 31) >> 2)) * DM + (l >> 5) * 32 + (l & 3) * 8) * 2), wo_v1 = wo_v0 ^ 32;
  const unsigned ldsw = lds0 + wid * 4096;
  const char* const o_b = ws + WS_O + (size_t)mb * 4 * 262144 + 4096;
  const unsigned o_v = (unsigned)(rbw * 262144 + kg * 8192 + l * 16);
  bf16x8 xf[XD];
  const int erow = mb * 128 + (t >> 2), ecol = nb * 64 + (t & 3) * 16;
  u32x4 fm, xe[2];
  f32x2 st;
  auto issue = [&](auto Sl) AI {
    constexpr int sl = Sl, q0 = SC.first(sl), nq = SC.count(sl);
    sfor<nq>([&](auto Qi) AI {
      constexpr int k = SC.kind[q0 + Qi], i = SC.idx[q0 + Qi];
      if constexpr (k == OX) gld16<(i & 7) * 1024 - 4096>(xf[i % XD], o_v, o_b + (size_t)(i >> 3) * 16384);
      if constexpr (k == OG) { constexpr int v = i >> 2, p = i & 3; glds<0>(ldsw + (v & 3) * 32768 + p * 1024, (p & 1) ? wo_v1 : wo_v0, wo_b + (size_t)(v * 256 + p * 8) * (DM * 2)); }
      if constexpr (k == OM) gld16<0>(fm, (unsigned)(l * 16), ws + WS_MX);
      if constexpr (k == OE && i < 2) gld16<16 * i>(xe[i], (unsigned)((erow * DM + ecol) * 2), x);
      if constexpr (k == OE && i == 2) gld8<0>(st, (unsigned)(erow * 8), ws + WS_ST);
    });
  };
  issue(IC(-1));

  unsigned xnp[8], gate[8];
  waitv<SC.younger(OM, 0, -1)>(xe[0], xe[1]);
  PIN(st); PIN(fm);
#pragma unroll
  for (int j = 0; j < 8; ++j) {
    const unsigned xw = xe[j >> 2][j & 3];
    const unsigned dd = pk2(bflo(xw) - st[0], bfhi(xw) - st[0]);
    xnp[j] = pk2(bflo(dd) * st[1], bfhi(dd) * st[1]);
    gate[j] = pk2(silu_f(bflo(xnp[j])), silu_f(bfhi(xnp[j])));
  }
  const unsigned key = wave_max_u32(umax(umax(fm[0], fm[1]), umax(fm[2], fm[3])));
  const int idx = SEQ - 1 - (int)(key & 0xffffu);
  const bool owner = (idx >> 7) == mb;
  f32x4 yp[7];
  if (owner) {
    const unsigned yv = (unsigned)(((t >> 4) * 1024 + nb * 64 + (t & 15) * 4) * 4);
    sfor<7>([&](auto I) AI { constexpr int q = I; gld16<0>(yp[q], yv, ws + WS_YP + (size_t)q * 32 * 4096); });
  }

  f32x4 acc[4][2];
#pragma unroll
  for (int d = 0; d < 4; ++d) { acc[d][0] = (f32x4){}; acc[d][1] = (f32x4){}; }
  LBase trb[2];
  trb[0].init(lds0 + kg * 16384 + g * 1024 + (c >> 2) * 64 + (g & 1) * 32 + (c & 3) * 8);
  trb[1].init(lds0 + kg * 16384 + g * 1024 + (c >> 2) * 64 + ((g & 1) ^ 1) * 32 + (c & 3) * 8);
  bf16x8 wf[2][4];
  auto WRD = [&](auto Si) AI {
    constexpr int s = Si, base = ((s >> 2) & 3) * 32768 + (s & 3) * 4096;
    sfor<4>([&](auto Dd) AI {
      constexpr int d = Dd, o = base + (d >> 1) * 512;
      wf[s & 1][d] = cat8(trd(trb[d & 1].template at<o>()), trd(trb[d & 1].template at<o + 256>()));
    });
  };

  waitbar<SC.younger(OG, 3, -1)>();
  STAMP(1);
  WRD(IC(0));
  SBAR();
  sfor<64>([&](auto Si) AI {
    constexpr int s = Si, u = s >> 2, x0 = (2 * s) % XD, x1 = (2 * s + 1) % XD;
    if constexpr (s + 1 < 64) WRD(IC(s + 1));
    SBAR();
    waitv<SC.younger(OX, 2 * s + 1, s - 1)>(xf[x0], xf[x1]);
    sfor<4>([&](auto Dd) AI {
      constexpr int d = Dd;
      acc[d][0] = MFMA(wf[s & 1][d], xf[x0], acc[d][0]);
      acc[d][1] = MFMA(wf[s & 1][d], xf[x1], acc[d][1]);
    });
    SBAR();
    issue(Si);
    SBAR();
    if constexpr ((s & 3) == 2 && u < 15) { waitbar<SC.younger(OG, 4 * u + 7, s)>(); STAMP(2 + u); }
  });
  waitbar<0>();
  STAMP(17);

  {
    LAS char* dst = lds + ((kg * 128 + rbw * 32 + c) * PITCH + 4 * g) * 4;
#pragma unroll
    for (int d = 0; d < 4; ++d) { *(LAS f32x4*)(dst + d * 64) = acc[d][0]; *(LAS f32x4*)(dst + 16 * PITCH * 4 + d * 64) = acc[d][1]; }
  }
  if (owner) {
    f32x4 s = {};
#pragma unroll
    for (int q = 0; q < 7; ++q) { PIN(yp[q]); s += yp[q]; }
    *(LAS f32x4*)(lds + YS + ((t >> 4) * 64 + (t & 15) * 4) * 4) = s;
  }
  __syncthreads();
  if (owner && erow == idx) {
    const unsigned ord = key >> 16, bits = (ord & 0x8000u) ? (ord & 0x7fffu) : (~ord & 0xffffu);
    const float sg = rbf(sigmoid_f(bflo(bits)));
#pragma unroll
    for (int q = 0; q < 4; ++q) {
      f32x4 a = {};
#pragma unroll 8
      for (int p = 0; p < 32; ++p) a += *(LAS const f32x4*)(lds + YS + (p * 64 + (t & 3) * 16 + q * 4) * 4);
#pragma unroll
      for (int j = 0; j < 2; ++j) {
        const float lo = rbf(bflo(xnp[2 * q + j]) + rbf(rbf(a[2 * j]) * sg)), hi = rbf(bfhi(xnp[2 * q + j]) + rbf(rbf(a[2 * j + 1]) * sg));
        gate[2 * q + j] = pk2(silu_f(lo), silu_f(hi));
      }
    }
  }
  u32x4 res[2];
  {
    LAS const char* src = lds + ((t >> 2) * PITCH + (t & 3) * 16) * 4;
#pragma unroll
    for (int q = 0; q < 4; ++q) {
      const f32x4 a = *(LAS const f32x4*)(src + q * 16) + *(LAS const f32x4*)(src + 128 * PITCH * 4 + q * 16);
#pragma unroll
      for (int j = 0; j < 2; ++j) {
        const unsigned sp = pk2(a[2 * j], a[2 * j + 1]), gp = gate[2 * q + j], xw = xe[q >> 1][(q & 1) * 2 + j];
        const unsigned pr = pk2(bflo(sp) * bflo(gp), bfhi(sp) * bfhi(gp));
        res[q >> 1][(q & 1) * 2 + j] = pk2(bflo(xw) + bflo(pr), bfhi(xw) + bfhi(pr));
      }
    }
  }
  u16* op = out + (size_t)erow * DM + ecol;
  *(u32x4*)op = res[0];
  *(u32x4*)(op + 8) = res[1];
  STAMP(18);
  STAMP_OUT(1, 19);
}

extern "C" void kernel_launch(void* const* d_in, const int* in_sizes, int n_in, void* d_out, int out_size, void* d_ws, size_t ws_size,
                              hipStream_t stream) {
  const u16* x = (const u16*)d_in[0];
  const u16* kc = (const u16*)d_in[1];
  const u16* vc = (const u16*)d_in[2];
  const u16* wq = (const u16*)d_in[3];
  const u16* wo = (const u16*)d_in[4];
  const u16* wr = (const u16*)d_in[6];
  const u16* wg = (const u16*)d_in[7];
  const u16* wu = (const u16*)d_in[8];
  const u16* wd = (const u16*)d_in[9];
  char* ws = (char*)d_ws;
  k1_prep<<<256, 768, 0, stream>>>(x, kc, vc, wr, wq, ws);
  k2_attn<<<256, 512, 0, stream>>>(wq, wg, wu, wd, wo, ws);
  k3_out<<<256, 512, 0, stream>>>(x, wo, ws, (u16*)d_out);
}
```

```cpp
#include <hip/hip_runtime.h>
#include <stdint.h>
#include <type_traits>

typedef unsigned short u16;
typedef short bf16x8 __attribute__((ext_vector_type(8)));
typedef short s16x4 __attribute__((ext_vector_type(4)));
typedef float f32x4 __attribute__((ext_vector_type(4)));
typedef float f32x2 __attribute__((ext_vector_type(2)));
typedef unsigned u32x4 __attribute__((ext_vector_type(4)));
typedef unsigned u32x2 __attribute__((ext_vector_type(2)));
typedef __bf16 bf2_t __attribute__((ext_vector_type(2)));
#define LAS __attribute__((address_space(3)))
#define DI __device__ __forceinline__
#define AI __attribute__((always_inline))
#define SBAR() __builtin_amdgcn_sched_barrier(0)
#define PIN(x) asm volatile("" : "+v"(x))
#define MFMA(a, b, c) __builtin_amdgcn_mfma_f32_16x16x32_bf16((a), (b), (c), 0, 0, 0)
#define MFMA16(a, b, c) __builtin_amdgcn_mfma_f32_16x16x16bf16_1k((a), (b), (c), 0, 0, 0)
#define IC(n) std::integral_constant<int, (n)>{}

constexpr int SEQ = 2048, DM = 1024, DH = 128, SK = 2048, WIN = 512, FF = 3584;
constexpr size_t WS_XN = 0;
constexpr size_t WS_KV = 4u << 20;
constexpr size_t WS_ST = 5u << 20;
constexpr size_t WS_MX = WS_ST + 16384;
constexpr size_t WS_YP = WS_ST + 65536;
constexpr size_t WS_FL = WS_ST + 32768;
constexpr size_t WS_XC = 24u << 20;
constexpr size_t WS_O = 8u << 20;

template <int I, int N, class F> DI void sfor_impl(F&& f) {
  if constexpr (I < N) { f(std::integral_constant<int, I>{}); sfor_impl<I + 1, N>(f); }
}
template <int N, class F> DI void sfor(F&& f) { sfor_impl<0, N>(f); }

DI unsigned pk2(float a, float b) { f32x2 v = {a, b}; return __builtin_bit_cast(unsigned, __builtin_convertvector(v, bf2_t)); }
DI float rbf(float a) { return (float)(__bf16)a; }
DI float bflo(unsigned u) { return __uint_as_float(u << 16); }
DI float bfhi(unsigned u) { return __uint_as_float(u & 0xffff0000u); }
DI float sigmoid_f(float v) { return __builtin_amdgcn_rcpf(1.f + __builtin_amdgcn_exp2f(v * -1.4426950408889634f)); }
DI float sel2(bool c, float a, float b) { PIN(a); PIN(b); return c ? a : b; }
DI float silu_f(float v) { return v * sigmoid_f(v); }
template <int CTRL> DI float dppf(float x) { return __builtin_bit_cast(float, __builtin_amdgcn_mov_dpp(__builtin_bit_cast(int, x), CTRL, 0xf, 0xf, true)); }
template <int CTRL> DI unsigned dppu(unsigned x) { return (unsigned)__builtin_amdgcn_mov_dpp((int)x, CTRL, 0xf, 0xf, true); }
constexpr int XOR1 = 0xB1, XOR2 = 0x4E, XOR7 = 0x141, XOR8 = 0x128;
DI float xg_sum(float x) {
  auto s = __builtin_amdgcn_permlane16_swap(__float_as_uint(x), __float_as_uint(x), false, false);
  x = __uint_as_float(s[0]) + __uint_as_float(s[1]);
  auto t = __builtin_amdgcn_permlane32_swap(__float_as_uint(x), __float_as_uint(x), false, false);
  return __uint_as_float(t[0]) + __uint_as_float(t[1]);
}
DI float xg_max(float x) {
  auto s = __builtin_amdgcn_permlane16_swap(__float_as_uint(x), __float_as_uint(x), false, false);
  x = __builtin_fmaxf(__uint_as_float(s[0]), __uint_as_float(s[1]));
  auto t = __builtin_amdgcn_permlane32_swap(__float_as_uint(x), __float_as_uint(x), false, false);
  return __builtin_fmaxf(__uint_as_float(t[0]), __uint_as_float(t[1]));
}
DI float wave_sum(float v) { v += dppf<XOR1>(v); v += dppf<XOR2>(v); v += dppf<XOR7>(v); v += dppf<XOR8>(v); return xg_sum(v); }
DI unsigned umax(unsigned a, unsigned b) { return a > b ? a : b; }
DI unsigned wave_max_u32(unsigned v) {
  v = umax(v, dppu<XOR1>(v)); v = umax(v, dppu<XOR2>(v)); v = umax(v, dppu<XOR7>(v)); v = umax(v, dppu<XOR8>(v));
  auto s = __builtin_amdgcn_permlane16_swap(v, v, false, false); v = umax(s[0], s[1]);
  auto t = __builtin_amdgcn_permlane32_swap(v, v, false, false); v = umax(t[0], t[1]);
  return __builtin_amdgcn_readfirstlane(v);
}

template <int OFF> DI void glds(unsigned ldsaddr, unsigned voff, const void* sbase) {
  asm volatile("s_mov_b32 m0, %0\n\ts_nop 0\n\tglobal_load_lds_dwordx4 %1, %2 offset:%3" ::"s"(ldsaddr), "v"(voff), "s"(sbase), "i"(OFF) : "memory");
}
DI void glds64(unsigned ldsaddr, const void* vaddr) {
  asm volatile("s_mov_b32 m0, %0\n\ts_nop 0\n\tglobal_load_lds_dwordx4 %1, off" ::"s"(ldsaddr), "v"(vaddr) : "memory");
}
template <int OFF, class T> DI void gld16(T& dst, unsigned voff, const void* sbase) {
  asm volatile("global_load_dwordx4 %0, %1, %2 offset:%3" : "=v"(dst) : "v"(voff), "s"(sbase), "i"(OFF) : "memory");
}
template <int OFF, class T> DI void gld8(T& dst, unsigned voff, const void* sbase) {
  asm volatile("global_load_dwordx2 %0, %1, %2 offset:%3" : "=v"(dst) : "v"(voff), "s"(sbase), "i"(OFF) : "memory");
}
template <int N> DI void waitv() { static_assert(N >= 0, "count"); asm volatile("s_waitcnt vmcnt(%0)" ::"n"(N > 63 ? 63 : N) : "memory"); }
template <int N, class T> DI void waitv(T& x) { static_assert(N >= 0, "count"); asm volatile("s_waitcnt vmcnt(%1)" : "+v"(x) : "n"(N > 63 ? 63 : N) : "memory"); }
template <int N, class T> DI void waitv(T& x, T& y) { static_assert(N >= 0, "count"); asm volatile("s_waitcnt vmcnt(%2)" : "+v"(x), "+v"(y) : "n"(N > 63 ? 63 : N) : "memory"); }
template <int N> DI void waitbar() { static_assert(N >= 0, "count"); asm volatile("s_waitcnt vmcnt(%0)\n\ts_barrier" ::"n"(N > 63 ? 63 : N) : "memory"); }
template <int N> DI void waitbar_l() { static_assert(N >= 0, "count"); asm volatile("s_waitcnt vmcnt(%0) lgkmcnt(0)\n\ts_barrier" ::"n"(N > 63 ? 63 : N) : "memory"); }
typedef short v4i16_t __attribute__((ext_vector_type(4)));
DI s16x4 trd(LAS const char* p) { return __builtin_amdgcn_ds_read_tr16_b64_v4i16((LAS v4i16_t*)p); }
struct LBase {
  unsigned lo, hi;
  DI void init(unsigned a) { lo = a; hi = a + 65536; PIN(lo); PIN(hi); }
  template <int OFF> DI LAS const char* at() const { return (LAS const char*)(uintptr_t)(OFF < 65536 ? lo : hi) + (OFF & 65535); }
};
DI bf16x8 cat8(s16x4 lo, s16x4 hi) { return __builtin_shufflevector(lo, hi, 0, 1, 2, 3, 4, 5, 6, 7); }

enum { OX, OG, OF, OM, OS, OP, OE };
struct Sched {
  int n; short kind[320], idx[320], slot[320];
  constexpr Sched() : n(0), kind{}, idx{}, slot{} {}
  constexpr void add(int s, int k, int i) { kind[n] = (short)k; idx[n] = (short)i; slot[n] = (short)s; ++n; }
  constexpr int younger(int k, int i, int s) const {
    int p = -1;
    for (int q = 0; q < n; ++q) if (kind[q] == k && idx[q] == i) p = q;
    if (p < 0 || slot[p] > s) return -1;
    int c = 0;
    for (int q = p + 1; q < n; ++q) if (slot[q] <= s) ++c;
    return c;
  }
  constexpr int first(int s) const { int q = 0; while (q < n && slot[q] < s) ++q; return q; }
  constexpr int count(int s) const { int c = 0; for (int q = 0; q < n; ++q) if (slot[q] == s) ++c; return c; }
};

#define STAMP_DECL
#define STAMP(i)
#define STAMP_OUT(kid, n)
__global__ __launch_bounds__(768) void k1_prep(const u16* __restrict__ x, const u16* __restrict__ kc, const u16* __restrict__ vc,
                                               const u16* __restrict__ wr, const u16* __restrict__ wq, char* __restrict__ ws) {
  __shared__ __attribute__((aligned(16))) char stage[16448 + 1024];
  const int t = threadIdx.x, w = t >> 6, l = t & 63, b = blockIdx.x;
  if (t >= 512) {
    const int tt = t - 512;
    const int ch = b * 256 + tt, kvh = ch >> 14, n = (ch >> 6) & 255, c = l & 15, g = l >> 4;
    int isv, un, i;
    if (n < 16) { isv = 0; un = n >> 3; i = n & 7; }
    else if (n >= 240) { isv = 1; un = 14 + ((n - 240) >> 3); i = n & 7; }
    else { const int m = n - 16; isv = m & 1; un = (m >> 4) + (isv ? 0 : 2); i = (m & 15) >> 1; }
    u32x4 o;
    if (!isv) {
      const u16* p = kc + ((size_t)kvh * SK + (SK - WIN) + un * 32 + (i >> 2) * 16 + c) * DH + (i & 3) * 32 + 4 * g;
      const u32x2 lo = *(const u32x2*)p, hi = *(const u32x2*)(p + 16);
      o = (u32x4){lo[0], lo[1], hi[0], hi[1]};
    } else {
      const int d = 32 * (i >> 1) + 8 * (c >> 2) + 4 * (i & 1) + (c & 3);
      const u16* p = vc + ((size_t)kvh * SK + (SK - WIN) + un * 32 + 4 * g) * DH + d;
      unsigned e[8];
#pragma unroll
      for (int j = 0; j < 8; ++j) e[j] = p[(16 * (j >> 2) + (j & 3)) * DH];
      o = (u32x4){e[0] | (e[1] << 16), e[2] | (e[3] << 16), e[4] | (e[5] << 16), e[6] | (e[7] << 16)};
    }
    *(u32x4*)(ws + WS_KV + (size_t)ch * 16) = o;
    if (tt == 0) *(unsigned*)(ws + WS_FL + b * 128) = 0u;
    return;
  }
#define PFN 4
  STAMP_DECL
  STAMP(0);
  const int row = 8 * b + w;
  u32x4 a0, a1, w0, w1;
  gld16<0>(a0, (unsigned)(row * DM * 2 + l * 16), x); gld16<1024>(a1, (unsigned)(row * DM * 2 + l * 16), x);
  gld16<0>(w0, (unsigned)(l * 16), wr); gld16<1024>(w1, (unsigned)(l * 16), wr);
  {
    const unsigned po = (unsigned)(((b >> 3) * 8 + w) * 8192 + (b & 7) * 1024 + l * 16);
    sfor<PFN>([&](auto I) AI { glds<0>((unsigned)(uintptr_t)(LAS char*)stage + 16448, po, (const char*)wq + (size_t)I * 256 * 8192); });
  }
  waitv<PFN>(a0, a1);
  PIN(w0); PIN(w1);
  float v[16];
#pragma unroll
  for (int j = 0; j < 4; ++j) { v[2 * j] = bflo(a0[j]); v[2 * j + 1] = bfhi(a0[j]); v[8 + 2 * j] = bflo(a1[j]); v[9 + 2 * j] = bfhi(a1[j]); }
  float s = 0.f;
#pragma unroll
  for (int j = 0; j < 16; ++j) s += v[j];
  PIN(s);
  STAMP(1);
  const float mean = wave_sum(s) * (1.f / DM);
  float q = 0.f;
#pragma unroll
  for (int j = 0; j < 16; ++j) { const float d = v[j] - mean; q += d * d; }
  const float var = wave_sum(q) * (1.f / DM);
  const float mb = rbf(mean), rden = 1.f / rbf(sqrtf(rbf(rbf(var) + 1e-5f)));
  float rr = 0.f;
  u32x4 o0, o1;
#pragma unroll
  for (int j = 0; j < 8; ++j) {
    const unsigned dd = pk2(v[2 * j] - mb, v[2 * j + 1] - mb);
    const unsigned nn = pk2(bflo(dd) * rden, bfhi(dd) * rden);
    const unsigned ww = j < 4 ? w0[j] : w1[j - 4];
    rr += bflo(nn) * bflo(ww); rr += bfhi(nn) * bfhi(ww);
    if (j < 4) o0[j] = nn; else o1[j - 4] = nn;
  }
  *(u32x4*)(stage + (l * 8 + (w ^ (l & 7))) * 16) = o0;
  *(u32x4*)(stage + 8192 + (l * 8 + (w ^ (l & 7))) * 16) = o1;
  rr = wave_sum(rr);
  if (l == 0) {
    const unsigned bits = pk2(rr, 0.f) & 0xffffu;
    const unsigned ord = (bits & 0x8000u) ? (~bits & 0xffffu) : (bits | 0x8000u);
    *(unsigned*)(stage + 16384 + w * 4) = (ord << 16) | (unsigned)(SEQ - 1 - row);
    *(f32x2*)(ws + WS_ST + row * 8) = (f32x2){mb, rden};
  }
  STAMP(2);
  asm volatile("s_waitcnt lgkmcnt(0)\n\ts_barrier" ::: "memory");
  STAMP(3);
#pragma unroll
  for (int hh = 0; hh < 2; ++hh) {
    const int ch = hh * 64 + (t >> 3), r = 8 * b + ((t & 7) ^ (ch & 7));
    const u32x4 o = *(const u32x4*)(stage + hh * 8192 + t * 16);
    *(u32x4*)(ws + WS_XN + ((size_t)(((r >> 5) * 32 + (ch >> 2)) * 2 + ((r >> 4) & 1)) * 64 + (ch & 3) * 16 + (r & 15)) * 16) = o;
  }
  if (t == 0) {
    const u32x4 ka = *(const u32x4*)(stage + 16384), kb = *(const u32x4*)(stage + 16400);
    *(unsigned*)(ws + WS_MX + b * 4) = umax(umax(umax(ka[0], ka[1]), umax(ka[2], ka[3])), umax(umax(kb[0], kb[1]), umax(kb[2], kb[3])));
  }
  STAMP(4);
  STAMP_OUT(2, 5);
}

namespace k2 {
constexpr int RING = 4 * 32768, FRING = RING, RED1 = FRING + 8 * 3072, HSC = RED1 + 1024, DUMMY = HSC + 512, LDS = DUMMY + 1024;
constexpr int XD = 16;
constexpr float LBOUND = 65536.f;
constexpr float QSCALE = 0.08838834764831845f * 1.4426950408889634f;
constexpr Sched make_sched() {
  Sched s;
  s.add(-1, OG, 0); s.add(-1, OG, 1);
  for (int i = 0; i < 4; ++i) s.add(-1, OX, i);
  s.add(-1, OG, 2); s.add(-1, OG, 3);
  for (int i = 4; i < 8; ++i) s.add(-1, OX, i);
  s.add(-1, OM, 0);
  for (int t = 0; t < 128; ++t) {
    if (t == 1 || t == 2) { s.add(t, OX, 6 + 2 * t); s.add(t, OX, 7 + 2 * t); }
    if (t == 4 || t == 5) { s.add(t, OX, 4 + 2 * t); s.add(t, OX, 5 + 2 * t); }
    for (int p = 0; p < 24; ++p) { const int a = 6 + p, b2 = 2 * p + 1; if ((a > b2 ? a : b2) == t) { s.add(t, OX, 16 + 2 * p); s.add(t, OX, 17 + 2 * p); } }
    if (t == 0) { s.add(t, OG, 4); s.add(t, OG, 5); }
    if (t == 3) { s.add(t, OG, 6); s.add(t, OG, 7); }
    if (t >= 6 && t <= 9) s.add(t, OG, 8 + t - 6);
    if (t >= 11 && t <= 14) s.add(t, OG, 12 + t - 11);
    for (int v = 4; v < 16; ++v) for (int p = 0; p < 4; ++p) if (8 * (v - 3) + 7 + p == t) s.add(t, OG, 4 * v + p);
    if ((t & 7) == 4 && (t >> 3) <= 10) s.add(t, OF, t >> 3);
    if (t == 8 * 12 + 4) s.add(t, OF, 11);
    if (t == 10) for (int i = 0; i < 4; ++i) s.add(t, OS, i);
    if (t >= 112 && t < 116) s.add(t, OP, t - 112);
  }
  return s;
}
constexpr Sched SC = make_sched();
}

__global__ __launch_bounds__(512, 2) void k2_attn(const u16* __restrict__ wq, const u16* __restrict__ wgate, const u16* __restrict__ wup,
                                                  const u16* __restrict__ wdown, const u16* __restrict__ wo, char* __restrict__ ws) {
  using namespace k2;
  __shared__ __attribute__((aligned(16))) char lds_raw[LDS];
  LAS char* const lds = (LAS char*)lds_raw;
  const int t = threadIdx.x, l = t & 63, bid = blockIdx.x, c = l & 15, g = l >> 4;
  const int wid = __builtin_amdgcn_readfirstlane(t >> 6);
  const int xcd = bid & 7, within = bid >> 3, head = xcd * 4 + (within & 3), rb = within >> 2;
  const int fj = xcd * 28 + (within < 28 ? within : within - 28);
  const unsigned lds0 = (unsigned)(uintptr_t)lds;
  STAMP_DECL
  STAMP(0);

  const char* const wq_b = (const char*)wq + head * 256;
  const unsigned wq_v = (unsigned)(((8 * wid + ((l & 31) >> 2)) * 4096 + (l >> 5) * 32 + ((l & 3) ^ (2 * (wid & 1))) * 8) * 2);
  const char* const kv_b = ws + WS_KV + (size_t)(head >> 3) * 262144;
  const unsigned kv_v = (unsigned)(wid * 4096 + l * 16);
  const char* const xn_b = ws + WS_XN + (size_t)rb * 8 * 65536 + 4096;
  const unsigned xn_v = (unsigned)(wid * 65536 + l * 16);
  const unsigned ldsw = lds0 + wid * 2048, ldsk = lds0 + wid * 4096, ldsf = lds0 + FRING + wid * 3072;
  const int frho = l >> 2, fch = (l & 3) ^ (frho & 4 ? 2 : 0);
  const char* fgu = (const char*)(fch & 2 ? wup : wgate) + ((size_t)(128 * wid + 32 * (frho >> 2) + (frho & 3)) * FF + fj * 16 + (fch & 1) * 8) * 2;
  const unsigned fd_v = (unsigned)((frho * DM + wid * 128 + fch * 8) * 2);
  bf16x8 xf[XD];
  u32x4 fm, fx[4];
  unsigned fx_v = 0;

  auto issue = [&](auto Sl) AI {
    constexpr int sl = Sl, q0 = SC.first(sl), nq = SC.count(sl);
    sfor<nq>([&](auto Qi) AI {
      constexpr int k = SC.kind[q0 + Qi], i = SC.idx[q0 + Qi];
      if constexpr (k == OX) gld16<(i & 7) * 1024 - 4096>(xf[i % XD], xn_v, xn_b + (size_t)(i >> 3) * 8192);
      if constexpr (k == OG) {
        constexpr int v = i >> 2, p = i & 3;
        if constexpr (v < 8) {
          const char* sb = wq_b + (size_t)(v * 128 + (p >> 1) * 64) * 8192;
          if constexpr (p & 1) glds<128>(ldsw + (v & 3) * 32768 + (p >> 1) * 16384 + 1024 - 128, wq_v, sb);
          else glds<0>(ldsw + (v & 3) * 32768 + (p >> 1) * 16384, wq_v, sb);
        } else {
          glds<p * 1024>(ldsk + (v & 3) * 32768, kv_v, kv_b + (size_t)(v - 8) * 32768);
        }
      }
      if constexpr (k == OF) {
        if constexpr (i < 8) glds64(ldsf + (i % 3) * 1024, fgu + (size_t)i * 4 * FF * 2);
        else glds<0>(ldsf + (i % 3) * 1024, fd_v, (const char*)wdown + (size_t)fj * 16 * DM * 2 + (i - 8) * 64);
      }
      if constexpr (k == OM) gld16<0>(fm, (unsigned)(l * 16), ws + WS_MX);
      if constexpr (k == OS) gld16<i * 256>(fx[i], fx_v, ws + WS_XN);
      if constexpr (k == OP) glds<0>(lds0 + DUMMY, kv_v, (const char*)wo + (size_t)bid * 32768 + i * 1024);
    });
  };

  LBase trb[2], frb;
  trb[0].init(lds0 + g * 2048 + (c >> 2) * 64 + (g & 1) * 32 + (c & 3) * 8);
  trb[1].init(lds0 + g * 2048 + (c >> 2) * 64 + ((g & 1) ^ 1) * 32 + (c & 3) * 8);
  frb.init(lds0 + l * 16);
  unsigned ftr[2];
  ftr[0] = ldsf + (4 * g + (c >> 2)) * 64 + (g & 1) * 32 + (c & 3) * 8;
  ftr[1] = ftr[0] ^ 32;
  f32x4 fa0 = {}, fa1 = {};
  s16x4 hfrag, fb[2];

  auto side = [&](auto Sl) AI {
    constexpr int sl = Sl, u = sl >> 3, i = sl & 7;
    if constexpr (sl == 9) {
      waitv<SC.younger(OM, 0, sl - 1)>(fm);
      const unsigned key = wave_max_u32(umax(umax(fm[0], fm[1]), umax(fm[2], fm[3])));
      const int idx = SEQ - 1 - (int)(key & 0xffffu);
      fx_v = (unsigned)(((((idx >> 5) * 32 + 4 * wid + g) * 2 + ((idx >> 4) & 1)) * 64 + (idx & 15)) * 16);
    }
    if constexpr (i == 1 && u >= 3 && u <= 10) {
      constexpr int p = u - 3;
      if constexpr (p == 0) { waitv<SC.younger(OS, 3, sl - 1)>(fx[0], fx[1]); PIN(fx[2]); PIN(fx[3]); }
      waitv<SC.younger(OF, p, sl - 1)>();
      fb[0] = trd((LAS const char*)(uintptr_t)ftr[0] + (p % 3) * 1024); fb[1] = trd((LAS const char*)(uintptr_t)ftr[1] + (p % 3) * 1024);
    }
    if constexpr (i == 2 && u >= 3 && u <= 10) {
      constexpr int p = u - 3;
      const u32x2 xa = {fx[p >> 1][2 * (p & 1)], fx[p >> 1][2 * (p & 1) + 1]};
      fa0 = MFMA16(__builtin_bit_cast(s16x4, xa), fb[0], fa0);
      fa1 = MFMA16(__builtin_bit_cast(s16x4, xa), fb[1], fa1);
    }
    if constexpr (sl == 8 * 10 + 4) *(LAS float*)(lds + RED1 + (wid * 32 + (l & 31)) * 4) = sel2(g & 1, fa1[0], fa0[0]);
    if constexpr (sl == 8 * 11 + 0) {
#pragma unroll
      for (int w = 0; w < 2; ++w) { fa0[w] = *(LAS const float*)(lds + RED1 + ((2 * g + w) * 32 + c) * 4); fa1[w] = *(LAS const float*)(lds + RED1 + ((2 * g + w) * 32 + 16 + c) * 4); }
    }
    if constexpr (sl == 8 * 11 + 1) { fa0[0] = xg_sum(fa0[0] + fa0[1]); fa1[0] = xg_sum(fa1[0] + fa1[1]); PIN(fa0); PIN(fa1); }
    if constexpr (sl == 8 * 11 + 2) {
      const float hh = rbf(silu_f(rbf(fa0[0]))) * rbf(fa1[0]);
      *(LAS u16*)(lds + HSC + wid * 64 + c * 2) = (u16)pk2(hh, hh);
    }
    if constexpr (sl == 8 * 11 + 3) hfrag = *(LAS const s16x4*)(lds + HSC + wid * 64 + g * 8);
    constexpr int dn = sl == 8 * 12 + 0 ? 8 : sl == 8 * 12 + 4 ? 9 : sl == 8 * 13 + 0 ? 10 : sl == 8 * 14 + 4 ? 11 : 0;
    if constexpr (dn) {
      waitv<SC.younger(OF, dn, sl - 1)>();
      fb[0] = trd((LAS const char*)(uintptr_t)ftr[0] + (dn % 3) * 1024); fb[1] = trd((LAS const char*)(uintptr_t)ftr[1] + (dn % 3) * 1024);
    }
    constexpr int dm = sl == 8 * 12 + 1 ? 8 : sl == 8 * 12 + 5 ? 9 : sl == 8 * 13 + 1 ? 10 : sl == 8 * 14 + 5 ? 11 : 0;
    if constexpr (dm) { fa0 = MFMA16(hfrag, fb[0], (f32x4){}); fa1 = MFMA16(hfrag, fb[1], (f32x4){}); }
    constexpr int dw = sl == 8 * 12 + 2 ? 8 : sl == 8 * 12 + 6 ? 9 : sl == 8 * 13 + 2 ? 10 : sl == 8 * 14 + 6 ? 11 : 0;
    if constexpr (dw) *(float*)(ws + WS_YP + ((size_t)(within < 28 ? fj : 224 + xcd * 4 + within - 28) * 1024 + wid * 128 + (dw - 8) * 32 + (l & 31)) * 4) = sel2(g & 1, fa1[0], fa0[0]);
  };
  auto endslot = [&](auto Sl) AI {
    constexpr int sl = Sl, u = sl >> 3, i = sl & 7;
    side(Sl);
    issue(Sl);
    SBAR();
    if constexpr (sl == 2) waitbar<SC.younger(OG, 3, sl)>();
    if constexpr (i == 6 && u < 15) {
      if constexpr (u == 10) waitbar_l<SC.younger(OG, 4 * u + 7, sl)>();
      else waitbar<SC.younger(OG, 4 * u + 7, sl)>();
      STAMP(2 + u);
    }
  };

  issue(IC(-1));
  f32x4 acc[8][2];
#pragma unroll
  for (int d = 0; d < 8; ++d) { acc[d][0] = (f32x4){}; acc[d][1] = (f32x4){}; }
  bf16x8 wf[2][4];
  auto WRD = [&](auto Si) AI {
    constexpr int s = Si, base = ((s >> 3) & 3) * 32768 + ((s >> 1) & 3) * 8192 + (s & 1) * 1024;
    sfor<4>([&](auto Dd) AI {
      constexpr int d = Dd, o = base + (d >> 1) * 512;
      wf[s & 1][d] = cat8(trd(trb[d & 1].template at<o>()), trd(trb[d & 1].template at<o + 256>()));
    });
  };
  bf16x8 fr[4];
  auto FRD = [&](auto Ni) AI { constexpr int n = Ni; if constexpr (n < 256) fr[n & 3] = *(LAS const bf16x8*)(frb.at<((n >> 5) & 3) * 32768 + (n & 31) * 1024>()); };

  waitbar<SC.younger(OG, 1, -1)>();
  STAMP(1);
  WRD(IC(0));
  SBAR();
  bf16x8 qf[4][2];
  auto QPACK = [&](auto Ds) AI {
    constexpr int ds = Ds;
#pragma unroll
    for (int qb = 0; qb < 2; ++qb) {
      const f32x4 a = acc[2 * ds][qb] * QSCALE, b = acc[2 * ds + 1][qb] * QSCALE;
      qf[ds][qb] = __builtin_bit_cast(bf16x8, (u32x4){pk2(a[0], a[1]), pk2(a[2], a[3]), pk2(b[0], b[1]), pk2(b[2], b[3])});
      PIN(qf[ds][qb]);
    }
  };
  sfor<64>([&](auto Si) AI {
    constexpr int s = Si, x0 = (s & ~1) % XD, x1 = (s | 1) % XD;
    if constexpr (s + 1 < 64) WRD(IC(s + 1));
    else { FRD(IC(0)); FRD(IC(1)); FRD(IC(2)); }
    SBAR();
    if constexpr (!(s & 1)) waitv<SC.younger(OX, s + 1, s - 1)>(xf[x0], xf[x1]);
    sfor<4>([&](auto Dd) AI {
      constexpr int d = Dd, dt = (s & 1) * 4 + d;
      acc[dt][0] = MFMA(wf[s & 1][d], xf[x0], acc[dt][0]);
      acc[dt][1] = MFMA(wf[s & 1][d], xf[x1], acc[dt][1]);
      if constexpr (s == 63 && (d & 1)) { QPACK(IC(d >> 1)); SBAR(); }
    });
    SBAR();
    endslot(Si);
  });

  f32x4 o[8][2], sc[2][2][2], negm[2];
#pragma unroll
  for (int d = 0; d < 8; ++d) { o[d][0] = (f32x4){}; o[d][1] = (f32x4){}; }
  u32x4 pw[2][2];
  float lsum[2] = {0.f, 0.f}, inv[2];
#define MX3(a, b, c) __builtin_fmaxf(__builtin_fmaxf((a), (b)), (c))
#define EXP2(x) __builtin_amdgcn_exp2f(x)
  char* const od = ws + WS_O + ((size_t)((rb * 8 + wid) * 128 + head * 4) * 128 + l) * 16;
  sfor<18>([&](auto Ui) AI {
    constexpr int H = Ui - 2, n0 = H < 0 ? 8 * (H + 2) : H < 14 ? 16 + 16 * H : 240 + 8 * (H - 14);
    constexpr bool DOK = H < 14, DOV = H >= 0, DOS = H >= -1 && H < 15;
    constexpr int sn = H & 1, ss = sn ^ 1, pc = H & 1, pn = pc ^ 1;
    const float lold[2] = {lsum[0], lsum[1]};
    auto SM = [&](auto Pi) AI {
      constexpr int p = Pi, kt = p >> 2, qb = (p >> 1) & 1, r = 2 * (p & 1);
      float a = sc[ss][kt][qb][r], b = sc[ss][kt][qb][r + 1];
      const float e0 = EXP2(a), e1 = EXP2(b);
      lsum[qb] += e0; lsum[qb] += e1;
      pw[pn][qb][kt * 2 + (p & 1)] = pk2(e0, e1);
      PIN(lsum[qb]); PIN(pw[pn][qb]);
    };
    sfor<8>([&](auto Ii) AI {
      constexpr int i = Ii, nk = n0 + ((DOK && DOV) ? 2 * i : i), nv = nk + (DOK ? 1 : 0), kt = i >> 2, ds = i & 3;
      if constexpr (DOK) {
        FRD(IC(nk + 3));
        if constexpr (H == -2 && i < 2) QPACK(IC(2 + i));
        sfor<2>([&](auto Qb) AI {
          constexpr int qb = Qb;
          if constexpr (ds == 0) sc[sn][kt][qb] = MFMA(fr[nk & 3], qf[0][qb], H == -2 ? (f32x4){} : negm[qb]);
          else sc[sn][kt][qb] = MFMA(fr[nk & 3], qf[ds][qb], sc[sn][kt][qb]);
        });
        if constexpr (H == -2 && (i == 5 || i == 6)) {
          constexpr int qb = i - 5;
          const float m = -xg_max(__builtin_fmaxf(MX3(sc[sn][0][qb][0], sc[sn][0][qb][1], sc[sn][0][qb][2]), sc[sn][0][qb][3]));
          negm[qb] = (f32x4){m, m, m, m};
          sc[sn][0][qb] += negm[qb];
          PIN(negm[qb]); PIN(sc[sn][0][qb]);
        }
        if constexpr (H == -1 && (i == 1 || i == 2)) { sc[ss][1][i - 1] += negm[i - 1]; PIN(sc[ss][1][i - 1]); }
        if constexpr (DOS && i == 0) SM(IC(0));
        if constexpr (DOS && !DOV && i < 7) SM(IC(i + 1));
        SBAR();
        if constexpr ((nk & 3) == 3) endslot(IC(64 + (nk >> 2)));
      }
      if constexpr (DOV) {
        FRD(IC(nv + 3));
        o[i][0] = MFMA(fr[nv & 3], __builtin_bit_cast(bf16x8, pw[pc][0]), o[i][0]);
        o[i][1] = MFMA(fr[nv & 3], __builtin_bit_cast(bf16x8, pw[pc][1]), o[i][1]);
        if constexpr (DOS && !DOK && i == 0) SM(IC(0));
        if constexpr (DOS && i < 7) SM(IC(i + 1));
        if constexpr (H == 15 && i == 0) { inv[0] = __builtin_amdgcn_rcpf(xg_sum(lsum[0])); inv[1] = __builtin_amdgcn_rcpf(xg_sum(lsum[1])); PIN(inv[0]); PIN(inv[1]); }
        if constexpr (H == 15 && i >= 3 && (i & 1)) {
          constexpr int os = (i - 3) >> 1;
#pragma unroll
          for (int qb = 0; qb < 2; ++qb) {
            const f32x4 a = o[2 * os][qb] * inv[qb], b = o[2 * os + 1][qb] * inv[qb];
            *(u32x4*)(od + (os * 2 + qb) * 1024) = (u32x4){pk2(a[0], a[1]), pk2(a[2], a[3]), pk2(b[0], b[1]), pk2(b[2], b[3])};
          }
        }
        SBAR();
        if constexpr ((nv & 3) == 3) endslot(IC(64 + (nv >> 2)));
      }
    });
    if constexpr (DOS) {
      if (__builtin_expect(__any(!(lsum[0] + lsum[1] <= LBOUND)), 0)) {
#pragma unroll
        for (int qb = 0; qb < 2; ++qb) {
          const f32x4 a = sc[ss][0][qb], b = sc[ss][1][qb];
          const float dl = xg_max(MX3(MX3(0.f, a[0], a[1]), MX3(a[2], a[3], b[0]), MX3(b[1], b[2], b[3])));
          const float f = EXP2(-dl);
          float ls = lold[qb];
          PIN(ls);
          ls *= f;
#pragma unroll
          for (int d = 0; d < 8; ++d) o[d][qb] *= f;
#pragma unroll
          for (int kt = 0; kt < 2; ++kt) {
            if constexpr (DOK) sc[sn][kt][qb] -= dl;
#pragma unroll
            for (int r = 0; r < 4; r += 2) {
              const float e0 = EXP2(sc[ss][kt][qb][r] - dl), e1 = EXP2(sc[ss][kt][qb][r + 1] - dl);
              ls += e0; ls += e1;
              PIN(ls);
              pw[pn][qb][kt * 2 + (r >> 1)] = pk2(e0, e1);
            }
          }
          lsum[qb] = ls;
          negm[qb] -= dl;
          PIN(pw[pn][qb]); PIN(negm[qb]);
        }
      }
      SBAR();
    }
  });
  STAMP(17);
#pragma unroll
  for (int qb = 0; qb < 2; ++qb) {
    const f32x4 a = o[6][qb] * inv[qb], b = o[7][qb] * inv[qb];
    *(u32x4*)(od + (6 + qb) * 1024) = (u32x4){pk2(a[0], a[1]), pk2(a[2], a[3]), pk2(b[0], b[1]), pk2(b[2], b[3])};
  }
  STAMP(18);
  STAMP_OUT(0, 19);
}

namespace k3 {
constexpr int PITCH = 132, YS = 2 * 128 * PITCH * 4, LDS = YS + 8 * 64 * 4;
constexpr int XD = 12;
constexpr Sched make_sched() {
  Sched s;
  s.add(-1, OE, 0); s.add(-1, OE, 1); s.add(-1, OE, 2); s.add(-1, OM, 0);
  for (int v = 0; v < 3; ++v) { for (int i = 0; i < 4; ++i) s.add(-1, OG, 4 * v + i); for (int i = 0; i < 4; ++i) s.add(-1, OX, 4 * v + i); }
  for (int t = 0; t < 64; ++t) {
    if (t < 2) { s.add(t, OG, 12 + 2 * t); s.add(t, OG, 13 + 2 * t); }
    if ((t & 1) && t - 1 + XD < 64) { s.add(t, OX, t - 1 + XD); s.add(t, OX, t + XD); }
    for (int v = 4; v < 16; ++v) for (int p = 0; p < 4; ++p) if (4 * (v - 3) + 3 + p == t) s.add(t, OG, 4 * v + p);
  }
  return s;
}
constexpr Sched SC = make_sched();
}

__global__ __launch_bounds__(512, 2) void k3_out(const u16* __restrict__ x, const u16* __restrict__ wo, char* __restrict__ ws,
                                                 u16* __restrict__ out) {
  using namespace k3;
  __shared__ __attribute__((aligned(16))) char lds_raw[LDS];
  LAS char* const lds = (LAS char*)lds_raw;
  const int t = threadIdx.x, l = t & 63, bid = blockIdx.x, c = l & 15, g = l >> 4;
  const int wid = __builtin_amdgcn_readfirstlane(t >> 6);
  const int xcd = bid & 7, within = bid >> 3;
  const int kh = xcd & 1, mb = 4 * (xcd >> 1) + (within >> 3), nb = within & 7;
  const int rbw = wid & 3, kg = wid >> 2;
  const unsigned lds0 = (unsigned)(uintptr_t)lds;
  STAMP_DECL
  STAMP(0);

  const char* const wo_b = (const char*)wo + (size_t)kh * 2048 * (DM * 2) + nb * 256;
  const unsigned wo_v = (unsigned)(((8 * wid + ((l & 31) >> 2)) * DM + (l >> 5) * 32 + ((l & 3) ^ (2 * (wid & 1))) * 8) * 2);
  const unsigned ldsw = lds0 + wid * 2048;
  const char* const o_b = ws + WS_O + (size_t)mb * 4 * 262144 + kh * 131072;
  const unsigned o_v = (unsigned)(rbw * 262144 + kg * 4096 + l * 16);
  bf16x8 xf[XD];
  const int erow = mb * 128 + (t >> 2), ecol = nb * 128 + kh * 64 + (t & 3) * 16;
  u32x4 fm, xe[2];
  f32x2 st;
  auto issue = [&](auto Sl) AI {
    constexpr int sl = Sl, q0 = SC.first(sl), nq = SC.count(sl);
    sfor<nq>([&](auto Qi) AI {
      constexpr int k = SC.kind[q0 + Qi], i = SC.idx[q0 + Qi];
      if constexpr (k == OX) gld16<(i & 3) * 1024>(xf[i % XD], o_v, o_b + (size_t)(i >> 2) * 8192);
      if constexpr (k == OG) {
        constexpr int v = i >> 2, p = i & 3;
        const char* sb = wo_b + (size_t)(v * 128 + (p >> 1) * 64) * (DM * 2);
        if constexpr (p & 1) glds<128>(ldsw + (v & 3) * 32768 + (p >> 1) * 16384 + 1024 - 128, wo_v, sb);
        else glds<0>(ldsw + (v & 3) * 32768 + (p >> 1) * 16384, wo_v, sb);
      }
      if constexpr (k == OM) gld16<0>(fm, (unsigned)(l * 16), ws + WS_MX);
      if constexpr (k == OE && i < 2) gld16<16 * i>(xe[i], (unsigned)((erow * DM + ecol) * 2), x);
      if constexpr (k == OE && i == 2) gld8<0>(st, (unsigned)(erow * 8), ws + WS_ST);
    });
  };
  issue(IC(-1));

  unsigned xnp[8], gate[8];
  waitv<SC.younger(OM, 0, -1)>(xe[0], xe[1]);
  PIN(st); PIN(fm);
#pragma unroll
  for (int j = 0; j < 8; ++j) {
    const unsigned xw = xe[j >> 2][j & 3];
    const unsigned dd = pk2(bflo(xw) - st[0], bfhi(xw) - st[0]);
    xnp[j] = pk2(bflo(dd) * st[1], bfhi(dd) * st[1]);
    gate[j] = pk2(silu_f(bflo(xnp[j])), silu_f(bfhi(xnp[j])));
  }
  const unsigned key = wave_max_u32(umax(umax(fm[0], fm[1]), umax(fm[2], fm[3])));
  const int idx = SEQ - 1 - (int)(key & 0xffffu);
  const bool owner = (idx >> 7) == mb;
  f32x4 yp[7];

  f32x4 acc[8][2];
#pragma unroll
  for (int d = 0; d < 8; ++d) { acc[d][0] = (f32x4){}; acc[d][1] = (f32x4){}; }
  LBase trb[2];
  trb[0].init(lds0 + kg * 16384 + g * 2048 + (c >> 2) * 64 + (g & 1) * 32 + (c & 3) * 8);
  trb[1].init(lds0 + kg * 16384 + g * 2048 + (c >> 2) * 64 + ((g & 1) ^ 1) * 32 + (c & 3) * 8);
  bf16x8 wf[2][4];
  auto WRD = [&](auto Si) AI {
    constexpr int s = Si, base = ((s >> 2) & 3) * 32768 + ((s >> 1) & 1) * 8192 + (s & 1) * 1024;
    sfor<4>([&](auto Dd) AI {
      constexpr int d = Dd, o = base + (d >> 1) * 512;
      wf[s & 1][d] = cat8(trd(trb[d & 1].template at<o>()), trd(trb[d & 1].template at<o + 256>()));
    });
  };

  waitbar<SC.younger(OG, 3, -1)>();
  STAMP(1);
  WRD(IC(0));
  SBAR();
  sfor<64>([&](auto Si) AI {
    constexpr int s = Si, u = s >> 2, x0 = (s & ~1) % XD, x1 = (s | 1) % XD;
    if constexpr (s + 1 < 64) WRD(IC(s + 1));
    SBAR();
    if constexpr (!(s & 1)) waitv<SC.younger(OX, s + 1, s - 1)>(xf[x0], xf[x1]);
    sfor<4>([&](auto Dd) AI {
      constexpr int d = Dd, dt = (s & 1) * 4 + d;
      acc[dt][0] = MFMA(wf[s & 1][d], xf[x0], acc[dt][0]);
      acc[dt][1] = MFMA(wf[s & 1][d], xf[x1], acc[dt][1]);
    });
    SBAR();
    issue(Si);
    if constexpr (s == 8) {
      if (owner) {
        const unsigned yv = (unsigned)(((t >> 4) * 1024 + nb * 128 + kh * 64 + (t & 15) * 4) * 4);
        sfor<7>([&](auto I) AI { constexpr int q = I; gld16<0>(yp[q], yv, ws + WS_YP + (size_t)q * 32 * 4096); });
      }
    }
    if constexpr (s == 25) {
      if (owner) {
        f32x4 a = {};
#pragma unroll
        for (int q = 0; q < 7; ++q) { PIN(yp[q]); a += yp[q]; }
#pragma unroll
        for (int j = 0; j < 4; ++j) a[j] = xg_sum(a[j]);
        *(LAS f32x4*)(lds + YS + (wid * 64 + c * 4) * 4) = a;
      }
    }
    if constexpr (s == 28) {
      if (owner && erow == idx) {
        const unsigned ord = key >> 16, bits = (ord & 0x8000u) ? (ord & 0x7fffu) : (~ord & 0xffffu);
        const float sg = rbf(sigmoid_f(bflo(bits)));
#pragma unroll
        for (int q = 0; q < 4; ++q) {
          f32x4 a = {};
#pragma unroll
          for (int p = 0; p < 8; ++p) a += *(LAS const f32x4*)(lds + YS + (p * 64 + (t & 3) * 16 + q * 4) * 4);
#pragma unroll
          for (int j = 0; j < 2; ++j) {
            const float lo = rbf(bflo(xnp[2 * q + j]) + rbf(rbf(a[2 * j]) * sg)), hi = rbf(bfhi(xnp[2 * q + j]) + rbf(rbf(a[2 * j + 1]) * sg));
            gate[2 * q + j] = pk2(silu_f(lo), silu_f(hi));
          }
        }
      }
    }
    SBAR();
    if constexpr ((s & 3) == 2 && u < 15) {
      if constexpr (s == 26) waitbar_l<SC.younger(OG, 4 * u + 7, s)>();
      else waitbar<SC.younger(OG, 4 * u + 7, s)>();
      if constexpr (u % 2 == 1) STAMP(2 + u / 2);
    }
  });
  waitbar<0>();
  STAMP(9);

  {
    LAS char* dst = lds + ((kg * 128 + rbw * 32 + c) * PITCH + 4 * g) * 4;
#pragma unroll
    for (int d = 0; d < 8; ++d) { *(LAS f32x4*)(dst + d * 64) = acc[d][0]; *(LAS f32x4*)(dst + 16 * PITCH * 4 + d * 64) = acc[d][1]; }
  }
  waitbar_l<0>();
  STAMP(10);
  f32x4 mine[4], oth[4];
  {
    LAS const char* src = lds + ((t >> 2) * PITCH + (t & 3) * 16) * 4;
    const char* xb = ws + WS_XC + (size_t)(bid ^ 1) * 32768;
#pragma unroll
    for (int q = 0; q < 4; ++q) {
      const f32x4 a = *(LAS const f32x4*)(src + (kh ^ 1) * 256 + q * 16) + *(LAS const f32x4*)(src + 128 * PITCH * 4 + (kh ^ 1) * 256 + q * 16);
      asm volatile("global_store_dwordx4 %0, %1, %2 sc1\n\ts_nop 1" ::"v"((unsigned)(q * 8192 + t * 16)), "v"(a), "s"(xb) : "memory");
    }
#pragma unroll
    for (int q = 0; q < 4; ++q) mine[q] = *(LAS const f32x4*)(src + kh * 256 + q * 16) + *(LAS const f32x4*)(src + 128 * PITCH * 4 + kh * 256 + q * 16);
  }
  waitbar<0>();
  STAMP(11);
  if (wid == 0) {
    if (l == 0) __hip_atomic_store((unsigned*)(ws + WS_FL + (bid ^ 1) * 128), 1u, __ATOMIC_RELAXED, __HIP_MEMORY_SCOPE_AGENT);
    for (int spin = 0; spin < (1 << 22); ++spin) {
      if (__builtin_amdgcn_readfirstlane(__hip_atomic_load((unsigned*)(ws + WS_FL + bid * 128), __ATOMIC_RELAXED, __HIP_MEMORY_SCOPE_AGENT)) != 0u) break;
      __builtin_amdgcn_s_sleep(1);
    }
  }
  waitbar<0>();
  STAMP(12);
  {
    const char* xb = ws + WS_XC + (size_t)bid * 32768;
#pragma unroll
    for (int q = 0; q < 4; ++q) asm volatile("global_load_dwordx4 %0, %1, %2 sc1" : "=v"(oth[q]) : "v"((unsigned)(q * 8192 + t * 16)), "s"(xb) : "memory");
    waitv<0>(oth[0], oth[1]);
    PIN(oth[2]); PIN(oth[3]);
  }
  STAMP(13);
  u32x4 res[2];
#pragma unroll
  for (int q = 0; q < 4; ++q) {
    const f32x4 a = kh ? oth[q] + mine[q] : mine[q] + oth[q];
#pragma unroll
    for (int j = 0; j < 2; ++j) {
      const unsigned sp = pk2(a[2 * j], a[2 * j + 1]), gp = gate[2 * q + j], xw = xe[q >> 1][(q & 1) * 2 + j];
      const unsigned pr = pk2(bflo(sp) * bflo(gp), bfhi(sp) * bfhi(gp));
      res[q >> 1][(q & 1) * 2 + j] = pk2(bflo(xw) + bflo(pr), bfhi(xw) + bfhi(pr));
    }
  }
  u16* op = out + (size_t)erow * DM + ecol;
  *(u32x4*)op = res[0];
  *(u32x4*)(op + 8) = res[1];
  STAMP(14);
  STAMP_OUT(1, 15);
}

extern "C" void kernel_launch(void* const* d_in, const int* in_sizes, int n_in, void* d_out, int out_size, void* d_ws, size_t ws_size,
                              hipStream_t stream) {
  const u16* x = (const u16*)d_in[0];
  const u16* kc = (const u16*)d_in[1];
  const u16* vc = (const u16*)d_in[2];
  const u16* wq = (const u16*)d_in[3];
  const u16* wo = (const u16*)d_in[4];
  const u16* wr = (const u16*)d_in[6];
  const u16* wg = (const u16*)d_in[7];
  const u16* wu = (const u16*)d_in[8];
  const u16* wd = (const u16*)d_in[9];
  char* ws = (char*)d_ws;
  k1_prep<<<256, 768, 0, stream>>>(x, kc, vc, wr, wq, ws);
  k2_attn<<<256, 512, 0, stream>>>(wq, wg, wu, wd, wo, ws);
  k3_out<<<256, 512, 0, stream>>>(x, wo, ws, (u16*)d_out);
}
```

```cpp
#include <hip/hip_runtime.h>
#include <stdint.h>
#include <type_traits>

typedef unsigned short u16;
typedef short bf16x8 __attribute__((ext_vector_type(8)));
typedef short s16x4 __attribute__((ext_vector_type(4)));
typedef float f32x4 __attribute__((ext_vector_type(4)));
typedef float f32x2 __attribute__((ext_vector_type(2)));
typedef unsigned u32x4 __attribute__((ext_vector_type(4)));
typedef unsigned u32x2 __attribute__((ext_vector_type(2)));
typedef __bf16 bf2_t __attribute__((ext_vector_type(2)));
#define LAS __attribute__((address_space(3)))
#define DI __device__ __forceinline__
#define AI __attribute__((always_inline))
#define SBAR() __builtin_amdgcn_sched_barrier(0)
template <class T> __device__ __forceinline__ void PIN(T& x) { asm volatile("" : "+v"(x)); }
template <class B> DI void MF(f32x4& acc, const bf16x8& a, const B& b) { asm volatile("v_mfma_f32_16x16x32_bf16 %0, %1, %2, %0" : "+v"(acc) : "v"(a), "v"(b)); }
template <class B> DI void MFC(f32x4& d, const bf16x8& a, const B& b, const f32x4& c) { asm volatile("v_mfma_f32_16x16x32_bf16 %0, %1, %2, %3" : "=&v"(d) : "v"(a), "v"(b), "v"(c)); }
template <class B> DI void MFZ(f32x4& d, const bf16x8& a, const B& b) { asm volatile("v_mfma_f32_16x16x32_bf16 %0, %1, %2, 0" : "=&v"(d) : "v"(a), "v"(b)); }
#define NOPS(n) asm volatile("s_nop " #n)
#define MFMA16(a, b, c) __builtin_amdgcn_mfma_f32_16x16x16bf16_1k((a), (b), (c), 0, 0, 0)
#define IC(n) std::integral_constant<int, (n)>{}

constexpr int SEQ = 2048, DM = 1024, DH = 128, SK = 2048, WIN = 512, FF = 3584;
constexpr size_t WS_XN = 0;
constexpr size_t WS_KV = 4u << 20;
constexpr size_t WS_ST = 5u << 20;
constexpr size_t WS_MX = WS_ST + 16384;
constexpr size_t WS_YP = WS_ST + 65536;
constexpr size_t WS_FL = 7u << 20;
constexpr size_t WS_XC = 24u << 20;
constexpr size_t WS_O = 8u << 20;

template <int I, int N, class F> DI void sfor_impl(F&& f) {
  if constexpr (I < N) { f(std::integral_constant<int, I>{}); sfor_impl<I + 1, N>(f); }
}
template <int N, class F> DI void sfor(F&& f) { sfor_impl<0, N>(f); }

DI unsigned pk2(float a, float b) { f32x2 v = {a, b}; return __builtin_bit_cast(unsigned, __builtin_convertvector(v, bf2_t)); }
DI float rbf(float a) { return (float)(__bf16)a; }
DI float bflo(unsigned u) { return __uint_as_float(u << 16); }
DI float bfhi(unsigned u) { return __uint_as_float(u & 0xffff0000u); }
DI float sigmoid_f(float v) { return __builtin_amdgcn_rcpf(1.f + __builtin_amdgcn_exp2f(v * -1.4426950408889634f)); }
DI float sel2(bool c, float a, float b) { PIN(a); PIN(b); return c ? a : b; }
DI float silu_f(float v) { return v * sigmoid_f(v); }
template <int CTRL> DI float dppf(float x) { return __builtin_bit_cast(float, __builtin_amdgcn_mov_dpp(__builtin_bit_cast(int, x), CTRL, 0xf, 0xf, true)); }
template <int CTRL> DI unsigned dppu(unsigned x) { return (unsigned)__builtin_amdgcn_mov_dpp((int)x, CTRL, 0xf, 0xf, true); }
constexpr int XOR1 = 0xB1, XOR2 = 0x4E, XOR7 = 0x141, XOR8 = 0x128;
DI float xg_sum(float x) {
  auto s = __builtin_amdgcn_permlane16_swap(__float_as_uint(x), __float_as_uint(x), false, false);
  x = __uint_as_float(s[0]) + __uint_as_float(s[1]);
  auto t = __builtin_amdgcn_permlane32_swap(__float_as_uint(x), __float_as_uint(x), false, false);
  return __uint_as_float(t[0]) + __uint_as_float(t[1]);
}
DI float xg_max(float x) {
  auto s = __builtin_amdgcn_permlane16_swap(__float_as_uint(x), __float_as_uint(x), false, false);
  x = __builtin_fmaxf(__uint_as_float(s[0]), __uint_as_float(s[1]));
  auto t = __builtin_amdgcn_permlane32_swap(__float_as_uint(x), __float_as_uint(x), false, false);
  return __builtin_fmaxf(__uint_as_float(t[0]), __uint_as_float(t[1]));
}
DI float wave_sum(float v) { v += dppf<XOR1>(v); v += dppf<XOR2>(v); v += dppf<XOR7>(v); v += dppf<XOR8>(v); return xg_sum(v); }
DI unsigned umax(unsigned a, unsigned b) { return a > b ? a : b; }
DI unsigned wave_max_u32(unsigned v) {
  v = umax(v, dppu<XOR1>(v)); v = umax(v, dppu<XOR2>(v)); v = umax(v, dppu<XOR7>(v)); v = umax(v, dppu<XOR8>(v));
  auto s = __builtin_amdgcn_permlane16_swap(v, v, false, false); v = umax(s[0], s[1]);
  auto t = __builtin_amdgcn_permlane32_swap(v, v, false, false); v = umax(t[0], t[1]);
  return __builtin_amdgcn_readfirstlane(v);
}

template <int OFF> DI void glds(unsigned ldsaddr, unsigned voff, const void* sbase) {
  asm volatile("s_mov_b32 m0, %0\n\ts_nop 0\n\tglobal_load_lds_dwordx4 %1, %2 offset:%3" ::"s"(ldsaddr), "v"(voff), "s"(sbase), "i"(OFF) : "memory");
}
DI void glds64(unsigned ldsaddr, const void* vaddr) {
  asm volatile("s_mov_b32 m0, %0\n\ts_nop 0\n\tglobal_load_lds_dwordx4 %1, off" ::"s"(ldsaddr), "v"(vaddr) : "memory");
}
template <int OFF, class T> DI void gld16(T& dst, unsigned voff, const void* sbase) {
  asm volatile("global_load_dwordx4 %0, %1, %2 offset:%3" : "=v"(dst) : "v"(voff), "s"(sbase), "i"(OFF) : "memory");
}
template <int OFF, class T> DI void gld8(T& dst, unsigned voff, const void* sbase) {
  asm volatile("global_load_dwordx2 %0, %1, %2 offset:%3" : "=v"(dst) : "v"(voff), "s"(sbase), "i"(OFF) : "memory");
}
template <int N> DI void waitv() { static_assert(N >= 0, "count"); asm volatile("s_waitcnt vmcnt(%0)" ::"n"(N > 63 ? 63 : N) : "memory"); }
template <int N, class T> DI void waitv(T& x) { static_assert(N >= 0, "count"); asm volatile("s_waitcnt vmcnt(%1)" : "+v"(x) : "n"(N > 63 ? 63 : N) : "memory"); }
template <int N, class T> DI void waitv(T& x, T& y) { static_assert(N >= 0, "count"); asm volatile("s_waitcnt vmcnt(%2)" : "+v"(x), "+v"(y) : "n"(N > 63 ? 63 : N) : "memory"); }
template <int N> DI void waitbar() { static_assert(N >= 0, "count"); asm volatile("s_waitcnt vmcnt(%0)\n\ts_barrier" ::"n"(N > 63 ? 63 : N) : "memory"); }
template <int N> DI void waitbar_l() { static_assert(N >= 0, "count"); asm volatile("s_waitcnt vmcnt(%0) lgkmcnt(0)\n\ts_barrier" ::"n"(N > 63 ? 63 : N) : "memory"); }
typedef short v4i16_t __attribute__((ext_vector_type(4)));
DI s16x4 trd(LAS const char* p) { return __builtin_amdgcn_ds_read_tr16_b64_v4i16((LAS v4i16_t*)p); }
struct LBase {
  unsigned lo, hi;
  DI void init(unsigned a) { lo = a; hi = a + 65536; PIN(lo); PIN(hi); }
  template <int OFF> DI LAS const char* at() const { return (LAS const char*)(uintptr_t)(OFF < 65536 ? lo : hi) + (OFF & 65535); }
};
DI bf16x8 cat8(s16x4 lo, s16x4 hi) { return __builtin_shufflevector(lo, hi, 0, 1, 2, 3, 4, 5, 6, 7); }

enum { OX, OG, OF, OM, OS, OP, OE };
struct Sched {
  int n; short kind[320], idx[320], slot[320];
  constexpr Sched() : n(0), kind{}, idx{}, slot{} {}
  constexpr void add(int s, int k, int i) { kind[n] = (short)k; idx[n] = (short)i; slot[n] = (short)s; ++n; }
  constexpr int younger(int k, int i, int s) const {
    int p = -1;
    for (int q = 0; q < n; ++q) if (kind[q] == k && idx[q] == i) p = q;
    if (p < 0 || slot[p] > s) return -1;
    int c = 0;
    for (int q = p + 1; q < n; ++q) if (slot[q] <= s) ++c;
    return c;
  }
  constexpr int first(int s) const { int q = 0; while (q < n && slot[q] < s) ++q; return q; }
  constexpr int count(int s) const { int c = 0; for (int q = 0; q < n; ++q) if (slot[q] == s) ++c; return c; }
};

#define STAMP_DECL
#define STAMP(i)
#define STAMP_OUT(kid, n)
__global__ __launch_bounds__(512) void k1_prep(const u16* __restrict__ x, const u16* __restrict__ kc, const u16* __restrict__ vc,
                                               const u16* __restrict__ wr, const u16* __restrict__ wq, char* __restrict__ ws) {
  __shared__ __attribute__((aligned(16))) char stage[16384 + 32];
  const int t = threadIdx.x, w = t >> 6, l = t & 63, b = blockIdx.x;
  if (b >= 256) {
    const int hb = b - 256;
    const int ch = hb * 512 + t, kvh = ch >> 14, n = (ch >> 6) & 255, c = l & 15, g = l >> 4;
    int isv, un, i;
    if (n < 16) { isv = 0; un = n >> 3; i = n & 7; }
    else if (n >= 240) { isv = 1; un = 14 + ((n - 240) >> 3); i = n & 7; }
    else { const int m = n - 16; isv = m & 1; un = (m >> 4) + (isv ? 0 : 2); i = (m & 15) >> 1; }
#define PFN 8
    {
      const unsigned po = (unsigned)(((hb >> 3) * 8 + w) * 8192 + (hb & 7) * 1024 + l * 16);
      sfor<PFN>([&](auto I) AI { glds<0>((unsigned)(uintptr_t)(LAS char*)stage, po, (const char*)wq + (size_t)I * 128 * 8192); });
    }
    u32x4 o;
    if (!isv) {
      const u16* p = kc + ((size_t)kvh * SK + (SK - WIN) + un * 32 + (i >> 2) * 16 + c) * DH + (i & 3) * 32 + 4 * g;
      const u32x2 lo = *(const u32x2*)p, hi = *(const u32x2*)(p + 16);
      o = (u32x4){lo[0], lo[1], hi[0], hi[1]};
    } else {
      const int d = 32 * (i >> 1) + 8 * (c >> 2) + 4 * (i & 1) + (c & 3);
      const u16* p = vc + ((size_t)kvh * SK + (SK - WIN) + un * 32 + 4 * g) * DH + d;
      unsigned e[8];
#pragma unroll
      for (int j = 0; j < 8; ++j) e[j] = p[(16 * (j >> 2) + (j & 3)) * DH];
      o = (u32x4){e[0] | (e[1] << 16), e[2] | (e[3] << 16), e[4] | (e[5] << 16), e[6] | (e[7] << 16)};
    }
    *(u32x4*)(ws + WS_KV + (size_t)ch * 16) = o;
    if (t < 16) *(unsigned*)(ws + WS_FL + (hb * 16 + t) * 64) = 0u;
    return;
  }
  STAMP_DECL
  STAMP(0);
  const int row = 8 * b + w;
  u32x4 a0, a1, w0, w1;
  gld16<0>(a0, (unsigned)(row * DM * 2 + l * 16), x); gld16<1024>(a1, (unsigned)(row * DM * 2 + l * 16), x);
  gld16<0>(w0, (unsigned)(l * 16), wr); gld16<1024>(w1, (unsigned)(l * 16), wr);
  waitv<0>(a0, a1);
  PIN(w0); PIN(w1);
  float v[16];
#pragma unroll
  for (int j = 0; j < 4; ++j) { v[2 * j] = bflo(a0[j]); v[2 * j + 1] = bfhi(a0[j]); v[8 + 2 * j] = bflo(a1[j]); v[9 + 2 * j] = bfhi(a1[j]); }
  float s = 0.f;
#pragma unroll
  for (int j = 0; j < 16; ++j) s += v[j];
  PIN(s);
  STAMP(1);
  const float mean = wave_sum(s) * (1.f / DM);
  float q = 0.f;
#pragma unroll
  for (int j = 0; j < 16; ++j) { const float d = v[j] - mean; q += d * d; }
  const float var = wave_sum(q) * (1.f / DM);
  const float mb = rbf(mean), rden = 1.f / rbf(sqrtf(rbf(rbf(var) + 1e-5f)));
  float rr = 0.f;
  u32x4 o0, o1;
#pragma unroll
  for (int j = 0; j < 8; ++j) {
    const unsigned dd = pk2(v[2 * j] - mb, v[2 * j + 1] - mb);
    const unsigned nn = pk2(bflo(dd) * rden, bfhi(dd) * rden);
    const unsigned ww = j < 4 ? w0[j] : w1[j - 4];
    rr += bflo(nn) * bflo(ww); rr += bfhi(nn) * bfhi(ww);
    if (j < 4) o0[j] = nn; else o1[j - 4] = nn;
  }
  *(u32x4*)(stage + (l * 8 + (w ^ (l & 7))) * 16) = o0;
  *(u32x4*)(stage + 8192 + (l * 8 + (w ^ (l & 7))) * 16) = o1;
  rr = wave_sum(rr);
  if (l == 0) {
    const unsigned bits = pk2(rr, 0.f) & 0xffffu;
    const unsigned ord = (bits & 0x8000u) ? (~bits & 0xffffu) : (bits | 0x8000u);
    *(unsigned*)(stage + 16384 + w * 4) = (ord << 16) | (unsigned)(SEQ - 1 - row);
    *(f32x2*)(ws + WS_ST + row * 8) = (f32x2){mb, rden};
  }
  STAMP(2);
  asm volatile("s_waitcnt lgkmcnt(0)\n\ts_barrier" ::: "memory");
  STAMP(3);
#pragma unroll
  for (int hh = 0; hh < 2; ++hh) {
    const int ch = hh * 64 + (t >> 3), r = 8 * b + ((t & 7) ^ (ch & 7));
    const u32x4 o = *(const u32x4*)(stage + hh * 8192 + t * 16);
    *(u32x4*)(ws + WS_XN + ((size_t)(((r >> 5) * 32 + (ch >> 2)) * 2 + ((r >> 4) & 1)) * 64 + (ch & 3) * 16 + (r & 15)) * 16) = o;
  }
  if (t == 0) {
    const u32x4 ka = *(const u32x4*)(stage + 16384), kb = *(const u32x4*)(stage + 16400);
    *(unsigned*)(ws + WS_MX + b * 4) = umax(umax(umax(ka[0], ka[1]), umax(ka[2], ka[3])), umax(umax(kb[0], kb[1]), umax(kb[2], kb[3])));
  }
  STAMP(4);
  STAMP_OUT(2, 5);
}

namespace k2 {
constexpr int RING = 4 * 32768, FRING = RING, RED1 = FRING + 8 * 3072, HSC = RED1 + 1024, DUMMY = HSC + 512, LDS = DUMMY + 1024;
constexpr int XD = 16;
constexpr float LBOUND = 65536.f;
constexpr float QSCALE = 0.08838834764831845f * 1.4426950408889634f;
constexpr int FISS[12] = {51, 52, 53, 67, 68, 69, 83, 84, 85, 99, 100, 109};
constexpr int FRDS[12] = {60, 62, 64, 76, 78, 80, 94, 96, 106, 112, 115, 121};
constexpr int FRED = 98;
constexpr int fpiece(int sl) { for (int p = 0; p < 12; ++p) if (FRDS[p] == sl) return p; return -1; }
constexpr Sched make_sched() {
  Sched s;
  s.add(-1, OG, 0); s.add(-1, OG, 1);
  for (int i = 0; i < 4; ++i) s.add(-1, OX, i);
  s.add(-1, OG, 2); s.add(-1, OG, 3);
  for (int i = 4; i < 8; ++i) s.add(-1, OX, i);
  for (int t = 0; t < 128; ++t) {
    if (t == 20) s.add(t, OM, 0);
    if (t == 1 || t == 2) { s.add(t, OX, 6 + 2 * t); s.add(t, OX, 7 + 2 * t); }
    if (t == 4 || t == 5) { s.add(t, OX, 4 + 2 * t); s.add(t, OX, 5 + 2 * t); }
    for (int p = 0; p < 24; ++p) { const int a = 6 + p, b2 = 2 * p + 1; if ((a > b2 ? a : b2) == t) { s.add(t, OX, 16 + 2 * p); s.add(t, OX, 17 + 2 * p); } }
    if (t == 0) { s.add(t, OG, 4); s.add(t, OG, 5); }
    if (t == 3) { s.add(t, OG, 6); s.add(t, OG, 7); }
    if (t >= 6 && t <= 9) s.add(t, OG, 8 + t - 6);
    if (t >= 11 && t <= 14) s.add(t, OG, 12 + t - 11);
    for (int v = 4; v < 16; ++v) for (int p = 0; p < 4; ++p) if (8 * (v - 3) + 7 + p == t) s.add(t, OG, 4 * v + p);
    for (int p = 0; p < 12; ++p) if (FISS[p] == t) s.add(t, OF, p);
    if (t == 44) for (int i = 0; i < 4; ++i) s.add(t, OS, i);
    if (t >= 112 && t < 116) s.add(t, OP, t - 112);
  }
  return s;
}
constexpr Sched SC = make_sched();
}

__global__ __launch_bounds__(512, 2) void k2_attn(const u16* __restrict__ wq, const u16* __restrict__ wgate, const u16* __restrict__ wup,
                                                  const u16* __restrict__ wdown, const u16* __restrict__ wo, char* __restrict__ ws) {
  using namespace k2;
  __shared__ __attribute__((aligned(16))) char lds_raw[LDS];
  LAS char* const lds = (LAS char*)lds_raw;
  const int t = threadIdx.x, l = t & 63, bid = blockIdx.x, c = l & 15, g = l >> 4;
  const int wid = __builtin_amdgcn_readfirstlane(t >> 6);
  const int xcd = bid & 7, within = bid >> 3, head = xcd * 4 + (within & 3), rb = within >> 2;
  const int fj = xcd * 28 + (within < 28 ? within : within - 28);
  const unsigned lds0 = (unsigned)(uintptr_t)lds;
  STAMP_DECL
  STAMP(0);

  const char* const wq_b = (const char*)wq + head * 256;
  const unsigned wq_v = (unsigned)(((8 * wid + ((l & 31) >> 2)) * 4096 + (l >> 5) * 32 + ((l & 3) ^ (2 * (wid & 1))) * 8) * 2);
  const char* const kv_b = ws + WS_KV + (size_t)(head >> 3) * 262144;
  const unsigned kv_v = (unsigned)(wid * 4096 + l * 16);
  const char* const xn_b = ws + WS_XN + (size_t)rb * 8 * 65536 + 4096;
  const unsigned xn_v = (unsigned)(wid * 65536 + l * 16);
  const unsigned ldsw = lds0 + wid * 2048, ldsk = lds0 + wid * 4096, ldsf = lds0 + FRING + wid * 3072;
  const int frho = l >> 2, fch = (l & 3) ^ (frho & 4 ? 2 : 0);
  const char* fgu = (const char*)(fch & 2 ? wup : wgate) + ((size_t)(128 * wid + 32 * (frho >> 2) + (frho & 3)) * FF + fj * 16 + (fch & 1) * 8) * 2;
  const unsigned fd_v = (unsigned)((frho * DM + wid * 128 + fch * 8) * 2);
  bf16x8 xf[XD];
  u32x4 fm, fx[4];
  unsigned fx_v = 0;

  auto issue = [&](auto Sl) AI {
    constexpr int sl = Sl, q0 = SC.first(sl), nq = SC.count(sl);
    sfor<nq>([&](auto Qi) AI {
      constexpr int k = SC.kind[q0 + Qi], i = SC.idx[q0 + Qi];
      if constexpr (k == OX) gld16<(i & 7) * 1024 - 4096>(xf[i % XD], xn_v, xn_b + (size_t)(i >> 3) * 8192);
      if constexpr (k == OG) {
        constexpr int v = i >> 2, p = i & 3;
        if constexpr (v < 8) {
          const char* sb = wq_b + (size_t)(v * 128 + (p >> 1) * 64) * 8192;
          if constexpr (p & 1) glds<128>(ldsw + (v & 3) * 32768 + (p >> 1) * 16384 + 1024 - 128, wq_v, sb);
          else glds<0>(ldsw + (v & 3) * 32768 + (p >> 1) * 16384, wq_v, sb);
        } else {
          glds<p * 1024>(ldsk + (v & 3) * 32768, kv_v, kv_b + (size_t)(v - 8) * 32768);
        }
      }
      if constexpr (k == OF) {
        if constexpr (i < 8) glds64(ldsf + (i % 3) * 1024, fgu + (size_t)i * 4 * FF * 2);
        else glds<0>(ldsf + (i % 3) * 1024, fd_v, (const char*)wdown + (size_t)fj * 16 * DM * 2 + (i - 8) * 64);
      }
      if constexpr (k == OM) gld16<0>(fm, (unsigned)(l * 16), ws + WS_MX);
      if constexpr (k == OS) gld16<i * 256>(fx[i], fx_v, ws + WS_XN);
      if constexpr (k == OP) glds<0>(lds0 + DUMMY, kv_v, (const char*)wo + (size_t)bid * 32768 + i * 1024);
    });
  };

  LBase trb[2], frb;
  trb[0].init(lds0 + g * 2048 + (c >> 2) * 64 + (g & 1) * 32 + (c & 3) * 8);
  trb[1].init(lds0 + g * 2048 + (c >> 2) * 64 + ((g & 1) ^ 1) * 32 + (c & 3) * 8);
  frb.init(lds0 + l * 16);
  unsigned ftr[2];
  ftr[0] = ldsf + (4 * g + (c >> 2)) * 64 + (g & 1) * 32 + (c & 3) * 8;
  ftr[1] = ftr[0] ^ 32;
  f32x4 fa0 = {}, fa1 = {};
  s16x4 hfrag, fb[2];

  auto side = [&](auto Sl) AI {
    constexpr int sl = Sl;
    if constexpr (sl == 43) {
      waitv<SC.younger(OM, 0, sl - 1)>(fm);
      const unsigned key = wave_max_u32(umax(umax(fm[0], fm[1]), umax(fm[2], fm[3])));
      const int idx = SEQ - 1 - (int)(key & 0xffffu);
      fx_v = (unsigned)(((((idx >> 5) * 32 + 4 * wid + g) * 2 + ((idx >> 4) & 1)) * 64 + (idx & 15)) * 16);
    }
    constexpr int pr = fpiece(sl), pm = fpiece(sl - 1), ps = fpiece(sl - 2);
    if constexpr (ps >= 8) *(float*)(ws + WS_YP + ((size_t)(within < 28 ? fj : 224 + xcd * 4 + within - 28) * 1024 + wid * 128 + (ps - 8) * 32 + (l & 31)) * 4) = sel2(g & 1, fa1[0], fa0[0]);
    if constexpr (pm >= 0 && pm < 8) {
      const u32x2 xa = {fx[pm >> 1][2 * (pm & 1)], fx[pm >> 1][2 * (pm & 1) + 1]};
      fa0 = MFMA16(__builtin_bit_cast(s16x4, xa), fb[0], fa0);
      fa1 = MFMA16(__builtin_bit_cast(s16x4, xa), fb[1], fa1);
    }
    if constexpr (pm >= 8) { fa0 = MFMA16(hfrag, fb[0], (f32x4){}); fa1 = MFMA16(hfrag, fb[1], (f32x4){}); }
    if constexpr (pr >= 0) {
      if constexpr (pr == 0) { waitv<SC.younger(OS, 3, sl - 1)>(fx[0], fx[1]); PIN(fx[2]); PIN(fx[3]); }
      waitv<SC.younger(OF, pr, sl - 1)>();
      fb[0] = trd((LAS const char*)(uintptr_t)ftr[0] + (pr % 3) * 1024); fb[1] = trd((LAS const char*)(uintptr_t)ftr[1] + (pr % 3) * 1024);
    }
    if constexpr (sl == FRED) *(LAS float*)(lds + RED1 + (wid * 32 + (l & 31)) * 4) = sel2(g & 1, fa1[0], fa0[0]);
    if constexpr (sl == 103) {
#pragma unroll
      for (int w = 0; w < 2; ++w) { fa0[w] = *(LAS const float*)(lds + RED1 + ((2 * g + w) * 32 + c) * 4); fa1[w] = *(LAS const float*)(lds + RED1 + ((2 * g + w) * 32 + 16 + c) * 4); }
    }
    if constexpr (sl == 104) { fa0[0] = xg_sum(fa0[0] + fa0[1]); fa1[0] = xg_sum(fa1[0] + fa1[1]); PIN(fa0); PIN(fa1); }
    if constexpr (sl == 105) {
      const float hh = rbf(silu_f(rbf(fa0[0]))) * rbf(fa1[0]);
      *(LAS u16*)(lds + HSC + wid * 64 + c * 2) = (u16)pk2(hh, hh);
    }
    if constexpr (sl == 106) hfrag = *(LAS const s16x4*)(lds + HSC + wid * 64 + g * 8);
  };
  auto endslot = [&](auto Sl) AI {
    constexpr int sl = Sl, u = sl >> 3, i = sl & 7;
    side(Sl);
    issue(Sl);
    SBAR();
    if constexpr (sl == 2) waitbar<SC.younger(OG, 3, sl)>();
    if constexpr (i == 6 && u < 15) {
      if constexpr (u == 12) waitbar_l<SC.younger(OG, 4 * u + 7, sl)>();
      else waitbar<SC.younger(OG, 4 * u + 7, sl)>();
      STAMP(2 + u);
    }
  };

  issue(IC(-1));
  f32x4 acc[8][2];
  bf16x8 wf[2][4];
  auto WRD = [&](auto Si) AI {
    constexpr int s = Si, base = ((s >> 3) & 3) * 32768 + ((s >> 1) & 3) * 8192 + (s & 1) * 1024;
    sfor<4>([&](auto Dd) AI {
      constexpr int d = Dd, o = base + (d >> 1) * 512;
      wf[s & 1][d] = cat8(trd(trb[d & 1].template at<o>()), trd(trb[d & 1].template at<o + 256>()));
    });
  };
  bf16x8 fr[4];
  auto FRD = [&](auto Ni) AI { constexpr int n = Ni; if constexpr (n < 256) fr[n & 3] = *(LAS const bf16x8*)(frb.at<((n >> 5) & 3) * 32768 + (n & 31) * 1024>()); };

  waitbar<SC.younger(OG, 1, -1)>();
  STAMP(1);
  WRD(IC(0));
  SBAR();
  bf16x8 qf[4][2];
  auto QPACK = [&](auto Ds) AI {
    constexpr int ds = Ds;
#pragma unroll
    for (int qb = 0; qb < 2; ++qb) {
      const f32x4 a = acc[2 * ds][qb] * QSCALE, b = acc[2 * ds + 1][qb] * QSCALE;
      qf[ds][qb] = __builtin_bit_cast(bf16x8, (u32x4){pk2(a[0], a[1]), pk2(a[2], a[3]), pk2(b[0], b[1]), pk2(b[2], b[3])});
      PIN(qf[ds][qb]);
    }
  };
  sfor<64>([&](auto Si) AI {
    constexpr int s = Si, x0 = (s & ~1) % XD, x1 = (s | 1) % XD;
    if constexpr (s + 1 < 64) WRD(IC(s + 1));
    else { FRD(IC(0)); FRD(IC(1)); FRD(IC(2)); }
    SBAR();
    if constexpr (!(s & 1)) waitv<SC.younger(OX, s + 1, s - 1)>(xf[x0], xf[x1]);
    sfor<4>([&](auto Dd) AI {
      constexpr int d = Dd, dt = (s & 1) * 4 + d;
      if constexpr (s < 2) { MFZ(acc[dt][0], wf[s & 1][d], xf[x0]); MFZ(acc[dt][1], wf[s & 1][d], xf[x1]); }
      else { MF(acc[dt][0], wf[s & 1][d], xf[x0]); MF(acc[dt][1], wf[s & 1][d], xf[x1]); }
      if constexpr (s == 63 && (d & 1)) { QPACK(IC(d >> 1)); SBAR(); }
    });
    SBAR();
    endslot(Si);
  });

  f32x4 o[8][2], sc[2][2][2], negm[2];
  u32x4 pw[2][2];
  float lsum[2] = {0.f, 0.f}, inv[2];
#define MX3(a, b, c) __builtin_fmaxf(__builtin_fmaxf((a), (b)), (c))
#define EXP2(x) __builtin_amdgcn_exp2f(x)
  char* const od = ws + WS_O + ((size_t)((rb * 8 + wid) * 128 + head * 4) * 128 + l) * 16;
  sfor<18>([&](auto Ui) AI {
    constexpr int H = Ui - 2, n0 = H < 0 ? 8 * (H + 2) : H < 14 ? 16 + 16 * H : 240 + 8 * (H - 14);
    constexpr bool DOK = H < 14, DOV = H >= 0, DOS = H >= -1 && H < 15;
    constexpr int sn = H & 1, ss = sn ^ 1, pc = H & 1, pn = pc ^ 1;
    const float lold[2] = {lsum[0], lsum[1]};
    auto SM = [&](auto Pi) AI {
      constexpr int p = Pi, kt = p >> 2, qb = (p >> 1) & 1, r = 2 * (p & 1);
      float a = sc[ss][kt][qb][r], b = sc[ss][kt][qb][r + 1];
      const float e0 = EXP2(a), e1 = EXP2(b);
      lsum[qb] += e0; lsum[qb] += e1;
      pw[pn][qb][kt * 2 + (p & 1)] = pk2(e0, e1);
      PIN(lsum[qb]); PIN(pw[pn][qb]);
    };
    sfor<8>([&](auto Ii) AI {
      constexpr int i = Ii, nk = n0 + ((DOK && DOV) ? 2 * i : i), nv = nk + (DOK ? 1 : 0), kt = i >> 2, ds = i & 3;
      if constexpr (DOK) {
        FRD(IC(nk + 3));
        if constexpr (H == -2 && i < 2) QPACK(IC(2 + i));
        sfor<2>([&](auto Qb) AI {
          constexpr int qb = Qb;
          if constexpr (ds == 0 && H == -2) MFZ(sc[sn][kt][qb], fr[nk & 3], qf[0][qb]);
          else if constexpr (ds == 0) MFC(sc[sn][kt][qb], fr[nk & 3], qf[0][qb], negm[qb]);
          else MF(sc[sn][kt][qb], fr[nk & 3], qf[ds][qb]);
        });
        if constexpr (H == -2 && (i == 5 || i == 6)) {
          constexpr int qb = i - 5;
          NOPS(3); PIN(sc[sn][0][qb]);
          const float m = -xg_max(__builtin_fmaxf(MX3(sc[sn][0][qb][0], sc[sn][0][qb][1], sc[sn][0][qb][2]), sc[sn][0][qb][3]));
          negm[qb] = (f32x4){m, m, m, m};
          sc[sn][0][qb] += negm[qb];
          PIN(negm[qb]); PIN(sc[sn][0][qb]);
        }
        if constexpr (H == -1 && (i == 1 || i == 2)) { NOPS(3); PIN(sc[ss][1][i - 1]); sc[ss][1][i - 1] += negm[i - 1]; PIN(sc[ss][1][i - 1]); }
        if constexpr (DOS && i == 0) SM(IC(0));
        if constexpr (DOS && !DOV && i < 7) SM(IC(i + 1));
        SBAR();
        if constexpr ((nk & 3) == 3) endslot(IC(64 + (nk >> 2)));
      }
      if constexpr (DOV) {
        FRD(IC(nv + 3));
        if constexpr (H == 0) { MFZ(o[i][0], fr[nv & 3], pw[pc][0]); MFZ(o[i][1], fr[nv & 3], pw[pc][1]); }
        else { MF(o[i][0], fr[nv & 3], pw[pc][0]); MF(o[i][1], fr[nv & 3], pw[pc][1]); }
        if constexpr (DOS && !DOK && i == 0) SM(IC(0));
        if constexpr (DOS && i < 7) SM(IC(i + 1));
        if constexpr (H == 15 && i == 0) { inv[0] = __builtin_amdgcn_rcpf(xg_sum(lsum[0])); inv[1] = __builtin_amdgcn_rcpf(xg_sum(lsum[1])); PIN(inv[0]); PIN(inv[1]); }
        if constexpr (H == 15 && i >= 3 && (i & 1)) {
          constexpr int os = (i - 3) >> 1;
          NOPS(3);
#pragma unroll
          for (int qb = 0; qb < 2; ++qb) {
            PIN(o[2 * os][qb]); PIN(o[2 * os + 1][qb]);
            const f32x4 a = o[2 * os][qb] * inv[qb], b = o[2 * os + 1][qb] * inv[qb];
            *(u32x4*)(od + (os * 2 + qb) * 1024) = (u32x4){pk2(a[0], a[1]), pk2(a[2], a[3]), pk2(b[0], b[1]), pk2(b[2], b[3])};
          }
        }
        SBAR();
        if constexpr ((nv & 3) == 3) endslot(IC(64 + (nv >> 2)));
      }
    });
    if constexpr (DOS) {
      if (__builtin_expect(__any(!(lsum[0] + lsum[1] <= LBOUND)), 0)) {
        NOPS(7);
        SBAR();
#pragma unroll
        for (int qb = 0; qb < 2; ++qb) {
          const f32x4 a = sc[ss][0][qb], b = sc[ss][1][qb];
          const float dl = xg_max(MX3(MX3(0.f, a[0], a[1]), MX3(a[2], a[3], b[0]), MX3(b[1], b[2], b[3])));
          const float f = EXP2(-dl);
          float ls = lold[qb];
          PIN(ls);
          ls *= f;
          if constexpr (DOV) {
#pragma unroll
            for (int d = 0; d < 8; ++d) o[d][qb] *= f;
          }
#pragma unroll
          for (int kt = 0; kt < 2; ++kt) {
            if constexpr (DOK) sc[sn][kt][qb] -= dl;
#pragma unroll
            for (int r = 0; r < 4; r += 2) {
              const float e0 = EXP2(sc[ss][kt][qb][r] - dl), e1 = EXP2(sc[ss][kt][qb][r + 1] - dl);
              ls += e0; ls += e1;
              PIN(ls);
              pw[pn][qb][kt * 2 + (r >> 1)] = pk2(e0, e1);
            }
          }
          lsum[qb] = ls;
          negm[qb] -= dl;
          PIN(pw[pn][qb]); PIN(negm[qb]);
        }
        SBAR();
        NOPS(1);
      }
      SBAR();
    }
  });
  STAMP(17);
  NOPS(7);
#pragma unroll
  for (int qb = 0; qb < 2; ++qb) {
    PIN(o[6][qb]); PIN(o[7][qb]);
    const f32x4 a = o[6][qb] * inv[qb], b = o[7][qb] * inv[qb];
    *(u32x4*)(od + (6 + qb) * 1024) = (u32x4){pk2(a[0], a[1]), pk2(a[2], a[3]), pk2(b[0], b[1]), pk2(b[2], b[3])};
  }
  STAMP(18);
  STAMP_OUT(0, 19);
}

namespace k3 {
constexpr int YS = 4 * 32768, LDS = YS + 8 * 64 * 4;
constexpr int XD = 12;
constexpr Sched make_sched() {
  Sched s;
  for (int i = 0; i < 4; ++i) s.add(-1, OG, i);
  for (int i = 0; i < 4; ++i) s.add(-1, OX, i);
  for (int i = 4; i < 8; ++i) s.add(-1, OG, i);
  for (int t = 0; t < 64; ++t) {
    if (t == 0) for (int i = 4; i < 8; ++i) s.add(t, OX, i);
    if (t == 1) for (int i = 8; i < 12; ++i) s.add(t, OG, i);
    if (t == 2) for (int i = 8; i < 12; ++i) s.add(t, OX, i);
    if (t == 4 || t == 5) { s.add(t, OG, 4 + 2 * t); s.add(t, OG, 5 + 2 * t); }
    if (t == 12) { for (int i = 0; i < 5; ++i) s.add(t, OE, i); s.add(t, OM, 0); }
    for (int p = 0; p < 26; ++p) { const int a = 3 + p, b2 = 2 * p + 1; if ((a > b2 ? a : b2) == t) { s.add(t, OX, XD + 2 * p); s.add(t, OX, XD + 1 + 2 * p); } }
    for (int v = 4; v < 16; ++v) for (int p = 0; p < 4; ++p) if (4 * (v - 3) + 3 + p == t) s.add(t, OG, 4 * v + p);
  }
  return s;
}
constexpr Sched SC = make_sched();
}

__global__ __launch_bounds__(512, 2) void k3_out(const u16* __restrict__ x, const u16* __restrict__ wo, char* __restrict__ ws,
                                                 u16* __restrict__ out) {
  using namespace k3;
  __shared__ __attribute__((aligned(16))) char lds_raw[LDS];
  LAS char* const lds = (LAS char*)lds_raw;
  const int t = threadIdx.x, l = t & 63, bid = blockIdx.x, c = l & 15, g = l >> 4;
  const int wid = __builtin_amdgcn_readfirstlane(t >> 6);
  const int xcd = bid & 7, within = bid >> 3;
  const int kh = xcd & 1, mb = 4 * (xcd >> 1) + (within >> 3), nb = within & 7;
  const int rbw = wid & 3, kg = wid >> 2;
  const unsigned lds0 = (unsigned)(uintptr_t)lds;
  STAMP_DECL
  STAMP(0);

  const char* const wo_b = (const char*)wo + (size_t)kh * 2048 * (DM * 2) + nb * 256;
  const unsigned wo_v = (unsigned)(((8 * wid + ((l & 31) >> 2)) * DM + (l >> 5) * 32 + ((l & 3) ^ (2 * (wid & 1))) * 8) * 2);
  const unsigned ldsw = lds0 + wid * 2048;
  const char* const o_b = ws + WS_O + (size_t)mb * 4 * 262144 + kh * 131072;
  const unsigned o_v = (unsigned)(rbw * 262144 + kg * 4096 + l * 16);
  bf16x8 xf[XD];
  const int erow = mb * 128 + rbw * 32 + kg * 16 + c, ecol = nb * 128 + kh * 64 + 4 * g;
  u32x4 fm;
  u32x2 xe[4];
  f32x2 st;
  auto issue = [&](auto Sl) AI {
    constexpr int sl = Sl, q0 = SC.first(sl), nq = SC.count(sl);
    sfor<nq>([&](auto Qi) AI {
      constexpr int k = SC.kind[q0 + Qi], i = SC.idx[q0 + Qi];
      if constexpr (k == OX) gld16<(i & 3) * 1024>(xf[i % XD], o_v, o_b + (size_t)(i >> 2) * 8192);
      if constexpr (k == OG) {
        constexpr int v = i >> 2, p = i & 3;
        const char* sb = wo_b + (size_t)(v * 128 + (p >> 1) * 64) * (DM * 2);
        if constexpr (p & 1) glds<128>(ldsw + (v & 3) * 32768 + (p >> 1) * 16384 + 1024 - 128, wo_v, sb);
        else glds<0>(ldsw + (v & 3) * 32768 + (p >> 1) * 16384, wo_v, sb);
      }
      if constexpr (k == OM) gld16<0>(fm, (unsigned)(l * 16), ws + WS_MX);
      if constexpr (k == OE && i < 4) gld8<32 * i>(xe[i], (unsigned)((erow * DM + ecol) * 2), x);
      if constexpr (k == OE && i == 4) gld8<0>(st, (unsigned)(erow * 8), ws + WS_ST);
    });
  };
  issue(IC(-1));

  unsigned xnp[8], gate[8], key = 0;
  int idx = 0;
  bool owner = false;
  f32x4 yp[7];

  f32x4 acc[8][2];
  LBase trb[2];
  trb[0].init(lds0 + kg * 16384 + g * 2048 + (c >> 2) * 64 + (g & 1) * 32 + (c & 3) * 8);
  trb[1].init(lds0 + kg * 16384 + g * 2048 + (c >> 2) * 64 + ((g & 1) ^ 1) * 32 + (c & 3) * 8);
  bf16x8 wf[2][4];
  auto WRD = [&](auto Si) AI {
    constexpr int s = Si, base = ((s >> 2) & 3) * 32768 + ((s >> 1) & 1) * 8192 + (s & 1) * 1024;
    sfor<4>([&](auto Dd) AI {
      constexpr int d = Dd, o = base + (d >> 1) * 512;
      wf[s & 1][d] = cat8(trd(trb[d & 1].template at<o>()), trd(trb[d & 1].template at<o + 256>()));
    });
  };

  waitbar<SC.younger(OG, 3, -1)>();
  STAMP(1);
  WRD(IC(0));
  SBAR();
  sfor<64>([&](auto Si) AI {
    constexpr int s = Si, u = s >> 2, x0 = (s & ~1) % XD, x1 = (s | 1) % XD;
    if constexpr (s + 1 < 64) WRD(IC(s + 1));
    SBAR();
    if constexpr (!(s & 1)) waitv<SC.younger(OX, s + 1, s - 1)>(xf[x0], xf[x1]);
    sfor<4>([&](auto Dd) AI {
      constexpr int d = Dd, dt = (s & 1) * 4 + d;
      if constexpr (s < 2) { MFZ(acc[dt][0], wf[s & 1][d], xf[x0]); MFZ(acc[dt][1], wf[s & 1][d], xf[x1]); }
      else { MF(acc[dt][0], wf[s & 1][d], xf[x0]); MF(acc[dt][1], wf[s & 1][d], xf[x1]); }
    });
    SBAR();
    issue(Si);
    if constexpr (s == 26) {
      waitv<SC.younger(OM, 0, s)>(fm);
      key = wave_max_u32(umax(umax(fm[0], fm[1]), umax(fm[2], fm[3])));
      idx = SEQ - 1 - (int)(key & 0xffffu);
      owner = (idx >> 7) == mb;
      if (owner) {
        const unsigned yv = (unsigned)(((t >> 4) * 1024 + nb * 128 + kh * 64 + (t & 15) * 4) * 4);
        sfor<7>([&](auto I) AI { constexpr int q = I; gld16<0>(yp[q], yv, ws + WS_YP + (size_t)q * 32 * 4096); });
      }
      PIN(st);
    }
    if constexpr (s >= 28 && s < 36) {
      constexpr int j = s - 28;
      PIN(xe[j >> 1]);
      const unsigned xw = xe[j >> 1][j & 1];
      const unsigned dd = pk2(bflo(xw) - st[0], bfhi(xw) - st[0]);
      xnp[j] = pk2(bflo(dd) * st[1], bfhi(dd) * st[1]);
      gate[j] = pk2(silu_f(bflo(xnp[j])), silu_f(bfhi(xnp[j])));
      PIN(xnp[j]); PIN(gate[j]);
    }
    if constexpr (s == 45) {
      if (owner) {
        f32x4 a = {};
#pragma unroll
        for (int q = 0; q < 7; ++q) { PIN(yp[q]); a += yp[q]; }
#pragma unroll
        for (int j = 0; j < 4; ++j) a[j] = xg_sum(a[j]);
        *(LAS f32x4*)(lds + YS + (wid * 64 + c * 4) * 4) = a;
      }
    }
    if constexpr (s == 48) {
      if (owner && erow == idx) {
        const unsigned ord = key >> 16, bits = (ord & 0x8000u) ? (ord & 0x7fffu) : (~ord & 0xffffu);
        const float sg = rbf(sigmoid_f(bflo(bits)));
#pragma unroll
        for (int q = 0; q < 4; ++q) {
          f32x4 a = {};
#pragma unroll
          for (int p = 0; p < 8; ++p) a += *(LAS const f32x4*)(lds + YS + (p * 64 + q * 16 + 4 * g) * 4);
#pragma unroll
          for (int j = 0; j < 2; ++j) {
            const float lo = rbf(bflo(xnp[2 * q + j]) + rbf(rbf(a[2 * j]) * sg)), hi = rbf(bfhi(xnp[2 * q + j]) + rbf(rbf(a[2 * j + 1]) * sg));
            gate[2 * q + j] = pk2(silu_f(lo), silu_f(hi));
          }
        }
      }
    }
    SBAR();
    if constexpr ((s & 3) == 2 && u < 15) {
      if constexpr (s == 46) waitbar_l<SC.younger(OG, 4 * u + 7, s)>();
      else waitbar<SC.younger(OG, 4 * u + 7, s)>();
      if constexpr (u % 2 == 1) STAMP(2 + u / 2);
    }
  });
  STAMP(9);

  f32x4 giv[4], kep[4], oth[4];
  NOPS(7);
  {
    LAS char* dst = lds + (wid ^ 4) * 8192 + l * 16;
    LAS const char* src = lds + wid * 8192 + l * 16;
#pragma unroll
    for (int d = 0; d < 8; ++d) { PIN(acc[d][0]); PIN(acc[d][1]); }
    if (kg) {
#pragma unroll
      for (int d = 0; d < 8; ++d) { *(LAS f32x4*)(dst + d * 1024) = acc[d][0]; acc[d][0] = acc[d][1]; }
    } else {
#pragma unroll
      for (int d = 0; d < 8; ++d) *(LAS f32x4*)(dst + d * 1024) = acc[d][1];
    }
    waitbar_l<63>();
    STAMP(10);
    if (kh) {
#pragma unroll
      for (int d = 0; d < 4; ++d) { giv[d] = acc[d][0]; kep[d] = acc[4 + d][0]; }
    } else {
#pragma unroll
      for (int d = 0; d < 4; ++d) { giv[d] = acc[4 + d][0]; kep[d] = acc[d][0]; }
    }
    const char* xb = ws + WS_XC + (size_t)(bid ^ 1) * 32768;
#pragma unroll
    for (int d = 0; d < 4; ++d) {
      const f32x4 a = giv[d] + *(LAS const f32x4*)(src + (kh ^ 1) * 4096 + d * 1024);
      asm volatile("global_store_dwordx4 %0, %1, %2 sc1\n\ts_nop 1" ::"v"((unsigned)(t * 16 + d * 8192)), "v"(a), "s"(xb) : "memory");
    }
#pragma unroll
    for (int d = 0; d < 4; ++d) kep[d] += *(LAS const f32x4*)(src + kh * 4096 + d * 1024);
  }
  waitv<0>();
  STAMP(11);
  if (l == 0) __hip_atomic_store((unsigned*)(ws + WS_FL + ((bid ^ 1) * 8 + wid) * 64), 1u, __ATOMIC_RELAXED, __HIP_MEMORY_SCOPE_AGENT);
  for (int spin = 0; spin < (1 << 16); ++spin) {
    if (__builtin_amdgcn_readfirstlane(__hip_atomic_load((unsigned*)(ws + WS_FL + (bid * 8 + wid) * 64), __ATOMIC_RELAXED, __HIP_MEMORY_SCOPE_AGENT)) != 0u) break;
    __builtin_amdgcn_s_sleep(1);
  }
  STAMP(12);
  {
    const char* xb = ws + WS_XC + (size_t)bid * 32768;
#pragma unroll
    for (int d = 0; d < 4; ++d) asm volatile("global_load_dwordx4 %0, %1, %2 sc1" : "=v"(oth[d]) : "v"((unsigned)(t * 16 + d * 8192)), "s"(xb) : "memory");
    waitv<0>(oth[0], oth[1]);
    PIN(oth[2]); PIN(oth[3]);
  }
  STAMP(13);
  u16* op = out + (size_t)erow * DM + ecol;
#pragma unroll
  for (int d = 0; d < 4; ++d) {
    const f32x4 a = kep[d] + oth[d];
    u32x2 res;
#pragma unroll
    for (int j = 0; j < 2; ++j) {
      const unsigned sp = pk2(a[2 * j], a[2 * j + 1]), gp = gate[2 * d + j], xw = xe[d][j];
      const unsigned pr = pk2(bflo(sp) * bflo(gp), bfhi(sp) * bfhi(gp));
      res[j] = pk2(bflo(xw) + bflo(pr), bfhi(xw) + bfhi(pr));
    }
    *(u32x2*)(op + d * 16) = res;
  }
  STAMP(14);
  STAMP_OUT(1, 15);
}

extern "C" void kernel_launch(void* const* d_in, const int* in_sizes, int n_in, void* d_out, int out_size, void* d_ws, size_t ws_size,
                              hipStream_t stream) {
  const u16* x = (const u16*)d_in[0];
  const u16* kc = (const u16*)d_in[1];
  const u16* vc = (const u16*)d_in[2];
  const u16* wq = (const u16*)d_in[3];
  const u16* wo = (const u16*)d_in[4];
  const u16* wr = (const u16*)d_in[6];
  const u16* wg = (const u16*)d_in[7];
  const u16* wu = (const u16*)d_in[8];
  const u16* wd = (const u16*)d_in[9];
  char* ws = (char*)d_ws;
  k1_prep<<<384, 512, 0, stream>>>(x, kc, vc, wr, wq, ws);
  k2_attn<<<256, 512, 0, stream>>>(wq, wg, wu, wd, wo, ws);
  k3_out<<<256, 512, 0, stream>>>(x, wo, ws, (u16*)d_out);
}
```

```cpp
#include <hip/hip_runtime.h>
#include <stdint.h>
#include <type_traits>

typedef unsigned short u16;
typedef short bf16x8 __attribute__((ext_vector_type(8)));
typedef short s16x4 __attribute__((ext_vector_type(4)));
typedef float f32x4 __attribute__((ext_vector_type(4)));
typedef float f32x2 __attribute__((ext_vector_type(2)));
typedef unsigned u32x4 __attribute__((ext_vector_type(4)));
typedef unsigned u32x2 __attribute__((ext_vector_type(2)));
typedef __bf16 bf2_t __attribute__((ext_vector_type(2)));
#define LAS __attribute__((address_space(3)))
#define DI __device__ __forceinline__
#define AI __attribute__((always_inline))
#define SBAR() __builtin_amdgcn_sched_barrier(0)
template <class T> __device__ __forceinline__ void PIN(T& x) { asm volatile("" : "+v"(x)); }
template <class B> DI void MF(f32x4& acc, const bf16x8& a, const B& b) { asm volatile("v_mfma_f32_16x16x32_bf16 %0, %1, %2, %0" : "+v"(acc) : "v"(a), "v"(b)); }
template <class B> DI void MFC(f32x4& d, const bf16x8& a, const B& b, const f32x4& c) { asm volatile("v_mfma_f32_16x16x32_bf16 %0, %1, %2, %3" : "=&v"(d) : "v"(a), "v"(b), "v"(c)); }
template <class B> DI void MFZ(f32x4& d, const bf16x8& a, const B& b) { asm volatile("v_mfma_f32_16x16x32_bf16 %0, %1, %2, 0" : "=&v"(d) : "v"(a), "v"(b)); }
#define NOPS(n) asm volatile("s_nop " #n)
#define MFMA16(a, b, c) __builtin_amdgcn_mfma_f32_16x16x16bf16_1k((a), (b), (c), 0, 0, 0)
#define IC(n) std::integral_constant<int, (n)>{}

constexpr int SEQ = 2048, DM = 1024, DH = 128, SK = 2048, WIN = 512, FF = 3584;
constexpr size_t WS_XN = 0;
constexpr size_t WS_KV = 4u << 20;
constexpr size_t WS_ST = 5u << 20;
constexpr size_t WS_EP = WS_ST + 16384;
constexpr size_t WS_G0 = WS_ST + 32768;
constexpr size_t WS_YP = WS_ST + 65536;
constexpr size_t WS_FO = 7u << 20;
constexpr size_t WS_FY = WS_FO + 32768;
constexpr size_t WS_FX = WS_FO + 65536;
constexpr size_t WS_O = 8u << 20;
constexpr size_t WS_XC = 24u << 20;

template <int I, int N, class F> DI void sfor_impl(F&& f) {
  if constexpr (I < N) { f(std::integral_constant<int, I>{}); sfor_impl<I + 1, N>(f); }
}
template <int N, class F> DI void sfor(F&& f) { sfor_impl<0, N>(f); }

DI unsigned pk2(float a, float b) { f32x2 v = {a, b}; return __builtin_bit_cast(unsigned, __builtin_convertvector(v, bf2_t)); }
DI float rbf(float a) { return (float)(__bf16)a; }
DI float bflo(unsigned u) { return __uint_as_float(u << 16); }
DI float bfhi(unsigned u) { return __uint_as_float(u & 0xffff0000u); }
DI float sigmoid_f(float v) { return __builtin_amdgcn_rcpf(1.f + __builtin_amdgcn_exp2f(v * -1.4426950408889634f)); }
DI float sel2(bool c, float a, float b) { PIN(a); PIN(b); return c ? a : b; }
DI float silu_f(float v) { return v * sigmoid_f(v); }
template <int CTRL> DI float dppf(float x) { return __builtin_bit_cast(float, __builtin_amdgcn_mov_dpp(__builtin_bit_cast(int, x), CTRL, 0xf, 0xf, true)); }
template <int CTRL> DI unsigned dppu(unsigned x) { return (unsigned)__builtin_amdgcn_mov_dpp((int)x, CTRL, 0xf, 0xf, true); }
constexpr int XOR1 = 0xB1, XOR2 = 0x4E, XOR7 = 0x141, XOR8 = 0x128;
DI float xg_sum(float x) {
  auto s = __builtin_amdgcn_permlane16_swap(__float_as_uint(x), __float_as_uint(x), false, false);
  x = __uint_as_float(s[0]) + __uint_as_float(s[1]);
  auto t = __builtin_amdgcn_permlane32_swap(__float_as_uint(x), __float_as_uint(x), false, false);
  return __uint_as_float(t[0]) + __uint_as_float(t[1]);
}
DI float xg_max(float x) {
  auto s = __builtin_amdgcn_permlane16_swap(__float_as_uint(x), __float_as_uint(x), false, false);
  x = __builtin_fmaxf(__uint_as_float(s[0]), __uint_as_float(s[1]));
  auto t = __builtin_amdgcn_permlane32_swap(__float_as_uint(x), __float_as_uint(x), false, false);
  return __builtin_fmaxf(__uint_as_float(t[0]), __uint_as_float(t[1]));
}
DI float wave_sum(float v) { v += dppf<XOR1>(v); v += dppf<XOR2>(v); v += dppf<XOR7>(v); v += dppf<XOR8>(v); return xg_sum(v); }
DI unsigned umax(unsigned a, unsigned b) { return a > b ? a : b; }
DI unsigned wave_max_u32(unsigned v) {
  v = umax(v, dppu<XOR1>(v)); v = umax(v, dppu<XOR2>(v)); v = umax(v, dppu<XOR7>(v)); v = umax(v, dppu<XOR8>(v));
  auto s = __builtin_amdgcn_permlane16_swap(v, v, false, false); v = umax(s[0], s[1]);
  auto t = __builtin_amdgcn_permlane32_swap(v, v, false, false); v = umax(t[0], t[1]);
  return __builtin_amdgcn_readfirstlane(v);
}

template <int OFF, bool DEV = false> DI void glds(unsigned ldsaddr, unsigned voff, const void* sbase) {
  if constexpr (DEV) asm volatile("s_mov_b32 m0, %0\n\ts_nop 0\n\tglobal_load_lds_dwordx4 %1, %2 offset:%3 sc1" ::"s"(ldsaddr), "v"(voff), "s"(sbase), "i"(OFF) : "memory");
  else asm volatile("s_mov_b32 m0, %0\n\ts_nop 0\n\tglobal_load_lds_dwordx4 %1, %2 offset:%3" ::"s"(ldsaddr), "v"(voff), "s"(sbase), "i"(OFF) : "memory");
}
DI void touch(unsigned ldsaddr, unsigned voff, const void* sbase) {
  asm volatile("s_mov_b32 m0, %0\n\ts_nop 0\n\tglobal_load_lds_dword %1, %2" ::"s"(ldsaddr), "v"(voff), "s"(sbase) : "memory");
}
template <int OFF, bool DEV = false, class T> DI void gld16(T& dst, unsigned voff, const void* sbase) {
  if constexpr (DEV) asm volatile("global_load_dwordx4 %0, %1, %2 offset:%3 sc1" : "=v"(dst) : "v"(voff), "s"(sbase), "i"(OFF) : "memory");
  else asm volatile("global_load_dwordx4 %0, %1, %2 offset:%3" : "=v"(dst) : "v"(voff), "s"(sbase), "i"(OFF) : "memory");
}
template <class T> DI void gld16v(T& dst, const void* vaddr) { asm volatile("global_load_dwordx4 %0, %1, off" : "=v"(dst) : "v"(vaddr) : "memory"); }
template <int OFF, bool DEV = false, class T> DI void gld8(T& dst, unsigned voff, const void* sbase) {
  if constexpr (DEV) asm volatile("global_load_dwordx2 %0, %1, %2 offset:%3 sc1" : "=v"(dst) : "v"(voff), "s"(sbase), "i"(OFF) : "memory");
  else asm volatile("global_load_dwordx2 %0, %1, %2 offset:%3" : "=v"(dst) : "v"(voff), "s"(sbase), "i"(OFF) : "memory");
}
template <int OFF> DI void gld2(unsigned& dst, unsigned voff, const void* sbase) {
  asm volatile("global_load_ushort %0, %1, %2 offset:%3" : "=v"(dst) : "v"(voff), "s"(sbase), "i"(OFF) : "memory");
}
template <int OFF, class T> DI void gst16(const T& v, unsigned voff, const void* sbase) {
  asm volatile("global_store_dwordx4 %0, %1, %2 offset:%3 sc1\n\ts_nop 1" ::"v"(voff), "v"(v), "s"(sbase), "i"(OFF) : "memory");
}
template <class T> DI void gst8(const T& v, unsigned voff, const void* sbase) {
  asm volatile("global_store_dwordx2 %0, %1, %2 sc1" ::"v"(voff), "v"(v), "s"(sbase) : "memory");
}
template <int OFF> DI void gst4(float v, unsigned voff, const void* sbase) {
  asm volatile("global_store_dword %0, %1, %2 offset:%3 sc1" ::"v"(voff), "v"(v), "s"(sbase), "i"(OFF) : "memory");
}
constexpr int SPINS = 1 << 16;
DI void poll8(unsigned voff, const void* sbase, unsigned lo, unsigned hi, unsigned lomask) {
  for (int spin = 0; spin < SPINS; ++spin) {
    u32x2 v;
    asm volatile("global_load_dwordx2 %0, %1, %2 sc1\n\ts_waitcnt vmcnt(0)" : "=&v"(v) : "v"(voff), "s"(sbase) : "memory");
    if (__all(((v[0] & lomask) == lo) & (v[1] == hi))) break;
    __builtin_amdgcn_s_sleep(1);
  }
}
template <int N> DI void waitv() { static_assert(N >= 0, "count"); asm volatile("s_waitcnt vmcnt(%0)" ::"n"(N > 63 ? 63 : N) : "memory"); }
template <int N, class T> DI void waitv(T& x) { static_assert(N >= 0, "count"); asm volatile("s_waitcnt vmcnt(%1)" : "+v"(x) : "n"(N > 63 ? 63 : N) : "memory"); }
template <int N, class T> DI void waitv(T& x, T& y) { static_assert(N >= 0, "count"); asm volatile("s_waitcnt vmcnt(%2)" : "+v"(x), "+v"(y) : "n"(N > 63 ? 63 : N) : "memory"); }
template <int N> DI void waitbar() { static_assert(N >= 0, "count"); asm volatile("s_waitcnt vmcnt(%0)\n\ts_barrier" ::"n"(N > 63 ? 63 : N) : "memory"); }
template <int N> DI void waitbar_l() { static_assert(N >= 0, "count"); asm volatile("s_waitcnt vmcnt(%0) lgkmcnt(0)\n\ts_barrier" ::"n"(N > 63 ? 63 : N) : "memory"); }
typedef short v4i16_t __attribute__((ext_vector_type(4)));
DI s16x4 trd(LAS const char* p) { return __builtin_amdgcn_ds_read_tr16_b64_v4i16((LAS v4i16_t*)p); }
struct LBase {
  unsigned lo, hi;
  DI void init(unsigned a) { lo = a; hi = a + 65536; PIN(lo); PIN(hi); }
  template <int OFF> DI LAS const char* at() const { return (LAS const char*)(uintptr_t)(OFF < 65536 ? lo : hi) + (OFF & 65535); }
};
DI bf16x8 cat8(s16x4 lo, s16x4 hi) { return __builtin_shufflevector(lo, hi, 0, 1, 2, 3, 4, 5, 6, 7); }

enum { OX, OG, OF, OM, OS, OP, OE, OW };
struct Sched {
  int n; short kind[400], idx[400], slot[400];
  constexpr Sched() : n(0), kind{}, idx{}, slot{} {}
  constexpr void add(int s, int k, int i) { kind[n] = (short)k; idx[n] = (short)i; slot[n] = (short)s; ++n; }
  constexpr int younger(int k, int i, int s) const {
    int p = -1;
    for (int q = 0; q < n; ++q) if (kind[q] == k && idx[q] == i) p = q;
    if (p < 0 || slot[p] > s) return -1;
    int c = 0;
    for (int q = p + 1; q < n; ++q) if (slot[q] <= s) ++c;
    return c;
  }
  constexpr int first(int s) const { int q = 0; while (q < n && slot[q] < s) ++q; return q; }
  constexpr int count(int s) const { int c = 0; for (int q = 0; q < n; ++q) if (slot[q] == s) ++c; return c; }
};

#define STAMP_DECL
#define STAMP(i)
#define STAMP_OUT(kid, n)

constexpr int RING = 4 * 32768, FB = RING, RED1 = FB + 8 * 3072, HSC = RED1 + 1024, YS = HSC + 512, DUMMY = YS + 2048, LDS_BYTES = DUMMY + 256;
constexpr int STAGE = 3 * 32768;
constexpr int XD = 16, XD2 = 12;
constexpr float LBOUND = 65536.f;
constexpr float QSCALE = 0.08838834764831845f * 1.4426950408889634f;
constexpr int B0 = 130;
constexpr Sched make_sched() {
  Sched s;
  for (int i = 0; i < 12; ++i) s.add(-2, OG, i);
  for (int i = 0; i < 12; ++i) s.add(-1, OX, i);
  for (int t = 0; t < 128; ++t) {
    if (t == 0 || t == 2) { s.add(t, OX, 12 + t); s.add(t, OX, 13 + t); }
    for (int p = 0; p < 24; ++p) { const int a = 3 + p, b2 = 2 * p + 1; if ((a > b2 ? a : b2) == t) { s.add(t, OX, 16 + 2 * p); s.add(t, OX, 17 + 2 * p); } }
    for (int v = 3; v < 18; ++v) for (int p = 0; p < 4; ++p) if (8 * (v - 3) + 7 + p == t) s.add(t, OG, 4 * v + p);
    if (t == 20) { s.add(t, OM, 0); s.add(t, OM, 1); }
    if (t == 59) s.add(t, OP, 0);
    if (t == 75) s.add(t, OP, 1);
    if (t == 83 || t == 85 || t == 91 || t == 93) s.add(t, OF, 8 + ((t - 83) >> 3) * 2 + (((t - 83) >> 1) & 1));
    if (t >= 99 && t < 123 && (t - 99) % 3 == 0) s.add(t, OF, (t - 99) / 3);
    if (t == 123) for (int i = 0; i < 4; ++i) s.add(t, OS, i);
  }
  for (int i = 72; i < 80; ++i) s.add(128, OG, i);
  for (int i = 0; i < 5; ++i) s.add(128, OE, i);
  for (int i = 0; i < XD2; ++i) s.add(129, OX, 64 + i);
  for (int t = 0; t < 64; ++t) {
    if ((t & 1) && t - 1 + XD2 < 64) { s.add(B0 + t, OX, 64 + t - 1 + XD2); s.add(B0 + t, OX, 64 + t + XD2); }
    for (int v = 4; v < 16; ++v) for (int p = 0; p < 4; ++p) if (4 * (v - 3) + 3 + p == t) s.add(B0 + t, OG, 64 + 4 * v + p);
  }
  return s;
}
constexpr Sched SC = make_sched();

__global__ __launch_bounds__(512, 2) void k_fused(const u16* __restrict__ x, const u16* __restrict__ kc, const u16* __restrict__ vc,
                                                  const u16* __restrict__ wq, const u16* __restrict__ wo, const u16* __restrict__ wr,
                                                  const u16* __restrict__ wgate, const u16* __restrict__ wup, const u16* __restrict__ wdown,
                                                  char* __restrict__ ws, u16* __restrict__ out) {
  __shared__ __attribute__((aligned(16))) char lds_raw[LDS_BYTES];
  LAS char* const lds = (LAS char*)lds_raw;
  const int t = threadIdx.x, l = t & 63, bid = blockIdx.x, c = l & 15, g = l >> 4;
  const int wid = __builtin_amdgcn_readfirstlane(t >> 6);
  const int xcd = bid & 7, within = bid >> 3, head = xcd * 4 + (within & 3), rb = within >> 2;
  const int fj = xcd * 28 + (within < 28 ? within : within - 28);
  const int kh = xcd >> 2, mb = 2 * rb + (xcd & 1), nb = ((xcd >> 1) & 1) * 4 + (within & 3);
  const int rbw = wid & 3, kg = wid >> 2;
  const unsigned lds0 = (unsigned)(uintptr_t)lds;
  STAMP_DECL
  STAMP(0);

  unsigned tlo, thi;
  {
    const int row = 8 * bid + wid;
    u32x4 a0, a1, w0, w1, kvo;
    u32x2 ep, klo, khi;
    unsigned e[8];
    gld16<0>(a0, (unsigned)(row * DM * 2 + l * 16), x); gld16<1024>(a1, (unsigned)(row * DM * 2 + l * 16), x);
    gld16<0>(w0, (unsigned)(l * 16), wr); gld16<1024>(w1, (unsigned)(l * 16), wr);
    gld8<0, true>(ep, 0u, ws + WS_EP);
    const int fn = (bid * 4 + (wid & 3)) & 255, kvh = bid >> 6;
    int isv, un, fi;
    if (fn < 16) { isv = 0; un = fn >> 3; fi = fn & 7; }
    else if (fn >= 240) { isv = 1; un = 14 + ((fn - 240) >> 3); fi = fn & 7; }
    else { const int m = fn - 16; isv = m & 1; un = (m >> 4) + (isv ? 0 : 2); fi = (m & 15) >> 1; }
    if (wid < 4) {
      touch(lds0 + DUMMY, (unsigned)((rb * 128 + wid * 32 + (l >> 1)) * 8192 + (l & 1) * 128), (const char*)wq + head * 256);
      waitv<1>();
    } else if (!isv) {
      const unsigned ko = (unsigned)(((kvh * SK + (SK - WIN) + un * 32 + (fi >> 2) * 16 + c) * DH + (fi & 3) * 32 + 4 * g) * 2);
      gld8<0>(klo, ko, kc); gld8<32>(khi, ko, kc);
      waitv<0>(klo, khi);
      kvo = (u32x4){klo[0], klo[1], khi[0], khi[1]};
    } else {
      const int d = 32 * (fi >> 1) + 8 * (c >> 2) + 4 * (fi & 1) + (c & 3);
      const unsigned vo = (unsigned)(((kvh * SK + (SK - WIN) + un * 32 + 4 * g + 8) * DH + d) * 2);
      sfor<8>([&](auto J) AI { constexpr int j = J; gld2<(16 * (j >> 2) + (j & 3) - 8) * DH * 2>(e[j], vo, vc); });
      waitv<0>(e[0], e[1]);
#pragma unroll
      for (int j = 2; j < 8; ++j) PIN(e[j]);
      kvo = (u32x4){e[0] | (e[1] << 16), e[2] | (e[3] << 16), e[4] | (e[5] << 16), e[6] | (e[7] << 16)};
    }
    PIN(a0); PIN(a1); PIN(w0); PIN(w1); PIN(ep);
    if (wid >= 4) gst16<0>(kvo, (unsigned)((bid * 4 + (wid & 3)) * 1024 + l * 16), ws + WS_KV);
    STAMP(1);
    {
      const unsigned long long tg = (((unsigned long long)(unsigned)__builtin_amdgcn_readfirstlane(ep[1]) << 32) | (unsigned)__builtin_amdgcn_readfirstlane(ep[0])) + 1ull;
      tlo = (unsigned)tg; thi = (unsigned)(tg >> 32);
    }
    float v[16];
#pragma unroll
    for (int j = 0; j < 4; ++j) { v[2 * j] = bflo(a0[j]); v[2 * j + 1] = bfhi(a0[j]); v[8 + 2 * j] = bflo(a1[j]); v[9 + 2 * j] = bfhi(a1[j]); }
    float s = 0.f;
#pragma unroll
    for (int j = 0; j < 16; ++j) s += v[j];
    const float mean = wave_sum(s) * (1.f / DM);
    float q = 0.f;
#pragma unroll
    for (int j = 0; j < 16; ++j) { const float d = v[j] - mean; q += d * d; }
    const float var = wave_sum(q) * (1.f / DM);
    const float mbf = rbf(mean), rden = 1.f / rbf(sqrtf(rbf(rbf(var) + 1e-5f)));
    float rr = 0.f;
    u32x4 o0, o1;
#pragma unroll
    for (int j = 0; j < 8; ++j) {
      const unsigned dd = pk2(v[2 * j] - mbf, v[2 * j + 1] - mbf);
      const unsigned nn = pk2(bflo(dd) * rden, bfhi(dd) * rden);
      const unsigned ww = j < 4 ? w0[j] : w1[j - 4];
      rr += bflo(nn) * bflo(ww); rr += bfhi(nn) * bfhi(ww);
      if (j < 4) o0[j] = nn; else o1[j - 4] = nn;
    }
    *(LAS u32x4*)(lds + STAGE + (l * 8 + (wid ^ (l & 7))) * 16) = o0;
    *(LAS u32x4*)(lds + STAGE + 8192 + (l * 8 + (wid ^ (l & 7))) * 16) = o1;
    rr = wave_sum(rr);
    if (l == 0) {
      const unsigned bits = pk2(rr, 0.f) & 0xffffu;
      const unsigned ord = (bits & 0x8000u) ? (~bits & 0xffffu) : (bits | 0x8000u);
      *(LAS unsigned*)(lds + STAGE + 16384 + wid * 4) = (ord << 11) | (unsigned)(SEQ - 1 - row);
      *(LAS f32x2*)(lds + STAGE + 16384 + 32 + wid * 8) = (f32x2){mbf, rden};
    }
    waitbar_l<63>();
    if (wid >= 4) {
#pragma unroll
      for (int hh = 0; hh < 4; ++hh) {
        const int p = hh * 256 + (t - 256), ch = p >> 3, r = 8 * bid + ((p & 7) ^ (ch & 7));
        const u32x4 o = *(LAS const u32x4*)(lds + STAGE + p * 16);
        gst16<0>(o, (unsigned)(((((r >> 5) * 32 + (ch >> 2)) * 2 + ((r >> 4) & 1)) * 64 + (ch & 3) * 16 + (r & 15)) * 16), ws + WS_XN);
      }
      if (t < 256 + 8) gst8(*(LAS const f32x2*)(lds + STAGE + 16384 + 32 + (t - 256) * 8), (unsigned)((8 * bid + t - 256) * 8), ws + WS_ST);
      waitv<0>();
    }
    waitbar_l<63>();
    if (t == 256) {
      const u32x4 ka = *(LAS const u32x4*)(lds + STAGE + 16384), kb = *(LAS const u32x4*)(lds + STAGE + 16400);
      const unsigned key = umax(umax(umax(ka[0], ka[1]), umax(ka[2], ka[3])), umax(umax(kb[0], kb[1]), umax(kb[2], kb[3])));
      gst8((u32x2){(tlo << 27) | key, (tlo >> 5) | (thi << 27)}, (unsigned)(bid * 8), ws + WS_G0);
    }
    STAMP(2);
  }
  const unsigned glo = tlo << 27, ghi = (tlo >> 5) | (thi << 27);

  const char* const wq_b = (const char*)wq + head * 256;
  const unsigned wq_v = (unsigned)(((8 * wid + ((l & 31) >> 2)) * 4096 + (l >> 5) * 32 + ((l & 3) ^ (2 * (wid & 1))) * 8) * 2);
  const char* const kv_b = ws + WS_KV + (size_t)(head >> 3) * 262144;
  const unsigned kv_v = (unsigned)(wid * 4096 + l * 16);
  const char* const xn_b = ws + WS_XN + (size_t)rb * 8 * 65536 + 4096;
  const unsigned xn_v = (unsigned)(wid * 65536 + l * 16);
  const char* const wo_b = (const char*)wo + (size_t)kh * 2048 * (DM * 2) + nb * 256;
  const char* const o_b = ws + WS_O + (size_t)mb * 4 * 262144 + kh * 131072;
  const unsigned ldsw = lds0 + wid * 2048, ldsk = lds0 + wid * 4096, ldsf = lds0 + FB + wid * 3072;
  const int erow = mb * 128 + rbw * 32 + kg * 16 + c, ecol = nb * 128 + kh * 64 + 4 * g;
  bf16x8 xf[XD];
  u32x4 fm[2], fx[4], fw[12];
  u32x2 xe[4];
  f32x2 st;
  unsigned key = 0;

  auto issue = [&](auto Sl) AI {
    constexpr int sl = Sl, q0 = SC.first(sl), nq = SC.count(sl);
    sfor<nq>([&](auto Qi) AI {
      constexpr int k = SC.kind[q0 + Qi], i = SC.idx[q0 + Qi];
      if constexpr (k == OX && i < 64) gld16<(i & 7) * 1024 - 4096, true>(xf[i % XD], xn_v, xn_b + (size_t)(i >> 3) * 8192);
      if constexpr (k == OX && i >= 64) gld16<(i & 3) * 1024, true>(xf[(i - 64) % XD2], (unsigned)(rbw * 262144 + kg * 4096 + l * 16), o_b + (size_t)((i - 64) >> 2) * 8192);
      if constexpr (k == OG) {
        constexpr int v = i >> 2, p = i & 3;
        if constexpr (v < 8) {
          const char* sb = wq_b + (size_t)(v * 128 + (p >> 1) * 64) * 8192;
          glds<(p & 1) * 128>(ldsw + (v & 3) * 32768 + (p >> 1) * 16384 + (p & 1) * (1024 - 128), wq_v, sb);
        } else if constexpr (v < 16) {
          glds<p * 1024, true>(ldsk + (v & 3) * 32768, kv_v, kv_b + (size_t)(v - 8) * 32768);
        } else {
          const unsigned wo_v = (unsigned)(((8 * wid + ((l & 31) >> 2)) * DM + (l >> 5) * 32 + ((l & 3) ^ (2 * (wid & 1))) * 8) * 2);
          const char* sb = wo_b + (size_t)((v - 16) * 128 + (p >> 1) * 64) * (DM * 2);
          glds<(p & 1) * 128>(ldsw + (v & 3) * 32768 + (p >> 1) * 16384 + (p & 1) * (1024 - 128), wo_v, sb);
        }
      }
      if constexpr (k == OF) {
        const int frho = l >> 2, fch = (l & 3) ^ (frho & 4 ? 2 : 0);
        if constexpr (i < 8) gld16v(fw[i], (const char*)(fch & 2 ? wup : wgate) + ((size_t)(128 * wid + 32 * (frho >> 2) + (frho & 3) + 4 * i) * FF + fj * 16 + (fch & 1) * 8) * 2);
        else gld16<(i - 8) * 64>(fw[i], (unsigned)((frho * DM + wid * 128 + fch * 8) * 2), (const char*)wdown + (size_t)fj * 16 * DM * 2);
      }
      if constexpr (k == OM) gld16<16 * i, true>(fm[i], (unsigned)(l * 32), ws + WS_G0);
      if constexpr (k == OS) {
        const int idx = SEQ - 1 - (int)(key & 2047u);
        gld16<i * 256, true>(fx[i], (unsigned)(((((idx >> 5) * 32 + 4 * wid + g) * 2 + ((idx >> 4) & 1)) * 64 + (idx & 15)) * 16), ws + WS_XN);
      }
      if constexpr (k == OP && i == 0)
        touch(lds0 + DUMMY, (unsigned)((within * 32 + (wid & 3) * 8 + (l >> 3)) * (FF * 2) + xcd * 896 + ((l & 7) < 7 ? (l & 7) : 6) * 128), wid < 4 ? wgate : wup);
      if constexpr (k == OP && i == 1)
        touch(lds0 + DUMMY, (unsigned)((kh * 2048 + within * 64 + wid * 8 + (l >> 3)) * (DM * 2) + ((xcd >> 1) & 1) * 1024 + (l & 7) * 128), wo);
      if constexpr (k == OE && i < 4) gld8<32 * i>(xe[i], (unsigned)((erow * DM + ecol) * 2), x);
      if constexpr (k == OE && i == 4) gld8<0, true>(st, (unsigned)(erow * 8), ws + WS_ST);
    });
  };
  auto endslot = [&](auto Sl) AI {
    constexpr int sl = Sl, u = sl >> 3, i = sl & 7;
    if constexpr (sl == 38) {
      waitv<SC.younger(OM, 1, sl - 1)>(fm[0], fm[1]);
      for (int spin = 0; spin < SPINS; ++spin) {
        const bool ok = ((fm[0][0] & 0xf8000000u) == glo) & (fm[0][1] == ghi) & ((fm[0][2] & 0xf8000000u) == glo) & (fm[0][3] == ghi) &
                        ((fm[1][0] & 0xf8000000u) == glo) & (fm[1][1] == ghi) & ((fm[1][2] & 0xf8000000u) == glo) & (fm[1][3] == ghi);
        if (__builtin_expect(__all(ok), 1)) break;
        __builtin_amdgcn_s_sleep(1);
        gld16<0, true>(fm[0], (unsigned)(l * 32), ws + WS_G0); gld16<16, true>(fm[1], (unsigned)(l * 32), ws + WS_G0);
        waitv<0>(fm[0], fm[1]);
      }
      key = wave_max_u32(umax(umax(fm[0][0], fm[0][2]), umax(fm[1][0], fm[1][2]))) & 0x07ffffffu;
      if (bid == 0 && t == 0) gst8((u32x2){tlo, thi}, 0u, ws + WS_EP);
    }
    issue(Sl);
    SBAR();
    if constexpr (i == 6 && u < 15) {
      waitbar<SC.younger(OG, 4 * u + 7, sl)>();
      if constexpr (u == 0 || u == 1 || u == 5 || u == 7 || u == 8 || u == 10 || u == 12 || u == 14)
        STAMP(u == 0 ? 4 : u == 1 ? 5 : u == 5 ? 6 : u == 7 ? 7 : u == 8 ? 8 : u == 10 ? 9 : u == 12 ? 10 : 11);
    }
  };

  issue(IC(-2));
  if (wid == 0) poll8((unsigned)((rb * 32 + (l & 31)) * 8), ws + WS_G0, glo, ghi, 0xf8000000u);
  waitbar<SC.younger(OG, 3, -2)>();
  issue(IC(-1));
  STAMP(3);

  LBase trb[2], frb;
  trb[0].init(lds0 + g * 2048 + (c >> 2) * 64 + (g & 1) * 32 + (c & 3) * 8);
  trb[1].init(lds0 + g * 2048 + (c >> 2) * 64 + ((g & 1) ^ 1) * 32 + (c & 3) * 8);
  frb.init(lds0 + l * 16);
  f32x4 acc[8][2];
  bf16x8 wf[2][4];
  auto WRD = [&](auto Si, auto Base) AI {
    constexpr int s = Si, base = Base;
    sfor<4>([&](auto Dd) AI {
      constexpr int d = Dd, o = base + (d >> 1) * 512;
      wf[s & 1][d] = cat8(trd(trb[d & 1].template at<o>()), trd(trb[d & 1].template at<o + 256>()));
    });
  };
#define WA(s) IC((((s) >> 3) & 3) * 32768 + (((s) >> 1) & 3) * 8192 + ((s) & 1) * 1024)
#define WB(s) IC((((s) >> 2) & 3) * 32768 + (((s) >> 1) & 1) * 8192 + ((s) & 1) * 1024)
  bf16x8 fr[4];
  auto FRD = [&](auto Ni) AI { constexpr int n = Ni; if constexpr (n < 256) fr[n & 3] = *(LAS const bf16x8*)(frb.at<((n >> 5) & 3) * 32768 + (n & 31) * 1024>()); };

  WRD(IC(0), WA(0));
  SBAR();
  bf16x8 qf[4][2];
  auto QPACK = [&](auto Ds) AI {
    constexpr int ds = Ds;
#pragma unroll
    for (int qb = 0; qb < 2; ++qb) {
      const f32x4 a = acc[2 * ds][qb] * QSCALE, b = acc[2 * ds + 1][qb] * QSCALE;
      qf[ds][qb] = __builtin_bit_cast(bf16x8, (u32x4){pk2(a[0], a[1]), pk2(a[2], a[3]), pk2(b[0], b[1]), pk2(b[2], b[3])});
      PIN(qf[ds][qb]);
    }
  };
  sfor<64>([&](auto Si) AI {
    constexpr int s = Si, x0 = (s & ~1) % XD, x1 = (s | 1) % XD;
    if constexpr (s + 1 < 64) WRD(IC(s + 1), WA(s + 1));
    else { FRD(IC(0)); FRD(IC(1)); FRD(IC(2)); }
    SBAR();
    if constexpr (!(s & 1)) waitv<SC.younger(OX, s + 1, s - 1)>(xf[x0], xf[x1]);
    sfor<4>([&](auto Dd) AI {
      constexpr int d = Dd, dt = (s & 1) * 4 + d;
      if constexpr (s < 2) { MFZ(acc[dt][0], wf[s & 1][d], xf[x0]); MFZ(acc[dt][1], wf[s & 1][d], xf[x1]); }
      else { MF(acc[dt][0], wf[s & 1][d], xf[x0]); MF(acc[dt][1], wf[s & 1][d], xf[x1]); }
      if constexpr (s == 63 && (d & 1)) { QPACK(IC(d >> 1)); SBAR(); }
    });
    SBAR();
    endslot(Si);
  });

  f32x4 o[8][2], sc[2][2][2], negm[2];
  u32x4 pw[2][2];
  float lsum[2] = {0.f, 0.f}, inv[2];
#define MX3(a, b, c) __builtin_fmaxf(__builtin_fmaxf((a), (b)), (c))
#define EXP2(x) __builtin_amdgcn_exp2f(x)
  const char* const od_b = ws + WS_O + ((size_t)(rb * 8) * 128 + head * 4) * 2048 + 4096;
  const unsigned od_v = (unsigned)(wid * 262144 + l * 16);
  auto OST = [&](auto Os) AI {
    constexpr int os = Os;
    sfor<2>([&](auto Qb) AI {
      constexpr int qb = Qb;
      PIN(o[2 * os][qb]); PIN(o[2 * os + 1][qb]);
      const f32x4 a = o[2 * os][qb] * inv[qb], b = o[2 * os + 1][qb] * inv[qb];
      gst16<(os * 2 + qb) * 1024 - 4096>((u32x4){pk2(a[0], a[1]), pk2(a[2], a[3]), pk2(b[0], b[1]), pk2(b[2], b[3])}, od_v, od_b);
    });
  };
  sfor<18>([&](auto Ui) AI {
    constexpr int H = Ui - 2, n0 = H < 0 ? 8 * (H + 2) : H < 14 ? 16 + 16 * H : 240 + 8 * (H - 14);
    constexpr bool DOK = H < 14, DOV = H >= 0, DOS = H >= -1 && H < 15;
    constexpr int sn = H & 1, ss = sn ^ 1, pc = H & 1, pn = pc ^ 1;
    const float lold[2] = {lsum[0], lsum[1]};
    auto SM = [&](auto Pi) AI {
      constexpr int p = Pi, kt = p >> 2, qb = (p >> 1) & 1, r = 2 * (p & 1);
      float a = sc[ss][kt][qb][r], b = sc[ss][kt][qb][r + 1];
      const float e0 = EXP2(a), e1 = EXP2(b);
      lsum[qb] += e0; lsum[qb] += e1;
      pw[pn][qb][kt * 2 + (p & 1)] = pk2(e0, e1);
      PIN(lsum[qb]); PIN(pw[pn][qb]);
    };
    sfor<8>([&](auto Ii) AI {
      constexpr int i = Ii, nk = n0 + ((DOK && DOV) ? 2 * i : i), nv = nk + (DOK ? 1 : 0), kt = i >> 2, ds = i & 3;
      if constexpr (DOK) {
        FRD(IC(nk + 3));
        if constexpr (H == -2 && i < 2) QPACK(IC(2 + i));
        sfor<2>([&](auto Qb) AI {
          constexpr int qb = Qb;
          if constexpr (ds == 0 && H == -2) MFZ(sc[sn][kt][qb], fr[nk & 3], qf[0][qb]);
          else if constexpr (ds == 0) MFC(sc[sn][kt][qb], fr[nk & 3], qf[0][qb], negm[qb]);
          else MF(sc[sn][kt][qb], fr[nk & 3], qf[ds][qb]);
        });
        if constexpr (H == -2 && (i == 5 || i == 6)) {
          constexpr int qb = i - 5;
          NOPS(3); PIN(sc[sn][0][qb]);
          const float m = -xg_max(__builtin_fmaxf(MX3(sc[sn][0][qb][0], sc[sn][0][qb][1], sc[sn][0][qb][2]), sc[sn][0][qb][3]));
          negm[qb] = (f32x4){m, m, m, m};
          sc[sn][0][qb] += negm[qb];
          PIN(negm[qb]); PIN(sc[sn][0][qb]);
        }
        if constexpr (H == -1 && (i == 1 || i == 2)) { NOPS(3); PIN(sc[ss][1][i - 1]); sc[ss][1][i - 1] += negm[i - 1]; PIN(sc[ss][1][i - 1]); }
        if constexpr (DOS && i == 0) SM(IC(0));
        if constexpr (DOS && !DOV && i < 7) SM(IC(i + 1));
        SBAR();
        if constexpr ((nk & 3) == 3) endslot(IC(64 + (nk >> 2)));
      }
      if constexpr (DOV) {
        FRD(IC(nv + 3));
        if constexpr (H == 0) { MFZ(o[i][0], fr[nv & 3], pw[pc][0]); MFZ(o[i][1], fr[nv & 3], pw[pc][1]); }
        else { MF(o[i][0], fr[nv & 3], pw[pc][0]); MF(o[i][1], fr[nv & 3], pw[pc][1]); }
        if constexpr (DOS && !DOK && i == 0) SM(IC(0));
        if constexpr (DOS && i < 7) SM(IC(i + 1));
        if constexpr (H == 15 && i == 0) { inv[0] = __builtin_amdgcn_rcpf(xg_sum(lsum[0])); inv[1] = __builtin_amdgcn_rcpf(xg_sum(lsum[1])); PIN(inv[0]); PIN(inv[1]); }
        if constexpr (H == 15 && i >= 3 && (i & 1)) {
          constexpr int os = (i - 3) >> 1;
          NOPS(3);
          OST(IC(os));
        }
        SBAR();
        if constexpr ((nv & 3) == 3) endslot(IC(64 + (nv >> 2)));
      }
    });
    if constexpr (DOS) {
      if (__builtin_expect(__any(!(lsum[0] + lsum[1] <= LBOUND)), 0)) {
        NOPS(7);
        SBAR();
#pragma unroll
        for (int qb = 0; qb < 2; ++qb) {
          const f32x4 a = sc[ss][0][qb], b = sc[ss][1][qb];
          const float dl = xg_max(MX3(MX3(0.f, a[0], a[1]), MX3(a[2], a[3], b[0]), MX3(b[1], b[2], b[3])));
          const float f = EXP2(-dl);
          float ls = lold[qb];
          PIN(ls);
          ls *= f;
          if constexpr (DOV) {
#pragma unroll
            for (int d = 0; d < 8; ++d) o[d][qb] *= f;
          }
#pragma unroll
          for (int kt = 0; kt < 2; ++kt) {
            if constexpr (DOK) sc[sn][kt][qb] -= dl;
#pragma unroll
            for (int r = 0; r < 4; r += 2) {
              const float e0 = EXP2(sc[ss][kt][qb][r] - dl), e1 = EXP2(sc[ss][kt][qb][r + 1] - dl);
              ls += e0; ls += e1;
              PIN(ls);
              pw[pn][qb][kt * 2 + (r >> 1)] = pk2(e0, e1);
            }
          }
          lsum[qb] = ls;
          negm[qb] -= dl;
          PIN(pw[pn][qb]); PIN(negm[qb]);
        }
        SBAR();
        NOPS(1);
      }
      SBAR();
    }
  });
  STAMP(12);
  NOPS(7);
  OST(IC(3));

  {
    LAS char* const fbw = (LAS char*)(uintptr_t)ldsf + l * 16;
    LAS const char* const ft0 = (LAS const char*)(uintptr_t)(ldsf + (4 * g + (c >> 2)) * 64 + (g & 1) * 32 + (c & 3) * 8);
    LAS const char* const ft1 = (LAS const char*)(uintptr_t)((ldsf + (4 * g + (c >> 2)) * 64 + (g & 1) * 32 + (c & 3) * 8) ^ 32);
    waitv<8>(fx[0], fx[1]);
    PIN(fx[2]); PIN(fx[3]);
    f32x4 fa0 = {}, fa1 = {};
#pragma unroll
    for (int p = 0; p < 8; ++p) {
      PIN(fw[p]);
      *(LAS u32x4*)(fbw + (p % 3) * 1024) = fw[p];
      const u32x2 xa = {fx[p >> 1][2 * (p & 1)], fx[p >> 1][2 * (p & 1) + 1]};
      fa0 = MFMA16(__builtin_bit_cast(s16x4, xa), trd(ft0 + (p % 3) * 1024), fa0);
      fa1 = MFMA16(__builtin_bit_cast(s16x4, xa), trd(ft1 + (p % 3) * 1024), fa1);
    }
    *(LAS float*)(lds + RED1 + (wid * 32 + (l & 31)) * 4) = sel2(g & 1, fa1[0], fa0[0]);
    waitv<0>();
    if (l == 0) gst8((u32x2){tlo, thi}, (unsigned)(((rb * 8 + wid) * 32 + head) * 8), ws + WS_FO);
    STAMP(13);
    waitbar_l<1>();
    issue(IC(128));
    float gs = 0.f, us = 0.f;
#pragma unroll
    for (int w = 0; w < 2; ++w) { gs += *(LAS const float*)(lds + RED1 + ((2 * g + w) * 32 + c) * 4); us += *(LAS const float*)(lds + RED1 + ((2 * g + w) * 32 + 16 + c) * 4); }
    const float hh = rbf(silu_f(rbf(xg_sum(gs)))) * rbf(xg_sum(us));
    *(LAS u16*)(lds + HSC + wid * 64 + c * 2) = (u16)pk2(hh, hh);
    const s16x4 hfrag = *(LAS const s16x4*)(lds + HSC + wid * 64 + g * 8);
    const int yrow = within < 28 ? fj : 224 + xcd * 4 + within - 28;
    sfor<4>([&](auto Pi) AI {
      constexpr int p = Pi;
      PIN(fw[8 + p]);
      *(LAS u32x4*)(fbw + (p % 3) * 1024) = fw[8 + p];
      const f32x4 y0 = MFMA16(hfrag, trd(ft0 + (p % 3) * 1024), (f32x4){}), y1 = MFMA16(hfrag, trd(ft1 + (p % 3) * 1024), (f32x4){});
      gst4<p * 128>(sel2(g & 1, y1[0], y0[0]), (unsigned)((wid * 128 + (l & 31)) * 4), ws + WS_YP + (size_t)yrow * 4096);
    });
    STAMP(14);
    poll8((unsigned)(((mb * 4 + rbw) * 32 + kh * 16 + c) * 8), ws + WS_FO, tlo, thi, 0xffffffffu);
    issue(IC(129));
    if (l == 0) gst8((u32x2){tlo, thi}, (unsigned)((wid * 256 + yrow) * 8), ws + WS_FY);
    STAMP(15);
  }

  unsigned xnp[8], gate[8];
  PIN(st);
#pragma unroll
  for (int j = 0; j < 8; ++j) {
    PIN(xe[j >> 1]);
    const unsigned xw = xe[j >> 1][j & 1];
    const unsigned dd = pk2(bflo(xw) - st[0], bfhi(xw) - st[0]);
    xnp[j] = pk2(bflo(dd) * st[1], bfhi(dd) * st[1]);
    gate[j] = pk2(silu_f(bflo(xnp[j])), silu_f(bfhi(xnp[j])));
    PIN(xnp[j]); PIN(gate[j]);
  }
  const int idx = SEQ - 1 - (int)(key & 2047u);
  const bool owner = (idx >> 7) == mb;
  f32x4 yp[7];
  trb[0].init(lds0 + kg * 16384 + g * 2048 + (c >> 2) * 64 + (g & 1) * 32 + (c & 3) * 8);
  trb[1].init(lds0 + kg * 16384 + g * 2048 + (c >> 2) * 64 + ((g & 1) ^ 1) * 32 + (c & 3) * 8);
  WRD(IC(0), WB(0));
  SBAR();
  sfor<64>([&](auto Si) AI {
    constexpr int s = Si, u = s >> 2, x0 = (s & ~1) % XD2, x1 = (s | 1) % XD2;
    if constexpr (s + 1 < 64) WRD(IC(s + 1), WB(s + 1));
    SBAR();
    if constexpr (!(s & 1)) waitv<SC.younger(OX, 64 + s + 1, B0 + s - 1)>(xf[x0], xf[x1]);
    sfor<4>([&](auto Dd) AI {
      constexpr int d = Dd, dt = (s & 1) * 4 + d;
      if constexpr (s < 2) { MFZ(acc[dt][0], wf[s & 1][d], xf[x0]); MFZ(acc[dt][1], wf[s & 1][d], xf[x1]); }
      else { MF(acc[dt][0], wf[s & 1][d], xf[x0]); MF(acc[dt][1], wf[s & 1][d], xf[x1]); }
    });
    SBAR();
    if constexpr (s == 12) {
      if (owner) { gld16<0, true>(fm[0], (unsigned)(nb * 2048 + (l < 56 ? l : 55) * 32), ws + WS_FY); gld16<16, true>(fm[1], (unsigned)(nb * 2048 + (l < 56 ? l : 55) * 32), ws + WS_FY); }
    }
    if constexpr (s == 28) {
      if (owner) {
        PIN(fm[0]); PIN(fm[1]);
        for (int spin = 0; spin < SPINS; ++spin) {
          const bool ok = (fm[0][0] == tlo) & (fm[0][1] == thi) & (fm[0][2] == tlo) & (fm[0][3] == thi) & (fm[1][0] == tlo) & (fm[1][1] == thi) & (fm[1][2] == tlo) & (fm[1][3] == thi);
          if (__builtin_expect(__all(ok), 1)) break;
          __builtin_amdgcn_s_sleep(1);
          gld16<0, true>(fm[0], (unsigned)(nb * 2048 + (l < 56 ? l : 55) * 32), ws + WS_FY); gld16<16, true>(fm[1], (unsigned)(nb * 2048 + (l < 56 ? l : 55) * 32), ws + WS_FY);
          waitv<0>(fm[0], fm[1]);
        }
        const unsigned yv = (unsigned)(((t >> 4) * 1024 + nb * 128 + kh * 64 + (t & 15) * 4) * 4);
        sfor<7>([&](auto I) AI { constexpr int q = I; gld16<0, true>(yp[q], yv, ws + WS_YP + (size_t)q * 32 * 4096); });
      }
    }
    if constexpr (s == 45) {
      if (owner) {
        f32x4 a = {};
#pragma unroll
        for (int q = 0; q < 7; ++q) { PIN(yp[q]); a += yp[q]; }
#pragma unroll
        for (int j = 0; j < 4; ++j) a[j] = xg_sum(a[j]);
        *(LAS f32x4*)(lds + YS + (wid * 64 + c * 4) * 4) = a;
      }
    }
    issue(IC(B0 + s));
    SBAR();
    if constexpr ((s & 3) == 2 && u < 15) {
      if constexpr (s == 46) waitbar_l<SC.younger(OG, 64 + 4 * u + 7, B0 + s)>();
      else waitbar<SC.younger(OG, 64 + 4 * u + 7, B0 + s)>();
      if constexpr (u % 4 == 1) STAMP(16 + u / 4);
    }
  });
  STAMP(20);

  f32x4 giv[4], kep[4], oth[4];
  NOPS(7);
  {
    LAS char* dst = lds + (wid ^ 4) * 8192 + l * 16;
    LAS const char* src = lds + wid * 8192 + l * 16;
#pragma unroll
    for (int d = 0; d < 8; ++d) { PIN(acc[d][0]); PIN(acc[d][1]); }
    if (kg) {
#pragma unroll
      for (int d = 0; d < 8; ++d) { *(LAS f32x4*)(dst + d * 1024) = acc[d][0]; acc[d][0] = acc[d][1]; }
    } else {
#pragma unroll
      for (int d = 0; d < 8; ++d) *(LAS f32x4*)(dst + d * 1024) = acc[d][1];
    }
    waitbar_l<63>();
    STAMP(21);
    if (kh) {
#pragma unroll
      for (int d = 0; d < 4; ++d) { giv[d] = acc[d][0]; kep[d] = acc[4 + d][0]; }
    } else {
#pragma unroll
      for (int d = 0; d < 4; ++d) { giv[d] = acc[4 + d][0]; kep[d] = acc[d][0]; }
    }
    sfor<4>([&](auto Dd) AI {
      constexpr int d = Dd;
      gst16<d * 1024>(giv[d] + *(LAS const f32x4*)(src + (kh ^ 1) * 4096 + d * 1024), (unsigned)(wid * 4096 + l * 16), ws + WS_XC + (size_t)(bid ^ 4) * 32768);
    });
#pragma unroll
    for (int d = 0; d < 4; ++d) kep[d] += *(LAS const f32x4*)(src + kh * 4096 + d * 1024);
  }
  if (owner && erow == idx) {
    const unsigned ord = key >> 11, bits = (ord & 0x8000u) ? (ord & 0x7fffu) : (~ord & 0xffffu);
    const float sg = rbf(sigmoid_f(bflo(bits)));
#pragma unroll
    for (int q = 0; q < 4; ++q) {
      f32x4 a = {};
#pragma unroll
      for (int p = 0; p < 8; ++p) a += *(LAS const f32x4*)(lds + YS + (p * 64 + q * 16 + 4 * g) * 4);
#pragma unroll
      for (int j = 0; j < 2; ++j) {
        const float lo = rbf(bflo(xnp[2 * q + j]) + rbf(rbf(a[2 * j]) * sg)), hi = rbf(bfhi(xnp[2 * q + j]) + rbf(rbf(a[2 * j + 1]) * sg));
        gate[2 * q + j] = pk2(silu_f(lo), silu_f(hi));
      }
    }
  }
  waitv<0>();
  STAMP(22);
  if (l == 0) gst8((u32x2){tlo, thi}, (unsigned)(((bid ^ 4) * 8 + wid) * 64), ws + WS_FX);
  poll8((unsigned)((bid * 8 + wid) * 64), ws + WS_FX, tlo, thi, 0xffffffffu);
  STAMP(23);
  sfor<4>([&](auto Dd) AI { constexpr int d = Dd; gld16<d * 1024, true>(oth[d], (unsigned)(wid * 4096 + l * 16), ws + WS_XC + (size_t)bid * 32768); });
  waitv<0>(oth[0], oth[1]);
  PIN(oth[2]); PIN(oth[3]);
  STAMP(24);
  u16* op = out + (size_t)erow * DM + ecol;
#pragma unroll
  for (int d = 0; d < 4; ++d) {
    const f32x4 a = kep[d] + oth[d];
    u32x2 res;
#pragma unroll
    for (int j = 0; j < 2; ++j) {
      const unsigned sp = pk2(a[2 * j], a[2 * j + 1]), gp = gate[2 * d + j], xw = xe[d][j];
      const unsigned pr = pk2(bflo(sp) * bflo(gp), bfhi(sp) * bfhi(gp));
      res[j] = pk2(bflo(xw) + bflo(pr), bfhi(xw) + bfhi(pr));
    }
    *(u32x2*)(op + d * 16) = res;
  }
  STAMP(25);
  STAMP_OUT(0, 26);
}

extern "C" void kernel_launch(void* const* d_in, const int* in_sizes, int n_in, void* d_out, int out_size, void* d_ws, size_t ws_size,
                              hipStream_t stream) {
  k_fused<<<256, 512, 0, stream>>>((const u16*)d_in[0], (const u16*)d_in[1], (const u16*)d_in[2], (const u16*)d_in[3], (const u16*)d_in[4],
                                   (const u16*)d_in[6], (const u16*)d_in[7], (const u16*)d_in[8], (const u16*)d_in[9], (char*)d_ws, (u16*)d_out);
}
```

```cpp
#include <hip/hip_runtime.h>
#include <stdint.h>
#include <type_traits>

typedef unsigned short u16;
typedef short bf16x8 __attribute__((ext_vector_type(8)));
typedef short s16x4 __attribute__((ext_vector_type(4)));
typedef float f32x4 __attribute__((ext_vector_type(4)));
typedef float f32x2 __attribute__((ext_vector_type(2)));
typedef unsigned u32x4 __attribute__((ext_vector_type(4)));
typedef unsigned u32x2 __attribute__((ext_vector_type(2)));
typedef __bf16 bf2_t __attribute__((ext_vector_type(2)));
#define LAS __attribute__((address_space(3)))
#define DI __device__ __forceinline__
#define AI __attribute__((always_inline))
#define SBAR() __builtin_amdgcn_sched_barrier(0)
template <class T> __device__ __forceinline__ void PIN(T& x) { asm volatile("" : "+v"(x)); }
template <class B> DI void MF(f32x4& acc, const bf16x8& a, const B& b) { asm volatile("v_mfma_f32_16x16x32_bf16 %0, %1, %2, %0" : "+v"(acc) : "v"(a), "v"(b)); }
template <class B> DI void MFC(f32x4& d, const bf16x8& a, const B& b, const f32x4& c) { asm volatile("v_mfma_f32_16x16x32_bf16 %0, %1, %2, %3" : "=&v"(d) : "v"(a), "v"(b), "v"(c)); }
template <class B> DI void MFZ(f32x4& d, const bf16x8& a, const B& b) { asm volatile("v_mfma_f32_16x16x32_bf16 %0, %1, %2, 0" : "=&v"(d) : "v"(a), "v"(b)); }
#define NOPS(n) asm volatile("s_nop " #n)
#define MFMA16(a, b, c) __builtin_amdgcn_mfma_f32_16x16x16bf16_1k((a), (b), (c), 0, 0, 0)
#define IC(n) std::integral_constant<int, (n)>{}

constexpr int SEQ = 2048, DM = 1024, DH = 128, SK = 2048, WIN = 512, FF = 3584;
constexpr size_t WS_XN = 0;
constexpr size_t WS_KV = 4u << 20;
constexpr size_t WS_ST = 5u << 20;
constexpr size_t WS_EP = WS_ST + 16384;
constexpr size_t WS_G0 = WS_ST + 32768;
constexpr size_t WS_FK = WS_ST + 49152;
constexpr size_t WS_YP = WS_ST + 65536;
constexpr size_t WS_FO = 7u << 20;
constexpr size_t WS_FY = WS_FO + 32768;
constexpr size_t WS_FX = WS_FO + 65536;
constexpr size_t WS_O = 8u << 20;
constexpr size_t WS_XC = 24u << 20;

template <int I, int N, class F> DI void sfor_impl(F&& f) {
  if constexpr (I < N) { f(std::integral_constant<int, I>{}); sfor_impl<I + 1, N>(f); }
}
template <int N, class F> DI void sfor(F&& f) { sfor_impl<0, N>(f); }

DI unsigned pk2(float a, float b) { f32x2 v = {a, b}; return __builtin_bit_cast(unsigned, __builtin_convertvector(v, bf2_t)); }
DI float rbf(float a) { return (float)(__bf16)a; }
DI float bflo(unsigned u) { return __uint_as_float(u << 16); }
DI float bfhi(unsigned u) { return __uint_as_float(u & 0xffff0000u); }
DI float sigmoid_f(float v) { return __builtin_amdgcn_rcpf(1.f + __builtin_amdgcn_exp2f(v * -1.4426950408889634f)); }
DI float sel2(bool c, float a, float b) { PIN(a); PIN(b); return c ? a : b; }
DI float silu_f(float v) { return v * sigmoid_f(v); }
template <int CTRL> DI float dppf(float x) { return __builtin_bit_cast(float, __builtin_amdgcn_mov_dpp(__builtin_bit_cast(int, x), CTRL, 0xf, 0xf, true)); }
template <int CTRL> DI unsigned dppu(unsigned x) { return (unsigned)__builtin_amdgcn_mov_dpp((int)x, CTRL, 0xf, 0xf, true); }
constexpr int XOR1 = 0xB1, XOR2 = 0x4E, XOR7 = 0x141, XOR8 = 0x128;
DI float xg_sum(float x) {
  auto s = __builtin_amdgcn_permlane16_swap(__float_as_uint(x), __float_as_uint(x), false, false);
  x = __uint_as_float(s[0]) + __uint_as_float(s[1]);
  auto t = __builtin_amdgcn_permlane32_swap(__float_as_uint(x), __float_as_uint(x), false, false);
  return __uint_as_float(t[0]) + __uint_as_float(t[1]);
}
DI float xg_max(float x) {
  auto s = __builtin_amdgcn_permlane16_swap(__float_as_uint(x), __float_as_uint(x), false, false);
  x = __builtin_fmaxf(__uint_as_float(s[0]), __uint_as_float(s[1]));
  auto t = __builtin_amdgcn_permlane32_swap(__float_as_uint(x), __float_as_uint(x), false, false);
  return __builtin_fmaxf(__uint_as_float(t[0]), __uint_as_float(t[1]));
}
DI float wave_sum(float v) { v += dppf<XOR1>(v); v += dppf<XOR2>(v); v += dppf<XOR7>(v); v += dppf<XOR8>(v); return xg_sum(v); }
DI unsigned umax(unsigned a, unsigned b) { return a > b ? a : b; }
DI unsigned wave_max_u32(unsigned v) {
  v = umax(v, dppu<XOR1>(v)); v = umax(v, dppu<XOR2>(v)); v = umax(v, dppu<XOR7>(v)); v = umax(v, dppu<XOR8>(v));
  auto s = __builtin_amdgcn_permlane16_swap(v, v, false, false); v = umax(s[0], s[1]);
  auto t = __builtin_amdgcn_permlane32_swap(v, v, false, false); v = umax(t[0], t[1]);
  return __builtin_amdgcn_readfirstlane(v);
}

template <int OFF, bool DEV = false> DI void glds(unsigned ldsaddr, unsigned voff, const void* sbase) {
  if constexpr (DEV) asm volatile("s_mov_b32 m0, %0\n\ts_nop 0\n\tglobal_load_lds_dwordx4 %1, %2 offset:%3 sc1" ::"s"(ldsaddr), "v"(voff), "s"(sbase), "i"(OFF) : "memory");
  else asm volatile("s_mov_b32 m0, %0\n\ts_nop 0\n\tglobal_load_lds_dwordx4 %1, %2 offset:%3" ::"s"(ldsaddr), "v"(voff), "s"(sbase), "i"(OFF) : "memory");
}
DI void touch(unsigned ldsaddr, unsigned voff, const void* sbase) {
  asm volatile("s_mov_b32 m0, %0\n\ts_nop 0\n\tglobal_load_lds_dword %1, %2" ::"s"(ldsaddr), "v"(voff), "s"(sbase) : "memory");
}
template <int OFF, bool DEV = false, class T> DI void gld16(T& dst, unsigned voff, const void* sbase) {
  if constexpr (DEV) asm volatile("global_load_dwordx4 %0, %1, %2 offset:%3 sc1" : "=v"(dst) : "v"(voff), "s"(sbase), "i"(OFF) : "memory");
  else asm volatile("global_load_dwordx4 %0, %1, %2 offset:%3" : "=v"(dst) : "v"(voff), "s"(sbase), "i"(OFF) : "memory");
}
template <class T> DI void gld16v(T& dst, const void* vaddr) { asm volatile("global_load_dwordx4 %0, %1, off" : "=v"(dst) : "v"(vaddr) : "memory"); }
template <int OFF, bool DEV = false, class T> DI void gld8(T& dst, unsigned voff, const void* sbase) {
  if constexpr (DEV) asm volatile("global_load_dwordx2 %0, %1, %2 offset:%3 sc1" : "=v"(dst) : "v"(voff), "s"(sbase), "i"(OFF) : "memory");
  else asm volatile("global_load_dwordx2 %0, %1, %2 offset:%3" : "=v"(dst) : "v"(voff), "s"(sbase), "i"(OFF) : "memory");
}
template <int OFF> DI void gld2(unsigned& dst, unsigned voff, const void* sbase) {
  asm volatile("global_load_ushort %0, %1, %2 offset:%3" : "=v"(dst) : "v"(voff), "s"(sbase), "i"(OFF) : "memory");
}
template <int OFF, class T> DI void gst16(const T& v, unsigned voff, const void* sbase) {
  asm volatile("global_store_dwordx4 %0, %1, %2 offset:%3 sc1\n\ts_nop 1" ::"v"(voff), "v"(v), "s"(sbase), "i"(OFF) : "memory");
}
template <class T> DI void gst8(const T& v, unsigned voff, const void* sbase) {
  asm volatile("global_store_dwordx2 %0, %1, %2 sc1" ::"v"(voff), "v"(v), "s"(sbase) : "memory");
}
template <int OFF> DI void gst4(float v, unsigned voff, const void* sbase) {
  asm volatile("global_store_dword %0, %1, %2 offset:%3 sc1" ::"v"(voff), "v"(v), "s"(sbase), "i"(OFF) : "memory");
}
constexpr int SPINS = 1 << 16;
DI void poll8(unsigned voff, const void* sbase, unsigned lo, unsigned hi, unsigned lomask) {
  for (int spin = 0; spin < SPINS; ++spin) {
    u32x2 v;
    asm volatile("global_load_dwordx2 %0, %1, %2 sc1\n\ts_waitcnt vmcnt(0)" : "=&v"(v) : "v"(voff), "s"(sbase) : "memory");
    if (__all(((v[0] & lomask) == lo) & (v[1] == hi))) break;
    __builtin_amdgcn_s_sleep(1);
  }
}
template <int N> DI void waitv() { static_assert(N >= 0, "count"); asm volatile("s_waitcnt vmcnt(%0)" ::"n"(N > 63 ? 63 : N) : "memory"); }
template <int N, class T> DI void waitv(T& x) { static_assert(N >= 0, "count"); asm volatile("s_waitcnt vmcnt(%1)" : "+v"(x) : "n"(N > 63 ? 63 : N) : "memory"); }
template <int N, class T> DI void waitv(T& x, T& y) { static_assert(N >= 0, "count"); asm volatile("s_waitcnt vmcnt(%2)" : "+v"(x), "+v"(y) : "n"(N > 63 ? 63 : N) : "memory"); }
template <int N> DI void waitbar() { static_assert(N >= 0, "count"); asm volatile("s_waitcnt vmcnt(%0)\n\ts_barrier" ::"n"(N > 63 ? 63 : N) : "memory"); }
template <int N> DI void waitbar_l() { static_assert(N >= 0, "count"); asm volatile("s_waitcnt vmcnt(%0) lgkmcnt(0)\n\ts_barrier" ::"n"(N > 63 ? 63 : N) : "memory"); }
typedef short v4i16_t __attribute__((ext_vector_type(4)));
DI s16x4 trd(LAS const char* p) { return __builtin_amdgcn_ds_read_tr16_b64_v4i16((LAS v4i16_t*)p); }
struct LBase {
  unsigned lo, hi;
  DI void init(unsigned a) { lo = a; hi = a + 65536; PIN(lo); PIN(hi); }
  template <int OFF> DI LAS const char* at() const { return (LAS const char*)(uintptr_t)(OFF < 65536 ? lo : hi) + (OFF & 65535); }
};
DI bf16x8 cat8(s16x4 lo, s16x4 hi) { return __builtin_shufflevector(lo, hi, 0, 1, 2, 3, 4, 5, 6, 7); }

enum { OX, OG, OF, OM, OS, OP, OE, OW };
struct Sched {
  int n; short kind[400], idx[400], slot[400];
  constexpr Sched() : n(0), kind{}, idx{}, slot{} {}
  constexpr void add(int s, int k, int i) { kind[n] = (short)k; idx[n] = (short)i; slot[n] = (short)s; ++n; }
  constexpr int younger(int k, int i, int s) const {
    int p = -1;
    for (int q = 0; q < n; ++q) if (kind[q] == k && idx[q] == i) p = q;
    if (p < 0 || slot[p] > s) return -1;
    int c = 0;
    for (int q = p + 1; q < n; ++q) if (slot[q] <= s) ++c;
    return c;
  }
  constexpr int first(int s) const { int q = 0; while (q < n && slot[q] < s) ++q; return q; }
  constexpr int count(int s) const { int c = 0; for (int q = 0; q < n; ++q) if (slot[q] == s) ++c; return c; }
};

#define STAMP_DECL
#define STAMP(i)
#define STAMP_OUT(kid, n)

constexpr int RING = 4 * 32768, FB = RING, RED1 = FB + 8 * 3072, HSC = RED1 + 1024, YS = HSC + 512, DUMMY = YS + 2048, LDS_BYTES = DUMMY + 256;
constexpr int STAGE = 3 * 32768;
constexpr int XD = 16, XD2 = 12;
constexpr float LBOUND = 65536.f;
constexpr float QSCALE = 0.08838834764831845f * 1.4426950408889634f;
constexpr int B0 = 130;
constexpr Sched make_sched() {
  Sched s;
  for (int i = 0; i < 12; ++i) s.add(-2, OG, i);
  for (int i = 0; i < 12; ++i) s.add(-1, OX, i);
  for (int t = 0; t < 128; ++t) {
    if (t == 0 || t == 2) { s.add(t, OX, 12 + t); s.add(t, OX, 13 + t); }
    for (int p = 0; p < 24; ++p) { const int a = 3 + p, b2 = 2 * p + 1; if ((a > b2 ? a : b2) == t) { s.add(t, OX, 16 + 2 * p); s.add(t, OX, 17 + 2 * p); } }
    for (int v = 3; v < 18; ++v) for (int p = 0; p < 4; ++p) if (8 * (v - 3) + 7 + p == t) s.add(t, OG, 4 * v + p);
    if (t == 20) s.add(t, OM, 0);
    if (t >= 51 && t < 55) s.add(t, OF, 8 + t - 51);
    if (t >= 99 && t < 123 && (t - 99) % 3 == 0) s.add(t, OF, (t - 99) / 3);
    if (t == 60) s.add(t, OS, 0);
  }
  for (int i = 0; i < XD2; ++i) s.add(128, OX, 64 + i);
  for (int i = 72; i < 80; ++i) s.add(129, OG, i);
  for (int t = 0; t < 64; ++t) {
    if (t == 53) for (int i = 0; i < 5; ++i) s.add(B0 + t, OE, i);
    if ((t & 1) && t - 1 + XD2 < 64) { s.add(B0 + t, OX, 64 + t - 1 + XD2); s.add(B0 + t, OX, 64 + t + XD2); }
    for (int v = 4; v < 16; ++v) for (int p = 0; p < 4; ++p) if ((v < 15 ? 4 * (v - 3) + 3 + (p >> 1) : 51) == t) s.add(B0 + t, OG, 64 + 4 * v + p);
  }
  return s;
}
constexpr Sched SC = make_sched();

__global__ __launch_bounds__(512, 2) void k_fused(const u16* __restrict__ x, const u16* __restrict__ kc, const u16* __restrict__ vc,
                                                  const u16* __restrict__ wq, const u16* __restrict__ wo, const u16* __restrict__ wr,
                                                  const u16* __restrict__ wgate, const u16* __restrict__ wup, const u16* __restrict__ wdown,
                                                  char* __restrict__ ws, u16* __restrict__ out) {
  __shared__ __attribute__((aligned(16))) char lds_raw[LDS_BYTES];
  LAS char* const lds = (LAS char*)lds_raw;
  const int t = threadIdx.x, l = t & 63, bid = blockIdx.x, c = l & 15, g = l >> 4;
  const int wid = __builtin_amdgcn_readfirstlane(t >> 6);
  const int xcd = bid & 7, within = bid >> 3, head = xcd * 4 + (within & 3), rb = within >> 2;
  const int fj = xcd * 28 + (within < 28 ? within : within - 28);
  const int kh = xcd >> 2, mb = 2 * rb + (xcd & 1), nb = ((xcd >> 1) & 1) * 4 + (within & 3);
  const int rbw = wid & 3, kg = wid >> 2;
  const unsigned lds0 = (unsigned)(uintptr_t)lds;
  STAMP_DECL
  STAMP(0);

  unsigned tlo, thi;
  {
    const int row = 8 * bid + wid;
    u32x4 a0, a1, w0, w1, kvo;
    u32x2 ep, klo, khi;
    unsigned e[8];
    gld16<0>(a0, (unsigned)(row * DM * 2 + l * 16), x); gld16<1024>(a1, (unsigned)(row * DM * 2 + l * 16), x);
    gld16<0>(w0, (unsigned)(l * 16), wr); gld16<1024>(w1, (unsigned)(l * 16), wr);
    gld8<0, true>(ep, 0u, ws + WS_EP);
    __builtin_amdgcn_s_barrier();
    const int fn = (bid * 4 + (wid & 3)) & 255, kvh = bid >> 6;
    int isv, un, fi;
    if (fn < 16) { isv = 0; un = fn >> 3; fi = fn & 7; }
    else if (fn >= 240) { isv = 1; un = 14 + ((fn - 240) >> 3); fi = fn & 7; }
    else { const int m = fn - 16; isv = m & 1; un = (m >> 4) + (isv ? 0 : 2); fi = (m & 15) >> 1; }
    if (wid < 4) {
      touch(lds0 + DUMMY, (unsigned)((wid * 256 + within * 8 + (l >> 3)) * 8192 + xcd * 1024 + (l & 7) * 128), wq);
      const int d = 32 * (fi >> 1) + 8 * (c >> 2) + 4 * (fi & 1) + (c & 3);
      const unsigned ko = (unsigned)(((kvh * SK + (SK - WIN) + un * 32 + (fi >> 2) * 16 + c) * DH + (fi & 3) * 32 + 4 * g) * 2);
      const unsigned vo = (unsigned)(((kvh * SK + (SK - WIN) + un * 32 + 4 * g + 8) * DH + d) * 2);
      if (!isv) { gld8<0>(klo, ko, kc); gld8<32>(khi, ko, kc); waitv<3>(); }
      else { sfor<8>([&](auto J) AI { constexpr int j = J; gld2<(16 * (j >> 2) + (j & 3) - 8) * DH * 2>(e[j], vo, vc); }); waitv<9>(); }
    } else {
      waitv<0>();
    }
    PIN(a0); PIN(a1); PIN(w0); PIN(w1); PIN(ep);
    STAMP(1);
    {
      const unsigned long long tg = (((unsigned long long)(unsigned)__builtin_amdgcn_readfirstlane(ep[1]) << 32) | (unsigned)__builtin_amdgcn_readfirstlane(ep[0])) + 1ull;
      tlo = (unsigned)tg; thi = (unsigned)(tg >> 32);
    }
    float v[16];
#pragma unroll
    for (int j = 0; j < 4; ++j) { v[2 * j] = bflo(a0[j]); v[2 * j + 1] = bfhi(a0[j]); v[8 + 2 * j] = bflo(a1[j]); v[9 + 2 * j] = bfhi(a1[j]); }
    float s = 0.f;
#pragma unroll
    for (int j = 0; j < 16; ++j) s += v[j];
    const float mean = wave_sum(s) * (1.f / DM);
    float q = 0.f;
#pragma unroll
    for (int j = 0; j < 16; ++j) { const float d = v[j] - mean; q += d * d; }
    const float var = wave_sum(q) * (1.f / DM);
    const float mbf = rbf(mean), rden = 1.f / rbf(sqrtf(rbf(rbf(var) + 1e-5f)));
    float rr = 0.f;
    u32x4 o0, o1;
#pragma unroll
    for (int j = 0; j < 8; ++j) {
      const unsigned dd = pk2(v[2 * j] - mbf, v[2 * j + 1] - mbf);
      const unsigned nn = pk2(bflo(dd) * rden, bfhi(dd) * rden);
      const unsigned ww = j < 4 ? w0[j] : w1[j - 4];
      rr += bflo(nn) * bflo(ww); rr += bfhi(nn) * bfhi(ww);
      if (j < 4) o0[j] = nn; else o1[j - 4] = nn;
    }
    *(LAS u32x4*)(lds + STAGE + (l * 8 + (wid ^ (l & 7))) * 16) = o0;
    *(LAS u32x4*)(lds + STAGE + 8192 + (l * 8 + (wid ^ (l & 7))) * 16) = o1;
    rr = wave_sum(rr);
    if (l == 0) {
      const unsigned bits = pk2(rr, 0.f) & 0xffffu;
      const unsigned ord = (bits & 0x8000u) ? (~bits & 0xffffu) : (bits | 0x8000u);
      *(LAS unsigned*)(lds + STAGE + 16384 + wid * 4) = (ord << 11) | (unsigned)(SEQ - 1 - row);
      *(LAS f32x2*)(lds + STAGE + 16384 + 32 + wid * 8) = (f32x2){mbf, rden};
    }
    waitbar_l<63>();
    if (wid >= 4) {
#pragma unroll
      for (int hh = 0; hh < 4; ++hh) {
        const int p = hh * 256 + (t - 256), ch = p >> 3, r = 8 * bid + ((p & 7) ^ (ch & 7));
        const u32x4 o = *(LAS const u32x4*)(lds + STAGE + p * 16);
        gst16<0>(o, (unsigned)(((((r >> 5) * 32 + (ch >> 2)) * 2 + ((r >> 4) & 1)) * 64 + (ch & 3) * 16 + (r & 15)) * 16), ws + WS_XN);
      }
      if (t < 256 + 8) gst8(*(LAS const f32x2*)(lds + STAGE + 16384 + 32 + (t - 256) * 8), (unsigned)((8 * bid + t - 256) * 8), ws + WS_ST);
      waitv<0>();
    }
    waitbar_l<63>();
    if (wid < 4) {
      if (!isv) {
        waitv<0>(klo, khi);
        kvo = (u32x4){klo[0], klo[1], khi[0], khi[1]};
      } else {
        waitv<0>(e[0], e[1]);
#pragma unroll
        for (int j = 2; j < 8; ++j) PIN(e[j]);
        kvo = (u32x4){e[0] | (e[1] << 16), e[2] | (e[3] << 16), e[4] | (e[5] << 16), e[6] | (e[7] << 16)};
      }
      gst16<0>(kvo, (unsigned)((bid * 4 + wid) * 1024 + l * 16), ws + WS_KV);
    }
    if (t == 256) {
      const u32x4 ka = *(LAS const u32x4*)(lds + STAGE + 16384), kb = *(LAS const u32x4*)(lds + STAGE + 16400);
      const unsigned key = umax(umax(umax(ka[0], ka[1]), umax(ka[2], ka[3])), umax(umax(kb[0], kb[1]), umax(kb[2], kb[3])));
      gst8((u32x2){(tlo << 27) | key, (tlo >> 5) | (thi << 27)}, (unsigned)(bid * 8), ws + WS_G0);
    }
    STAMP(2);
  }
  const unsigned glo = tlo << 27, ghi = (tlo >> 5) | (thi << 27);

  const char* const wq_b = (const char*)wq + head * 256;
  const unsigned wq_v = (unsigned)(((8 * wid + ((l & 31) >> 2)) * 4096 + (l >> 5) * 32 + ((l & 3) ^ (2 * (wid & 1))) * 8) * 2);
  const char* const kv_b = ws + WS_KV + (size_t)(head >> 3) * 262144;
  const unsigned kv_v = (unsigned)(wid * 4096 + l * 16);
  const char* const xn_b = ws + WS_XN + (size_t)rb * 8 * 65536 + 4096;
  const unsigned xn_v = (unsigned)(wid * 65536 + l * 16);
  const char* const wo_b = (const char*)wo + (size_t)kh * 2048 * (DM * 2) + nb * 256;
  const char* const o_b = ws + WS_O + (size_t)mb * 4 * 262144 + kh * 131072;
  const unsigned ldsw = lds0 + wid * 2048, ldsk = lds0 + wid * 4096, ldsf = lds0 + FB + wid * 3072;
  const int erow = mb * 128 + rbw * 32 + kg * 16 + c, ecol = nb * 128 + kh * 64 + 4 * g;
  bf16x8 xf[XD];
  u32x4 fm[2], fw[12];
  u32x2 fg;
  u32x2 xe[4];
  f32x2 st;
  unsigned key = 0;

  auto issue = [&](auto Sl) AI {
    constexpr int sl = Sl, q0 = SC.first(sl), nq = SC.count(sl);
    sfor<nq>([&](auto Qi) AI {
      constexpr int k = SC.kind[q0 + Qi], i = SC.idx[q0 + Qi];
      if constexpr (k == OX && i < 64) gld16<(i & 7) * 1024 - 4096, true>(xf[i % XD], xn_v, xn_b + (size_t)(i >> 3) * 8192);
      if constexpr (k == OX && i >= 64) gld16<(i & 3) * 1024, true>(xf[(i - 64) % XD2], (unsigned)(rbw * 262144 + kg * 4096 + l * 16), o_b + (size_t)((i - 64) >> 2) * 8192);
      if constexpr (k == OG) {
        constexpr int v = i >> 2, p = i & 3;
        if constexpr (v < 8) {
          const char* sb = wq_b + (size_t)(v * 128 + (p >> 1) * 64) * 8192;
          glds<(p & 1) * 128>(ldsw + (v & 3) * 32768 + (p >> 1) * 16384 + (p & 1) * (1024 - 128), wq_v, sb);
        } else if constexpr (v < 16) {
          glds<p * 1024, true>(ldsk + (v & 3) * 32768, kv_v, kv_b + (size_t)(v - 8) * 32768);
        } else {
          const unsigned wo_v = (unsigned)(((8 * wid + ((l & 31) >> 2)) * DM + (l >> 5) * 32 + ((l & 3) ^ (2 * (wid & 1))) * 8) * 2);
          const char* sb = wo_b + (size_t)((v - 16) * 128 + (p >> 1) * 64) * (DM * 2) + (((p & 1) ^ kh ^ 1) << 7);
          glds<0>(ldsw + (v & 3) * 32768 + (p >> 1) * 16384 + (p & 1) * 1024, wo_v, sb);
        }
      }
      if constexpr (k == OF) {
        const int frho = l >> 2, fch = (l & 3) ^ (frho & 4 ? 2 : 0);
        if constexpr (i < 8) gld16v(fw[i], (const char*)(fch & 2 ? wup : wgate) + ((size_t)(128 * wid + 32 * (frho >> 2) + (frho & 3) + 4 * i) * FF + fj * 16 + (fch & 1) * 8) * 2);
        else gld16<(i - 8) * 64>(fw[i], (unsigned)((frho * DM + wid * 128 + fch * 8) * 2), (const char*)wdown + (size_t)fj * 16 * DM * 2);
      }
      if constexpr (k == OM) gld8<0, true>(fg, (unsigned)((t & 255) * 8), wid < 4 ? ws + WS_G0 : ws + WS_FK + (head >> 3) * 2048);
      if constexpr (k == OS) {
        const u32x4 kk = *(LAS const u32x4*)(lds + HSC);
        key = __builtin_amdgcn_readfirstlane(umax(umax(kk[0], kk[1]), umax(kk[2], kk[3])));
        const int idx = SEQ - 1 - (int)(key & 2047u);
        glds<0, true>(ldsf, (unsigned)(((((idx >> 5) * 32 + 4 * wid + ((l >> 2) & 3)) * 2 + ((idx >> 4) & 1)) * 64 + (l & 3) * 16 + (idx & 15)) * 16), ws + WS_XN);
      }
      if constexpr (k == OE && i < 4) gld8<32 * i>(xe[i], (unsigned)((erow * DM + ecol) * 2), x);
      if constexpr (k == OE && i == 4) gld8<0, true>(st, (unsigned)(erow * 8), ws + WS_ST);
    });
  };
  auto endslot = [&](auto Sl) AI {
    constexpr int sl = Sl, u = sl >> 3, i = sl & 7;
    if constexpr (sl == 38) {
      waitv<SC.younger(OM, 0, sl - 1)>(fg);
      const unsigned lo = wid < 4 ? glo : tlo, hi = wid < 4 ? ghi : thi, mk = wid < 4 ? 0xf8000000u : 0xffffffffu;
      for (int spin = 0; spin < SPINS; ++spin) {
        if (__builtin_expect(__all(((fg[0] & mk) == lo) & (fg[1] == hi)), 1)) break;
        __builtin_amdgcn_s_sleep(1);
        gld8<0, true>(fg, (unsigned)((t & 255) * 8), wid < 4 ? ws + WS_G0 : ws + WS_FK + (head >> 3) * 2048);
        waitv<0>(fg);
      }
      const unsigned km = wave_max_u32(fg[0] & 0x07ffffffu);
      *(LAS unsigned*)(lds + HSC + wid * 4) = km;
    }
    if constexpr (sl == 47) { if (bid == 0 && t == 0) gst8((u32x2){tlo, thi}, 0u, ws + WS_EP); }
    if constexpr (sl >= 59 && sl < 83 && (sl - 59) % 3 == 0) {
      if (wid == (sl - 59) / 3)
        touch(lds0 + DUMMY, (unsigned)((within * 32 + (wid & 3) * 8 + (l >> 3)) * (FF * 2) + xcd * 896 + ((l & 7) < 7 ? (l & 7) : 6) * 128), wid < 4 ? wgate : wup);
    }
    if constexpr (sl >= 84 && sl < 100 && (sl - 84) % 4 == 0) {
      if (wid == (sl - 84) / 4)
        touch(lds0 + DUMMY, (unsigned)((kh * 2048 + wid * 512 + within * 16 + (xcd & 1) * 8 + (l >> 3)) * (DM * 2) + ((xcd >> 1) & 1) * 1024 + (l & 7) * 128), wo);
    }
    issue(Sl);
    SBAR();
    if constexpr (i == 6 && u < 15) {
      if constexpr (u == 5) waitbar_l<SC.younger(OG, 4 * u + 7, sl)>();
      else waitbar<SC.younger(OG, 4 * u + 7, sl)>();
      if constexpr (u == 0 || u == 1 || u == 5 || u == 7 || u == 8 || u == 10 || u == 12 || u == 14)
        STAMP(u == 0 ? 4 : u == 1 ? 5 : u == 5 ? 6 : u == 7 ? 7 : u == 8 ? 8 : u == 10 ? 9 : u == 12 ? 10 : 11);
    }
  };

  issue(IC(-2));
  if (wid < 4) {
    waitv<0>();
    if (l == 0) gst8((u32x2){tlo, thi}, (unsigned)((bid * 4 + wid) * 8), ws + WS_FK);
  }
  if (wid == 5) poll8((unsigned)((rb * 32 + (l & 31)) * 8), ws + WS_G0, glo, ghi, 0xf8000000u);
  waitbar<SC.younger(OG, 3, -2)>();
  issue(IC(-1));
  STAMP(3);

  LBase trb[2], frb;
  trb[0].init(lds0 + g * 2048 + (c >> 2) * 64 + (g & 1) * 32 + (c & 3) * 8);
  trb[1].init(lds0 + g * 2048 + (c >> 2) * 64 + ((g & 1) ^ 1) * 32 + (c & 3) * 8);
  frb.init(lds0 + l * 16);
  f32x4 acc[8][2];
  bf16x8 wf[2][4];
  auto WRD = [&](auto Si, auto Base) AI {
    constexpr int s = Si, base = Base;
    sfor<4>([&](auto Dd) AI {
      constexpr int d = Dd, o = base + (d >> 1) * 512;
      wf[s & 1][d] = cat8(trd(trb[d & 1].template at<o>()), trd(trb[d & 1].template at<o + 256>()));
    });
  };
#define WA(s) IC((((s) >> 3) & 3) * 32768 + (((s) >> 1) & 3) * 8192 + ((s) & 1) * 1024)
#define WB(s) IC((((s) >> 2) & 3) * 32768 + (((s) >> 1) & 1) * 8192 + ((s) & 1) * 1024)
  bf16x8 fr[4];
  auto FRD = [&](auto Ni) AI { constexpr int n = Ni; if constexpr (n < 256) fr[n & 3] = *(LAS const bf16x8*)(frb.at<((n >> 5) & 3) * 32768 + (n & 31) * 1024>()); };

  WRD(IC(0), WA(0));
  SBAR();
  bf16x8 qf[4][2];
  auto QPACK = [&](auto Ds) AI {
    constexpr int ds = Ds;
#pragma unroll
    for (int qb = 0; qb < 2; ++qb) {
      const f32x4 a = acc[2 * ds][qb] * QSCALE, b = acc[2 * ds + 1][qb] * QSCALE;
      qf[ds][qb] = __builtin_bit_cast(bf16x8, (u32x4){pk2(a[0], a[1]), pk2(a[2], a[3]), pk2(b[0], b[1]), pk2(b[2], b[3])});
      PIN(qf[ds][qb]);
    }
  };
  sfor<64>([&](auto Si) AI {
    constexpr int s = Si, x0 = (s & ~1) % XD, x1 = (s | 1) % XD;
    if constexpr (s + 1 < 64) WRD(IC(s + 1), WA(s + 1));
    else { FRD(IC(0)); FRD(IC(1)); FRD(IC(2)); }
    SBAR();
    if constexpr (!(s & 1)) waitv<SC.younger(OX, s + 1, s - 1)>(xf[x0], xf[x1]);
    sfor<4>([&](auto Dd) AI {
      constexpr int d = Dd, dt = (s & 1) * 4 + d;
      if constexpr (s < 2) { MFZ(acc[dt][0], wf[s & 1][d], xf[x0]); MFZ(acc[dt][1], wf[s & 1][d], xf[x1]); }
      else { MF(acc[dt][0], wf[s & 1][d], xf[x0]); MF(acc[dt][1], wf[s & 1][d], xf[x1]); }
      if constexpr (s == 63 && (d & 1)) { QPACK(IC(d >> 1)); SBAR(); }
    });
    SBAR();
    endslot(Si);
  });

  f32x4 o[8][2], sc[2][2][2], negm[2];
  u32x4 pw[2][2];
  float lsum[2] = {0.f, 0.f}, inv[2];
#define MX3(a, b, c) __builtin_fmaxf(__builtin_fmaxf((a), (b)), (c))
#define EXP2(x) __builtin_amdgcn_exp2f(x)
  const char* const od_b = ws + WS_O + ((size_t)(rb * 8) * 128 + head * 4) * 2048 + 4096;
  const unsigned od_v = (unsigned)(wid * 262144 + l * 16);
  auto OST = [&](auto Os) AI {
    constexpr int os = Os;
    sfor<2>([&](auto Qb) AI {
      constexpr int qb = Qb;
      PIN(o[2 * os][qb]); PIN(o[2 * os + 1][qb]);
      const f32x4 a = o[2 * os][qb] * inv[qb], b = o[2 * os + 1][qb] * inv[qb];
      gst16<(os * 2 + qb) * 1024 - 4096>((u32x4){pk2(a[0], a[1]), pk2(a[2], a[3]), pk2(b[0], b[1]), pk2(b[2], b[3])}, od_v, od_b);
    });
  };
  sfor<18>([&](auto Ui) AI {
    constexpr int H = Ui - 2, n0 = H < 0 ? 8 * (H + 2) : H < 14 ? 16 + 16 * H : 240 + 8 * (H - 14);
    constexpr bool DOK = H < 14, DOV = H >= 0, DOS = H >= -1 && H < 15;
    constexpr int sn = H & 1, ss = sn ^ 1, pc = H & 1, pn = pc ^ 1;
    const float lold[2] = {lsum[0], lsum[1]};
    auto SM = [&](auto Pi) AI {
      constexpr int p = Pi, kt = p >> 2, qb = (p >> 1) & 1, r = 2 * (p & 1);
      float a = sc[ss][kt][qb][r], b = sc[ss][kt][qb][r + 1];
      const float e0 = EXP2(a), e1 = EXP2(b);
      lsum[qb] += e0; lsum[qb] += e1;
      pw[pn][qb][kt * 2 + (p & 1)] = pk2(e0, e1);
      PIN(lsum[qb]); PIN(pw[pn][qb]);
    };
    sfor<8>([&](auto Ii) AI {
      constexpr int i = Ii, nk = n0 + ((DOK && DOV) ? 2 * i : i), nv = nk + (DOK ? 1 : 0), kt = i >> 2, ds = i & 3;
      if constexpr (DOK) {
        FRD(IC(nk + 3));
        if constexpr (H == -2 && i < 2) QPACK(IC(2 + i));
        sfor<2>([&](auto Qb) AI {
          constexpr int qb = Qb;
          if constexpr (ds == 0 && H == -2) MFZ(sc[sn][kt][qb], fr[nk & 3], qf[0][qb]);
          else if constexpr (ds == 0) MFC(sc[sn][kt][qb], fr[nk & 3], qf[0][qb], negm[qb]);
          else MF(sc[sn][kt][qb], fr[nk & 3], qf[ds][qb]);
        });
        if constexpr (H == -2 && (i == 5 || i == 6)) {
          constexpr int qb = i - 5;
          NOPS(3); PIN(sc[sn][0][qb]);
          const float m = -xg_max(__builtin_fmaxf(MX3(sc[sn][0][qb][0], sc[sn][0][qb][1], sc[sn][0][qb][2]), sc[sn][0][qb][3]));
          negm[qb] = (f32x4){m, m, m, m};
          sc[sn][0][qb] += negm[qb];
          PIN(negm[qb]); PIN(sc[sn][0][qb]);
        }
        if constexpr (H == -1 && (i == 1 || i == 2)) { NOPS(3); PIN(sc[ss][1][i - 1]); sc[ss][1][i - 1] += negm[i - 1]; PIN(sc[ss][1][i - 1]); }
        if constexpr (DOS && i == 0) SM(IC(0));
        if constexpr (DOS && !DOV && i < 7) SM(IC(i + 1));
        SBAR();
        if constexpr ((nk & 3) == 3) endslot(IC(64 + (nk >> 2)));
      }
      if constexpr (DOV) {
        FRD(IC(nv + 3));
        if constexpr (H == 0) { MFZ(o[i][0], fr[nv & 3], pw[pc][0]); MFZ(o[i][1], fr[nv & 3], pw[pc][1]); }
        else { MF(o[i][0], fr[nv & 3], pw[pc][0]); MF(o[i][1], fr[nv & 3], pw[pc][1]); }
        if constexpr (DOS && !DOK && i == 0) SM(IC(0));
        if constexpr (DOS && i < 7) SM(IC(i + 1));
        if constexpr (H == 15 && i == 0) { inv[0] = __builtin_amdgcn_rcpf(xg_sum(lsum[0])); inv[1] = __builtin_amdgcn_rcpf(xg_sum(lsum[1])); PIN(inv[0]); PIN(inv[1]); }
        if constexpr (H == 15 && i >= 3 && (i & 1)) {
          constexpr int os = (i - 3) >> 1;
          NOPS(3);
          OST(IC(os));
        }
        SBAR();
        if constexpr ((nv & 3) == 3) endslot(IC(64 + (nv >> 2)));
      }
    });
    if constexpr (DOS) {
      if (__builtin_expect(__any(!(lsum[0] + lsum[1] <= LBOUND)), 0)) {
        NOPS(7);
        SBAR();
#pragma unroll
        for (int qb = 0; qb < 2; ++qb) {
          const f32x4 a = sc[ss][0][qb], b = sc[ss][1][qb];
          const float dl = xg_max(MX3(MX3(0.f, a[0], a[1]), MX3(a[2], a[3], b[0]), MX3(b[1], b[2], b[3])));
          const float f = EXP2(-dl);
          float ls = lold[qb];
          PIN(ls);
          ls *= f;
          if constexpr (DOV) {
#pragma unroll
            for (int d = 0; d < 8; ++d) o[d][qb] *= f;
          }
#pragma unroll
          for (int kt = 0; kt < 2; ++kt) {
            if constexpr (DOK) sc[sn][kt][qb] -= dl;
#pragma unroll
            for (int r = 0; r < 4; r += 2) {
              const float e0 = EXP2(sc[ss][kt][qb][r] - dl), e1 = EXP2(sc[ss][kt][qb][r + 1] - dl);
              ls += e0; ls += e1;
              PIN(ls);
              pw[pn][qb][kt * 2 + (r >> 1)] = pk2(e0, e1);
            }
          }
          lsum[qb] = ls;
          negm[qb] -= dl;
          PIN(pw[pn][qb]); PIN(negm[qb]);
        }
        SBAR();
        NOPS(1);
      }
      SBAR();
    }
  });
  STAMP(12);
  NOPS(7);
  OST(IC(3));

  const int yrow = within < 28 ? fj : 224 + xcd * 4 + within - 28;
  {
    LAS char* const fbw = (LAS char*)(uintptr_t)ldsf + l * 16;
    LAS const char* const ft0 = (LAS const char*)(uintptr_t)(ldsf + (4 * g + (c >> 2)) * 64 + (g & 1) * 32 + (c & 3) * 8);
    LAS const char* const ft1 = (LAS const char*)(uintptr_t)((ldsf + (4 * g + (c >> 2)) * 64 + (g & 1) * 32 + (c & 3) * 8) ^ 32);
    waitv<8>();
    u32x4 fx[4];
#pragma unroll
    for (int i = 0; i < 4; ++i) fx[i] = *(LAS const u32x4*)((LAS const char*)(uintptr_t)ldsf + (g * 4 + i) * 16);
    f32x4 fa0 = {}, fa1 = {};
    s16x4 fb[8];
#pragma unroll
    for (int r = 0; r < 4; ++r) {
#pragma unroll
      for (int q = 0; q < 3; ++q) { PIN(fw[3 * r + q]); *(LAS u32x4*)(fbw + q * 1024) = fw[3 * r + q]; }
#pragma unroll
      for (int q = 0; q < 3; ++q) {
        const int p = 3 * r + q;
        const s16x4 b0 = trd(ft0 + q * 1024), b1 = trd(ft1 + q * 1024);
        if (p < 8) {
          const u32x2 xa = {fx[p >> 1][2 * (p & 1)], fx[p >> 1][2 * (p & 1) + 1]};
          fa0 = MFMA16(__builtin_bit_cast(s16x4, xa), b0, fa0);
          fa1 = MFMA16(__builtin_bit_cast(s16x4, xa), b1, fa1);
        } else { fb[2 * (p - 8)] = b0; fb[2 * (p - 8) + 1] = b1; }
      }
    }
    *(LAS float*)(lds + RED1 + (wid * 32 + (l & 31)) * 4) = sel2(g & 1, fa1[0], fa0[0]);
    waitv<0>();
    if (l == 0) gst8((u32x2){tlo, thi}, (unsigned)(((rb * 8 + wid) * 32 + head) * 8), ws + WS_FO);
    STAMP(13);
    poll8((unsigned)(((mb * 4 + rbw) * 32 + kh * 16 + c) * 8), ws + WS_FO, tlo, thi, 0xffffffffu);
    issue(IC(128));
    STAMP(14);
    waitbar_l<63>();
    issue(IC(129));
    f32x4 gs = *(LAS const f32x4*)(lds + RED1 + ((c & 7) * 32 + 4 * g) * 4), us = *(LAS const f32x4*)(lds + RED1 + ((c & 7) * 32 + 16 + 4 * g) * 4);
    float hh[4];
#pragma unroll
    for (int j = 0; j < 4; ++j) {
      float a = gs[j], b = us[j];
      a += dppf<XOR1>(a); a += dppf<XOR2>(a); a += dppf<XOR7>(a);
      b += dppf<XOR1>(b); b += dppf<XOR2>(b); b += dppf<XOR7>(b);
      hh[j] = rbf(silu_f(rbf(a))) * rbf(b);
    }
    const s16x4 hfrag = __builtin_bit_cast(s16x4, (u32x2){pk2(hh[0], hh[1]), pk2(hh[2], hh[3])});
    sfor<2>([&](auto Pi) AI {
      constexpr int p = Pi;
      const f32x4 y0 = MFMA16(hfrag, fb[4 * p], (f32x4){}), y1 = MFMA16(hfrag, fb[4 * p + 1], (f32x4){});
      const f32x4 y2 = MFMA16(hfrag, fb[4 * p + 2], (f32x4){}), y3 = MFMA16(hfrag, fb[4 * p + 3], (f32x4){});
      gst4<p * 256>(sel2(g & 2, sel2(g & 1, y3[0], y2[0]), sel2(g & 1, y1[0], y0[0])), (unsigned)((wid * 128 + l) * 4), ws + WS_YP + (size_t)yrow * 4096);
    });
    STAMP(15);
  }

  const int idx = SEQ - 1 - (int)(key & 2047u);
  const bool owner = (idx >> 7) == mb;
  f32x4 yp[7];
  trb[0].init(lds0 + kg * 16384 + g * 2048 + (c >> 2) * 64 + (g & 1) * 32 + (c & 3) * 8);
  trb[1].init(lds0 + kg * 16384 + g * 2048 + (c >> 2) * 64 + ((g & 1) ^ 1) * 32 + (c & 3) * 8);
  auto item = [](int s) { return s < 52 ? s : s < 58 ? 52 + 2 * (s - 52) : 53 + 2 * (s - 58); };
  u32x2 pf;
  WRD(IC(0), WB(0));
  SBAR();
  sfor<64>([&](auto Si) AI {
    constexpr int s = Si, u = s >> 2, it = item(s), x0 = (it & ~1) % XD2, x1 = (it | 1) % XD2;
    if constexpr (s + 1 < 64) WRD(IC(s + 1), WB(item(s + 1)));
    SBAR();
    if constexpr (!(it & 1)) waitv<SC.younger(OX, 64 + it + 1, B0 + s - 1)>(xf[x0], xf[x1]);
    sfor<4>([&](auto Dd) AI {
      constexpr int d = Dd, dt = (it & 1) * 4 + d;
      if constexpr (s < 2) { MFZ(acc[dt][0], wf[s & 1][d], xf[x0]); MFZ(acc[dt][1], wf[s & 1][d], xf[x1]); }
      else { MF(acc[dt][0], wf[s & 1][d], xf[x0]); MF(acc[dt][1], wf[s & 1][d], xf[x1]); }
      if constexpr (s == 58) {
        gst16<d * 1024>(acc[d][0], (unsigned)((rbw * 2 + kg) * 4096 + l * 16), ws + WS_XC + (size_t)(bid ^ 4) * 65536);
        gst16<d * 1024>(acc[d][1], (unsigned)((rbw * 2 + kg) * 4096 + l * 16), ws + WS_XC + (size_t)(bid ^ 4) * 65536 + 32768);
      }
    });
    SBAR();
    if constexpr (s == 61) {
      waitv<0>();
      if (l == 0) gst8((u32x2){tlo, thi}, (unsigned)(((bid ^ 4) * 4 + rbw) * 64 + kg * 8), ws + WS_FX);
    }
    if constexpr (s == 62) gld8<0, true>(pf, (unsigned)((bid * 4 + rbw) * 64 + (l & 1) * 8), ws + WS_FX);
    if constexpr (s == 14) {
      if (l == 0) gst8((u32x2){tlo, thi}, (unsigned)((wid * 256 + yrow) * 8), ws + WS_FY);
    }
    if constexpr (s == 22) {
      if (owner) { gld16<0, true>(fm[0], (unsigned)(nb * 2048 + (l < 56 ? l : 55) * 32), ws + WS_FY); gld16<16, true>(fm[1], (unsigned)(nb * 2048 + (l < 56 ? l : 55) * 32), ws + WS_FY); }
    }
    if constexpr (s == 36) {
      if (owner) {
        PIN(fm[0]); PIN(fm[1]);
        for (int spin = 0; spin < SPINS; ++spin) {
          const bool ok = (fm[0][0] == tlo) & (fm[0][1] == thi) & (fm[0][2] == tlo) & (fm[0][3] == thi) & (fm[1][0] == tlo) & (fm[1][1] == thi) & (fm[1][2] == tlo) & (fm[1][3] == thi);
          if (__builtin_expect(__all(ok), 1)) break;
          __builtin_amdgcn_s_sleep(1);
          gld16<0, true>(fm[0], (unsigned)(nb * 2048 + (l < 56 ? l : 55) * 32), ws + WS_FY); gld16<16, true>(fm[1], (unsigned)(nb * 2048 + (l < 56 ? l : 55) * 32), ws + WS_FY);
          waitv<0>(fm[0], fm[1]);
        }
        const unsigned yv = (unsigned)(((t >> 4) * 1024 + nb * 128 + kh * 64 + (t & 15) * 4) * 4);
        sfor<7>([&](auto I) AI { constexpr int q = I; gld16<0, true>(yp[q], yv, ws + WS_YP + (size_t)q * 32 * 4096); });
      }
    }
    if constexpr (s == 51) {
      if (owner) {
        f32x4 a = {};
#pragma unroll
        for (int q = 0; q < 7; ++q) { PIN(yp[q]); a += yp[q]; }
#pragma unroll
        for (int j = 0; j < 4; ++j) a[j] = xg_sum(a[j]);
        *(LAS f32x4*)(lds + YS + (wid * 64 + c * 4) * 4) = a;
      }
    }
    issue(IC(B0 + s));
    SBAR();
    if constexpr ((s & 3) == 2 && u < 13) {
      waitbar<SC.younger(OG, 64 + 4 * u + 7, B0 + s)>();
      if constexpr (u % 4 == 1) STAMP(16 + u / 4);
    }
    if constexpr (s == 52) waitbar<SC.younger(OG, 64 + 59, B0 + s)>();
    if constexpr (s == 54) { waitbar_l<SC.younger(OG, 64 + 63, B0 + s)>(); STAMP(19); }
  });
  STAMP(20);

  f32x4 kep[4], oth[8];
  unsigned gate[8];
  waitv<0>(pf);
  if (!__all((pf[0] == tlo) & (pf[1] == thi))) poll8((unsigned)((bid * 4 + rbw) * 64 + (l & 1) * 8), ws + WS_FX, tlo, thi, 0xffffffffu);
  STAMP(21);
  sfor<8>([&](auto Dd) AI { constexpr int d = Dd; gld16<d * 1024 - 4096, true>(oth[d], (unsigned)(kg * 32768 + rbw * 8192 + l * 16), ws + WS_XC + (size_t)bid * 65536 + 4096); });
  {
    LAS char* dst = lds + (wid ^ 4) * 4096 + l * 16;
    LAS const char* src = lds + wid * 4096 + l * 16;
#pragma unroll
    for (int d = 4; d < 8; ++d) { PIN(acc[d][0]); PIN(acc[d][1]); }
    if (kg) {
#pragma unroll
      for (int d = 0; d < 4; ++d) { *(LAS f32x4*)(dst + d * 1024) = acc[4 + d][0]; kep[d] = acc[4 + d][1]; }
    } else {
#pragma unroll
      for (int d = 0; d < 4; ++d) { *(LAS f32x4*)(dst + d * 1024) = acc[4 + d][1]; kep[d] = acc[4 + d][0]; }
    }
    unsigned xnp[8];
    PIN(st);
#pragma unroll
    for (int j = 0; j < 8; ++j) {
      PIN(xe[j >> 1]);
      const unsigned xw = xe[j >> 1][j & 1];
      const unsigned dd = pk2(bflo(xw) - st[0], bfhi(xw) - st[0]);
      xnp[j] = pk2(bflo(dd) * st[1], bfhi(dd) * st[1]);
      gate[j] = pk2(silu_f(bflo(xnp[j])), silu_f(bfhi(xnp[j])));
      PIN(xnp[j]); PIN(gate[j]);
    }
    if (owner && erow == idx) {
      const unsigned ord = key >> 11, bits = (ord & 0x8000u) ? (ord & 0x7fffu) : (~ord & 0xffffu);
      const float sg = rbf(sigmoid_f(bflo(bits)));
#pragma unroll
      for (int q = 0; q < 4; ++q) {
        f32x4 a = {};
#pragma unroll
        for (int p = 0; p < 8; ++p) a += *(LAS const f32x4*)(lds + YS + (p * 64 + q * 16 + 4 * g) * 4);
#pragma unroll
        for (int j = 0; j < 2; ++j) {
          const float lo = rbf(bflo(xnp[2 * q + j]) + rbf(rbf(a[2 * j]) * sg)), hi = rbf(bfhi(xnp[2 * q + j]) + rbf(rbf(a[2 * j + 1]) * sg));
          gate[2 * q + j] = pk2(silu_f(lo), silu_f(hi));
        }
      }
    }
    waitbar_l<63>();
    STAMP(22);
#pragma unroll
    for (int d = 0; d < 4; ++d) kep[d] += *(LAS const f32x4*)(src + d * 1024);
  }
  STAMP(23);
  waitv<0>(oth[0], oth[1]);
#pragma unroll
  for (int d = 2; d < 8; ++d) PIN(oth[d]);
  STAMP(24);
  u16* op = out + (size_t)erow * DM + ecol;
#pragma unroll
  for (int d = 0; d < 4; ++d) {
    const f32x4 a = kep[d] + (oth[d] + oth[4 + d]);
    u32x2 res;
#pragma unroll
    for (int j = 0; j < 2; ++j) {
      const unsigned sp = pk2(a[2 * j], a[2 * j + 1]), gp = gate[2 * d + j], xw = xe[d][j];
      const unsigned pr = pk2(bflo(sp) * bflo(gp), bfhi(sp) * bfhi(gp));
      res[j] = pk2(bflo(xw) + bflo(pr), bfhi(xw) + bfhi(pr));
    }
    *(u32x2*)(op + d * 16) = res;
  }
  STAMP(25);
  STAMP_OUT(0, 26);
}

extern "C" void kernel_launch(void* const* d_in, const int* in_sizes, int n_in, void* d_out, int out_size, void* d_ws, size_t ws_size,
                              hipStream_t stream) {
  k_fused<<<256, 512, 0, stream>>>((const u16*)d_in[0], (const u16*)d_in[1], (const u16*)d_in[2], (const u16*)d_in[3], (const u16*)d_in[4],
                                   (const u16*)d_in[6], (const u16*)d_in[7], (const u16*)d_in[8], (const u16*)d_in[9], (char*)d_ws, (u16*)d_out);
}
```

```cpp
#include <hip/hip_runtime.h>
#include <stdint.h>
#include <type_traits>

typedef unsigned short u16;
typedef short bf16x8 __attribute__((ext_vector_type(8)));
typedef short s16x4 __attribute__((ext_vector_type(4)));
typedef float f32x4 __attribute__((ext_vector_type(4)));
typedef float f32x2 __attribute__((ext_vector_type(2)));
typedef unsigned u32x4 __attribute__((ext_vector_type(4)));
typedef unsigned u32x2 __attribute__((ext_vector_type(2)));
typedef __bf16 bf2_t __attribute__((ext_vector_type(2)));
#define LAS __attribute__((address_space(3)))
#define DI __device__ __forceinline__
#define AI __attribute__((always_inline))
#define SBAR() __builtin_amdgcn_sched_barrier(0)
template <class T> __device__ __forceinline__ void PIN(T& x) { asm volatile("" : "+v"(x)); }
template <class B> DI void MF(f32x4& acc, const bf16x8& a, const B& b) { asm volatile("v_mfma_f32_16x16x32_bf16 %0, %1, %2, %0" : "+v"(acc) : "v"(a), "v"(b)); }
template <class B> DI void MFC(f32x4& d, const bf16x8& a, const B& b, const f32x4& c) { asm volatile("v_mfma_f32_16x16x32_bf16 %0, %1, %2, %3" : "=&v"(d) : "v"(a), "v"(b), "v"(c)); }
template <class B> DI void MFZ(f32x4& d, const bf16x8& a, const B& b) { asm volatile("v_mfma_f32_16x16x32_bf16 %0, %1, %2, 0" : "=&v"(d) : "v"(a), "v"(b)); }
#define NOPS(n) asm volatile("s_nop " #n)
#define MFMA16(a, b, c) __builtin_amdgcn_mfma_f32_16x16x16bf16_1k((a), (b), (c), 0, 0, 0)
#define IC(n) std::integral_constant<int, (n)>{}

constexpr int SEQ = 2048, DM = 1024, DH = 128, SK = 2048, WIN = 512, FF = 3584;
constexpr size_t WS_XN = 0;
constexpr size_t WS_KV = 4u << 20;
constexpr size_t WS_ST = 5u << 20;
constexpr size_t WS_EP = WS_ST + 16384;
constexpr size_t WS_G0 = WS_ST + 32768;
constexpr size_t WS_FK = WS_ST + 49152;
constexpr size_t WS_YP = WS_ST + 65536;
constexpr size_t WS_FO = 7u << 20;
constexpr size_t WS_FY = WS_FO + 32768;
constexpr size_t WS_FX = WS_FO + 65536;
constexpr size_t WS_O = 8u << 20;
constexpr size_t WS_XC = 24u << 20;

template <int I, int N, class F> DI void sfor_impl(F&& f) {
  if constexpr (I < N) { f(std::integral_constant<int, I>{}); sfor_impl<I + 1, N>(f); }
}
template <int N, class F> DI void sfor(F&& f) { sfor_impl<0, N>(f); }

DI unsigned pk2(float a, float b) { f32x2 v = {a, b}; return __builtin_bit_cast(unsigned, __builtin_convertvector(v, bf2_t)); }
DI float rbf(float a) { return (float)(__bf16)a; }
DI float bflo(unsigned u) { return __uint_as_float(u << 16); }
DI float bfhi(unsigned u) { return __uint_as_float(u & 0xffff0000u); }
DI float sigmoid_f(float v) { return __builtin_amdgcn_rcpf(1.f + __builtin_amdgcn_exp2f(v * -1.4426950408889634f)); }
DI float sel2(bool c, float a, float b) { PIN(a); PIN(b); return c ? a : b; }
DI float silu_f(float v) { return v * sigmoid_f(v); }
template <int CTRL> DI float dppf(float x) { return __builtin_bit_cast(float, __builtin_amdgcn_mov_dpp(__builtin_bit_cast(int, x), CTRL, 0xf, 0xf, true)); }
template <int CTRL> DI unsigned dppu(unsigned x) { return (unsigned)__builtin_amdgcn_mov_dpp((int)x, CTRL, 0xf, 0xf, true); }
constexpr int XOR1 = 0xB1, XOR2 = 0x4E, XOR7 = 0x141, XOR8 = 0x128;
DI float xg_sum(float x) {
  auto s = __builtin_amdgcn_permlane16_swap(__float_as_uint(x), __float_as_uint(x), false, false);
  x = __uint_as_float(s[0]) + __uint_as_float(s[1]);
  auto t = __builtin_amdgcn_permlane32_swap(__float_as_uint(x), __float_as_uint(x), false, false);
  return __uint_as_float(t[0]) + __uint_as_float(t[1]);
}
DI float xg_max(float x) {
  auto s = __builtin_amdgcn_permlane16_swap(__float_as_uint(x), __float_as_uint(x), false, false);
  x = __builtin_fmaxf(__uint_as_float(s[0]), __uint_as_float(s[1]));
  auto t = __builtin_amdgcn_permlane32_swap(__float_as_uint(x), __float_as_uint(x), false, false);
  return __builtin_fmaxf(__uint_as_float(t[0]), __uint_as_float(t[1]));
}
DI float wave_sum(float v) { v += dppf<XOR1>(v); v += dppf<XOR2>(v); v += dppf<XOR7>(v); v += dppf<XOR8>(v); return xg_sum(v); }
DI unsigned umax(unsigned a, unsigned b) { return a > b ? a : b; }
DI unsigned wave_max_u32(unsigned v) {
  v = umax(v, dppu<XOR1>(v)); v = umax(v, dppu<XOR2>(v)); v = umax(v, dppu<XOR7>(v)); v = umax(v, dppu<XOR8>(v));
  auto s = __builtin_amdgcn_permlane16_swap(v, v, false, false); v = umax(s[0], s[1]);
  auto t = __builtin_amdgcn_permlane32_swap(v, v, false, false); v = umax(t[0], t[1]);
  return __builtin_amdgcn_readfirstlane(v);
}

template <int OFF, bool DEV = false> DI void glds(unsigned ldsaddr, unsigned voff, const void* sbase) {
  if constexpr (DEV) asm volatile("s_mov_b32 m0, %0\n\ts_nop 0\n\tglobal_load_lds_dwordx4 %1, %2 offset:%3 sc1" ::"s"(ldsaddr), "v"(voff), "s"(sbase), "i"(OFF) : "memory");
  else asm volatile("s_mov_b32 m0, %0\n\ts_nop 0\n\tglobal_load_lds_dwordx4 %1, %2 offset:%3" ::"s"(ldsaddr), "v"(voff), "s"(sbase), "i"(OFF) : "memory");
}
DI void touch(unsigned ldsaddr, unsigned voff, const void* sbase) {
  asm volatile("s_mov_b32 m0, %0\n\ts_nop 0\n\tglobal_load_lds_dword %1, %2" ::"s"(ldsaddr), "v"(voff), "s"(sbase) : "memory");
}
template <int OFF, bool DEV = false, class T> DI void gld16(T& dst, unsigned voff, const void* sbase) {
  if constexpr (DEV) asm volatile("global_load_dwordx4 %0, %1, %2 offset:%3 sc1" : "=v"(dst) : "v"(voff), "s"(sbase), "i"(OFF) : "memory");
  else asm volatile("global_load_dwordx4 %0, %1, %2 offset:%3" : "=v"(dst) : "v"(voff), "s"(sbase), "i"(OFF) : "memory");
}
template <class T> DI void gld16v(T& dst, const void* vaddr) { asm volatile("global_load_dwordx4 %0, %1, off" : "=v"(dst) : "v"(vaddr) : "memory"); }
template <int OFF, bool DEV = false, class T> DI void gld8(T& dst, unsigned voff, const void* sbase) {
  if constexpr (DEV) asm volatile("global_load_dwordx2 %0, %1, %2 offset:%3 sc1" : "=v"(dst) : "v"(voff), "s"(sbase), "i"(OFF) : "memory");
  else asm volatile("global_load_dwordx2 %0, %1, %2 offset:%3" : "=v"(dst) : "v"(voff), "s"(sbase), "i"(OFF) : "memory");
}
template <int OFF> DI void gld2(unsigned& dst, unsigned voff, const void* sbase) {
  asm volatile("global_load_ushort %0, %1, %2 offset:%3" : "=v"(dst) : "v"(voff), "s"(sbase), "i"(OFF) : "memory");
}
template <int OFF, class T> DI void gst16(const T& v, unsigned voff, const void* sbase) {
  asm volatile("global_store_dwordx4 %0, %1, %2 offset:%3 sc1\n\ts_nop 1" ::"v"(voff), "v"(v), "s"(sbase), "i"(OFF) : "memory");
}
template <class T> DI void gst8(const T& v, unsigned voff, const void* sbase) {
  asm volatile("global_store_dwordx2 %0, %1, %2 sc1" ::"v"(voff), "v"(v), "s"(sbase) : "memory");
}
template <int OFF> DI void gst4(float v, unsigned voff, const void* sbase) {
  asm volatile("global_store_dword %0, %1, %2 offset:%3 sc1" ::"v"(voff), "v"(v), "s"(sbase), "i"(OFF) : "memory");
}
constexpr int SPINS = 1 << 16;
DI void poll8(unsigned voff, const void* sbase, unsigned lo, unsigned hi, unsigned lomask) {
  for (int spin = 0; spin < SPINS; ++spin) {
    u32x2 v;
    asm volatile("global_load_dwordx2 %0, %1, %2 sc1\n\ts_waitcnt vmcnt(0)" : "=&v"(v) : "v"(voff), "s"(sbase) : "memory");
    if (__all(((v[0] & lomask) == lo) & (v[1] == hi))) break;
    __builtin_amdgcn_s_sleep(1);
  }
}
template <int N> DI void waitv() { static_assert(N >= 0, "count"); asm volatile("s_waitcnt vmcnt(%0)" ::"n"(N > 63 ? 63 : N) : "memory"); }
template <int N, class T> DI void waitv(T& x) { static_assert(N >= 0, "count"); asm volatile("s_waitcnt vmcnt(%1)" : "+v"(x) : "n"(N > 63 ? 63 : N) : "memory"); }
template <int N, class T> DI void waitv(T& x, T& y) { static_assert(N >= 0, "count"); asm volatile("s_waitcnt vmcnt(%2)" : "+v"(x), "+v"(y) : "n"(N > 63 ? 63 : N) : "memory"); }
template <int N> DI void waitbar() { static_assert(N >= 0, "count"); asm volatile("s_waitcnt vmcnt(%0)\n\ts_barrier" ::"n"(N > 63 ? 63 : N) : "memory"); }
template <int N> DI void waitbar_l() { static_assert(N >= 0, "count"); asm volatile("s_waitcnt vmcnt(%0) lgkmcnt(0)\n\ts_barrier" ::"n"(N > 63 ? 63 : N) : "memory"); }
typedef short v4i16_t __attribute__((ext_vector_type(4)));
DI s16x4 trd(LAS const char* p) { return __builtin_amdgcn_ds_read_tr16_b64_v4i16((LAS v4i16_t*)p); }
struct LBase {
  unsigned lo, hi;
  DI void init(unsigned a) { lo = a; hi = a + 65536; PIN(lo); PIN(hi); }
  template <int OFF> DI LAS const char* at() const { return (LAS const char*)(uintptr_t)(OFF < 65536 ? lo : hi) + (OFF & 65535); }
};
DI bf16x8 cat8(s16x4 lo, s16x4 hi) { return __builtin_shufflevector(lo, hi, 0, 1, 2, 3, 4, 5, 6, 7); }

enum { OX, OG, OF, OM, OS, OP, OE, OW };
struct Sched {
  int n; short kind[400], idx[400], slot[400];
  constexpr Sched() : n(0), kind{}, idx{}, slot{} {}
  constexpr void add(int s, int k, int i) { kind[n] = (short)k; idx[n] = (short)i; slot[n] = (short)s; ++n; }
  constexpr int younger(int k, int i, int s) const {
    int p = -1;
    for (int q = 0; q < n; ++q) if (kind[q] == k && idx[q] == i) p = q;
    if (p < 0 || slot[p] > s) return -1;
    int c = 0;
    for (int q = p + 1; q < n; ++q) if (slot[q] <= s) ++c;
    return c;
  }
  constexpr int first(int s) const { int q = 0; while (q < n && slot[q] < s) ++q; return q; }
  constexpr int count(int s) const { int c = 0; for (int q = 0; q < n; ++q) if (slot[q] == s) ++c; return c; }
};

#define STAMP_DECL
#define STAMP(i)
#define STAMP_OUT(kid, n)

constexpr int RING = 4 * 32768, FB = RING, RED1 = FB + 8 * 3072, HSC = RED1 + 1024, YS = HSC + 512, DUMMY = YS + 2048, LDS_BYTES = DUMMY + 256;
constexpr int STAGE = 3 * 32768;
constexpr int XD = 16, XD2 = 16;
constexpr float LBOUND = 65536.f;
constexpr float QSCALE = 0.08838834764831845f * 1.4426950408889634f;
constexpr int B0 = 130;
constexpr Sched make_sched() {
  Sched s;
  for (int i = 0; i < 12; ++i) s.add(-2, OG, i);
  for (int i = 0; i < 12; ++i) s.add(-1, OX, i);
  for (int t = 0; t < 128; ++t) {
    if (t == 0 || t == 2) { s.add(t, OX, 12 + t); s.add(t, OX, 13 + t); }
    for (int p = 0; p < 24; ++p) { const int a = 3 + p, b2 = 2 * p + 1; if ((a > b2 ? a : b2) == t) { s.add(t, OX, 16 + 2 * p); s.add(t, OX, 17 + 2 * p); } }
    for (int v = 3; v < 18; ++v) for (int p = 0; p < 4; ++p) if (8 * (v - 3) + 7 + p == t) s.add(t, OG, 4 * v + p);
    if (t == 20) s.add(t, OM, 0);
    if (t >= 51 && t < 55) s.add(t, OF, 8 + t - 51);
    if (t >= 99 && t < 123 && (t - 99) % 3 == 0) s.add(t, OF, (t - 99) / 3);
    if (t == 60) s.add(t, OS, 0);
  }
  for (int i = 0; i < 12; ++i) s.add(128, OX, 64 + i);
  s.add(128, OM, 1);
  for (int i = 72; i < 76; ++i) s.add(129, OG, i);
  for (int i = 12; i < 16; ++i) s.add(129, OX, 64 + i);
  for (int i = 76; i < 80; ++i) s.add(129, OG, i);
  for (int t = 0; t < 64; ++t) {
    if (t == 52) for (int i = 0; i < 5; ++i) s.add(B0 + t, OE, i);
    if (t == 8 || t == 20) s.add(B0 + t, OM, t == 8 ? 2 : 3);
    for (int p = 0; p < 24; ++p) { const int a = 8 + p, b2 = 2 * p + 1; if ((a > b2 ? a : b2) == t) { s.add(B0 + t, OX, 80 + 2 * p); s.add(B0 + t, OX, 81 + 2 * p); } }
    for (int v = 4; v < 16; ++v) for (int p = 0; p < 4; ++p) if ((v < 14 ? 4 * (v - 3) + 3 + (p >> 1) : v == 14 ? 45 : 49) == t) s.add(B0 + t, OG, 64 + 4 * v + p);
  }
  return s;
}
constexpr Sched SC = make_sched();

__global__ __launch_bounds__(512, 2) void k_fused(const u16* __restrict__ x, const u16* __restrict__ kc, const u16* __restrict__ vc,
                                                  const u16* __restrict__ wq, const u16* __restrict__ wo, const u16* __restrict__ wr,
                                                  const u16* __restrict__ wgate, const u16* __restrict__ wup, const u16* __restrict__ wdown,
                                                  char* __restrict__ ws, u16* __restrict__ out) {
  __shared__ __attribute__((aligned(16))) char lds_raw[LDS_BYTES];
  LAS char* const lds = (LAS char*)lds_raw;
  const int t = threadIdx.x, l = t & 63, bid = blockIdx.x, c = l & 15, g = l >> 4;
  const int wid = __builtin_amdgcn_readfirstlane(t >> 6);
  const int xcd = bid & 7, within = bid >> 3, head = xcd * 4 + (within & 3), rb = within >> 2;
  const int fj = xcd * 28 + (within < 28 ? within : within - 28);
  const int kh = xcd >> 2, mb = 2 * rb + (xcd & 1), nb = ((xcd >> 1) & 1) * 4 + (within & 3);
  const int rbw = wid & 3, kg = wid >> 2, hrot = (xcd & 3) * 4;
  const unsigned lds0 = (unsigned)(uintptr_t)lds;
  STAMP_DECL
  STAMP(0);

  unsigned tlo, thi;
  {
    const int row = 8 * bid + wid;
    u32x4 a0, a1, w0, w1, kvo;
    u32x2 ep, klo, khi;
    unsigned e[8];
    gld16<0>(a0, (unsigned)(row * DM * 2 + l * 16), x); gld16<1024>(a1, (unsigned)(row * DM * 2 + l * 16), x);
    gld16<0>(w0, (unsigned)(l * 16), wr); gld16<1024>(w1, (unsigned)(l * 16), wr);
    gld8<0, true>(ep, 0u, ws + WS_EP);
    __builtin_amdgcn_s_barrier();
    const int fn = (bid * 4 + (wid & 3)) & 255, kvh = bid >> 6;
    int isv, un, fi;
    if (fn < 16) { isv = 0; un = fn >> 3; fi = fn & 7; }
    else if (fn >= 240) { isv = 1; un = 14 + ((fn - 240) >> 3); fi = fn & 7; }
    else { const int m = fn - 16; isv = m & 1; un = (m >> 4) + (isv ? 0 : 2); fi = (m & 15) >> 1; }
    if (wid < 4) {
      touch(lds0 + DUMMY, (unsigned)((wid * 256 + within * 8 + (l >> 3)) * 8192 + xcd * 1024 + (l & 7) * 128), wq);
      const int d = 32 * (fi >> 1) + 8 * (c >> 2) + 4 * (fi & 1) + (c & 3);
      const unsigned ko = (unsigned)(((kvh * SK + (SK - WIN) + un * 32 + (fi >> 2) * 16 + c) * DH + (fi & 3) * 32 + 4 * g) * 2);
      const unsigned vo = (unsigned)(((kvh * SK + (SK - WIN) + un * 32 + 4 * g + 8) * DH + d) * 2);
      if (!isv) { gld8<0>(klo, ko, kc); gld8<32>(khi, ko, kc); waitv<3>(); }
      else { sfor<8>([&](auto J) AI { constexpr int j = J; gld2<(16 * (j >> 2) + (j & 3) - 8) * DH * 2>(e[j], vo, vc); }); waitv<9>(); }
    } else {
      waitv<0>();
    }
    PIN(a0); PIN(a1); PIN(w0); PIN(w1); PIN(ep);
    STAMP(1);
    {
      const unsigned long long tg = (((unsigned long long)(unsigned)__builtin_amdgcn_readfirstlane(ep[1]) << 32) | (unsigned)__builtin_amdgcn_readfirstlane(ep[0])) + 1ull;
      tlo = (unsigned)tg; thi = (unsigned)(tg >> 32);
    }
    float v[16];
#pragma unroll
    for (int j = 0; j < 4; ++j) { v[2 * j] = bflo(a0[j]); v[2 * j + 1] = bfhi(a0[j]); v[8 + 2 * j] = bflo(a1[j]); v[9 + 2 * j] = bfhi(a1[j]); }
    float s = 0.f;
#pragma unroll
    for (int j = 0; j < 16; ++j) s += v[j];
    const float mean = wave_sum(s) * (1.f / DM);
    float q = 0.f;
#pragma unroll
    for (int j = 0; j < 16; ++j) { const float d = v[j] - mean; q += d * d; }
    const float var = wave_sum(q) * (1.f / DM);
    const float mbf = rbf(mean), rden = 1.f / rbf(sqrtf(rbf(rbf(var) + 1e-5f)));
    float rr = 0.f;
    u32x4 o0, o1;
#pragma unroll
    for (int j = 0; j < 8; ++j) {
      const unsigned dd = pk2(v[2 * j] - mbf, v[2 * j + 1] - mbf);
      const unsigned nn = pk2(bflo(dd) * rden, bfhi(dd) * rden);
      const unsigned ww = j < 4 ? w0[j] : w1[j - 4];
      rr += bflo(nn) * bflo(ww); rr += bfhi(nn) * bfhi(ww);
      if (j < 4) o0[j] = nn; else o1[j - 4] = nn;
    }
    *(LAS u32x4*)(lds + STAGE + (l * 8 + (wid ^ (l & 7))) * 16) = o0;
    *(LAS u32x4*)(lds + STAGE + 8192 + (l * 8 + (wid ^ (l & 7))) * 16) = o1;
    rr = wave_sum(rr);
    if (l == 0) {
      const unsigned bits = pk2(rr, 0.f) & 0xffffu;
      const unsigned ord = (bits & 0x8000u) ? (~bits & 0xffffu) : (bits | 0x8000u);
      *(LAS unsigned*)(lds + STAGE + 16384 + wid * 4) = (ord << 11) | (unsigned)(SEQ - 1 - row);
      *(LAS f32x2*)(lds + STAGE + 16384 + 32 + wid * 8) = (f32x2){mbf, rden};
    }
    waitbar_l<63>();
    if (wid >= 4) {
#pragma unroll
      for (int hh = 0; hh < 4; ++hh) {
        const int p = hh * 256 + (t - 256), ch = p >> 3, r = 8 * bid + ((p & 7) ^ (ch & 7));
        const u32x4 o = *(LAS const u32x4*)(lds + STAGE + p * 16);
        gst16<0>(o, (unsigned)(((((r >> 5) * 32 + (ch >> 2)) * 2 + ((r >> 4) & 1)) * 64 + (ch & 3) * 16 + (r & 15)) * 16), ws + WS_XN);
      }
      if (t < 256 + 8) gst8(*(LAS const f32x2*)(lds + STAGE + 16384 + 32 + (t - 256) * 8), (unsigned)((8 * bid + t - 256) * 8), ws + WS_ST);
      waitv<0>();
    }
    waitbar_l<63>();
    if (wid < 4) {
      if (!isv) {
        waitv<0>(klo, khi);
        kvo = (u32x4){klo[0], klo[1], khi[0], khi[1]};
      } else {
        waitv<0>(e[0], e[1]);
#pragma unroll
        for (int j = 2; j < 8; ++j) PIN(e[j]);
        kvo = (u32x4){e[0] | (e[1] << 16), e[2] | (e[3] << 16), e[4] | (e[5] << 16), e[6] | (e[7] << 16)};
      }
      gst16<0>(kvo, (unsigned)((bid * 4 + wid) * 1024 + l * 16), ws + WS_KV);
    }
    if (t == 256) {
      const u32x4 ka = *(LAS const u32x4*)(lds + STAGE + 16384), kb = *(LAS const u32x4*)(lds + STAGE + 16400);
      const unsigned key = umax(umax(umax(ka[0], ka[1]), umax(ka[2], ka[3])), umax(umax(kb[0], kb[1]), umax(kb[2], kb[3])));
      gst8((u32x2){(tlo << 27) | key, (tlo >> 5) | (thi << 27)}, (unsigned)(bid * 8), ws + WS_G0);
    }
    STAMP(2);
  }
  const unsigned glo = tlo << 27, ghi = (tlo >> 5) | (thi << 27);

  const char* const wq_b = (const char*)wq + head * 256;
  const unsigned wq_v = (unsigned)(((8 * wid + ((l & 31) >> 2)) * 4096 + (l >> 5) * 32 + ((l & 3) ^ (2 * (wid & 1))) * 8) * 2);
  const char* const kv_b = ws + WS_KV + (size_t)(head >> 3) * 262144;
  const unsigned kv_v = (unsigned)(wid * 4096 + l * 16);
  const char* const xn_b = ws + WS_XN + (size_t)rb * 8 * 65536 + 4096;
  const unsigned xn_v = (unsigned)(wid * 65536 + l * 16);
  const char* const wo_b = (const char*)wo + (size_t)kh * 2048 * (DM * 2) + nb * 256;
  const char* const o_b = ws + WS_O + (size_t)mb * 4 * 262144 + kh * 131072;
  const unsigned ldsw = lds0 + wid * 2048, ldsk = lds0 + wid * 4096, ldsf = lds0 + FB + wid * 3072;
  const int erow = mb * 128 + rbw * 32 + kg * 16 + c, ecol = nb * 128 + kh * 64 + 4 * g;
  bf16x8 xf[XD];
  u32x4 fm[2], fw[12];
  u32x2 fg;
  u32x2 xe[4];
  f32x2 st;
  unsigned key = 0;

  auto fo_v = [&](int j) AI { return (unsigned)(((mb * 4 + rbw) * 32 + kh * 16 + ((hrot + 4 * j + (l & 3)) & 15)) * 8); };
  auto issue = [&](auto Sl) AI {
    constexpr int sl = Sl, q0 = SC.first(sl), nq = SC.count(sl);
    sfor<nq>([&](auto Qi) AI {
      constexpr int k = SC.kind[q0 + Qi], i = SC.idx[q0 + Qi];
      if constexpr (k == OX && i < 64) gld16<(i & 7) * 1024 - 4096, true>(xf[i % XD], xn_v, xn_b + (size_t)(i >> 3) * 8192);
      if constexpr (k == OX && i >= 64) gld16<(i & 3) * 1024, true>(xf[(i - 64) % XD2], (unsigned)(rbw * 262144 + kg * 4096 + l * 16), o_b + (size_t)(((((i - 64) >> 2) + hrot) & 15) * 8192));
      if constexpr (k == OG) {
        constexpr int v = i >> 2, p = i & 3;
        if constexpr (v < 8) {
          const char* sb = wq_b + (size_t)(v * 128 + (p >> 1) * 64) * 8192;
          glds<(p & 1) * 128>(ldsw + (v & 3) * 32768 + (p >> 1) * 16384 + (p & 1) * (1024 - 128), wq_v, sb);
        } else if constexpr (v < 16) {
          glds<p * 1024, true>(ldsk + (v & 3) * 32768, kv_v, kv_b + (size_t)(v - 8) * 32768);
        } else {
          const unsigned wo_v = (unsigned)(((8 * wid + ((l & 31) >> 2)) * DM + (l >> 5) * 32 + ((l & 3) ^ (2 * (wid & 1))) * 8) * 2);
          const char* sb = wo_b + (size_t)(((v - 16 + hrot) & 15) * 128 + (p >> 1) * 64) * (DM * 2) + (((p & 1) ^ kh ^ 1) << 7);
          glds<0>(ldsw + (v & 3) * 32768 + (p >> 1) * 16384 + (p & 1) * 1024, wo_v, sb);
        }
      }
      if constexpr (k == OF) {
        const int frho = l >> 2, fch = (l & 3) ^ (frho & 4 ? 2 : 0);
        if constexpr (i < 8) gld16v(fw[i], (const char*)(fch & 2 ? wup : wgate) + ((size_t)(128 * wid + 32 * (frho >> 2) + (frho & 3) + 4 * i) * FF + fj * 16 + (fch & 1) * 8) * 2);
        else gld16<(i - 8) * 64>(fw[i], (unsigned)((frho * DM + wid * 128 + fch * 8) * 2), (const char*)wdown + (size_t)fj * 16 * DM * 2);
      }
      if constexpr (k == OM && i > 0) gld8<0, true>(fg, fo_v(i), ws + WS_FO);
      if constexpr (k == OM && i == 0) gld8<0, true>(fg, (unsigned)((t & 255) * 8), wid < 4 ? ws + WS_G0 : ws + WS_FK + (head >> 3) * 2048);
      if constexpr (k == OS) {
        const u32x4 kk = *(LAS const u32x4*)(lds + HSC);
        key = __builtin_amdgcn_readfirstlane(umax(umax(kk[0], kk[1]), umax(kk[2], kk[3])));
        const int idx = SEQ - 1 - (int)(key & 2047u);
        glds<0, true>(ldsf, (unsigned)(((((idx >> 5) * 32 + 4 * wid + ((l >> 2) & 3)) * 2 + ((idx >> 4) & 1)) * 64 + (l & 3) * 16 + (idx & 15)) * 16), ws + WS_XN);
      }
      if constexpr (k == OE && i < 4) gld8<32 * i>(xe[i], (unsigned)((erow * DM + ecol) * 2), x);
      if constexpr (k == OE && i == 4) gld8<0, true>(st, (unsigned)(erow * 8), ws + WS_ST);
    });
  };
  auto endslot = [&](auto Sl) AI {
    constexpr int sl = Sl, u = sl >> 3, i = sl & 7;
    if constexpr (sl == 38) {
      waitv<SC.younger(OM, 0, sl - 1)>(fg);
      const unsigned lo = wid < 4 ? glo : tlo, hi = wid < 4 ? ghi : thi, mk = wid < 4 ? 0xf8000000u : 0xffffffffu;
      for (int spin = 0; spin < SPINS; ++spin) {
        if (__builtin_expect(__all(((fg[0] & mk) == lo) & (fg[1] == hi)), 1)) break;
        __builtin_amdgcn_s_sleep(1);
        gld8<0, true>(fg, (unsigned)((t & 255) * 8), wid < 4 ? ws + WS_G0 : ws + WS_FK + (head >> 3) * 2048);
        waitv<0>(fg);
      }
      const unsigned km = wave_max_u32(fg[0] & 0x07ffffffu);
      *(LAS unsigned*)(lds + HSC + wid * 4) = km;
    }
    if constexpr (sl == 47) { if (bid == 0 && t == 0) gst8((u32x2){tlo, thi}, 0u, ws + WS_EP); }
    if constexpr (sl >= 59 && sl < 83 && (sl - 59) % 3 == 0) {
      if (wid == (sl - 59) / 3)
        touch(lds0 + DUMMY, (unsigned)((within * 32 + (wid & 3) * 8 + (l >> 3)) * (FF * 2) + xcd * 896 + ((l & 7) < 7 ? (l & 7) : 6) * 128), wid < 4 ? wgate : wup);
    }
    if constexpr (sl >= 84 && sl < 100 && (sl - 84) % 4 == 0) {
      if (wid == (sl - 84) / 4)
        touch(lds0 + DUMMY, (unsigned)((kh * 2048 + wid * 512 + within * 16 + (xcd & 1) * 8 + (l >> 3)) * (DM * 2) + ((xcd >> 1) & 1) * 1024 + (l & 7) * 128), wo);
    }
    issue(Sl);
    SBAR();
    if constexpr (i == 6 && u < 15) {
      if constexpr (u == 5) waitbar_l<SC.younger(OG, 4 * u + 7, sl)>();
      else waitbar<SC.younger(OG, 4 * u + 7, sl)>();
      if constexpr (u == 0 || u == 1 || u == 5 || u == 7 || u == 8 || u == 10 || u == 12 || u == 14)
        STAMP(u == 0 ? 4 : u == 1 ? 5 : u == 5 ? 6 : u == 7 ? 7 : u == 8 ? 8 : u == 10 ? 9 : u == 12 ? 10 : 11);
    }
  };

  issue(IC(-2));
  if (wid < 4) {
    waitv<0>();
    if (l == 0) gst8((u32x2){tlo, thi}, (unsigned)((bid * 4 + wid) * 8), ws + WS_FK);
  }
  if (wid == 5) poll8((unsigned)((rb * 32 + (l & 31)) * 8), ws + WS_G0, glo, ghi, 0xf8000000u);
  waitbar<SC.younger(OG, 3, -2)>();
  issue(IC(-1));
  STAMP(3);

  LBase trb[2], frb;
  trb[0].init(lds0 + g * 2048 + (c >> 2) * 64 + (g & 1) * 32 + (c & 3) * 8);
  trb[1].init(lds0 + g * 2048 + (c >> 2) * 64 + ((g & 1) ^ 1) * 32 + (c & 3) * 8);
  frb.init(lds0 + l * 16);
  f32x4 acc[8][2];
  bf16x8 wf[2][4];
  auto WRD = [&](auto Si, auto Base) AI {
    constexpr int s = Si, base = Base;
    sfor<4>([&](auto Dd) AI {
      constexpr int d = Dd, o = base + (d >> 1) * 512;
      wf[s & 1][d] = cat8(trd(trb[d & 1].template at<o>()), trd(trb[d & 1].template at<o + 256>()));
    });
  };
#define WA(s) IC((((s) >> 3) & 3) * 32768 + (((s) >> 1) & 3) * 8192 + ((s) & 1) * 1024)
#define WB(s) IC((((s) >> 2) & 3) * 32768 + (((s) >> 1) & 1) * 8192 + ((s) & 1) * 1024)
  bf16x8 fr[4];
  auto FRD = [&](auto Ni) AI { constexpr int n = Ni; if constexpr (n < 256) fr[n & 3] = *(LAS const bf16x8*)(frb.at<((n >> 5) & 3) * 32768 + (n & 31) * 1024>()); };

  WRD(IC(0), WA(0));
  SBAR();
  bf16x8 qf[4][2];
  auto QPACK = [&](auto Ds) AI {
    constexpr int ds = Ds;
#pragma unroll
    for (int qb = 0; qb < 2; ++qb) {
      const f32x4 a = acc[2 * ds][qb] * QSCALE, b = acc[2 * ds + 1][qb] * QSCALE;
      qf[ds][qb] = __builtin_bit_cast(bf16x8, (u32x4){pk2(a[0], a[1]), pk2(a[2], a[3]), pk2(b[0], b[1]), pk2(b[2], b[3])});
      PIN(qf[ds][qb]);
    }
  };
  sfor<64>([&](auto Si) AI {
    constexpr int s = Si, x0 = (s & ~1) % XD, x1 = (s | 1) % XD;
    if constexpr (s + 1 < 64) WRD(IC(s + 1), WA(s + 1));
    else { FRD(IC(0)); FRD(IC(1)); FRD(IC(2)); }
    SBAR();
    if constexpr (!(s & 1)) waitv<SC.younger(OX, s + 1, s - 1)>(xf[x0], xf[x1]);
    sfor<4>([&](auto Dd) AI {
      constexpr int d = Dd, dt = (s & 1) * 4 + d;
      if constexpr (s < 2) { MFZ(acc[dt][0], wf[s & 1][d], xf[x0]); MFZ(acc[dt][1], wf[s & 1][d], xf[x1]); }
      else { MF(acc[dt][0], wf[s & 1][d], xf[x0]); MF(acc[dt][1], wf[s & 1][d], xf[x1]); }
      if constexpr (s == 63 && (d & 1)) { QPACK(IC(d >> 1)); SBAR(); }
    });
    SBAR();
    endslot(Si);
  });

  f32x4 o[8][2], sc[2][2][2], negm[2];
  u32x4 pw[2][2];
  float lsum[2] = {0.f, 0.f}, inv[2];
#define MX3(a, b, c) __builtin_fmaxf(__builtin_fmaxf((a), (b)), (c))
#define EXP2(x) __builtin_amdgcn_exp2f(x)
  const char* const od_b = ws + WS_O + ((size_t)(rb * 8) * 128 + head * 4) * 2048 + 4096;
  const unsigned od_v = (unsigned)(wid * 262144 + l * 16);
  auto OST = [&](auto Os) AI {
    constexpr int os = Os;
    sfor<2>([&](auto Qb) AI {
      constexpr int qb = Qb;
      PIN(o[2 * os][qb]); PIN(o[2 * os + 1][qb]);
      const f32x4 a = o[2 * os][qb] * inv[qb], b = o[2 * os + 1][qb] * inv[qb];
      gst16<(os * 2 + qb) * 1024 - 4096>((u32x4){pk2(a[0], a[1]), pk2(a[2], a[3]), pk2(b[0], b[1]), pk2(b[2], b[3])}, od_v, od_b);
    });
  };
  sfor<18>([&](auto Ui) AI {
    constexpr int H = Ui - 2, n0 = H < 0 ? 8 * (H + 2) : H < 14 ? 16 + 16 * H : 240 + 8 * (H - 14);
    constexpr bool DOK = H < 14, DOV = H >= 0, DOS = H >= -1 && H < 15;
    constexpr int sn = H & 1, ss = sn ^ 1, pc = H & 1, pn = pc ^ 1;
    const float lold[2] = {lsum[0], lsum[1]};
    auto SM = [&](auto Pi) AI {
      constexpr int p = Pi, kt = p >> 2, qb = (p >> 1) & 1, r = 2 * (p & 1);
      float a = sc[ss][kt][qb][r], b = sc[ss][kt][qb][r + 1];
      const float e0 = EXP2(a), e1 = EXP2(b);
      lsum[qb] += e0; lsum[qb] += e1;
      pw[pn][qb][kt * 2 + (p & 1)] = pk2(e0, e1);
      PIN(lsum[qb]); PIN(pw[pn][qb]);
    };
    sfor<8>([&](auto Ii) AI {
      constexpr int i = Ii, nk = n0 + ((DOK && DOV) ? 2 * i : i), nv = nk + (DOK ? 1 : 0), kt = i >> 2, ds = i & 3;
      if constexpr (DOK) {
        FRD(IC(nk + 3));
        if constexpr (H == -2 && i < 2) QPACK(IC(2 + i));
        sfor<2>([&](auto Qb) AI {
          constexpr int qb = Qb;
          if constexpr (ds == 0 && H == -2) MFZ(sc[sn][kt][qb], fr[nk & 3], qf[0][qb]);
          else if constexpr (ds == 0) MFC(sc[sn][kt][qb], fr[nk & 3], qf[0][qb], negm[qb]);
          else MF(sc[sn][kt][qb], fr[nk & 3], qf[ds][qb]);
        });
        if constexpr (H == -2 && (i == 5 || i == 6)) {
          constexpr int qb = i - 5;
          NOPS(3); PIN(sc[sn][0][qb]);
          const float m = -xg_max(__builtin_fmaxf(MX3(sc[sn][0][qb][0], sc[sn][0][qb][1], sc[sn][0][qb][2]), sc[sn][0][qb][3]));
          negm[qb] = (f32x4){m, m, m, m};
          sc[sn][0][qb] += negm[qb];
          PIN(negm[qb]); PIN(sc[sn][0][qb]);
        }
        if constexpr (H == -1 && (i == 1 || i == 2)) { NOPS(3); PIN(sc[ss][1][i - 1]); sc[ss][1][i - 1] += negm[i - 1]; PIN(sc[ss][1][i - 1]); }
        if constexpr (DOS && i == 0) SM(IC(0));
        if constexpr (DOS && !DOV && i < 7) SM(IC(i + 1));
        SBAR();
        if constexpr ((nk & 3) == 3) endslot(IC(64 + (nk >> 2)));
      }
      if constexpr (DOV) {
        FRD(IC(nv + 3));
        if constexpr (H == 0) { MFZ(o[i][0], fr[nv & 3], pw[pc][0]); MFZ(o[i][1], fr[nv & 3], pw[pc][1]); }
        else { MF(o[i][0], fr[nv & 3], pw[pc][0]); MF(o[i][1], fr[nv & 3], pw[pc][1]); }
        if constexpr (DOS && !DOK && i == 0) SM(IC(0));
        if constexpr (DOS && i < 7) SM(IC(i + 1));
        if constexpr (H == 15 && i == 0) { inv[0] = __builtin_amdgcn_rcpf(xg_sum(lsum[0])); inv[1] = __builtin_amdgcn_rcpf(xg_sum(lsum[1])); PIN(inv[0]); PIN(inv[1]); }
        if constexpr (H == 15 && i >= 3 && (i & 1)) {
          constexpr int os = (i - 3) >> 1;
          NOPS(3);
          OST(IC(os));
        }
        SBAR();
        if constexpr ((nv & 3) == 3) endslot(IC(64 + (nv >> 2)));
      }
    });
    if constexpr (DOS) {
      if (__builtin_expect(__any(!(lsum[0] + lsum[1] <= LBOUND)), 0)) {
        NOPS(7);
        SBAR();
#pragma unroll
        for (int qb = 0; qb < 2; ++qb) {
          const f32x4 a = sc[ss][0][qb], b = sc[ss][1][qb];
          const float dl = xg_max(MX3(MX3(0.f, a[0], a[1]), MX3(a[2], a[3], b[0]), MX3(b[1], b[2], b[3])));
          const float f = EXP2(-dl);
          float ls = lold[qb];
          PIN(ls);
          ls *= f;
          if constexpr (DOV) {
#pragma unroll
            for (int d = 0; d < 8; ++d) o[d][qb] *= f;
          }
#pragma unroll
          for (int kt = 0; kt < 2; ++kt) {
            if constexpr (DOK) sc[sn][kt][qb] -= dl;
#pragma unroll
            for (int r = 0; r < 4; r += 2) {
              const float e0 = EXP2(sc[ss][kt][qb][r] - dl), e1 = EXP2(sc[ss][kt][qb][r + 1] - dl);
              ls += e0; ls += e1;
              PIN(ls);
              pw[pn][qb][kt * 2 + (r >> 1)] = pk2(e0, e1);
            }
          }
          lsum[qb] = ls;
          negm[qb] -= dl;
          PIN(pw[pn][qb]); PIN(negm[qb]);
        }
        SBAR();
        NOPS(1);
      }
      SBAR();
    }
  });
  STAMP(12);
  NOPS(7);
  OST(IC(3));

  const int yrow = within < 28 ? fj : 224 + xcd * 4 + within - 28;
  {
    LAS char* const fbw = (LAS char*)(uintptr_t)ldsf + l * 16;
    LAS const char* const ft0 = (LAS const char*)(uintptr_t)(ldsf + (4 * g + (c >> 2)) * 64 + (g & 1) * 32 + (c & 3) * 8);
    LAS const char* const ft1 = (LAS const char*)(uintptr_t)((ldsf + (4 * g + (c >> 2)) * 64 + (g & 1) * 32 + (c & 3) * 8) ^ 32);
    waitv<8>();
    u32x4 fx[4];
#pragma unroll
    for (int i = 0; i < 4; ++i) fx[i] = *(LAS const u32x4*)((LAS const char*)(uintptr_t)ldsf + (g * 4 + i) * 16);
    f32x4 fa0 = {}, fa1 = {};
    s16x4 fb[8];
#pragma unroll
    for (int r = 0; r < 4; ++r) {
#pragma unroll
      for (int q = 0; q < 3; ++q) { PIN(fw[3 * r + q]); *(LAS u32x4*)(fbw + q * 1024) = fw[3 * r + q]; }
#pragma unroll
      for (int q = 0; q < 3; ++q) {
        const int p = 3 * r + q;
        const s16x4 b0 = trd(ft0 + q * 1024), b1 = trd(ft1 + q * 1024);
        if (p < 8) {
          const u32x2 xa = {fx[p >> 1][2 * (p & 1)], fx[p >> 1][2 * (p & 1) + 1]};
          fa0 = MFMA16(__builtin_bit_cast(s16x4, xa), b0, fa0);
          fa1 = MFMA16(__builtin_bit_cast(s16x4, xa), b1, fa1);
        } else { fb[2 * (p - 8)] = b0; fb[2 * (p - 8) + 1] = b1; }
      }
    }
    *(LAS float*)(lds + RED1 + (wid * 32 + (l & 31)) * 4) = sel2(g & 1, fa1[0], fa0[0]);
    waitv<0>();
    if (l == 0) gst8((u32x2){tlo, thi}, (unsigned)(((rb * 8 + wid) * 32 + head) * 8), ws + WS_FO);
    STAMP(13);
    poll8(fo_v(0), ws + WS_FO, tlo, thi, 0xffffffffu);
    issue(IC(128));
    STAMP(14);
    waitbar_l<63>();
    issue(IC(129));
    f32x4 gs = *(LAS const f32x4*)(lds + RED1 + ((c & 7) * 32 + 4 * g) * 4), us = *(LAS const f32x4*)(lds + RED1 + ((c & 7) * 32 + 16 + 4 * g) * 4);
    float hh[4];
#pragma unroll
    for (int j = 0; j < 4; ++j) {
      float a = gs[j], b = us[j];
      a += dppf<XOR1>(a); a += dppf<XOR2>(a); a += dppf<XOR7>(a);
      b += dppf<XOR1>(b); b += dppf<XOR2>(b); b += dppf<XOR7>(b);
      hh[j] = rbf(silu_f(rbf(a))) * rbf(b);
    }
    const s16x4 hfrag = __builtin_bit_cast(s16x4, (u32x2){pk2(hh[0], hh[1]), pk2(hh[2], hh[3])});
    sfor<2>([&](auto Pi) AI {
      constexpr int p = Pi;
      const f32x4 y0 = MFMA16(hfrag, fb[4 * p], (f32x4){}), y1 = MFMA16(hfrag, fb[4 * p + 1], (f32x4){});
      const f32x4 y2 = MFMA16(hfrag, fb[4 * p + 2], (f32x4){}), y3 = MFMA16(hfrag, fb[4 * p + 3], (f32x4){});
      gst4<p * 256>(sel2(g & 2, sel2(g & 1, y3[0], y2[0]), sel2(g & 1, y1[0], y0[0])), (unsigned)((wid * 128 + l) * 4), ws + WS_YP + (size_t)yrow * 4096);
    });
    STAMP(15);
  }

  const int idx = SEQ - 1 - (int)(key & 2047u);
  const bool owner = (idx >> 7) == mb;
  f32x4 yp[7];
  trb[0].init(lds0 + kg * 16384 + g * 2048 + (c >> 2) * 64 + (g & 1) * 32 + (c & 3) * 8);
  trb[1].init(lds0 + kg * 16384 + g * 2048 + (c >> 2) * 64 + ((g & 1) ^ 1) * 32 + (c & 3) * 8);
  auto item = [](int s) { return s < 48 ? s : s < 56 ? 48 + 2 * (s - 48) : 49 + 2 * (s - 56); };
  u32x2 pf;
  f32x4 giv[4];
  WRD(IC(0), WB(0));
  SBAR();
  sfor<64>([&](auto Si) AI {
    constexpr int s = Si, u = s >> 2, it = item(s), x0 = (it & ~1) % XD2, x1 = (it | 1) % XD2;
    if constexpr (s + 1 < 64) WRD(IC(s + 1), WB(item(s + 1)));
    SBAR();
    if constexpr (!(it & 1)) waitv<SC.younger(OX, 64 + it + 1, B0 + s - 1)>(xf[x0], xf[x1]);
    sfor<4>([&](auto Dd) AI {
      constexpr int d = Dd, dt = (it & 1) * 4 + d;
      if constexpr (s < 2) { MFZ(acc[dt][0], wf[s & 1][d], xf[x0]); MFZ(acc[dt][1], wf[s & 1][d], xf[x1]); }
      else { MF(acc[dt][0], wf[s & 1][d], xf[x0]); MF(acc[dt][1], wf[s & 1][d], xf[x1]); }
    });
    SBAR();
    if constexpr (s == 8 || s == 17 || s == 33) {
      constexpr int j = s == 8 ? 1 : s == 17 ? 2 : 3;
      waitv<SC.younger(OM, j, B0 + s - 1)>(fg);
      if (__builtin_expect(!__all((fg[0] == tlo) & (fg[1] == thi)), 0)) poll8(fo_v(j), ws + WS_FO, tlo, thi, 0xffffffffu);
    }
    if constexpr (s == 56) {
      LAS char* dst = lds + (wid ^ 4) * 4096 + l * 16;
      if (kg) {
#pragma unroll
        for (int d = 0; d < 4; ++d) { *(LAS f32x4*)(dst + (d & 1) * 2048 + (d >> 1) * 32768) = acc[d][0]; giv[d] = acc[d][1]; }
      } else {
#pragma unroll
        for (int d = 0; d < 4; ++d) { *(LAS f32x4*)(dst + (d & 1) * 2048 + (d >> 1) * 32768) = acc[d][1]; giv[d] = acc[d][0]; }
      }
      waitbar_l<63>();
    }
    if constexpr (s == 57) {
      LAS const char* src = lds + wid * 4096 + l * 16;
      sfor<4>([&](auto Dd) AI {
        constexpr int d = Dd;
        gst16<d * 1024>(giv[d] + *(LAS const f32x4*)(src + (d & 1) * 2048 + (d >> 1) * 32768), (unsigned)(wid * 4096 + l * 16), ws + WS_XC + (size_t)(bid ^ 4) * 32768);
      });
    }
    if constexpr (s == 62) {
      waitv<0>();
      if (l == 0) gst8((u32x2){tlo, thi}, (unsigned)(((bid ^ 4) * 8 + wid) * 64), ws + WS_FX);
      gld8<0, true>(pf, (unsigned)((bid * 8 + wid) * 64), ws + WS_FX);
    }
    if constexpr (s == 18) {
      if (l == 0) gst8((u32x2){tlo, thi}, (unsigned)((wid * 256 + yrow) * 8), ws + WS_FY);
    }
    if constexpr (s == 24) {
      if (owner) { gld16<0, true>(fm[0], (unsigned)(nb * 2048 + (l < 56 ? l : 55) * 32), ws + WS_FY); gld16<16, true>(fm[1], (unsigned)(nb * 2048 + (l < 56 ? l : 55) * 32), ws + WS_FY); }
    }
    if constexpr (s == 42) {
      if (owner) {
        PIN(fm[0]); PIN(fm[1]);
        for (int spin = 0; spin < SPINS; ++spin) {
          const bool ok = (fm[0][0] == tlo) & (fm[0][1] == thi) & (fm[0][2] == tlo) & (fm[0][3] == thi) & (fm[1][0] == tlo) & (fm[1][1] == thi) & (fm[1][2] == tlo) & (fm[1][3] == thi);
          if (__builtin_expect(__all(ok), 1)) break;
          __builtin_amdgcn_s_sleep(1);
          gld16<0, true>(fm[0], (unsigned)(nb * 2048 + (l < 56 ? l : 55) * 32), ws + WS_FY); gld16<16, true>(fm[1], (unsigned)(nb * 2048 + (l < 56 ? l : 55) * 32), ws + WS_FY);
          waitv<0>(fm[0], fm[1]);
        }
        const unsigned yv = (unsigned)(((t >> 4) * 1024 + nb * 128 + kh * 64 + (t & 15) * 4) * 4);
        sfor<7>([&](auto I) AI { constexpr int q = I; gld16<0, true>(yp[q], yv, ws + WS_YP + (size_t)q * 32 * 4096); });
      }
    }
    if constexpr (s == 55) {
      if (owner) {
        f32x4 a = {};
#pragma unroll
        for (int q = 0; q < 7; ++q) { PIN(yp[q]); a += yp[q]; }
#pragma unroll
        for (int j = 0; j < 4; ++j) a[j] = xg_sum(a[j]);
        *(LAS f32x4*)(lds + YS + (wid * 64 + c * 4) * 4) = a;
      }
    }
    issue(IC(B0 + s));
    SBAR();
    if constexpr (((s & 3) == 2 && u < 11) || s == 44) {
      waitbar<SC.younger(OG, 64 + 4 * u + 7, B0 + s)>();
      if constexpr (u % 4 == 1) STAMP(16 + u / 4);
    }
    if constexpr (s == 48) waitbar<SC.younger(OG, 64 + 55, B0 + s)>();
    if constexpr (s == 50) waitbar<SC.younger(OG, 64 + 59, B0 + s)>();
    if constexpr (s == 52) { waitbar_l<SC.younger(OG, 64 + 63, B0 + s)>(); STAMP(19); }
  });
  STAMP(20);

  f32x4 kep[4], oth[4];
  unsigned gate[8];
  waitv<0>(pf);
  if (!__all((pf[0] == tlo) & (pf[1] == thi))) poll8((unsigned)((bid * 8 + wid) * 64), ws + WS_FX, tlo, thi, 0xffffffffu);
  STAMP(21);
  sfor<4>([&](auto Dd) AI { constexpr int d = Dd; gld16<d * 1024, true>(oth[d], (unsigned)(wid * 4096 + l * 16), ws + WS_XC + (size_t)bid * 32768); });
  {
    LAS char* dst = lds + 65536 + (wid ^ 4) * 4096 + l * 16;
    LAS const char* src = lds + 65536 + wid * 4096 + l * 16;
#pragma unroll
    for (int d = 4; d < 8; ++d) { PIN(acc[d][0]); PIN(acc[d][1]); }
    if (kg) {
#pragma unroll
      for (int d = 0; d < 4; ++d) { *(LAS f32x4*)(dst + (d & 1) * 2048 + (d >> 1) * 32768) = acc[4 + d][0]; kep[d] = acc[4 + d][1]; }
    } else {
#pragma unroll
      for (int d = 0; d < 4; ++d) { *(LAS f32x4*)(dst + (d & 1) * 2048 + (d >> 1) * 32768) = acc[4 + d][1]; kep[d] = acc[4 + d][0]; }
    }
    unsigned xnp[8];
    PIN(st);
#pragma unroll
    for (int j = 0; j < 8; ++j) {
      PIN(xe[j >> 1]);
      const unsigned xw = xe[j >> 1][j & 1];
      const unsigned dd = pk2(bflo(xw) - st[0], bfhi(xw) - st[0]);
      xnp[j] = pk2(bflo(dd) * st[1], bfhi(dd) * st[1]);
      gate[j] = pk2(silu_f(bflo(xnp[j])), silu_f(bfhi(xnp[j])));
      PIN(xnp[j]); PIN(gate[j]);
    }
    if (owner && erow == idx) {
      const unsigned ord = key >> 11, bits = (ord & 0x8000u) ? (ord & 0x7fffu) : (~ord & 0xffffu);
      const float sg = rbf(sigmoid_f(bflo(bits)));
#pragma unroll
      for (int q = 0; q < 4; ++q) {
        f32x4 a = {};
#pragma unroll
        for (int p = 0; p < 8; ++p) a += *(LAS const f32x4*)(lds + YS + (p * 64 + q * 16 + 4 * g) * 4);
#pragma unroll
        for (int j = 0; j < 2; ++j) {
          const float lo = rbf(bflo(xnp[2 * q + j]) + rbf(rbf(a[2 * j]) * sg)), hi = rbf(bfhi(xnp[2 * q + j]) + rbf(rbf(a[2 * j + 1]) * sg));
          gate[2 * q + j] = pk2(silu_f(lo), silu_f(hi));
        }
      }
    }
    waitbar_l<63>();
    STAMP(22);
#pragma unroll
    for (int d = 0; d < 4; ++d) kep[d] += *(LAS const f32x4*)(src + (d & 1) * 2048 + (d >> 1) * 32768);
  }
  STAMP(23);
  waitv<0>(oth[0], oth[1]);
#pragma unroll
  for (int d = 2; d < 4; ++d) PIN(oth[d]);
  STAMP(24);
  u16* op = out + (size_t)erow * DM + ecol;
#pragma unroll
  for (int d = 0; d < 4; ++d) {
    const f32x4 a = kep[d] + oth[d];
    u32x2 res;
#pragma unroll
    for (int j = 0; j < 2; ++j) {
      const unsigned sp = pk2(a[2 * j], a[2 * j + 1]), gp = gate[2 * d + j], xw = xe[d][j];
      const unsigned pr = pk2(bflo(sp) * bflo(gp), bfhi(sp) * bfhi(gp));
      res[j] = pk2(bflo(xw) + bflo(pr), bfhi(xw) + bfhi(pr));
    }
    *(u32x2*)(op + d * 16) = res;
  }
  STAMP(25);
  STAMP_OUT(0, 26);
}

extern "C" void kernel_launch(void* const* d_in, const int* in_sizes, int n_in, void* d_out, int out_size, void* d_ws, size_t ws_size,
                              hipStream_t stream) {
  k_fused<<<256, 512, 0, stream>>>((const u16*)d_in[0], (const u16*)d_in[1], (const u16*)d_in[2], (const u16*)d_in[3], (const u16*)d_in[4],
                                   (const u16*)d_in[6], (const u16*)d_in[7], (const u16*)d_in[8], (const u16*)d_in[9], (char*)d_ws, (u16*)d_out);
}
```

```cpp
#include <hip/hip_runtime.h>
#include <stdint.h>
#include <type_traits>

typedef unsigned short u16;
typedef short bf16x8 __attribute__((ext_vector_type(8)));
typedef short s16x4 __attribute__((ext_vector_type(4)));
typedef float f32x4 __attribute__((ext_vector_type(4)));
typedef float f32x2 __attribute__((ext_vector_type(2)));
typedef unsigned u32x4 __attribute__((ext_vector_type(4)));
typedef unsigned u32x2 __attribute__((ext_vector_type(2)));
typedef __bf16 bf2_t __attribute__((ext_vector_type(2)));
#define LAS __attribute__((address_space(3)))
#define DI __device__ __forceinline__
#define AI __attribute__((always_inline))
#define SBAR() __builtin_amdgcn_sched_barrier(0)
template <class T> __device__ __forceinline__ void PIN(T& x) { asm volatile("" : "+v"(x)); }
template <class B> DI void MF(f32x4& acc, const bf16x8& a, const B& b) { asm volatile("v_mfma_f32_16x16x32_bf16 %0, %1, %2, %0" : "+v"(acc) : "v"(a), "v"(b)); }
template <class B> DI void MFC(f32x4& d, const bf16x8& a, const B& b, const f32x4& c) { asm volatile("v_mfma_f32_16x16x32_bf16 %0, %1, %2, %3" : "=&v"(d) : "v"(a), "v"(b), "v"(c)); }
template <class B> DI void MFZ(f32x4& d, const bf16x8& a, const B& b) { asm volatile("v_mfma_f32_16x16x32_bf16 %0, %1, %2, 0" : "=&v"(d) : "v"(a), "v"(b)); }
#define NOPS(n) asm volatile("s_nop " #n)
#define MFMA16(a, b, c) __builtin_amdgcn_mfma_f32_16x16x16bf16_1k((a), (b), (c), 0, 0, 0)
#define IC(n) std::integral_constant<int, (n)>{}

constexpr int SEQ = 2048, DM = 1024, DH = 128, SK = 2048, WIN = 512, FF = 3584;
constexpr size_t WS_XN = 0;
constexpr size_t WS_KV = 4u << 20;
constexpr size_t WS_ST = 5u << 20;
constexpr size_t WS_EP = WS_ST + 16384;
constexpr size_t WS_G0 = WS_ST + 32768;
constexpr size_t WS_FK = WS_ST + 49152;
constexpr size_t WS_YP = WS_ST + 65536;
constexpr size_t WS_FO = 7u << 20;
constexpr size_t WS_FY = WS_FO + 32768;
constexpr size_t WS_FX = WS_FO + 65536;
constexpr size_t WS_O = 8u << 20;
constexpr size_t WS_XC = 24u << 20;

template <int I, int N, class F> DI void sfor_impl(F&& f) {
  if constexpr (I < N) { f(std::integral_constant<int, I>{}); sfor_impl<I + 1, N>(f); }
}
template <int N, class F> DI void sfor(F&& f) { sfor_impl<0, N>(f); }

DI unsigned pk2(float a, float b) { f32x2 v = {a, b}; return __builtin_bit_cast(unsigned, __builtin_convertvector(v, bf2_t)); }
DI float rbf(float a) { return (float)(__bf16)a; }
DI float bflo(unsigned u) { return __uint_as_float(u << 16); }
DI float bfhi(unsigned u) { return __uint_as_float(u & 0xffff0000u); }
DI float sigmoid_f(float v) { return __builtin_amdgcn_rcpf(1.f + __builtin_amdgcn_exp2f(v * -1.4426950408889634f)); }
DI float sel2(bool c, float a, float b) { PIN(a); PIN(b); return c ? a : b; }
DI float silu_f(float v) { return v * sigmoid_f(v); }
template <int CTRL> DI float dppf(float x) { return __builtin_bit_cast(float, __builtin_amdgcn_mov_dpp(__builtin_bit_cast(int, x), CTRL, 0xf, 0xf, true)); }
template <int CTRL> DI unsigned dppu(unsigned x) { return (unsigned)__builtin_amdgcn_mov_dpp((int)x, CTRL, 0xf, 0xf, true); }
constexpr int XOR1 = 0xB1, XOR2 = 0x4E, XOR7 = 0x141, XOR8 = 0x128;
DI float xg_sum(float x) {
  auto s = __builtin_amdgcn_permlane16_swap(__float_as_uint(x), __float_as_uint(x), false, false);
  x = __uint_as_float(s[0]) + __uint_as_float(s[1]);
  auto t = __builtin_amdgcn_permlane32_swap(__float_as_uint(x), __float_as_uint(x), false, false);
  return __uint_as_float(t[0]) + __uint_as_float(t[1]);
}
DI float xg_max(float x) {
  auto s = __builtin_amdgcn_permlane16_swap(__float_as_uint(x), __float_as_uint(x), false, false);
  x = __builtin_fmaxf(__uint_as_float(s[0]), __uint_as_float(s[1]));
  auto t = __builtin_amdgcn_permlane32_swap(__float_as_uint(x), __float_as_uint(x), false, false);
  return __builtin_fmaxf(__uint_as_float(t[0]), __uint_as_float(t[1]));
}
DI float wave_sum(float v) { v += dppf<XOR1>(v); v += dppf<XOR2>(v); v += dppf<XOR7>(v); v += dppf<XOR8>(v); return xg_sum(v); }
DI unsigned umax(unsigned a, unsigned b) { return a > b ? a : b; }
DI unsigned wave_max_u32(unsigned v) {
  v = umax(v, dppu<XOR1>(v)); v = umax(v, dppu<XOR2>(v)); v = umax(v, dppu<XOR7>(v)); v = umax(v, dppu<XOR8>(v));
  auto s = __builtin_amdgcn_permlane16_swap(v, v, false, false); v = umax(s[0], s[1]);
  auto t = __builtin_amdgcn_permlane32_swap(v, v, false, false); v = umax(t[0], t[1]);
  return __builtin_amdgcn_readfirstlane(v);
}

template <int OFF, bool DEV = false> DI void glds(unsigned ldsaddr, unsigned voff, const void* sbase) {
  if constexpr (DEV) asm volatile("s_mov_b32 m0, %0\n\ts_nop 0\n\tglobal_load_lds_dwordx4 %1, %2 offset:%3 sc1" ::"s"(ldsaddr), "v"(voff), "s"(sbase), "i"(OFF) : "memory");
  else asm volatile("s_mov_b32 m0, %0\n\ts_nop 0\n\tglobal_load_lds_dwordx4 %1, %2 offset:%3" ::"s"(ldsaddr), "v"(voff), "s"(sbase), "i"(OFF) : "memory");
}
DI void touch(unsigned ldsaddr, unsigned voff, const void* sbase) {
  asm volatile("s_mov_b32 m0, %0\n\ts_nop 0\n\tglobal_load_lds_dword %1, %2" ::"s"(ldsaddr), "v"(voff), "s"(sbase) : "memory");
}
template <int OFF, bool DEV = false, class T> DI void gld16(T& dst, unsigned voff, const void* sbase) {
  if constexpr (DEV) asm volatile("global_load_dwordx4 %0, %1, %2 offset:%3 sc1" : "=v"(dst) : "v"(voff), "s"(sbase), "i"(OFF) : "memory");
  else asm volatile("global_load_dwordx4 %0, %1, %2 offset:%3" : "=v"(dst) : "v"(voff), "s"(sbase), "i"(OFF) : "memory");
}
template <class T> DI void gld16v(T& dst, const void* vaddr) { asm volatile("global_load_dwordx4 %0, %1, off" : "=v"(dst) : "v"(vaddr) : "memory"); }
template <int OFF, bool DEV = false, class T> DI void gld8(T& dst, unsigned voff, const void* sbase) {
  if constexpr (DEV) asm volatile("global_load_dwordx2 %0, %1, %2 offset:%3 sc1" : "=v"(dst) : "v"(voff), "s"(sbase), "i"(OFF) : "memory");
  else asm volatile("global_load_dwordx2 %0, %1, %2 offset:%3" : "=v"(dst) : "v"(voff), "s"(sbase), "i"(OFF) : "memory");
}
template <int OFF> DI void gld2(unsigned& dst, unsigned voff, const void* sbase) {
  asm volatile("global_load_ushort %0, %1, %2 offset:%3" : "=v"(dst) : "v"(voff), "s"(sbase), "i"(OFF) : "memory");
}
template <int OFF, class T> DI void gst16(const T& v, unsigned voff, const void* sbase) {
  asm volatile("global_store_dwordx4 %0, %1, %2 offset:%3 sc1\n\ts_nop 1" ::"v"(voff), "v"(v), "s"(sbase), "i"(OFF) : "memory");
}
template <class T> DI void gst8(const T& v, unsigned voff, const void* sbase) {
  asm volatile("global_store_dwordx2 %0, %1, %2 sc1" ::"v"(voff), "v"(v), "s"(sbase) : "memory");
}
template <int OFF> DI void gst4(float v, unsigned voff, const void* sbase) {
  asm volatile("global_store_dword %0, %1, %2 offset:%3 sc1" ::"v"(voff), "v"(v), "s"(sbase), "i"(OFF) : "memory");
}
constexpr int SPINS = 1 << 16;
DI void poll8(unsigned voff, const void* sbase, unsigned lo, unsigned hi, unsigned lomask) {
  for (int spin = 0; spin < SPINS; ++spin) {
    u32x2 v;
    asm volatile("global_load_dwordx2 %0, %1, %2 sc1\n\ts_waitcnt vmcnt(0)" : "=&v"(v) : "v"(voff), "s"(sbase) : "memory");
    if (__all(((v[0] & lomask) == lo) & (v[1] == hi))) break;
    __builtin_amdgcn_s_sleep(1);
  }
}
template <int N> DI void waitv() { static_assert(N >= 0, "count"); asm volatile("s_waitcnt vmcnt(%0)" ::"n"(N > 63 ? 63 : N) : "memory"); }
template <int N, class T> DI void waitv(T& x) { static_assert(N >= 0, "count"); asm volatile("s_waitcnt vmcnt(%1)" : "+v"(x) : "n"(N > 63 ? 63 : N) : "memory"); }
template <int N, class T> DI void waitv(T& x, T& y) { static_assert(N >= 0, "count"); asm volatile("s_waitcnt vmcnt(%2)" : "+v"(x), "+v"(y) : "n"(N > 63 ? 63 : N) : "memory"); }
template <int N> DI void waitbar() { static_assert(N >= 0, "count"); asm volatile("s_waitcnt vmcnt(%0)\n\ts_barrier" ::"n"(N > 63 ? 63 : N) : "memory"); }
template <int N> DI void waitbar_l() { static_assert(N >= 0, "count"); asm volatile("s_waitcnt vmcnt(%0) lgkmcnt(0)\n\ts_barrier" ::"n"(N > 63 ? 63 : N) : "memory"); }
typedef short v4i16_t __attribute__((ext_vector_type(4)));
DI s16x4 trd(LAS const char* p) { return __builtin_amdgcn_ds_read_tr16_b64_v4i16((LAS v4i16_t*)p); }
struct LBase {
  unsigned lo, hi;
  DI void init(unsigned a) { lo = a; hi = a + 65536; PIN(lo); PIN(hi); }
  template <int OFF> DI LAS const char* at() const { return (LAS const char*)(uintptr_t)(OFF < 65536 ? lo : hi) + (OFF & 65535); }
};
DI bf16x8 cat8(s16x4 lo, s16x4 hi) { return __builtin_shufflevector(lo, hi, 0, 1, 2, 3, 4, 5, 6, 7); }

enum { OX, OG, OF, OM, OS, OP, OE, OW };
struct Sched {
  int n; short kind[400], idx[400], slot[400];
  constexpr Sched() : n(0), kind{}, idx{}, slot{} {}
  constexpr void add(int s, int k, int i) { kind[n] = (short)k; idx[n] = (short)i; slot[n] = (short)s; ++n; }
  constexpr int younger(int k, int i, int s) const {
    int p = -1;
    for (int q = 0; q < n; ++q) if (kind[q] == k && idx[q] == i) p = q;
    if (p < 0 || slot[p] > s) return -1;
    int c = 0;
    for (int q = p + 1; q < n; ++q) if (slot[q] <= s) ++c;
    return c;
  }
  constexpr int first(int s) const { int q = 0; while (q < n && slot[q] < s) ++q; return q; }
  constexpr int count(int s) const { int c = 0; for (int q = 0; q < n; ++q) if (slot[q] == s) ++c; return c; }
};

#define STAMP_DECL
#define STAMP(i)
#define STAMP_OUT(kid, n)

constexpr int RING = 4 * 32768, FB = RING, RED1 = FB + 8 * 3072, HSC = RED1 + 1024, YS = HSC + 512, DUMMY = YS + 2048, LDS_BYTES = DUMMY + 256;
constexpr int STAGE = 3 * 32768;
constexpr int XD = 16, XD2 = 16;
constexpr float LBOUND = 65536.f;
constexpr float QSCALE = 0.08838834764831845f * 1.4426950408889634f;
constexpr int B0 = 130;
constexpr Sched make_sched() {
  Sched s;
  for (int i = 0; i < 12; ++i) s.add(-2, OG, i);
  for (int i = 0; i < 12; ++i) s.add(-1, OX, i);
  for (int t = 0; t < 128; ++t) {
    if (t == 0 || t == 2) { s.add(t, OX, 12 + t); s.add(t, OX, 13 + t); }
    for (int p = 0; p < 24; ++p) { const int a = 3 + p, b2 = 2 * p + 1; if ((a > b2 ? a : b2) == t) { s.add(t, OX, 16 + 2 * p); s.add(t, OX, 17 + 2 * p); } }
    for (int v = 3; v < 18; ++v) for (int p = 0; p < 4; ++p) if (8 * (v - 3) + 7 + p == t) s.add(t, OG, 4 * v + p);
    if (t == 20) s.add(t, OM, 0);
    if (t >= 98 && t < 122 && !(t & 1)) s.add(t, OF, (t - 98) / 2);
    if (t == 60) s.add(t, OS, 0);
  }
  for (int i = 0; i < 12; ++i) s.add(128, OX, 64 + i);
  s.add(128, OM, 1);
  for (int i = 72; i < 76; ++i) s.add(129, OG, i);
  for (int i = 12; i < 16; ++i) s.add(129, OX, 64 + i);
  for (int i = 76; i < 80; ++i) s.add(129, OG, i);
  for (int t = 0; t < 64; ++t) {
    if (t == 52) for (int i = 0; i < 5; ++i) s.add(B0 + t, OE, i);
    if (t == 8 || t == 20) s.add(B0 + t, OM, t == 8 ? 2 : 3);
    for (int p = 0; p < 24; ++p) { const int a = 8 + p, b2 = 2 * p + 1; if ((a > b2 ? a : b2) == t) { s.add(B0 + t, OX, 80 + 2 * p); s.add(B0 + t, OX, 81 + 2 * p); } }
    for (int v = 4; v < 16; ++v) for (int p = 0; p < 4; ++p) if ((v < 14 ? 4 * (v - 3) + 3 + (p >> 1) : v == 14 ? 45 : 49) == t) s.add(B0 + t, OG, 64 + 4 * v + p);
  }
  return s;
}
constexpr Sched SC = make_sched();

__global__ __launch_bounds__(512, 2) void k_fused(const u16* __restrict__ x, const u16* __restrict__ kc, const u16* __restrict__ vc,
                                                  const u16* __restrict__ wq, const u16* __restrict__ wo, const u16* __restrict__ wr,
                                                  const u16* __restrict__ wgate, const u16* __restrict__ wup, const u16* __restrict__ wdown,
                                                  char* __restrict__ ws, u16* __restrict__ out) {
  __shared__ __attribute__((aligned(16))) char lds_raw[LDS_BYTES];
  LAS char* const lds = (LAS char*)lds_raw;
  const int t = threadIdx.x, l = t & 63, bid = blockIdx.x, c = l & 15, g = l >> 4;
  const int wid = __builtin_amdgcn_readfirstlane(t >> 6);
  const int xcd = bid & 7, within = bid >> 3, head = xcd * 4 + (within & 3), rb = within >> 2;
  const int fj = xcd * 28 + (within < 28 ? within : within - 28);
  const int kh = xcd >> 2, mb = 2 * rb + (xcd & 1), nb = ((xcd >> 1) & 1) * 4 + (within & 3);
  const int rbw = wid & 3, kg = wid >> 2, hrot = (xcd & 3) * 4;
  const unsigned lds0 = (unsigned)(uintptr_t)lds;
  STAMP_DECL
  STAMP(0);

  unsigned tlo, thi;
  {
    const int row = 8 * bid + wid;
    u32x4 a0, a1, w0, w1, kvo;
    u32x2 ep, klo, khi;
    unsigned e[8];
    gld16<0>(a0, (unsigned)(row * DM * 2 + l * 16), x); gld16<1024>(a1, (unsigned)(row * DM * 2 + l * 16), x);
    gld16<0>(w0, (unsigned)(l * 16), wr); gld16<1024>(w1, (unsigned)(l * 16), wr);
    gld8<0, true>(ep, 0u, ws + WS_EP);
    __builtin_amdgcn_s_barrier();
    if (wid == 0) { touch(lds0 + DUMMY, (unsigned)((within * 8 + (l >> 3)) * 8192 + xcd * 1024 + (l & 7) * 128), wq); waitv<1>(); }
    else waitv<0>();
    PIN(a0); PIN(a1); PIN(w0); PIN(w1); PIN(ep);
    STAMP(1);
    {
      const unsigned long long tg = (((unsigned long long)(unsigned)__builtin_amdgcn_readfirstlane(ep[1]) << 32) | (unsigned)__builtin_amdgcn_readfirstlane(ep[0])) + 1ull;
      tlo = (unsigned)tg; thi = (unsigned)(tg >> 32);
    }
    float v[16];
#pragma unroll
    for (int j = 0; j < 4; ++j) { v[2 * j] = bflo(a0[j]); v[2 * j + 1] = bfhi(a0[j]); v[8 + 2 * j] = bflo(a1[j]); v[9 + 2 * j] = bfhi(a1[j]); }
    float s = 0.f;
#pragma unroll
    for (int j = 0; j < 16; ++j) s += v[j];
    const float mean = wave_sum(s) * (1.f / DM);
    float q = 0.f;
#pragma unroll
    for (int j = 0; j < 16; ++j) { const float d = v[j] - mean; q += d * d; }
    const float var = wave_sum(q) * (1.f / DM);
    const float mbf = rbf(mean), rden = 1.f / rbf(sqrtf(rbf(rbf(var) + 1e-5f)));
    float rr = 0.f;
    u32x4 o0, o1;
#pragma unroll
    for (int j = 0; j < 8; ++j) {
      const unsigned dd = pk2(v[2 * j] - mbf, v[2 * j + 1] - mbf);
      const unsigned nn = pk2(bflo(dd) * rden, bfhi(dd) * rden);
      const unsigned ww = j < 4 ? w0[j] : w1[j - 4];
      rr += bflo(nn) * bflo(ww); rr += bfhi(nn) * bfhi(ww);
      if (j < 4) o0[j] = nn; else o1[j - 4] = nn;
    }
    *(LAS u32x4*)(lds + STAGE + (l * 8 + (wid ^ (l & 7))) * 16) = o0;
    *(LAS u32x4*)(lds + STAGE + 8192 + (l * 8 + (wid ^ (l & 7))) * 16) = o1;
    rr = wave_sum(rr);
    if (l == 0) {
      const unsigned bits = pk2(rr, 0.f) & 0xffffu;
      const unsigned ord = (bits & 0x8000u) ? (~bits & 0xffffu) : (bits | 0x8000u);
      *(LAS unsigned*)(lds + STAGE + 16384 + wid * 4) = (ord << 11) | (unsigned)(SEQ - 1 - row);
      *(LAS f32x2*)(lds + STAGE + 16384 + 32 + wid * 8) = (f32x2){mbf, rden};
    }
    waitbar_l<63>();
    if (wid >= 4) {
#pragma unroll
      for (int hh = 0; hh < 4; ++hh) {
        const int p = hh * 256 + (t - 256), ch = p >> 3, r = 8 * bid + ((p & 7) ^ (ch & 7));
        const u32x4 o = *(LAS const u32x4*)(lds + STAGE + p * 16);
        gst16<0>(o, (unsigned)(((((r >> 5) * 32 + (ch >> 2)) * 2 + ((r >> 4) & 1)) * 64 + (ch & 3) * 16 + (r & 15)) * 16), ws + WS_XN);
      }
      if (t < 256 + 8) gst8(*(LAS const f32x2*)(lds + STAGE + 16384 + 32 + (t - 256) * 8), (unsigned)((8 * bid + t - 256) * 8), ws + WS_ST);
      waitv<0>();
    }
    waitbar_l<63>();
    if (wid < 4) {
      if (wid) touch(lds0 + DUMMY, (unsigned)((wid * 256 + within * 8 + (l >> 3)) * 8192 + xcd * 1024 + (l & 7) * 128), wq);
      const int fn = (bid * 4 + wid) & 255, kvh = bid >> 6;
      if (fn < 128) {
        const unsigned ko = (unsigned)(((kvh * SK + (SK - WIN) + (fn >> 2) * 16 + c) * DH + (fn & 3) * 32 + 4 * g) * 2);
        gld8<0>(klo, ko, kc); gld8<32>(khi, ko, kc);
        waitv<0>(klo, khi);
        kvo = (u32x4){klo[0], klo[1], khi[0], khi[1]};
      } else {
        const int d = 32 * ((fn - 128) >> 5) + 8 * (c >> 2) + 4 * (fn & 1) + (c & 3);
        const unsigned vo = (unsigned)(((kvh * SK + (SK - WIN) + ((fn >> 1) & 15) * 32 + 4 * g + 8) * DH + d) * 2);
        sfor<8>([&](auto J) AI { constexpr int j = J; gld2<(16 * (j >> 2) + (j & 3) - 8) * DH * 2>(e[j], vo, vc); });
        waitv<0>(e[0], e[1]);
#pragma unroll
        for (int j = 2; j < 8; ++j) PIN(e[j]);
        kvo = (u32x4){e[0] | (e[1] << 16), e[2] | (e[3] << 16), e[4] | (e[5] << 16), e[6] | (e[7] << 16)};
      }
      gst16<0>(kvo, (unsigned)((bid * 4 + wid) * 1024 + l * 16), ws + WS_KV);
    }
    if (t == 256) {
      const u32x4 ka = *(LAS const u32x4*)(lds + STAGE + 16384), kb = *(LAS const u32x4*)(lds + STAGE + 16400);
      const unsigned key = umax(umax(umax(ka[0], ka[1]), umax(ka[2], ka[3])), umax(umax(kb[0], kb[1]), umax(kb[2], kb[3])));
      gst8((u32x2){(tlo << 27) | key, (tlo >> 5) | (thi << 27)}, (unsigned)(bid * 8), ws + WS_G0);
    }
    STAMP(2);
  }
  const unsigned glo = tlo << 27, ghi = (tlo >> 5) | (thi << 27);

  const char* const wq_b = (const char*)wq + head * 256;
  const unsigned wq_v = (unsigned)(((8 * wid + ((l & 31) >> 2)) * 4096 + (l >> 5) * 32 + ((l & 3) ^ (2 * (wid & 1))) * 8) * 2);
  const char* const kv_b = ws + WS_KV + (size_t)(head >> 3) * 262144;
  const unsigned kv_v = (unsigned)(wid * 4096 + l * 16);
  const char* const xn_b = ws + WS_XN + (size_t)rb * 8 * 65536 + 4096;
  const unsigned xn_v = (unsigned)(wid * 65536 + l * 16);
  const char* const wo_b = (const char*)wo + (size_t)kh * 2048 * (DM * 2) + nb * 256;
  const char* const o_b = ws + WS_O + (size_t)mb * 4 * 262144 + kh * 131072;
  const unsigned ldsw = lds0 + wid * 2048, ldsk = lds0 + wid * 4096, ldsf = lds0 + FB + wid * 3072;
  const int erow = mb * 128 + rbw * 32 + kg * 16 + c, ecol = nb * 128 + kh * 64 + 4 * g;
  bf16x8 xf[XD];
  u32x4 fm[2], fw[12];
  u32x2 fg;
  u32x2 xe[4];
  f32x2 st;
  unsigned key = 0;

  auto fo_v = [&](int j) AI { return (unsigned)(((mb * 4 + rbw) * 32 + kh * 16 + ((hrot + 4 * j + (l & 3)) & 15)) * 8); };
  auto issue = [&](auto Sl) AI {
    constexpr int sl = Sl, q0 = SC.first(sl), nq = SC.count(sl);
    sfor<nq>([&](auto Qi) AI {
      constexpr int k = SC.kind[q0 + Qi], i = SC.idx[q0 + Qi];
      if constexpr (k == OX && i < 64) gld16<(i & 7) * 1024 - 4096, true>(xf[i % XD], xn_v, xn_b + (size_t)(i >> 3) * 8192);
      if constexpr (k == OX && i >= 64) gld16<(i & 3) * 1024, true>(xf[(i - 64) % XD2], (unsigned)(rbw * 262144 + kg * 4096 + l * 16), o_b + (size_t)(((((i - 64) >> 2) + hrot) & 15) * 8192));
      if constexpr (k == OG) {
        constexpr int v = i >> 2, p = i & 3;
        if constexpr (v < 8) {
          const char* sb = wq_b + (size_t)(v * 128 + (p >> 1) * 64) * 8192;
          glds<(p & 1) * 128>(ldsw + (v & 3) * 32768 + (p >> 1) * 16384 + (p & 1) * (1024 - 128), wq_v, sb);
        } else if constexpr (v < 16) {
          glds<p * 1024, true>(ldsk + (v & 3) * 32768, kv_v, kv_b + (size_t)(v - 8) * 32768);
        } else {
          const unsigned wo_v = (unsigned)(((8 * wid + ((l & 31) >> 2)) * DM + (l >> 5) * 32 + ((l & 3) ^ (2 * (wid & 1))) * 8) * 2);
          const char* sb = wo_b + (size_t)(((v - 16 + hrot) & 15) * 128 + (p >> 1) * 64) * (DM * 2) + (((p & 1) ^ kh ^ 1) << 7);
          glds<0>(ldsw + (v & 3) * 32768 + (p >> 1) * 16384 + (p & 1) * 1024, wo_v, sb);
        }
      }
      if constexpr (k == OF) {
        const int frho = l >> 2, fch = (l & 3) ^ (frho & 4 ? 2 : 0);
        if constexpr (i < 8) gld16v(fw[i], (const char*)(fch & 2 ? wup : wgate) + ((size_t)(128 * wid + 32 * (frho >> 2) + (frho & 3) + 4 * i) * FF + fj * 16 + (fch & 1) * 8) * 2);
        else gld16<(i - 8) * 64>(fw[i], (unsigned)((frho * DM + wid * 128 + fch * 8) * 2), (const char*)wdown + (size_t)fj * 16 * DM * 2);
      }
      if constexpr (k == OM && i > 0) gld8<0, true>(fg, fo_v(i), ws + WS_FO);
      if constexpr (k == OM && i == 0) gld8<0, true>(fg, (unsigned)((t & 255) * 8), wid < 4 ? ws + WS_G0 : ws + WS_FK + (head >> 3) * 2048);
      if constexpr (k == OS) {
        const u32x4 kk = *(LAS const u32x4*)(lds + HSC);
        key = __builtin_amdgcn_readfirstlane(umax(umax(kk[0], kk[1]), umax(kk[2], kk[3])));
        const int idx = SEQ - 1 - (int)(key & 2047u);
        glds<0, true>(ldsf, (unsigned)(((((idx >> 5) * 32 + 4 * wid + ((l >> 2) & 3)) * 2 + ((idx >> 4) & 1)) * 64 + (l & 3) * 16 + (idx & 15)) * 16), ws + WS_XN);
      }
      if constexpr (k == OE && i < 4) gld8<32 * i>(xe[i], (unsigned)((erow * DM + ecol) * 2), x);
      if constexpr (k == OE && i == 4) gld8<0, true>(st, (unsigned)(erow * 8), ws + WS_ST);
    });
  };
  auto endslot = [&](auto Sl) AI {
    constexpr int sl = Sl, u = sl >> 3, i = sl & 7;
    if constexpr (sl == 2) {
      if (wid < 4 && l == 0) gst8((u32x2){tlo, thi}, (unsigned)((bid * 4 + wid) * 8), ws + WS_FK);
    }
    if constexpr (sl >= 50 && sl < 58 && !(sl & 1)) {
      if (wid == 4 + (sl - 50) / 2) touch(lds0 + DUMMY, (unsigned)(((wid & 3) * 64 + l) * 128), (const char*)wdown + (size_t)fj * 16 * DM * 2);
    }
    if constexpr (sl == 38) {
      waitv<SC.younger(OM, 0, sl - 1)>(fg);
      const unsigned lo = wid < 4 ? glo : tlo, hi = wid < 4 ? ghi : thi, mk = wid < 4 ? 0xf8000000u : 0xffffffffu;
      for (int spin = 0; spin < SPINS; ++spin) {
        if (__builtin_expect(__all(((fg[0] & mk) == lo) & (fg[1] == hi)), 1)) break;
        __builtin_amdgcn_s_sleep(1);
        gld8<0, true>(fg, (unsigned)((t & 255) * 8), wid < 4 ? ws + WS_G0 : ws + WS_FK + (head >> 3) * 2048);
        waitv<0>(fg);
      }
      const unsigned km = wave_max_u32(fg[0] & 0x07ffffffu);
      *(LAS unsigned*)(lds + HSC + wid * 4) = km;
    }
    if constexpr (sl == 47) { if (bid == 0 && t == 0) gst8((u32x2){tlo, thi}, 0u, ws + WS_EP); }
    if constexpr (sl >= 59 && sl < 83 && (sl - 59) % 3 == 0) {
      if (wid == (sl - 59) / 3)
        touch(lds0 + DUMMY, (unsigned)((within * 32 + (wid & 3) * 8 + (l >> 3)) * (FF * 2) + xcd * 896 + ((l & 7) < 7 ? (l & 7) : 6) * 128), wid < 4 ? wgate : wup);
    }
    if constexpr (sl >= 84 && sl < 100 && (sl - 84) % 4 == 0) {
      if (wid == (sl - 84) / 4)
        touch(lds0 + DUMMY, (unsigned)((kh * 2048 + wid * 512 + within * 16 + (xcd & 1) * 8 + (l >> 3)) * (DM * 2) + ((xcd >> 1) & 1) * 1024 + (l & 7) * 128), wo);
    }
    issue(Sl);
    SBAR();
    if constexpr (i == 6 && u < 15) {
      if constexpr (u == 5) waitbar_l<SC.younger(OG, 4 * u + 7, sl)>();
      else waitbar<SC.younger(OG, 4 * u + 7, sl)>();
      if constexpr (u == 0 || u == 1 || u == 5 || u == 7 || u == 8 || u == 10 || u == 12 || u == 14)
        STAMP(u == 0 ? 4 : u == 1 ? 5 : u == 5 ? 6 : u == 7 ? 7 : u == 8 ? 8 : u == 10 ? 9 : u == 12 ? 10 : 11);
    }
  };

  issue(IC(-2));
  if (wid == 5) poll8((unsigned)((rb * 32 + (l & 31)) * 8), ws + WS_G0, glo, ghi, 0xf8000000u);
  waitbar<SC.younger(OG, 3, -2)>();
  issue(IC(-1));
  STAMP(3);

  LBase trb[2], frb;
  trb[0].init(lds0 + g * 2048 + (c >> 2) * 64 + (g & 1) * 32 + (c & 3) * 8);
  trb[1].init(lds0 + g * 2048 + (c >> 2) * 64 + ((g & 1) ^ 1) * 32 + (c & 3) * 8);
  frb.init(lds0 + l * 16);
  f32x4 acc[8][2];
  bf16x8 wf[2][4];
  auto WRD1 = [&](auto Si, auto Base, auto Dd) AI {
    constexpr int s = Si, d = Dd, o = Base + (d >> 1) * 512;
    wf[s & 1][d] = cat8(trd(trb[d & 1].template at<o>()), trd(trb[d & 1].template at<o + 256>()));
  };
  auto WRD = [&](auto Si, auto Base) AI { sfor<4>([&](auto Dd) AI { WRD1(Si, Base, Dd); }); };
#define WA(s) IC((((s) >> 3) & 3) * 32768 + (((s) >> 1) & 3) * 8192 + ((s) & 1) * 1024)
#define WB(s) IC((((s) >> 2) & 3) * 32768 + (((s) >> 1) & 1) * 8192 + ((s) & 1) * 1024)
  bf16x8 fr[4];
  auto FRD = [&](auto Ni) AI { constexpr int n = Ni; if constexpr (n < 256) fr[n & 3] = *(LAS const bf16x8*)(frb.at<((n >> 5) & 3) * 32768 + (n & 31) * 1024>()); };

  WRD(IC(0), WA(0));
  SBAR();
  bf16x8 qf[4][2];
  auto QPACK = [&](auto Ds) AI {
    constexpr int ds = Ds;
#pragma unroll
    for (int qb = 0; qb < 2; ++qb) {
      const f32x4 a = acc[2 * ds][qb] * QSCALE, b = acc[2 * ds + 1][qb] * QSCALE;
      qf[ds][qb] = __builtin_bit_cast(bf16x8, (u32x4){pk2(a[0], a[1]), pk2(a[2], a[3]), pk2(b[0], b[1]), pk2(b[2], b[3])});
      PIN(qf[ds][qb]);
    }
  };
  sfor<64>([&](auto Si) AI {
    constexpr int s = Si, x0 = (s & ~1) % XD, x1 = (s | 1) % XD;
    if constexpr (!(s & 1)) waitv<SC.younger(OX, s + 1, s - 1)>(xf[x0], xf[x1]);
    sfor<4>([&](auto Dd) AI {
      constexpr int d = Dd, dt = (s & 1) * 4 + d;
      if constexpr (s + 1 < 64) WRD1(IC(s + 1), WA(s + 1), Dd);
      else if constexpr (d < 3) FRD(Dd);
      SBAR();
      if constexpr (s < 2) { MFZ(acc[dt][0], wf[s & 1][d], xf[x0]); MFZ(acc[dt][1], wf[s & 1][d], xf[x1]); }
      else { MF(acc[dt][0], wf[s & 1][d], xf[x0]); MF(acc[dt][1], wf[s & 1][d], xf[x1]); }
      if constexpr (s == 63 && (d & 1)) { QPACK(IC(d >> 1)); SBAR(); }
    });
    SBAR();
    endslot(Si);
  });

  f32x4 sc[2][2][2], negm[2], o[2][2][2];
  u32x4 pw[16][2];
  float lsum[2] = {0.f, 0.f}, inv[2];
#define MX3(a, b, c) __builtin_fmaxf(__builtin_fmaxf((a), (b)), (c))
#define EXP2(x) __builtin_amdgcn_exp2f(x)
  const char* const od_b = ws + WS_O + ((size_t)(rb * 8) * 128 + head * 4) * 2048 + 4096;
  const unsigned od_v = (unsigned)(wid * 262144 + l * 16);
  auto SM = [&](auto Hi, auto Pi) AI {
    constexpr int H = Hi, p = Pi, kt = p >> 2, qb = (p >> 1) & 1, r = 2 * (p & 1);
    const float e0 = EXP2(sc[H & 1][kt][qb][r]), e1 = EXP2(sc[H & 1][kt][qb][r + 1]);
    lsum[qb] += e0; lsum[qb] += e1;
    unsigned w = pk2(e0, e1);
    PIN(lsum[qb]); PIN(w);
    pw[H][qb][kt * 2 + (p & 1)] = w;
  };
  sfor<128>([&](auto Ni) AI {
    constexpr int n = Ni, H = n >> 3, i = n & 7, kt = i >> 2, ds = i & 3, sn = H & 1;
    FRD(IC(n + 3));
    if constexpr (n < 2) QPACK(IC(2 + n));
    sfor<2>([&](auto Qb) AI {
      constexpr int qb = Qb;
      if constexpr (ds == 0 && H == 0) MFZ(sc[sn][kt][qb], fr[n & 3], qf[0][qb]);
      else if constexpr (ds == 0) MFC(sc[sn][kt][qb], fr[n & 3], qf[0][qb], negm[qb]);
      else MF(sc[sn][kt][qb], fr[n & 3], qf[ds][qb]);
    });
    if constexpr (n == 5 || n == 6) {
      constexpr int qb = n - 5;
      NOPS(3); PIN(sc[0][0][qb]);
      const float m = -xg_max(__builtin_fmaxf(MX3(sc[0][0][qb][0], sc[0][0][qb][1], sc[0][0][qb][2]), sc[0][0][qb][3]));
      negm[qb] = (f32x4){m, m, m, m};
      sc[0][0][qb] += negm[qb];
      PIN(negm[qb]); PIN(sc[0][0][qb]);
    }
    if constexpr (n == 9 || n == 10) { NOPS(3); PIN(sc[0][1][n - 9]); sc[0][1][n - 9] += negm[n - 9]; PIN(sc[0][1][n - 9]); }
    if constexpr (H >= 1) SM(IC(H - 1), IC(i));
    if constexpr (n == 125) { PIN(negm[0]); PIN(negm[1]); }
    SBAR();
    if constexpr ((n & 3) == 3) endslot(IC(64 + (n >> 2)));
  });
  sfor<8>([&](auto Pi) AI { if constexpr (Pi == 4) { NOPS(3); PIN(sc[1][1][0]); PIN(sc[1][1][1]); } SM(IC(15), Pi); });
  if (__builtin_expect(__any(!(lsum[0] + lsum[1] <= LBOUND)), 0)) {
    auto tile = [&](unsigned off, f32x4& s0, f32x4& s1) AI {
      bf16x8 kf[4];
      SBAR(); NOPS(7);
      sfor<4>([&](auto Ds) AI { constexpr int ds = Ds; gld16<ds * 1024, true>(kf[ds], off, kv_b); });
      waitv<0>(kf[0], kf[1]);
      PIN(kf[2]); PIN(kf[3]);
#pragma unroll
      for (int ds = 0; ds < 4; ++ds) {
        s0 = __builtin_amdgcn_mfma_f32_16x16x32_bf16(kf[ds], qf[ds][0], s0, 0, 0, 0);
        s1 = __builtin_amdgcn_mfma_f32_16x16x32_bf16(kf[ds], qf[ds][1], s1, 0, 0, 0);
      }
    };
    float m0 = -__builtin_inff(), m1 = m0;
#pragma unroll 1
    for (int kt = 0; kt < 32; ++kt) {
      f32x4 s0 = {}, s1 = {};
      tile((unsigned)(kt * 4096 + l * 16), s0, s1);
      m0 = MX3(MX3(m0, s0[0], s0[1]), s0[2], s0[3]); m1 = MX3(MX3(m1, s1[0], s1[1]), s1[2], s1[3]);
    }
    m0 = -xg_max(m0); m1 = -xg_max(m1);
    lsum[0] = 0.f; lsum[1] = 0.f;
    sfor<16>([&](auto Hi) AI {
      constexpr int H = Hi;
#pragma unroll
      for (int kt = 0; kt < 2; ++kt) {
        sc[H & 1][kt][0] = (f32x4){m0, m0, m0, m0}; sc[H & 1][kt][1] = (f32x4){m1, m1, m1, m1};
        tile((unsigned)((2 * H + kt) * 4096 + l * 16), sc[H & 1][kt][0], sc[H & 1][kt][1]);
      }
      sfor<8>([&](auto Pi) AI { SM(Hi, Pi); });
      PIN(pw[H][0]); PIN(pw[H][1]);
    });
  }
  SBAR();
  NOPS(1);
  auto OST = [&](auto Os, auto Qb) AI {
    constexpr int os = Os, qb = Qb;
    PIN(o[os & 1][0][qb]); PIN(o[os & 1][1][qb]);
    const f32x4 a = o[os & 1][0][qb] * inv[qb], b = o[os & 1][1][qb] * inv[qb];
    gst16<(os * 2 + qb) * 1024 - 4096>((u32x4){pk2(a[0], a[1]), pk2(a[2], a[3]), pk2(b[0], b[1]), pk2(b[2], b[3])}, od_v, od_b);
  };
  sfor<128>([&](auto Mi) AI {
    constexpr int m = Mi, n = 128 + m, os = m >> 5, H = (m >> 1) & 15, par = m & 1;
    FRD(IC(n + 3));
    if constexpr (H == 0) { MFZ(o[os & 1][par][0], fr[n & 3], pw[H][0]); MFZ(o[os & 1][par][1], fr[n & 3], pw[H][1]); }
    else { MF(o[os & 1][par][0], fr[n & 3], pw[H][0]); MF(o[os & 1][par][1], fr[n & 3], pw[H][1]); }
    if constexpr (m == 2 || m == 3) { inv[m - 2] = __builtin_amdgcn_rcpf(xg_sum(lsum[m - 2])); PIN(inv[m - 2]); }
    if constexpr (os >= 1 && ((m & 31) == 3 || (m & 31) == 5)) OST(IC(os - 1), IC(((m & 31) - 3) >> 1));
    SBAR();
    if constexpr ((n & 3) == 3) endslot(IC(64 + (n >> 2)));
  });
  STAMP(12);
  NOPS(7);
  OST(IC(3), IC(0)); OST(IC(3), IC(1));

  const int yrow = within < 28 ? fj : 224 + xcd * 4 + within - 28;
  {
    LAS char* const fbw = (LAS char*)(uintptr_t)ldsf + l * 16;
    LAS const char* const ft0 = (LAS const char*)(uintptr_t)(ldsf + (4 * g + (c >> 2)) * 64 + (g & 1) * 32 + (c & 3) * 8);
    LAS const char* const ft1 = (LAS const char*)(uintptr_t)((ldsf + (4 * g + (c >> 2)) * 64 + (g & 1) * 32 + (c & 3) * 8) ^ 32);
    waitv<2>();
    u32x4 fx[4];
#pragma unroll
    for (int i = 0; i < 4; ++i) fx[i] = *(LAS const u32x4*)((LAS const char*)(uintptr_t)ldsf + (g * 4 + i) * 16);
    f32x4 fa0 = {}, fa1 = {};
    s16x4 fb[8];
#pragma unroll
    for (int r = 0; r < 4; ++r) {
#pragma unroll
      for (int q = 0; q < 3; ++q) { PIN(fw[3 * r + q]); *(LAS u32x4*)(fbw + q * 1024) = fw[3 * r + q]; }
#pragma unroll
      for (int q = 0; q < 3; ++q) {
        const int p = 3 * r + q;
        const s16x4 b0 = trd(ft0 + q * 1024), b1 = trd(ft1 + q * 1024);
        if (p < 8) {
          const u32x2 xa = {fx[p >> 1][2 * (p & 1)], fx[p >> 1][2 * (p & 1) + 1]};
          fa0 = MFMA16(__builtin_bit_cast(s16x4, xa), b0, fa0);
          fa1 = MFMA16(__builtin_bit_cast(s16x4, xa), b1, fa1);
        } else { fb[2 * (p - 8)] = b0; fb[2 * (p - 8) + 1] = b1; }
      }
    }
    *(LAS float*)(lds + RED1 + (wid * 32 + (l & 31)) * 4) = sel2(g & 1, fa1[0], fa0[0]);
    waitv<0>();
    if (l == 0) gst8((u32x2){tlo, thi}, (unsigned)(((rb * 8 + wid) * 32 + head) * 8), ws + WS_FO);
    STAMP(13);
    poll8(fo_v(0), ws + WS_FO, tlo, thi, 0xffffffffu);
    issue(IC(128));
    STAMP(14);
    waitbar_l<63>();
    issue(IC(129));
    f32x4 gs = *(LAS const f32x4*)(lds + RED1 + ((c & 7) * 32 + 4 * g) * 4), us = *(LAS const f32x4*)(lds + RED1 + ((c & 7) * 32 + 16 + 4 * g) * 4);
    float hh[4];
#pragma unroll
    for (int j = 0; j < 4; ++j) {
      float a = gs[j], b = us[j];
      a += dppf<XOR1>(a); a += dppf<XOR2>(a); a += dppf<XOR7>(a);
      b += dppf<XOR1>(b); b += dppf<XOR2>(b); b += dppf<XOR7>(b);
      hh[j] = rbf(silu_f(rbf(a))) * rbf(b);
    }
    const s16x4 hfrag = __builtin_bit_cast(s16x4, (u32x2){pk2(hh[0], hh[1]), pk2(hh[2], hh[3])});
    sfor<2>([&](auto Pi) AI {
      constexpr int p = Pi;
      const f32x4 y0 = MFMA16(hfrag, fb[4 * p], (f32x4){}), y1 = MFMA16(hfrag, fb[4 * p + 1], (f32x4){});
      const f32x4 y2 = MFMA16(hfrag, fb[4 * p + 2], (f32x4){}), y3 = MFMA16(hfrag, fb[4 * p + 3], (f32x4){});
      gst4<p * 256>(sel2(g & 2, sel2(g & 1, y3[0], y2[0]), sel2(g & 1, y1[0], y0[0])), (unsigned)((wid * 128 + l) * 4), ws + WS_YP + (size_t)yrow * 4096);
    });
    STAMP(15);
  }

  const int idx = SEQ - 1 - (int)(key & 2047u);
  const bool owner = (idx >> 7) == mb;
  f32x4 yp[7];
  trb[0].init(lds0 + kg * 16384 + g * 2048 + (c >> 2) * 64 + (g & 1) * 32 + (c & 3) * 8);
  trb[1].init(lds0 + kg * 16384 + g * 2048 + (c >> 2) * 64 + ((g & 1) ^ 1) * 32 + (c & 3) * 8);
  auto item = [](int s) { return s < 48 ? s : s < 56 ? 48 + 2 * (s - 48) : 49 + 2 * (s - 56); };
  u32x2 pf;
  f32x4 giv[4];
  WRD(IC(0), WB(0));
  SBAR();
  sfor<64>([&](auto Si) AI {
    constexpr int s = Si, u = s >> 2, it = item(s), x0 = (it & ~1) % XD2, x1 = (it | 1) % XD2;
    if constexpr (!(it & 1)) waitv<SC.younger(OX, 64 + it + 1, B0 + s - 1)>(xf[x0], xf[x1]);
    sfor<4>([&](auto Dd) AI {
      constexpr int d = Dd, dt = (it & 1) * 4 + d;
      if constexpr (s + 1 < 64) WRD1(IC(s + 1), WB(item(s + 1)), Dd);
      SBAR();
      if constexpr (s < 2) { MFZ(acc[dt][0], wf[s & 1][d], xf[x0]); MFZ(acc[dt][1], wf[s & 1][d], xf[x1]); }
      else { MF(acc[dt][0], wf[s & 1][d], xf[x0]); MF(acc[dt][1], wf[s & 1][d], xf[x1]); }
    });
    SBAR();
    if constexpr (s == 8 || s == 17 || s == 33) {
      constexpr int j = s == 8 ? 1 : s == 17 ? 2 : 3;
      waitv<SC.younger(OM, j, B0 + s - 1)>(fg);
      if (__builtin_expect(!__all((fg[0] == tlo) & (fg[1] == thi)), 0)) poll8(fo_v(j), ws + WS_FO, tlo, thi, 0xffffffffu);
    }
    if constexpr (s == 56) {
      LAS char* dst = lds + (wid ^ 4) * 4096 + l * 16;
      if (kg) {
#pragma unroll
        for (int d = 0; d < 4; ++d) { *(LAS f32x4*)(dst + (d & 1) * 2048 + (d >> 1) * 32768) = acc[d][0]; giv[d] = acc[d][1]; }
      } else {
#pragma unroll
        for (int d = 0; d < 4; ++d) { *(LAS f32x4*)(dst + (d & 1) * 2048 + (d >> 1) * 32768) = acc[d][1]; giv[d] = acc[d][0]; }
      }
      waitbar_l<63>();
    }
    if constexpr (s == 57) {
      LAS const char* src = lds + wid * 4096 + l * 16;
      sfor<4>([&](auto Dd) AI {
        constexpr int d = Dd;
        gst16<d * 1024>(giv[d] + *(LAS const f32x4*)(src + (d & 1) * 2048 + (d >> 1) * 32768), (unsigned)(wid * 4096 + l * 16), ws + WS_XC + (size_t)(bid ^ 4) * 32768);
      });
    }
    if constexpr (s == 62) {
      waitv<0>();
      if (l == 0) gst8((u32x2){tlo, thi}, (unsigned)(((bid ^ 4) * 8 + wid) * 64), ws + WS_FX);
      gld8<0, true>(pf, (unsigned)((bid * 8 + wid) * 64), ws + WS_FX);
    }
    if constexpr (s == 18) {
      if (l == 0) gst8((u32x2){tlo, thi}, (unsigned)((wid * 256 + yrow) * 8), ws + WS_FY);
    }
    if constexpr (s == 24) {
      if (owner) { gld16<0, true>(fm[0], (unsigned)(nb * 2048 + (l < 56 ? l : 55) * 32), ws + WS_FY); gld16<16, true>(fm[1], (unsigned)(nb * 2048 + (l < 56 ? l : 55) * 32), ws + WS_FY); }
    }
    if constexpr (s == 42) {
      if (owner) {
        PIN(fm[0]); PIN(fm[1]);
        for (int spin = 0; spin < SPINS; ++spin) {
          const bool ok = (fm[0][0] == tlo) & (fm[0][1] == thi) & (fm[0][2] == tlo) & (fm[0][3] == thi) & (fm[1][0] == tlo) & (fm[1][1] == thi) & (fm[1][2] == tlo) & (fm[1][3] == thi);
          if (__builtin_expect(__all(ok), 1)) break;
          __builtin_amdgcn_s_sleep(1);
          gld16<0, true>(fm[0], (unsigned)(nb * 2048 + (l < 56 ? l : 55) * 32), ws + WS_FY); gld16<16, true>(fm[1], (unsigned)(nb * 2048 + (l < 56 ? l : 55) * 32), ws + WS_FY);
          waitv<0>(fm[0], fm[1]);
        }
        const unsigned yv = (unsigned)(((t >> 4) * 1024 + nb * 128 + kh * 64 + (t & 15) * 4) * 4);
        sfor<7>([&](auto I) AI { constexpr int q = I; gld16<0, true>(yp[q], yv, ws + WS_YP + (size_t)q * 32 * 4096); });
      }
    }
    if constexpr (s == 55) {
      if (owner) {
        f32x4 a = {};
#pragma unroll
        for (int q = 0; q < 7; ++q) { PIN(yp[q]); a += yp[q]; }
#pragma unroll
        for (int j = 0; j < 4; ++j) a[j] = xg_sum(a[j]);
        *(LAS f32x4*)(lds + YS + (wid * 64 + c * 4) * 4) = a;
      }
    }
    issue(IC(B0 + s));
    SBAR();
    if constexpr (((s & 3) == 2 && u < 11) || s == 44) {
      waitbar<SC.younger(OG, 64 + 4 * u + 7, B0 + s)>();
      if constexpr (u % 4 == 1) STAMP(16 + u / 4);
    }
    if constexpr (s == 48) waitbar<SC.younger(OG, 64 + 55, B0 + s)>();
    if constexpr (s == 50) waitbar<SC.younger(OG, 64 + 59, B0 + s)>();
    if constexpr (s == 52) { waitbar_l<SC.younger(OG, 64 + 63, B0 + s)>(); STAMP(19); }
  });
  STAMP(20);

  f32x4 kep[4], oth[4];
  unsigned gate[8];
  waitv<0>(pf);
  if (!__all((pf[0] == tlo) & (pf[1] == thi))) poll8((unsigned)((bid * 8 + wid) * 64), ws + WS_FX, tlo, thi, 0xffffffffu);
  STAMP(21);
  sfor<4>([&](auto Dd) AI { constexpr int d = Dd; gld16<d * 1024, true>(oth[d], (unsigned)(wid * 4096 + l * 16), ws + WS_XC + (size_t)bid * 32768); });
  {
    LAS char* dst = lds + 65536 + (wid ^ 4) * 4096 + l * 16;
    LAS const char* src = lds + 65536 + wid * 4096 + l * 16;
#pragma unroll
    for (int d = 4; d < 8; ++d) { PIN(acc[d][0]); PIN(acc[d][1]); }
    if (kg) {
#pragma unroll
      for (int d = 0; d < 4; ++d) { *(LAS f32x4*)(dst + (d & 1) * 2048 + (d >> 1) * 32768) = acc[4 + d][0]; kep[d] = acc[4 + d][1]; }
    } else {
#pragma unroll
      for (int d = 0; d < 4; ++d) { *(LAS f32x4*)(dst + (d & 1) * 2048 + (d >> 1) * 32768) = acc[4 + d][1]; kep[d] = acc[4 + d][0]; }
    }
    unsigned xnp[8];
    PIN(st);
#pragma unroll
    for (int j = 0; j < 8; ++j) {
      PIN(xe[j >> 1]);
      const unsigned xw = xe[j >> 1][j & 1];
      const unsigned dd = pk2(bflo(xw) - st[0], bfhi(xw) - st[0]);
      xnp[j] = pk2(bflo(dd) * st[1], bfhi(dd) * st[1]);
      gate[j] = pk2(silu_f(bflo(xnp[j])), silu_f(bfhi(xnp[j])));
      PIN(xnp[j]); PIN(gate[j]);
    }
    if (owner && erow == idx) {
      const unsigned ord = key >> 11, bits = (ord & 0x8000u) ? (ord & 0x7fffu) : (~ord & 0xffffu);
      const float sg = rbf(sigmoid_f(bflo(bits)));
#pragma unroll
      for (int q = 0; q < 4; ++q) {
        f32x4 a = {};
#pragma unroll
        for (int p = 0; p < 8; ++p) a += *(LAS const f32x4*)(lds + YS + (p * 64 + q * 16 + 4 * g) * 4);
#pragma unroll
        for (int j = 0; j < 2; ++j) {
          const float lo = rbf(bflo(xnp[2 * q + j]) + rbf(rbf(a[2 * j]) * sg)), hi = rbf(bfhi(xnp[2 * q + j]) + rbf(rbf(a[2 * j + 1]) * sg));
          gate[2 * q + j] = pk2(silu_f(lo), silu_f(hi));
        }
      }
    }
    waitbar_l<63>();
    STAMP(22);
#pragma unroll
    for (int d = 0; d < 4; ++d) kep[d] += *(LAS const f32x4*)(src + (d & 1) * 2048 + (d >> 1) * 32768);
  }
  STAMP(23);
  waitv<0>(oth[0], oth[1]);
#pragma unroll
  for (int d = 2; d < 4; ++d) PIN(oth[d]);
  STAMP(24);
  u16* op = out + (size_t)erow * DM + ecol;
#pragma unroll
  for (int d = 0; d < 4; ++d) {
    const f32x4 a = kep[d] + oth[d];
    u32x2 res;
#pragma unroll
    for (int j = 0; j < 2; ++j) {
      const unsigned sp = pk2(a[2 * j], a[2 * j + 1]), gp = gate[2 * d + j], xw = xe[d][j];
      const unsigned pr = pk2(bflo(sp) * bflo(gp), bfhi(sp) * bfhi(gp));
      res[j] = pk2(bflo(xw) + bflo(pr), bfhi(xw) + bfhi(pr));
    }
    *(u32x2*)(op + d * 16) = res;
  }
  STAMP(25);
  STAMP_OUT(0, 26);
}

extern "C" void kernel_launch(void* const* d_in, const int* in_sizes, int n_in, void* d_out, int out_size, void* d_ws, size_t ws_size,
                              hipStream_t stream) {
  k_fused<<<256, 512, 0, stream>>>((const u16*)d_in[0], (const u16*)d_in[1], (const u16*)d_in[2], (const u16*)d_in[3], (const u16*)d_in[4],
                                   (const u16*)d_in[6], (const u16*)d_in[7], (const u16*)d_in[8], (const u16*)d_in[9], (char*)d_ws, (u16*)d_out);
}
```

```cpp
#include <hip/hip_runtime.h>
#include <stdint.h>
#include <type_traits>

typedef unsigned short u16;
typedef short bf16x8 __attribute__((ext_vector_type(8)));
typedef short s16x4 __attribute__((ext_vector_type(4)));
typedef float f32x4 __attribute__((ext_vector_type(4)));
typedef float f32x2 __attribute__((ext_vector_type(2)));
typedef unsigned u32x4 __attribute__((ext_vector_type(4)));
typedef unsigned u32x2 __attribute__((ext_vector_type(2)));
typedef __bf16 bf2_t __attribute__((ext_vector_type(2)));
#define LAS __attribute__((address_space(3)))
#define DI __device__ __forceinline__
#define AI __attribute__((always_inline))
#define SBAR() __builtin_amdgcn_sched_barrier(0)
template <class T> __device__ __forceinline__ void PIN(T& x) { asm volatile("" : "+v"(x)); }
template <class B> DI void MF(f32x4& acc, const bf16x8& a, const B& b) { asm volatile("v_mfma_f32_16x16x32_bf16 %0, %1, %2, %0" : "+v"(acc) : "v"(a), "v"(b)); }
template <class B> DI void MFC(f32x4& d, const bf16x8& a, const B& b, const f32x4& c) { asm volatile("v_mfma_f32_16x16x32_bf16 %0, %1, %2, %3" : "=&v"(d) : "v"(a), "v"(b), "v"(c)); }
template <class B> DI void MFZ(f32x4& d, const bf16x8& a, const B& b) { asm volatile("v_mfma_f32_16x16x32_bf16 %0, %1, %2, 0" : "=&v"(d) : "v"(a), "v"(b)); }
#define NOPS(n) asm volatile("s_nop " #n)
#define MFMA16(a, b, c) __builtin_amdgcn_mfma_f32_16x16x16bf16_1k((a), (b), (c), 0, 0, 0)
#define IC(n) std::integral_constant<int, (n)>{}

constexpr int SEQ = 2048, DM = 1024, DH = 128, SK = 2048, WIN = 512, FF = 3584;
constexpr size_t WS_XN = 0;
constexpr size_t WS_KV = 4u << 20;
constexpr size_t WS_ST = 5u << 20;
constexpr size_t WS_EP = WS_ST + 16384;
constexpr size_t WS_G0 = WS_ST + 32768;
constexpr size_t WS_FK = WS_ST + 49152;
constexpr size_t WS_YP = WS_ST + 65536;
constexpr size_t WS_FO = 7u << 20;
constexpr size_t WS_FY = WS_FO + 65536;
constexpr size_t WS_FX = WS_FO + 131072;
constexpr size_t WS_O = 8u << 20;
constexpr size_t WS_XC = 24u << 20;

template <int I, int N, class F> DI void sfor_impl(F&& f) {
  if constexpr (I < N) { f(std::integral_constant<int, I>{}); sfor_impl<I + 1, N>(f); }
}
template <int N, class F> DI void sfor(F&& f) { sfor_impl<0, N>(f); }

DI unsigned pk2(float a, float b) { f32x2 v = {a, b}; return __builtin_bit_cast(unsigned, __builtin_convertvector(v, bf2_t)); }
DI float rbf(float a) { return (float)(__bf16)a; }
DI float bflo(unsigned u) { return __uint_as_float(u << 16); }
DI float bfhi(unsigned u) { return __uint_as_float(u & 0xffff0000u); }
DI float sigmoid_f(float v) { return __builtin_amdgcn_rcpf(1.f + __builtin_amdgcn_exp2f(v * -1.4426950408889634f)); }
DI float sel2(bool c, float a, float b) { PIN(a); PIN(b); return c ? a : b; }
DI float silu_f(float v) { return v * sigmoid_f(v); }
template <int CTRL> DI float dppf(float x) { return __builtin_bit_cast(float, __builtin_amdgcn_mov_dpp(__builtin_bit_cast(int, x), CTRL, 0xf, 0xf, true)); }
template <int CTRL> DI unsigned dppu(unsigned x) { return (unsigned)__builtin_amdgcn_mov_dpp((int)x, CTRL, 0xf, 0xf, true); }
constexpr int XOR1 = 0xB1, XOR2 = 0x4E, XOR7 = 0x141, XOR8 = 0x128;
DI float xg_sum(float x) {
  auto s = __builtin_amdgcn_permlane16_swap(__float_as_uint(x), __float_as_uint(x), false, false);
  x = __uint_as_float(s[0]) + __uint_as_float(s[1]);
  auto t = __builtin_amdgcn_permlane32_swap(__float_as_uint(x), __float_as_uint(x), false, false);
  return __uint_as_float(t[0]) + __uint_as_float(t[1]);
}
DI float xg_max(float x) {
  auto s = __builtin_amdgcn_permlane16_swap(__float_as_uint(x), __float_as_uint(x), false, false);
  x = __builtin_fmaxf(__uint_as_float(s[0]), __uint_as_float(s[1]));
  auto t = __builtin_amdgcn_permlane32_swap(__float_as_uint(x), __float_as_uint(x), false, false);
  return __builtin_fmaxf(__uint_as_float(t[0]), __uint_as_float(t[1]));
}
DI float wave_sum(float v) { v += dppf<XOR1>(v); v += dppf<XOR2>(v); v += dppf<XOR7>(v); v += dppf<XOR8>(v); return xg_sum(v); }
DI unsigned umax(unsigned a, unsigned b) { return a > b ? a : b; }
DI unsigned wave_max_u32(unsigned v) {
  v = umax(v, dppu<XOR1>(v)); v = umax(v, dppu<XOR2>(v)); v = umax(v, dppu<XOR7>(v)); v = umax(v, dppu<XOR8>(v));
  auto s = __builtin_amdgcn_permlane16_swap(v, v, false, false); v = umax(s[0], s[1]);
  auto t = __builtin_amdgcn_permlane32_swap(v, v, false, false); v = umax(t[0], t[1]);
  return __builtin_amdgcn_readfirstlane(v);
}

template <int OFF, bool DEV = false> DI void glds(unsigned ldsaddr, unsigned voff, const void* sbase) {
  if constexpr (DEV) asm volatile("s_mov_b32 m0, %0\n\ts_nop 0\n\tglobal_load_lds_dwordx4 %1, %2 offset:%3 sc1" ::"s"(ldsaddr), "v"(voff), "s"(sbase), "i"(OFF) : "memory");
  else asm volatile("s_mov_b32 m0, %0\n\ts_nop 0\n\tglobal_load_lds_dwordx4 %1, %2 offset:%3" ::"s"(ldsaddr), "v"(voff), "s"(sbase), "i"(OFF) : "memory");
}
DI void touch(unsigned ldsaddr, unsigned voff, const void* sbase) {
  asm volatile("s_mov_b32 m0, %0\n\ts_nop 0\n\tglobal_load_lds_dword %1, %2" ::"s"(ldsaddr), "v"(voff), "s"(sbase) : "memory");
}
template <int OFF, bool DEV = false, class T> DI void gld16(T& dst, unsigned voff, const void* sbase) {
  if constexpr (DEV) asm volatile("global_load_dwordx4 %0, %1, %2 offset:%3 sc1" : "=v"(dst) : "v"(voff), "s"(sbase), "i"(OFF) : "memory");
  else asm volatile("global_load_dwordx4 %0, %1, %2 offset:%3" : "=v"(dst) : "v"(voff), "s"(sbase), "i"(OFF) : "memory");
}
DI void glds64(unsigned ldsaddr, const void* vaddr) {
  asm volatile("s_mov_b32 m0, %0\n\ts_nop 0\n\tglobal_load_lds_dwordx4 %1, off" ::"s"(ldsaddr), "v"(vaddr) : "memory");
}
template <int OFF, bool DEV = false, class T> DI void gld8(T& dst, unsigned voff, const void* sbase) {
  if constexpr (DEV) asm volatile("global_load_dwordx2 %0, %1, %2 offset:%3 sc1" : "=v"(dst) : "v"(voff), "s"(sbase), "i"(OFF) : "memory");
  else asm volatile("global_load_dwordx2 %0, %1, %2 offset:%3" : "=v"(dst) : "v"(voff), "s"(sbase), "i"(OFF) : "memory");
}
template <int OFF> DI void gld2(unsigned& dst, unsigned voff, const void* sbase) {
  asm volatile("global_load_ushort %0, %1, %2 offset:%3" : "=v"(dst) : "v"(voff), "s"(sbase), "i"(OFF) : "memory");
}
template <int OFF, class T> DI void gst16(const T& v, unsigned voff, const void* sbase) {
  asm volatile("global_store_dwordx4 %0, %1, %2 offset:%3 sc1\n\ts_nop 1" ::"v"(voff), "v"(v), "s"(sbase), "i"(OFF) : "memory");
}
template <class T> DI void gst8(const T& v, unsigned voff, const void* sbase) {
  asm volatile("global_store_dwordx2 %0, %1, %2 sc1" ::"v"(voff), "v"(v), "s"(sbase) : "memory");
}
template <int OFF> DI void gst4(float v, unsigned voff, const void* sbase) {
  asm volatile("global_store_dword %0, %1, %2 offset:%3 sc1" ::"v"(voff), "v"(v), "s"(sbase), "i"(OFF) : "memory");
}
constexpr int SPINS = 1 << 16;
DI void poll8(unsigned voff, const void* sbase, unsigned lo, unsigned hi, unsigned lomask) {
  for (int spin = 0; spin < SPINS; ++spin) {
    u32x2 v;
    asm volatile("global_load_dwordx2 %0, %1, %2 sc1\n\ts_waitcnt vmcnt(0)" : "=&v"(v) : "v"(voff), "s"(sbase) : "memory");
    if (__all(((v[0] & lomask) == lo) & (v[1] == hi))) break;
    __builtin_amdgcn_s_sleep(1);
  }
}
template <int N> DI void waitv() { static_assert(N >= 0, "count"); asm volatile("s_waitcnt vmcnt(%0)" ::"n"(N > 63 ? 63 : N) : "memory"); }
template <int N, class T> DI void waitv(T& x) { static_assert(N >= 0, "count"); asm volatile("s_waitcnt vmcnt(%1)" : "+v"(x) : "n"(N > 63 ? 63 : N) : "memory"); }
template <int N, class T> DI void waitv(T& x, T& y) { static_assert(N >= 0, "count"); asm volatile("s_waitcnt vmcnt(%2)" : "+v"(x), "+v"(y) : "n"(N > 63 ? 63 : N) : "memory"); }
template <int N> DI void waitbar() { static_assert(N >= 0, "count"); asm volatile("s_waitcnt vmcnt(%0)\n\ts_barrier" ::"n"(N > 63 ? 63 : N) : "memory"); }
template <int N> DI void waitbar_l() { static_assert(N >= 0, "count"); asm volatile("s_waitcnt vmcnt(%0) lgkmcnt(0)\n\ts_barrier" ::"n"(N > 63 ? 63 : N) : "memory"); }
typedef short v4i16_t __attribute__((ext_vector_type(4)));
DI s16x4 trd(LAS const char* p) { return __builtin_amdgcn_ds_read_tr16_b64_v4i16((LAS v4i16_t*)p); }
struct LBase {
  unsigned lo, hi;
  DI void init(unsigned a) { lo = a; hi = a + 65536; PIN(lo); PIN(hi); }
  template <int OFF> DI LAS const char* at() const { return (LAS const char*)(uintptr_t)(OFF < 65536 ? lo : hi) + (OFF & 65535); }
};
DI bf16x8 cat8(s16x4 lo, s16x4 hi) { return __builtin_shufflevector(lo, hi, 0, 1, 2, 3, 4, 5, 6, 7); }

enum { OX, OG, OF, OM, OS, OP, OE, OW };
struct Sched {
  int n; short kind[400], idx[400], slot[400];
  constexpr Sched() : n(0), kind{}, idx{}, slot{} {}
  constexpr void add(int s, int k, int i) { kind[n] = (short)k; idx[n] = (short)i; slot[n] = (short)s; ++n; }
  constexpr int younger(int k, int i, int s) const {
    int p = -1;
    for (int q = 0; q < n; ++q) if (kind[q] == k && idx[q] == i) p = q;
    if (p < 0 || slot[p] > s) return -1;
    int c = 0;
    for (int q = p + 1; q < n; ++q) if (slot[q] <= s) ++c;
    return c;
  }
  constexpr int first(int s) const { int q = 0; while (q < n && slot[q] < s) ++q; return q; }
  constexpr int count(int s) const { int c = 0; for (int q = 0; q < n; ++q) if (slot[q] == s) ++c; return c; }
};

#define STAMP_DECL
#define STAMP(i)
#define STAMP_OUT(kid, n)

constexpr int RING = 4 * 32768, FB = RING, RED1 = FB + 8 * 3072, HSC = RED1 + 1024, YS = HSC + 512, DUMMY = YS + 2048, LDS_BYTES = DUMMY + 256;
constexpr int STAGE = 3 * 32768;
constexpr int XD = 16, XD2 = 16;
constexpr float LBOUND = 65536.f;
constexpr float QSCALE = 0.08838834764831845f * 1.4426950408889634f;
constexpr int B0 = 128;
constexpr int FISS[12] = {72, 75, 78, 81, 84, 87, 90, 93, 96, 99, 102, 110};
constexpr int FRDS[12] = {79, 82, 85, 88, 91, 94, 97, 100, 107, 110, 113, 117};
constexpr int fpiece(int sl) { for (int p = 0; p < 12; ++p) if (FRDS[p] == sl) return p; return -1; }
constexpr Sched make_sched() {
  Sched s;
  for (int i = 0; i < 12; ++i) s.add(-2, OG, i);
  for (int i = 0; i < 16; ++i) s.add(-1, OX, i);
  for (int t = 0; t < 128; ++t) {
    if (t == 104 || t == 112 || t == 120) { s.add(t, OW, (t - 104) / 4); s.add(t, OW, (t - 104) / 4 + 1); }
    for (int p = 0; p < 24; ++p) { const int a = 3 + p, b2 = 2 * p + 1; if ((a > b2 ? a : b2) == t) { s.add(t, OX, 16 + 2 * p); s.add(t, OX, 17 + 2 * p); } }
    if (t >= 124) { s.add(t, OX, 64 + 2 * (t - 124)); s.add(t, OX, 65 + 2 * (t - 124)); }
    for (int v = 3; v < 18; ++v) for (int p = 0; p < 4; ++p) if (8 * (v - 3) + 7 + p == t) s.add(t, OG, 4 * v + p);
    if (t == 127) { s.add(t, OG, 72); s.add(t, OG, 73); }
    if (t == 20) s.add(t, OM, 0);
    if (t == 118) s.add(t, OM, 1);
    for (int p = 0; p < 12; ++p) if (FISS[p] == t) s.add(t, OF, p);
  }
  for (int t = 0; t < 64; ++t) {
    if (t == 0) { s.add(B0, OW, 6); s.add(B0, OW, 7); s.add(B0, OG, 74); s.add(B0, OG, 75); }
    if (t == 0 || t == 9 || t == 20) s.add(B0 + t, OM, t == 0 ? 2 : t == 9 ? 3 : 4);
    if (t == 1 || t == 2 || t == 5 || t == 6) { const int q = t < 3 ? t - 1 : t - 3; s.add(B0 + t, OX, 72 + 2 * q); s.add(B0 + t, OX, 73 + 2 * q); }
    if (t == 52) for (int i = 0; i < 5; ++i) s.add(B0 + t, OE, i);
    for (int p = 0; p < 24; ++p) { const int a = 8 + p, b2 = 2 * p + 1; if ((a > b2 ? a : b2) == t) { s.add(B0 + t, OX, 80 + 2 * p); s.add(B0 + t, OX, 81 + 2 * p); } }
    for (int v = 3; v < 16; ++v) for (int p = 0; p < 4; ++p) if ((v < 14 ? 4 * (v - 3) + 3 + (p >> 1) : v == 14 ? 45 : 49) == t) s.add(B0 + t, OG, 64 + 4 * v + p);
  }
  return s;
}
constexpr Sched SC = make_sched();

__global__ __launch_bounds__(512, 2) void k_fused(const u16* __restrict__ x, const u16* __restrict__ kc, const u16* __restrict__ vc,
                                                  const u16* __restrict__ wq, const u16* __restrict__ wo, const u16* __restrict__ wr,
                                                  const u16* __restrict__ wgate, const u16* __restrict__ wup, const u16* __restrict__ wdown,
                                                  char* __restrict__ ws, u16* __restrict__ out) {
  __shared__ __attribute__((aligned(16))) char lds_raw[LDS_BYTES];
  LAS char* const lds = (LAS char*)lds_raw;
  const int t = threadIdx.x, l = t & 63, bid = blockIdx.x, c = l & 15, g = l >> 4;
  const int wid = __builtin_amdgcn_readfirstlane(t >> 6);
  const int xcd = bid & 7, within = bid >> 3, head = xcd * 4 + (within & 3), rb = within >> 2;
  const int fj = xcd * 28 + (within < 28 ? within : within - 28);
  const int kh = xcd >> 2, mb = 2 * rb + (xcd & 1), nb = ((xcd >> 1) & 1) * 4 + (within & 3);
  const int rbw = wid & 3, kg = wid >> 2;
  const unsigned lds0 = (unsigned)(uintptr_t)lds;
  STAMP_DECL
  STAMP(0);

  unsigned tlo, thi;
  {
    const int row = 8 * bid + wid;
    u32x4 a0, a1, w0, w1, kvo;
    u32x2 ep, klo, khi;
    unsigned e[8];
    gld16<0>(a0, (unsigned)(row * DM * 2 + l * 16), x); gld16<1024>(a1, (unsigned)(row * DM * 2 + l * 16), x);
    gld16<0>(w0, (unsigned)(l * 16), wr); gld16<1024>(w1, (unsigned)(l * 16), wr);
    gld8<0, true>(ep, 0u, ws + WS_EP);
    __builtin_amdgcn_s_barrier();
    const int fn = (bid * 4 + wid) & 255, kvh = bid >> 6;
    if (wid < 4) {
      if (fn < 128) {
        const unsigned ko = (unsigned)(((kvh * SK + (SK - WIN) + (fn >> 2) * 16 + c) * DH + (fn & 3) * 32 + 4 * g) * 2);
        gld8<0>(klo, ko, kc); gld8<32>(khi, ko, kc);
      } else {
        const int d = 32 * ((fn - 128) >> 5) + 8 * (c >> 2) + 4 * (fn & 1) + (c & 3);
        const unsigned vo = (unsigned)(((kvh * SK + (SK - WIN) + ((fn >> 1) & 15) * 32 + 4 * g + 8) * DH + d) * 2);
        sfor<8>([&](auto J) AI { constexpr int j = J; gld2<(16 * (j >> 2) + (j & 3) - 8) * DH * 2>(e[j], vo, vc); });
      }
      if (wid < 2) touch(lds0 + DUMMY, (unsigned)((wid * 256 + within * 8 + (l >> 3)) * 8192 + xcd * 1024 + (l & 7) * 128), wq);
      if (fn < 128) waitv<2>(); else waitv<8>();
    } else {
      waitv<0>();
    }
    PIN(a0); PIN(a1); PIN(w0); PIN(w1); PIN(ep);
    STAMP(1);
    {
      const unsigned long long tg = (((unsigned long long)(unsigned)__builtin_amdgcn_readfirstlane(ep[1]) << 32) | (unsigned)__builtin_amdgcn_readfirstlane(ep[0])) + 1ull;
      tlo = (unsigned)tg; thi = (unsigned)(tg >> 32);
    }
    float v[16];
#pragma unroll
    for (int j = 0; j < 4; ++j) { v[2 * j] = bflo(a0[j]); v[2 * j + 1] = bfhi(a0[j]); v[8 + 2 * j] = bflo(a1[j]); v[9 + 2 * j] = bfhi(a1[j]); }
    float s = 0.f;
#pragma unroll
    for (int j = 0; j < 16; ++j) s += v[j];
    const float mean = wave_sum(s) * (1.f / DM);
    float q = 0.f;
#pragma unroll
    for (int j = 0; j < 16; ++j) { const float d = v[j] - mean; q += d * d; }
    const float var = wave_sum(q) * (1.f / DM);
    const float mbf = rbf(mean), rden = 1.f / rbf(sqrtf(rbf(rbf(var) + 1e-5f)));
    float rr = 0.f;
    u32x4 o0, o1;
#pragma unroll
    for (int j = 0; j < 8; ++j) {
      const unsigned dd = pk2(v[2 * j] - mbf, v[2 * j + 1] - mbf);
      const unsigned nn = pk2(bflo(dd) * rden, bfhi(dd) * rden);
      const unsigned ww = j < 4 ? w0[j] : w1[j - 4];
      rr += bflo(nn) * bflo(ww); rr += bfhi(nn) * bfhi(ww);
      if (j < 4) o0[j] = nn; else o1[j - 4] = nn;
    }
    *(LAS u32x4*)(lds + STAGE + (l * 8 + (wid ^ (l & 7))) * 16) = o0;
    *(LAS u32x4*)(lds + STAGE + 8192 + (l * 8 + (wid ^ (l & 7))) * 16) = o1;
    rr = wave_sum(rr);
    if (l == 0) {
      const unsigned bits = pk2(rr, 0.f) & 0xffffu;
      const unsigned ord = (bits & 0x8000u) ? (~bits & 0xffffu) : (bits | 0x8000u);
      *(LAS unsigned*)(lds + STAGE + 16384 + wid * 4) = (ord << 11) | (unsigned)(SEQ - 1 - row);
      *(LAS f32x2*)(lds + STAGE + 16384 + 32 + wid * 8) = (f32x2){mbf, rden};
    }
    waitbar_l<63>();
    if (wid >= 4) {
#pragma unroll
      for (int hh = 0; hh < 4; ++hh) {
        const int p = hh * 256 + (t - 256), ch = p >> 3, r = 8 * bid + ((p & 7) ^ (ch & 7));
        const u32x4 o = *(LAS const u32x4*)(lds + STAGE + p * 16);
        gst16<0>(o, (unsigned)(((((r >> 5) * 32 + (ch >> 2)) * 2 + ((r >> 4) & 1)) * 64 + (ch & 3) * 16 + (r & 15)) * 16), ws + WS_XN);
      }
      if (t < 256 + 8) gst8(*(LAS const f32x2*)(lds + STAGE + 16384 + 32 + (t - 256) * 8), (unsigned)((8 * bid + t - 256) * 8), ws + WS_ST);
      waitv<0>();
    } else {
      if (wid < 2) waitv<1>(); else waitv<0>();
      if (fn < 128) {
        PIN(klo); PIN(khi);
        kvo = (u32x4){klo[0], klo[1], khi[0], khi[1]};
      } else {
#pragma unroll
        for (int j = 0; j < 8; ++j) PIN(e[j]);
        kvo = (u32x4){e[0] | (e[1] << 16), e[2] | (e[3] << 16), e[4] | (e[5] << 16), e[6] | (e[7] << 16)};
      }
      gst16<0>(kvo, (unsigned)((bid * 4 + wid) * 1024 + l * 16), ws + WS_KV);
    }
    waitbar_l<63>();
    if (t == 256) {
      const u32x4 ka = *(LAS const u32x4*)(lds + STAGE + 16384), kb = *(LAS const u32x4*)(lds + STAGE + 16400);
      const unsigned key = umax(umax(umax(ka[0], ka[1]), umax(ka[2], ka[3])), umax(umax(kb[0], kb[1]), umax(kb[2], kb[3])));
      gst8((u32x2){(tlo << 27) | key, (tlo >> 5) | (thi << 27)}, (unsigned)(bid * 8), ws + WS_G0);
    }
    STAMP(2);
  }
  const unsigned glo = tlo << 27, ghi = (tlo >> 5) | (thi << 27);

  const char* const wq_b = (const char*)wq + head * 256;
  const unsigned wq_v = (unsigned)(((8 * wid + ((l & 31) >> 2)) * 4096 + (l >> 5) * 32 + ((l & 3) ^ (2 * (wid & 1))) * 8) * 2);
  const char* const kv_b = ws + WS_KV + (size_t)(head >> 3) * 262144;
  const unsigned kv_v = (unsigned)(wid * 4096 + l * 16);
  const char* const xn_b = ws + WS_XN + (size_t)rb * 8 * 65536 + 4096;
  const unsigned xn_v = (unsigned)(wid * 65536 + l * 16);
  const char* const wo_b = (const char*)wo + (size_t)kh * 2048 * (DM * 2) + nb * 256;
  const char* const o_b = ws + WS_O + (size_t)mb * 4 * 262144 + kh * 131072;
  const unsigned ldsw = lds0 + wid * 2048, ldsk = lds0 + wid * 4096, ldsf = lds0 + FB + wid * 3072;
  const int erow = mb * 128 + rbw * 32 + kg * 16 + c, ecol = nb * 128 + kh * 64 + 4 * g;
  bf16x8 xf[XD];
  u32x4 fm[2];
  u32x2 fg;
  u32x2 xe[4];
  f32x2 st;
  unsigned key = 0;

  const unsigned fo_v = (unsigned)(((mb * 4 + rbw) * 32 + kh * 16 + c) * 8);
  const int yrow = within < 28 ? fj : 224 + xcd * 4 + within - 28;
  auto issue = [&](auto Sl) AI {
    constexpr int sl = Sl, q0 = SC.first(sl), nq = SC.count(sl);
    sfor<nq>([&](auto Qi) AI {
      constexpr int k = SC.kind[q0 + Qi], i = SC.idx[q0 + Qi];
      if constexpr (k == OX && i < 64) gld16<(i & 7) * 1024 - 4096, true>(xf[i % XD], xn_v, xn_b + (size_t)(i >> 3) * 8192);
      if constexpr (k == OX && i >= 64) {
        constexpr int q = i - 64, v = q >> 2;
        gld16<(q & 1) * 1024, true>(xf[q % XD2], (unsigned)(rbw * 262144 + kg * 16384 + l * 16), o_b + (size_t)(((v & 3) * 4 + ((q >> 1) & 1)) * 8192 + (v >> 2) * 2048));
      }
      if constexpr (k == OG) {
        constexpr int v = i >> 2, p = i & 3;
        if constexpr (v < 8) {
          const char* sb = wq_b + (size_t)(v * 128 + (p >> 1) * 64) * 8192;
          glds<(p & 1) * 128>(ldsw + (v & 3) * 32768 + (p >> 1) * 16384 + (p & 1) * (1024 - 128), wq_v, sb);
        } else if constexpr (v < 16) {
          glds<p * 1024, true>(ldsk + (v & 3) * 32768, kv_v, kv_b + (size_t)(v - 8) * 32768);
        } else {
          const unsigned wo_v = (unsigned)((((wid >> 2) * 128 + (wid & 3) * 8 + ((l & 31) >> 2)) * DM + (l >> 5) * 32 + ((l & 3) ^ (2 * (wid & 1))) * 8) * 2);
          const char* sb = wo_b + (size_t)(((v & 3) * 4 + (p >> 1) * 2) * 128 + ((v - 16) >> 2) * 32) * (DM * 2) + (((p & 1) ^ kh ^ 1) << 7);
          glds<0>(ldsw + (v & 3) * 32768 + (p >> 1) * 16384 + (p & 1) * 1024, wo_v, sb);
        }
      }
      if constexpr (k == OF) {
        const int frho = l >> 2, fch = (l & 3) ^ (frho & 4 ? 2 : 0);
        if constexpr (i < 8) glds64(ldsf + (i % 3) * 1024, (const char*)(fch & 2 ? wup : wgate) + ((size_t)(128 * wid + 32 * (frho >> 2) + (frho & 3) + 4 * i) * FF + fj * 16 + (fch & 1) * 8) * 2);
        else glds<0>(ldsf + (i % 3) * 1024, (unsigned)((frho * DM + wid * 128 + fch * 8) * 2), (const char*)wdown + (size_t)fj * 16 * DM * 2 + (i - 8) * 64);
      }
      if constexpr (k == OM && i > 0) gld8<0, true>(fg, fo_v, ws + WS_FO + (i - 1) * 16384);
      if constexpr (k == OM && i == 0) gld8<0, true>(fg, (unsigned)((t & 255) * 8), wid < 4 ? ws + WS_G0 : ws + WS_FK + (head >> 3) * 2048);
      if constexpr (k == OE && i < 4) gld8<32 * i>(xe[i], (unsigned)((erow * DM + ecol) * 2), x);
      if constexpr (k == OE && i == 4) gld8<0, true>(st, (unsigned)(erow * 8), ws + WS_ST);
    });
  };
  f32x4 fa0, fa1;
  s16x4 fb0, fb1, fxa;
  auto endslot = [&](auto Sl) AI {
    constexpr int sl = Sl, u = sl >> 3, i = sl & 7;
    if constexpr (sl == 2) {
      if (wid < 4 && l == 0) gst8((u32x2){tlo, thi}, (unsigned)((bid * 4 + wid) * 8), ws + WS_FK);
    }
    if constexpr (sl == 38) {
      waitv<SC.younger(OM, 0, sl - 1)>(fg);
      const unsigned lo = wid < 4 ? glo : tlo, hi = wid < 4 ? ghi : thi, mk = wid < 4 ? 0xf8000000u : 0xffffffffu;
      for (int spin = 0; spin < SPINS; ++spin) {
        if (__builtin_expect(__all(((fg[0] & mk) == lo) & (fg[1] == hi)), 1)) break;
        __builtin_amdgcn_s_sleep(1);
        gld8<0, true>(fg, (unsigned)((t & 255) * 8), wid < 4 ? ws + WS_G0 : ws + WS_FK + (head >> 3) * 2048);
        waitv<0>(fg);
      }
      const unsigned km = wave_max_u32(fg[0] & 0x07ffffffu);
      *(LAS unsigned*)(lds + HSC + wid * 4) = km;
    }
    if constexpr (sl == 47) {
      if (bid == 0 && t == 0) gst8((u32x2){tlo, thi}, 0u, ws + WS_EP);
      const u32x4 kk = *(LAS const u32x4*)(lds + HSC);
      key = __builtin_amdgcn_readfirstlane(umax(umax(kk[0], kk[1]), umax(kk[2], kk[3])));
      const int idx = SEQ - 1 - (int)(key & 2047u), ch = (wid & 1) * 64 + l;
      if (wid < 2) glds<0, true>(lds0 + YS + wid * 1024, (unsigned)(((((idx >> 5) * 32 + (ch >> 2)) * 2 + ((idx >> 4) & 1)) * 64 + (ch & 3) * 16 + (idx & 15)) * 16), ws + WS_XN);
    }
    if constexpr (sl >= 48 && sl < 64 && !(sl & 1)) {
      if (wid == (sl - 48) / 2)
        touch(lds0 + DUMMY, (unsigned)((within * 32 + (wid & 3) * 8 + (l >> 3)) * (FF * 2) + xcd * 896 + ((l & 7) < 7 ? (l & 7) : 6) * 128), wid < 4 ? wgate : wup);
    }
    if constexpr (sl >= 65 && sl < 73 && (sl & 1)) {
      if (wid == 4 + (sl - 65) / 2) touch(lds0 + DUMMY, (unsigned)(((wid & 3) * 64 + l) * 128), (const char*)wdown + (size_t)fj * 16 * DM * 2);
    }
    if constexpr (sl >= 76 && sl < 92 && (sl - 76) % 4 == 0) {
      if (wid == (sl - 76) / 4)
        touch(lds0 + DUMMY, (unsigned)((kh * 2048 + wid * 512 + within * 16 + (xcd & 1) * 8 + (l >> 3)) * (DM * 2) + ((xcd >> 1) & 1) * 1024 + (l & 7) * 128), wo);
    }
    constexpr int pr = fpiece(sl), pm = fpiece(sl - 1), ps = fpiece(sl - 2);
    if constexpr (ps >= 8) gst4<(ps - 8) * 128>(sel2(g & 1, fa1[0], fa0[0]), (unsigned)((wid * 128 + (l & 31)) * 4), ws + WS_YP + (size_t)yrow * 4096);
    if constexpr (pm == 0 || pm >= 8) { fa0 = MFMA16(fxa, fb0, (f32x4){}); fa1 = MFMA16(fxa, fb1, (f32x4){}); PIN(fa0); PIN(fa1); }
    else if constexpr (pm > 0) { fa0 = MFMA16(fxa, fb0, fa0); fa1 = MFMA16(fxa, fb1, fa1); PIN(fa0); PIN(fa1); }
    if constexpr (pr >= 0) {
      waitv<SC.younger(OF, pr, sl - 1)>();
      const unsigned ft = ldsf + (4 * g + (c >> 2)) * 64 + (g & 1) * 32 + (c & 3) * 8;
      fb0 = trd((LAS const char*)(uintptr_t)ft + (pr % 3) * 1024); fb1 = trd((LAS const char*)(uintptr_t)(ft ^ 32) + (pr % 3) * 1024);
      if constexpr (pr < 8) fxa = *(LAS const s16x4*)(lds + YS + (wid * 128 + g * 32 + pr * 4) * 2);
    }
    if constexpr (sl == 102) *(LAS float*)(lds + RED1 + (wid * 32 + (l & 31)) * 4) = sel2(g & 1, fa1[0], fa0[0]);
    if constexpr (sl == 103) { fa0 = *(LAS const f32x4*)(lds + RED1 + ((c & 7) * 32 + 4 * g) * 4); fa1 = *(LAS const f32x4*)(lds + RED1 + ((c & 7) * 32 + 16 + 4 * g) * 4); }
    if constexpr (sl == 104 || sl == 105) {
#pragma unroll
      for (int j = 2 * (sl - 104); j < 2 * (sl - 104) + 2; ++j) {
        float a = fa0[j], b = fa1[j];
        a += dppf<XOR1>(a); a += dppf<XOR2>(a); a += dppf<XOR7>(a);
        b += dppf<XOR1>(b); b += dppf<XOR2>(b); b += dppf<XOR7>(b);
        fa0[j] = rbf(silu_f(rbf(a))) * rbf(b);
      }
      PIN(fa0);
    }
    if constexpr (sl == 106) { fxa = __builtin_bit_cast(s16x4, (u32x2){pk2(fa0[0], fa0[1]), pk2(fa0[2], fa0[3])}); PIN(fxa); }
    if constexpr (sl == 110 || sl == 118 || sl == 126) {
      waitv<SC.younger(OW, (sl - 110) / 4 + 1, sl - 1)>();
      if (l == 0) gst8((u32x2){tlo, thi}, (unsigned)(((rb * 8 + wid) * 32 + head) * 8), ws + WS_FO + ((sl - 110) / 8) * 16384);
    }
    if constexpr (sl == 123) {
      waitv<SC.younger(OM, 1, sl - 1)>(fg);
      if (__builtin_expect(!__all((fg[0] == tlo) & (fg[1] == thi)), 0)) poll8(fo_v, ws + WS_FO, tlo, thi, 0xffffffffu);
    }
    issue(Sl);
    SBAR();
    if constexpr (i == 6) {
      if constexpr (u == 5 || u == 12) waitbar_l<SC.younger(OG, 4 * u + 7, sl)>();
      else waitbar<SC.younger(OG, 4 * u + 7, sl)>();
      if constexpr (u == 0 || u == 1 || u == 5 || u == 7 || u == 8 || u == 10 || u == 12 || u == 14)
        STAMP(u == 0 ? 4 : u == 1 ? 5 : u == 5 ? 6 : u == 7 ? 7 : u == 8 ? 8 : u == 10 ? 9 : u == 12 ? 10 : 11);
    }
  };

  issue(IC(-2));
  if (wid == 5) poll8((unsigned)((rb * 32 + (l & 31)) * 8), ws + WS_G0, glo, ghi, 0xf8000000u);
  waitbar<SC.younger(OG, 3, -2)>();
  issue(IC(-1));
  if (wid == 2 || wid == 3) touch(lds0 + DUMMY, (unsigned)((wid * 256 + within * 8 + (l >> 3)) * 8192 + xcd * 1024 + (l & 7) * 128), wq);
  STAMP(3);

  LBase trb[2], frb;
  trb[0].init(lds0 + g * 2048 + (c >> 2) * 64 + (g & 1) * 32 + (c & 3) * 8);
  trb[1].init(lds0 + g * 2048 + (c >> 2) * 64 + ((g & 1) ^ 1) * 32 + (c & 3) * 8);
  frb.init(lds0 + l * 16);
  f32x4 acc[8][2];
  bf16x8 wf[2][4];
  auto WRD1 = [&](auto Si, auto Base, auto Dd) AI {
    constexpr int s = Si, d = Dd, o = Base + (d >> 1) * 512;
    wf[s & 1][d] = cat8(trd(trb[d & 1].template at<o>()), trd(trb[d & 1].template at<o + 256>()));
  };
  auto WRD = [&](auto Si, auto Base) AI { sfor<4>([&](auto Dd) AI { WRD1(Si, Base, Dd); }); };
#define WA(s) IC((((s) >> 3) & 3) * 32768 + (((s) >> 1) & 3) * 8192 + ((s) & 1) * 1024)
#define WB(s) IC((((s) >> 2) & 3) * 32768 + (((s) >> 1) & 1) * 8192 + ((s) & 1) * 1024)
  bf16x8 fr[4];
  auto FRD = [&](auto Ni) AI { constexpr int n = Ni; if constexpr (n < 256) fr[n & 3] = *(LAS const bf16x8*)(frb.at<((n >> 5) & 3) * 32768 + (n & 31) * 1024>()); };

  WRD(IC(0), WA(0));
  SBAR();
  bf16x8 qf[4][2];
  auto QPACK = [&](auto Ds) AI {
    constexpr int ds = Ds;
#pragma unroll
    for (int qb = 0; qb < 2; ++qb) {
      const f32x4 a = acc[2 * ds][qb] * QSCALE, b = acc[2 * ds + 1][qb] * QSCALE;
      qf[ds][qb] = __builtin_bit_cast(bf16x8, (u32x4){pk2(a[0], a[1]), pk2(a[2], a[3]), pk2(b[0], b[1]), pk2(b[2], b[3])});
      PIN(qf[ds][qb]);
    }
  };
  sfor<64>([&](auto Si) AI {
    constexpr int s = Si, x0 = (s & ~1) % XD, x1 = (s | 1) % XD;
    if constexpr (!(s & 1)) waitv<SC.younger(OX, s + 1, s - 1)>(xf[x0], xf[x1]);
    sfor<4>([&](auto Dd) AI {
      constexpr int d = Dd, dt = (s & 1) * 4 + d;
      if constexpr (s + 1 < 64) WRD1(IC(s + 1), WA(s + 1), Dd);
      else if constexpr (d < 3) FRD(Dd);
      SBAR();
      if constexpr (s < 2) { MFZ(acc[dt][0], wf[s & 1][d], xf[x0]); MFZ(acc[dt][1], wf[s & 1][d], xf[x1]); }
      else { MF(acc[dt][0], wf[s & 1][d], xf[x0]); MF(acc[dt][1], wf[s & 1][d], xf[x1]); }
      if constexpr (s == 63 && (d & 1)) { QPACK(IC(d >> 1)); SBAR(); }
    });
    SBAR();
    endslot(Si);
  });

  f32x4 sc[2][2][2], negm[2], o[2][2][2];
  u32x4 pw[16][2];
  float lsum[2] = {0.f, 0.f}, inv[2];
#define MX3(a, b, c) __builtin_fmaxf(__builtin_fmaxf((a), (b)), (c))
#define EXP2(x) __builtin_amdgcn_exp2f(x)
  const char* const od_b = ws + WS_O + ((size_t)(rb * 8) * 128 + head * 4) * 2048 + 4096;
  const unsigned od_v = (unsigned)(wid * 262144 + l * 16);
  auto SM = [&](auto Hi, auto Pi) AI {
    constexpr int H = Hi, p = Pi, kt = p >> 2, qb = (p >> 1) & 1, r = 2 * (p & 1);
    const float e0 = EXP2(sc[H & 1][kt][qb][r]), e1 = EXP2(sc[H & 1][kt][qb][r + 1]);
    lsum[qb] += e0; lsum[qb] += e1;
    unsigned w = pk2(e0, e1);
    PIN(lsum[qb]); PIN(w);
    pw[H][qb][kt * 2 + (p & 1)] = w;
  };
  sfor<128>([&](auto Ni) AI {
    constexpr int n = Ni, H = n >> 3, i = n & 7, kt = i >> 2, ds = i & 3, sn = H & 1;
    FRD(IC(n + 3));
    if constexpr (n < 2) QPACK(IC(2 + n));
    sfor<2>([&](auto Qb) AI {
      constexpr int qb = Qb;
      if constexpr (ds == 0 && H == 0) MFZ(sc[sn][kt][qb], fr[n & 3], qf[0][qb]);
      else if constexpr (ds == 0) MFC(sc[sn][kt][qb], fr[n & 3], qf[0][qb], negm[qb]);
      else MF(sc[sn][kt][qb], fr[n & 3], qf[ds][qb]);
    });
    if constexpr (n == 5 || n == 6) {
      constexpr int qb = n - 5;
      NOPS(3); PIN(sc[0][0][qb]);
      const float m = -xg_max(__builtin_fmaxf(MX3(sc[0][0][qb][0], sc[0][0][qb][1], sc[0][0][qb][2]), sc[0][0][qb][3]));
      negm[qb] = (f32x4){m, m, m, m};
      sc[0][0][qb] += negm[qb];
      PIN(negm[qb]); PIN(sc[0][0][qb]);
    }
    if constexpr (n == 9 || n == 10) { NOPS(3); PIN(sc[0][1][n - 9]); sc[0][1][n - 9] += negm[n - 9]; PIN(sc[0][1][n - 9]); }
    if constexpr (H >= 1) SM(IC(H - 1), IC(i));
    if constexpr (n == 125) { PIN(negm[0]); PIN(negm[1]); }
    SBAR();
    if constexpr ((n & 3) == 3) endslot(IC(64 + (n >> 2)));
  });
  sfor<8>([&](auto Pi) AI { if constexpr (Pi == 4) { NOPS(3); PIN(sc[1][1][0]); PIN(sc[1][1][1]); } SM(IC(15), Pi); });
  if (__builtin_expect(__any(!(lsum[0] + lsum[1] <= LBOUND)), 0)) {
    auto tile = [&](unsigned off, f32x4& s0, f32x4& s1) AI {
      bf16x8 kf[4];
      SBAR(); NOPS(7);
      sfor<4>([&](auto Ds) AI { constexpr int ds = Ds; gld16<ds * 1024, true>(kf[ds], off, kv_b); });
      waitv<0>(kf[0], kf[1]);
      PIN(kf[2]); PIN(kf[3]);
#pragma unroll
      for (int ds = 0; ds < 4; ++ds) {
        s0 = __builtin_amdgcn_mfma_f32_16x16x32_bf16(kf[ds], qf[ds][0], s0, 0, 0, 0);
        s1 = __builtin_amdgcn_mfma_f32_16x16x32_bf16(kf[ds], qf[ds][1], s1, 0, 0, 0);
      }
    };
    float m0 = -__builtin_inff(), m1 = m0;
#pragma unroll 1
    for (int kt = 0; kt < 32; ++kt) {
      f32x4 s0 = {}, s1 = {};
      tile((unsigned)(kt * 4096 + l * 16), s0, s1);
      m0 = MX3(MX3(m0, s0[0], s0[1]), s0[2], s0[3]); m1 = MX3(MX3(m1, s1[0], s1[1]), s1[2], s1[3]);
    }
    m0 = -xg_max(m0); m1 = -xg_max(m1);
    lsum[0] = 0.f; lsum[1] = 0.f;
    sfor<16>([&](auto Hi) AI {
      constexpr int H = Hi;
#pragma unroll
      for (int kt = 0; kt < 2; ++kt) {
        sc[H & 1][kt][0] = (f32x4){m0, m0, m0, m0}; sc[H & 1][kt][1] = (f32x4){m1, m1, m1, m1};
        tile((unsigned)((2 * H + kt) * 4096 + l * 16), sc[H & 1][kt][0], sc[H & 1][kt][1]);
      }
      sfor<8>([&](auto Pi) AI { SM(Hi, Pi); });
      PIN(pw[H][0]); PIN(pw[H][1]);
    });
  }
  SBAR();
  NOPS(1);
  auto OST = [&](auto Os, auto Qb) AI {
    constexpr int os = Os, qb = Qb;
    PIN(o[os & 1][0][qb]); PIN(o[os & 1][1][qb]);
    const f32x4 a = o[os & 1][0][qb] * inv[qb], b = o[os & 1][1][qb] * inv[qb];
    gst16<(os * 2 + qb) * 1024 - 4096>((u32x4){pk2(a[0], a[1]), pk2(a[2], a[3]), pk2(b[0], b[1]), pk2(b[2], b[3])}, od_v, od_b);
  };
  sfor<128>([&](auto Mi) AI {
    constexpr int m = Mi, n = 128 + m, os = m >> 5, H = (m >> 1) & 15, par = m & 1;
    FRD(IC(n + 3));
    if constexpr (H == 0) { MFZ(o[os & 1][par][0], fr[n & 3], pw[H][0]); MFZ(o[os & 1][par][1], fr[n & 3], pw[H][1]); }
    else { MF(o[os & 1][par][0], fr[n & 3], pw[H][0]); MF(o[os & 1][par][1], fr[n & 3], pw[H][1]); }
    if constexpr (m == 2 || m == 3) { inv[m - 2] = __builtin_amdgcn_rcpf(xg_sum(lsum[m - 2])); PIN(inv[m - 2]); }
    if constexpr (os >= 1 && ((m & 31) == 2 || (m & 31) == 3)) OST(IC(os - 1), IC(m & 1));
    if constexpr (m == 123) {
      trb[0].init(lds0 + kg * 16384 + g * 2048 + (c >> 2) * 64 + (g & 1) * 32 + (c & 3) * 8);
      trb[1].init(lds0 + kg * 16384 + g * 2048 + (c >> 2) * 64 + ((g & 1) ^ 1) * 32 + (c & 3) * 8);
    }
    if constexpr (m >= 124) WRD1(IC(0), WB(0), IC(m - 124));
    SBAR();
    if constexpr ((n & 3) == 3) endslot(IC(64 + (n >> 2)));
  });
  STAMP(12); STAMP(13); STAMP(14); STAMP(15);

  const int idx = SEQ - 1 - (int)(key & 2047u);
  const bool owner = (idx >> 7) == mb;
  f32x4 yp[7];
  auto item = [](int s) { return s < 48 ? s : s < 56 ? 48 + 2 * (s - 48) : 49 + 2 * (s - 56); };
  u32x2 pf;
  f32x4 giv[4];
  sfor<64>([&](auto Si) AI {
    constexpr int s = Si, u = s >> 2, it = item(s), x0 = (it & ~1) % XD2, x1 = (it | 1) % XD2;
    if constexpr (!(it & 1)) waitv<SC.younger(OX, 64 + it + 1, B0 + s - 1)>(xf[x0], xf[x1]);
    sfor<4>([&](auto Dd) AI {
      constexpr int d = Dd, dt = (it & 1) * 4 + d;
      if constexpr (s + 1 < 64) WRD1(IC(s + 1), WB(item(s + 1)), Dd);
      SBAR();
      if constexpr (s < 2) { MFZ(acc[dt][0], wf[s & 1][d], xf[x0]); MFZ(acc[dt][1], wf[s & 1][d], xf[x1]); }
      else { MF(acc[dt][0], wf[s & 1][d], xf[x0]); MF(acc[dt][1], wf[s & 1][d], xf[x1]); }
      if constexpr (s == 0 && d >= 2) OST(IC(3), IC(d - 2));
    });
    SBAR();
    if constexpr (s == 7) {
      waitv<SC.younger(OW, 7, B0 + s - 1)>();
      if (l == 0) {
        gst8((u32x2){tlo, thi}, (unsigned)(((rb * 8 + wid) * 32 + head) * 8), ws + WS_FO + 3 * 16384);
        gst8((u32x2){tlo, thi}, (unsigned)((wid * 256 + yrow) * 8), ws + WS_FY);
      }
    }
    if constexpr (s == 8 || s == 17 || s == 33) {
      constexpr int j = s == 8 ? 1 : s == 17 ? 2 : 3;
      waitv<SC.younger(OM, j + 1, B0 + s - 1)>(fg);
      if (__builtin_expect(!__all((fg[0] == tlo) & (fg[1] == thi)), 0)) poll8(fo_v, ws + WS_FO + j * 16384, tlo, thi, 0xffffffffu);
    }
    if constexpr (s == 56) {
      LAS char* dst = lds + (wid ^ 4) * 4096 + l * 16;
      if (kg) {
#pragma unroll
        for (int d = 0; d < 4; ++d) { *(LAS f32x4*)(dst + (d & 1) * 2048 + (d >> 1) * 32768) = acc[d][0]; giv[d] = acc[d][1]; }
      } else {
#pragma unroll
        for (int d = 0; d < 4; ++d) { *(LAS f32x4*)(dst + (d & 1) * 2048 + (d >> 1) * 32768) = acc[d][1]; giv[d] = acc[d][0]; }
      }
      waitbar_l<63>();
    }
    if constexpr (s == 57) {
      LAS const char* src = lds + wid * 4096 + l * 16;
      sfor<4>([&](auto Dd) AI {
        constexpr int d = Dd;
        gst16<d * 1024>(giv[d] + *(LAS const f32x4*)(src + (d & 1) * 2048 + (d >> 1) * 32768), (unsigned)(wid * 4096 + l * 16), ws + WS_XC + (size_t)(bid ^ 4) * 32768);
      });
    }
    if constexpr (s == 62) {
      waitv<0>();
      if (l == 0) gst8((u32x2){tlo, thi}, (unsigned)(((bid ^ 4) * 8 + wid) * 64), ws + WS_FX);
      gld8<0, true>(pf, (unsigned)((bid * 8 + wid) * 64), ws + WS_FX);
    }
    if constexpr (s == 24) {
      if (owner) { gld16<0, true>(fm[0], (unsigned)(nb * 2048 + (l < 56 ? l : 55) * 32), ws + WS_FY); gld16<16, true>(fm[1], (unsigned)(nb * 2048 + (l < 56 ? l : 55) * 32), ws + WS_FY); }
    }
    if constexpr (s == 42) {
      if (owner) {
        PIN(fm[0]); PIN(fm[1]);
        for (int spin = 0; spin < SPINS; ++spin) {
          const bool ok = (fm[0][0] == tlo) & (fm[0][1] == thi) & (fm[0][2] == tlo) & (fm[0][3] == thi) & (fm[1][0] == tlo) & (fm[1][1] == thi) & (fm[1][2] == tlo) & (fm[1][3] == thi);
          if (__builtin_expect(__all(ok), 1)) break;
          __builtin_amdgcn_s_sleep(1);
          gld16<0, true>(fm[0], (unsigned)(nb * 2048 + (l < 56 ? l : 55) * 32), ws + WS_FY); gld16<16, true>(fm[1], (unsigned)(nb * 2048 + (l < 56 ? l : 55) * 32), ws + WS_FY);
          waitv<0>(fm[0], fm[1]);
        }
        const unsigned yv = (unsigned)(((t >> 4) * 1024 + nb * 128 + kh * 64 + (t & 15) * 4) * 4);
        sfor<7>([&](auto I) AI { constexpr int q = I; gld16<0, true>(yp[q], yv, ws + WS_YP + (size_t)q * 32 * 4096); });
      }
    }
    if constexpr (s == 55) {
      if (owner) {
        f32x4 a = {};
#pragma unroll
        for (int q = 0; q < 7; ++q) { PIN(yp[q]); a += yp[q]; }
#pragma unroll
        for (int j = 0; j < 4; ++j) a[j] = xg_sum(a[j]);
        *(LAS f32x4*)(lds + YS + (wid * 64 + c * 4) * 4) = a;
      }
    }
    issue(IC(B0 + s));
    SBAR();
    if constexpr (((s & 3) == 2 && u < 11) || s == 44) {
      waitbar<SC.younger(OG, 64 + 4 * u + 7, B0 + s)>();
      if constexpr (u % 4 == 1) STAMP(16 + u / 4);
    }
    if constexpr (s == 48) waitbar<SC.younger(OG, 64 + 55, B0 + s)>();
    if constexpr (s == 50) waitbar<SC.younger(OG, 64 + 59, B0 + s)>();
    if constexpr (s == 52) { waitbar_l<SC.younger(OG, 64 + 63, B0 + s)>(); STAMP(19); }
  });
  STAMP(20);

  f32x4 kep[4], oth[4];
  unsigned gate[8];
  {
    unsigned xnp[8];
    PIN(st);
#pragma unroll
    for (int j = 0; j < 8; ++j) {
      PIN(xe[j >> 1]);
      const unsigned xw = xe[j >> 1][j & 1];
      const unsigned dd = pk2(bflo(xw) - st[0], bfhi(xw) - st[0]);
      xnp[j] = pk2(bflo(dd) * st[1], bfhi(dd) * st[1]);
      gate[j] = pk2(silu_f(bflo(xnp[j])), silu_f(bfhi(xnp[j])));
      PIN(xnp[j]); PIN(gate[j]);
    }
    if (owner && erow == idx) {
      const unsigned ord = key >> 11, bits = (ord & 0x8000u) ? (ord & 0x7fffu) : (~ord & 0xffffu);
      const float sg = rbf(sigmoid_f(bflo(bits)));
#pragma unroll
      for (int q = 0; q < 4; ++q) {
        f32x4 a = {};
#pragma unroll
        for (int p = 0; p < 8; ++p) a += *(LAS const f32x4*)(lds + YS + (p * 64 + q * 16 + 4 * g) * 4);
#pragma unroll
        for (int j = 0; j < 2; ++j) {
          const float lo = rbf(bflo(xnp[2 * q + j]) + rbf(rbf(a[2 * j]) * sg)), hi = rbf(bfhi(xnp[2 * q + j]) + rbf(rbf(a[2 * j + 1]) * sg));
          gate[2 * q + j] = pk2(silu_f(lo), silu_f(hi));
        }
      }
    }
  }
  waitv<0>(pf);
  if (!__all((pf[0] == tlo) & (pf[1] == thi))) poll8((unsigned)((bid * 8 + wid) * 64), ws + WS_FX, tlo, thi, 0xffffffffu);
  STAMP(21);
  sfor<4>([&](auto Dd) AI { constexpr int d = Dd; gld16<d * 1024, true>(oth[d], (unsigned)(wid * 4096 + l * 16), ws + WS_XC + (size_t)bid * 32768); });
  {
    LAS char* dst = lds + 65536 + (wid ^ 4) * 4096 + l * 16;
    LAS const char* src = lds + 65536 + wid * 4096 + l * 16;
#pragma unroll
    for (int d = 4; d < 8; ++d) { PIN(acc[d][0]); PIN(acc[d][1]); }
    if (kg) {
#pragma unroll
      for (int d = 0; d < 4; ++d) { *(LAS f32x4*)(dst + (d & 1) * 2048 + (d >> 1) * 32768) = acc[4 + d][0]; kep[d] = acc[4 + d][1]; }
    } else {
#pragma unroll
      for (int d = 0; d < 4; ++d) { *(LAS f32x4*)(dst + (d & 1) * 2048 + (d >> 1) * 32768) = acc[4 + d][1]; kep[d] = acc[4 + d][0]; }
    }
    waitbar_l<63>();
    STAMP(22);
#pragma unroll
    for (int d = 0; d < 4; ++d) kep[d] += *(LAS const f32x4*)(src + (d & 1) * 2048 + (d >> 1) * 32768);
  }
  STAMP(23);
  waitv<0>(oth[0], oth[1]);
#pragma unroll
  for (int d = 2; d < 4; ++d) PIN(oth[d]);
  STAMP(24);
  u16* op = out + (size_t)erow * DM + ecol;
#pragma unroll
  for (int d = 0; d < 4; ++d) {
    const f32x4 a = kep[d] + oth[d];
    u32x2 res;
#pragma unroll
    for (int j = 0; j < 2; ++j) {
      const unsigned sp = pk2(a[2 * j], a[2 * j + 1]), gp = gate[2 * d + j], xw = xe[d][j];
      const unsigned pr = pk2(bflo(sp) * bflo(gp), bfhi(sp) * bfhi(gp));
      res[j] = pk2(bflo(xw) + bflo(pr), bfhi(xw) + bfhi(pr));
    }
    *(u32x2*)(op + d * 16) = res;
  }
  STAMP(25);
  STAMP_OUT(0, 26);
}

extern "C" void kernel_launch(void* const* d_in, const int* in_sizes, int n_in, void* d_out, int out_size, void* d_ws, size_t ws_size,
                              hipStream_t stream) {
  k_fused<<<256, 512, 0, stream>>>((const u16*)d_in[0], (const u16*)d_in[1], (const u16*)d_in[2], (const u16*)d_in[3], (const u16*)d_in[4],
                                   (const u16*)d_in[6], (const u16*)d_in[7], (const u16*)d_in[8], (const u16*)d_in[9], (char*)d_ws, (u16*)d_out);
}
```

```cpp
#include <hip/hip_runtime.h>
#include <stdint.h>
#include <type_traits>

typedef unsigned short u16;
typedef short bf16x8 __attribute__((ext_vector_type(8)));
typedef short s16x4 __attribute__((ext_vector_type(4)));
typedef float f32x4 __attribute__((ext_vector_type(4)));
typedef float f32x2 __attribute__((ext_vector_type(2)));
typedef unsigned u32x4 __attribute__((ext_vector_type(4)));
typedef unsigned u32x2 __attribute__((ext_vector_type(2)));
typedef __bf16 bf2_t __attribute__((ext_vector_type(2)));
#define LAS __attribute__((address_space(3)))
#define DI __device__ __forceinline__
#define AI __attribute__((always_inline))
#define SBAR() __builtin_amdgcn_sched_barrier(0)
template <class T> __device__ __forceinline__ void PIN(T& x) { asm volatile("" : "+v"(x)); }
template <class B> DI void MF(f32x4& acc, const bf16x8& a, const B& b) { asm volatile("v_mfma_f32_16x16x32_bf16 %0, %1, %2, %0" : "+v"(acc) : "v"(a), "v"(b)); }
template <class B> DI void MFC(f32x4& d, const bf16x8& a, const B& b, const f32x4& c) { asm volatile("v_mfma_f32_16x16x32_bf16 %0, %1, %2, %3" : "=&v"(d) : "v"(a), "v"(b), "v"(c)); }
template <class B> DI void MFZ(f32x4& d, const bf16x8& a, const B& b) { asm volatile("v_mfma_f32_16x16x32_bf16 %0, %1, %2, 0" : "=&v"(d) : "v"(a), "v"(b)); }
#define NOPS(n) asm volatile("s_nop " #n)
#define MFMA16(a, b, c) __builtin_amdgcn_mfma_f32_16x16x16bf16_1k((a), (b), (c), 0, 0, 0)
#define IC(n) std::integral_constant<int, (n)>{}

constexpr int SEQ = 2048, DM = 1024, DH = 128, SK = 2048, WIN = 512, FF = 3584;
constexpr size_t WS_XN = 0;
constexpr size_t WS_KV = 4u << 20;
constexpr size_t WS_ST = 5u << 20;
constexpr size_t WS_EP = WS_ST + 16384;
constexpr size_t WS_G0 = WS_ST + 32768;
constexpr size_t WS_FK = WS_ST + 49152;
constexpr size_t WS_YP = WS_ST + 65536;
constexpr size_t WS_FO = 7u << 20;
constexpr size_t WS_FY = WS_FO + 65536;
constexpr size_t WS_FX = WS_FO + 131072;
constexpr size_t WS_O = 8u << 20;
constexpr size_t WS_GU = 32u << 20;
constexpr size_t WS_XC = 24u << 20;

template <int I, int N, class F> DI void sfor_impl(F&& f) {
  if constexpr (I < N) { f(std::integral_constant<int, I>{}); sfor_impl<I + 1, N>(f); }
}
template <int N, class F> DI void sfor(F&& f) { sfor_impl<0, N>(f); }

DI unsigned pk2(float a, float b) { f32x2 v = {a, b}; return __builtin_bit_cast(unsigned, __builtin_convertvector(v, bf2_t)); }
DI float rbf(float a) { return (float)(__bf16)a; }
DI float bflo(unsigned u) { return __uint_as_float(u << 16); }
DI float bfhi(unsigned u) { return __uint_as_float(u & 0xffff0000u); }
DI float sigmoid_f(float v) { return __builtin_amdgcn_rcpf(1.f + __builtin_amdgcn_exp2f(v * -1.4426950408889634f)); }
DI float sel2(bool c, float a, float b) { PIN(a); PIN(b); return c ? a : b; }
DI float silu_f(float v) { return v * sigmoid_f(v); }
template <int CTRL> DI float dppf(float x) { return __builtin_bit_cast(float, __builtin_amdgcn_mov_dpp(__builtin_bit_cast(int, x), CTRL, 0xf, 0xf, true)); }
template <int CTRL> DI unsigned dppu(unsigned x) { return (unsigned)__builtin_amdgcn_mov_dpp((int)x, CTRL, 0xf, 0xf, true); }
constexpr int XOR1 = 0xB1, XOR2 = 0x4E, XOR7 = 0x141, XOR8 = 0x128;
DI float xg_sum(float x) {
  auto s = __builtin_amdgcn_permlane16_swap(__float_as_uint(x), __float_as_uint(x), false, false);
  x = __uint_as_float(s[0]) + __uint_as_float(s[1]);
  auto t = __builtin_amdgcn_permlane32_swap(__float_as_uint(x), __float_as_uint(x), false, false);
  return __uint_as_float(t[0]) + __uint_as_float(t[1]);
}
DI float xg_max(float x) {
  auto s = __builtin_amdgcn_permlane16_swap(__float_as_uint(x), __float_as_uint(x), false, false);
  x = __builtin_fmaxf(__uint_as_float(s[0]), __uint_as_float(s[1]));
  auto t = __builtin_amdgcn_permlane32_swap(__float_as_uint(x), __float_as_uint(x), false, false);
  return __builtin_fmaxf(__uint_as_float(t[0]), __uint_as_float(t[1]));
}
DI float wave_sum(float v) { v += dppf<XOR1>(v); v += dppf<XOR2>(v); v += dppf<XOR7>(v); v += dppf<XOR8>(v); return xg_sum(v); }
DI unsigned umax(unsigned a, unsigned b) { return a > b ? a : b; }
DI unsigned wave_max_u32(unsigned v) {
  v = umax(v, dppu<XOR1>(v)); v = umax(v, dppu<XOR2>(v)); v = umax(v, dppu<XOR7>(v)); v = umax(v, dppu<XOR8>(v));
  auto s = __builtin_amdgcn_permlane16_swap(v, v, false, false); v = umax(s[0], s[1]);
  auto t = __builtin_amdgcn_permlane32_swap(v, v, false, false); v = umax(t[0], t[1]);
  return __builtin_amdgcn_readfirstlane(v);
}

template <int OFF, bool DEV = false> DI void glds(unsigned ldsaddr, unsigned voff, const void* sbase) {
  if constexpr (DEV) asm volatile("s_mov_b32 m0, %0\n\ts_nop 0\n\tglobal_load_lds_dwordx4 %1, %2 offset:%3 sc1" ::"s"(ldsaddr), "v"(voff), "s"(sbase), "i"(OFF) : "memory");
  else asm volatile("s_mov_b32 m0, %0\n\ts_nop 0\n\tglobal_load_lds_dwordx4 %1, %2 offset:%3" ::"s"(ldsaddr), "v"(voff), "s"(sbase), "i"(OFF) : "memory");
}
DI void touch(unsigned ldsaddr, unsigned voff, const void* sbase) {
  asm volatile("s_mov_b32 m0, %0\n\ts_nop 0\n\tglobal_load_lds_dword %1, %2" ::"s"(ldsaddr), "v"(voff), "s"(sbase) : "memory");
}
template <int OFF, bool DEV = false, class T> DI void gld16(T& dst, unsigned voff, const void* sbase) {
  if constexpr (DEV) asm volatile("global_load_dwordx4 %0, %1, %2 offset:%3 sc1" : "=v"(dst) : "v"(voff), "s"(sbase), "i"(OFF) : "memory");
  else asm volatile("global_load_dwordx4 %0, %1, %2 offset:%3" : "=v"(dst) : "v"(voff), "s"(sbase), "i"(OFF) : "memory");
}
template <int OFF, bool DEV = false, class T> DI void gld8(T& dst, unsigned voff, const void* sbase) {
  if constexpr (DEV) asm volatile("global_load_dwordx2 %0, %1, %2 offset:%3 sc1" : "=v"(dst) : "v"(voff), "s"(sbase), "i"(OFF) : "memory");
  else asm volatile("global_load_dwordx2 %0, %1, %2 offset:%3" : "=v"(dst) : "v"(voff), "s"(sbase), "i"(OFF) : "memory");
}
template <int OFF> DI void gld2(unsigned& dst, unsigned voff, const void* sbase) {
  asm volatile("global_load_ushort %0, %1, %2 offset:%3" : "=v"(dst) : "v"(voff), "s"(sbase), "i"(OFF) : "memory");
}
template <int OFF, class T> DI void gst16(const T& v, unsigned voff, const void* sbase) {
  asm volatile("global_store_dwordx4 %0, %1, %2 offset:%3 sc1\n\ts_nop 1" ::"v"(voff), "v"(v), "s"(sbase), "i"(OFF) : "memory");
}
template <class T> DI void gst8(const T& v, unsigned voff, const void* sbase) {
  asm volatile("global_store_dwordx2 %0, %1, %2 sc1" ::"v"(voff), "v"(v), "s"(sbase) : "memory");
}
template <int OFF> DI void gst4(float v, unsigned voff, const void* sbase) {
  asm volatile("global_store_dword %0, %1, %2 offset:%3 sc1" ::"v"(voff), "v"(v), "s"(sbase), "i"(OFF) : "memory");
}
constexpr int SPINS = 1 << 16;
DI void poll8(unsigned voff, const void* sbase, unsigned lo, unsigned hi, unsigned lomask) {
  for (int spin = 0; spin < SPINS; ++spin) {
    u32x2 v;
    asm volatile("global_load_dwordx2 %0, %1, %2 sc1\n\ts_waitcnt vmcnt(0)" : "=&v"(v) : "v"(voff), "s"(sbase) : "memory");
    if (__all(((v[0] & lomask) == lo) & (v[1] == hi))) break;
    __builtin_amdgcn_s_sleep(1);
  }
}
template <int N> DI void waitv() { static_assert(N >= 0, "count"); asm volatile("s_waitcnt vmcnt(%0)" ::"n"(N > 63 ? 63 : N) : "memory"); }
template <int N, class T> DI void waitv(T& x) { static_assert(N >= 0, "count"); asm volatile("s_waitcnt vmcnt(%1)" : "+v"(x) : "n"(N > 63 ? 63 : N) : "memory"); }
template <int N, class T> DI void waitv(T& x, T& y) { static_assert(N >= 0, "count"); asm volatile("s_waitcnt vmcnt(%2)" : "+v"(x), "+v"(y) : "n"(N > 63 ? 63 : N) : "memory"); }
template <int N> DI void waitbar() { static_assert(N >= 0, "count"); asm volatile("s_waitcnt vmcnt(%0)\n\ts_barrier" ::"n"(N > 63 ? 63 : N) : "memory"); }
template <int N> DI void waitbar_l() { static_assert(N >= 0, "count"); asm volatile("s_waitcnt vmcnt(%0) lgkmcnt(0)\n\ts_barrier" ::"n"(N > 63 ? 63 : N) : "memory"); }
typedef short v4i16_t __attribute__((ext_vector_type(4)));
DI s16x4 trd(LAS const char* p) { return __builtin_amdgcn_ds_read_tr16_b64_v4i16((LAS v4i16_t*)p); }
struct LBase {
  unsigned lo, hi;
  DI void init(unsigned a) { lo = a; hi = a + 65536; PIN(lo); PIN(hi); }
  template <int OFF> DI LAS const char* at() const { return (LAS const char*)(uintptr_t)(OFF < 65536 ? lo : hi) + (OFF & 65535); }
};
DI bf16x8 cat8(s16x4 lo, s16x4 hi) { return __builtin_shufflevector(lo, hi, 0, 1, 2, 3, 4, 5, 6, 7); }

enum { OX, OG, OF, OM, OS, OP, OE, OW };
struct Sched {
  int n; short kind[400], idx[400], slot[400];
  constexpr Sched() : n(0), kind{}, idx{}, slot{} {}
  constexpr void add(int s, int k, int i) { kind[n] = (short)k; idx[n] = (short)i; slot[n] = (short)s; ++n; }
  constexpr int younger(int k, int i, int s) const {
    int p = -1;
    for (int q = 0; q < n; ++q) if (kind[q] == k && idx[q] == i) p = q;
    if (p < 0 || slot[p] > s) return -1;
    int c = 0;
    for (int q = p + 1; q < n; ++q) if (slot[q] <= s) ++c;
    return c;
  }
  constexpr int first(int s) const { int q = 0; while (q < n && slot[q] < s) ++q; return q; }
  constexpr int count(int s) const { int c = 0; for (int q = 0; q < n; ++q) if (slot[q] == s) ++c; return c; }
};

#define STAMP_DECL
#define STAMP(i)
#define STAMP_OUT(kid, n)

constexpr int RING = 4 * 32768, FB = RING, RED1 = FB + 8 * 3072, HSC = RED1 + 1024, YS = HSC + 512, DUMMY = YS + 2048, LDS_BYTES = DUMMY + 256;
constexpr int STAGE = 3 * 32768;
constexpr int XD = 16, XD2 = 16;
constexpr float LBOUND = 65536.f;
constexpr float QSCALE = 0.08838834764831845f * 1.4426950408889634f;
constexpr int B0 = 128;
constexpr int FISS[4] = {96, 97, 98, 110}, FRDS[4] = {107, 110, 113, 117};
constexpr int fpiece(int sl) { for (int p = 0; p < 4; ++p) if (FRDS[p] == sl) return p; return -1; }
constexpr Sched make_sched() {
  Sched s;
  for (int i = 0; i < 12; ++i) s.add(-2, OG, i);
  for (int i = 0; i < 16; ++i) s.add(-1, OX, i);
  for (int t = 0; t < 128; ++t) {
    if (t == 104 || t == 112 || t == 120) { s.add(t, OW, (t - 104) / 4); s.add(t, OW, (t - 104) / 4 + 1); }
    for (int p = 0; p < 24; ++p) { const int a = 3 + p, b2 = 2 * p + 1; if ((a > b2 ? a : b2) == t) { s.add(t, OX, 16 + 2 * p); s.add(t, OX, 17 + 2 * p); } }
    if (t >= 120) { s.add(t, OX, 64 + 2 * (t - 120)); s.add(t, OX, 65 + 2 * (t - 120)); }
    for (int v = 3; v < 18; ++v) for (int p = 0; p < 4; ++p) if (8 * (v - 3) + 7 + p == t) s.add(t, OG, 4 * v + p);
    if (t == 127) { s.add(t, OG, 72); s.add(t, OG, 73); }
    if (t == 20) s.add(t, OM, 0);
    if (t == 115 || t == 123) s.add(t, OM, t == 115 ? 1 : 2);
    if (t >= 49 && t < 57) s.add(t, OF, t - 49);
    if (t == 57) { s.add(t, OS, 0); s.add(t, OS, 1); }
    if (t == 90) s.add(t, OS, 2);
    for (int p = 0; p < 4; ++p) if (FISS[p] == t) s.add(t, OF, 8 + p);
  }
  for (int t = 0; t < 64; ++t) {
    if (t == 0) { s.add(B0, OW, 6); s.add(B0, OW, 7); s.add(B0, OG, 74); s.add(B0, OG, 75); }
    if (t == 5 || t == 20) s.add(B0 + t, OM, t == 5 ? 3 : 4);
    if (t == 52) for (int i = 0; i < 5; ++i) s.add(B0 + t, OE, i);
    if ((t & 1) && t < 48) { s.add(B0 + t, OX, 79 + t); s.add(B0 + t, OX, 80 + t); }
    for (int v = 3; v < 16; ++v) for (int p = 0; p < 4; ++p) if ((v < 14 ? 4 * (v - 3) + 3 + (p >> 1) : v == 14 ? 45 : 49) == t) s.add(B0 + t, OG, 64 + 4 * v + p);
  }
  return s;
}
constexpr Sched SC = make_sched();

__global__ __launch_bounds__(512, 2) void k_fused(const u16* __restrict__ x, const u16* __restrict__ kc, const u16* __restrict__ vc,
                                                  const u16* __restrict__ wq, const u16* __restrict__ wo, const u16* __restrict__ wr,
                                                  const u16* __restrict__ wgate, const u16* __restrict__ wup, const u16* __restrict__ wdown,
                                                  char* __restrict__ ws, u16* __restrict__ out) {
  __shared__ __attribute__((aligned(16))) char lds_raw[LDS_BYTES];
  LAS char* const lds = (LAS char*)lds_raw;
  const int t = threadIdx.x, l = t & 63, bid = blockIdx.x, c = l & 15, g = l >> 4;
  const int wid = __builtin_amdgcn_readfirstlane(t >> 6);
  const int xcd = bid & 7, within = bid >> 3, head = xcd * 4 + (within & 3), rb = within >> 2;
  const int fj = xcd * 28 + (within < 28 ? within : within - 28);
  const int kh = xcd >> 2, mb = 2 * rb + (xcd & 1), nb = ((xcd >> 1) & 1) * 4 + (within & 3);
  const int rbw = wid & 3, kg = wid >> 2;
  const unsigned lds0 = (unsigned)(uintptr_t)lds;
  STAMP_DECL
  STAMP(0);

  unsigned tlo, thi;
  {
    const int row = 8 * bid + wid;
    u32x4 a0, a1, w0, w1, kvo;
    u32x2 ep, klo, khi;
    unsigned e[8];
    gld16<0>(a0, (unsigned)(row * DM * 2 + l * 16), x); gld16<1024>(a1, (unsigned)(row * DM * 2 + l * 16), x);
    gld16<0>(w0, (unsigned)(l * 16), wr); gld16<1024>(w1, (unsigned)(l * 16), wr);
    gld8<0, true>(ep, 0u, ws + WS_EP);
    __builtin_amdgcn_s_barrier();
    const int fn = (bid * 4 + wid) & 255, kvh = bid >> 6;
    if (wid < 4) {
      if (fn < 128) {
        const unsigned ko = (unsigned)(((kvh * SK + (SK - WIN) + (fn >> 2) * 16 + c) * DH + (fn & 3) * 32 + 4 * g) * 2);
        gld8<0>(klo, ko, kc); gld8<32>(khi, ko, kc);
      } else {
        const int d = 32 * ((fn - 128) >> 5) + 8 * (c >> 2) + 4 * (fn & 1) + (c & 3);
        const unsigned vo = (unsigned)(((kvh * SK + (SK - WIN) + ((fn >> 1) & 15) * 32 + 4 * g + 8) * DH + d) * 2);
        sfor<8>([&](auto J) AI { constexpr int j = J; gld2<(16 * (j >> 2) + (j & 3) - 8) * DH * 2>(e[j], vo, vc); });
      }
      if (wid < 2) touch(lds0 + DUMMY, (unsigned)((wid * 256 + within * 8 + (l >> 3)) * 8192 + xcd * 1024 + (l & 7) * 128), wq);
      if (fn < 128) waitv<2>(); else waitv<8>();
    } else {
      waitv<0>();
    }
    PIN(a0); PIN(a1); PIN(w0); PIN(w1); PIN(ep);
    STAMP(1);
    {
      const unsigned long long tg = (((unsigned long long)(unsigned)__builtin_amdgcn_readfirstlane(ep[1]) << 32) | (unsigned)__builtin_amdgcn_readfirstlane(ep[0])) + 1ull;
      tlo = (unsigned)tg; thi = (unsigned)(tg >> 32);
    }
    float v[16];
#pragma unroll
    for (int j = 0; j < 4; ++j) { v[2 * j] = bflo(a0[j]); v[2 * j + 1] = bfhi(a0[j]); v[8 + 2 * j] = bflo(a1[j]); v[9 + 2 * j] = bfhi(a1[j]); }
    float s = 0.f;
#pragma unroll
    for (int j = 0; j < 16; ++j) s += v[j];
    const float mean = wave_sum(s) * (1.f / DM);
    float q = 0.f;
#pragma unroll
    for (int j = 0; j < 16; ++j) { const float d = v[j] - mean; q += d * d; }
    const float var = wave_sum(q) * (1.f / DM);
    const float mbf = rbf(mean), rden = 1.f / rbf(sqrtf(rbf(rbf(var) + 1e-5f)));
    float rr = 0.f;
    u32x4 o0, o1;
#pragma unroll
    for (int j = 0; j < 8; ++j) {
      const unsigned dd = pk2(v[2 * j] - mbf, v[2 * j + 1] - mbf);
      const unsigned nn = pk2(bflo(dd) * rden, bfhi(dd) * rden);
      const unsigned ww = j < 4 ? w0[j] : w1[j - 4];
      rr += bflo(nn) * bflo(ww); rr += bfhi(nn) * bfhi(ww);
      if (j < 4) o0[j] = nn; else o1[j - 4] = nn;
    }
    *(LAS u32x4*)(lds + STAGE + (l * 8 + (wid ^ (l & 7))) * 16) = o0;
    *(LAS u32x4*)(lds + STAGE + 8192 + (l * 8 + (wid ^ (l & 7))) * 16) = o1;
    rr = wave_sum(rr);
    if (l == 0) {
      const unsigned bits = pk2(rr, 0.f) & 0xffffu;
      const unsigned ord = (bits & 0x8000u) ? (~bits & 0xffffu) : (bits | 0x8000u);
      *(LAS unsigned*)(lds + STAGE + 16384 + wid * 4) = (ord << 11) | (unsigned)(SEQ - 1 - row);
      *(LAS f32x2*)(lds + STAGE + 16384 + 32 + wid * 8) = (f32x2){mbf, rden};
    }
    waitbar_l<63>();
    if (wid >= 4) {
#pragma unroll
      for (int hh = 0; hh < 4; ++hh) {
        const int p = hh * 256 + (t - 256), ch = p >> 3, r = 8 * bid + ((p & 7) ^ (ch & 7));
        const u32x4 o = *(LAS const u32x4*)(lds + STAGE + p * 16);
        gst16<0>(o, (unsigned)(((((r >> 5) * 32 + (ch >> 2)) * 2 + ((r >> 4) & 1)) * 64 + (ch & 3) * 16 + (r & 15)) * 16), ws + WS_XN);
      }
      if (t < 256 + 8) gst8(*(LAS const f32x2*)(lds + STAGE + 16384 + 32 + (t - 256) * 8), (unsigned)((8 * bid + t - 256) * 8), ws + WS_ST);
      waitv<0>();
    } else {
      if (wid < 2) waitv<1>(); else waitv<0>();
      if (fn < 128) {
        PIN(klo); PIN(khi);
        kvo = (u32x4){klo[0], klo[1], khi[0], khi[1]};
      } else {
#pragma unroll
        for (int j = 0; j < 8; ++j) PIN(e[j]);
        kvo = (u32x4){e[0] | (e[1] << 16), e[2] | (e[3] << 16), e[4] | (e[5] << 16), e[6] | (e[7] << 16)};
      }
      gst16<0>(kvo, (unsigned)((bid * 4 + wid) * 1024 + l * 16), ws + WS_KV);
    }
    waitbar_l<63>();
    if (t == 256) {
      const u32x4 ka = *(LAS const u32x4*)(lds + STAGE + 16384), kb = *(LAS const u32x4*)(lds + STAGE + 16400);
      const unsigned key = umax(umax(umax(ka[0], ka[1]), umax(ka[2], ka[3])), umax(umax(kb[0], kb[1]), umax(kb[2], kb[3])));
      gst8((u32x2){(tlo << 27) | key, (tlo >> 5) | (thi << 27)}, (unsigned)(bid * 8), ws + WS_G0);
    }
    STAMP(2);
  }
  const unsigned glo = tlo << 27, ghi = (tlo >> 5) | (thi << 27);

  const char* const wq_b = (const char*)wq + head * 256;
  const unsigned wq_v = (unsigned)(((8 * wid + ((l & 31) >> 2)) * 4096 + (l >> 5) * 32 + ((l & 3) ^ (2 * (wid & 1))) * 8) * 2);
  const char* const kv_b = ws + WS_KV + (size_t)(head >> 3) * 262144;
  const unsigned kv_v = (unsigned)(wid * 4096 + l * 16);
  const char* const xn_b = ws + WS_XN + (size_t)rb * 8 * 65536 + 4096;
  const unsigned xn_v = (unsigned)(wid * 65536 + l * 16);
  const char* const wo_b = (const char*)wo + (size_t)kh * 2048 * (DM * 2) + nb * 256;
  const char* const o_b = ws + WS_O + (size_t)mb * 4 * 262144 + kh * 131072;
  const unsigned ldsw = lds0 + wid * 2048, ldsk = lds0 + wid * 4096, ldsf = lds0 + FB + wid * 3072;
  const int erow = mb * 128 + rbw * 32 + kg * 16 + c, ecol = nb * 128 + kh * 64 + 4 * g;
  bf16x8 xf[XD];
  u32x4 fm[2], fw[8];
  s16x4 fxs[2];
  const unsigned gu_v = (unsigned)((((4 * wid + g) * 2 + ((l >> 3) & 1)) * FF + fj * 16 + (l & 7) * 2) * 8);
  u32x2 fg;
  u32x2 xe[4];
  f32x2 st;
  unsigned key = 0;

  const unsigned fo_v = (unsigned)(((mb * 4 + rbw) * 32 + kh * 16 + c) * 8);
  const int yrow = within < 28 ? fj : 224 + xcd * 4 + within - 28;
  auto issue = [&](auto Sl) AI {
    constexpr int sl = Sl, q0 = SC.first(sl), nq = SC.count(sl);
    sfor<nq>([&](auto Qi) AI {
      constexpr int k = SC.kind[q0 + Qi], i = SC.idx[q0 + Qi];
      if constexpr (k == OX && i < 64) gld16<(i & 7) * 1024 - 4096, true>(xf[i % XD], xn_v, xn_b + (size_t)(i >> 3) * 8192);
      if constexpr (k == OX && i >= 64) {
        constexpr int q = i - 64, v = q >> 2;
        gld16<(q & 1) * 1024, true>(xf[q % XD2], (unsigned)(rbw * 262144 + kg * 16384 + l * 16), o_b + (size_t)(((v & 3) * 4 + ((q >> 1) & 1)) * 8192 + (v >> 2) * 2048));
      }
      if constexpr (k == OG) {
        constexpr int v = i >> 2, p = i & 3;
        if constexpr (v < 8) {
          const char* sb = wq_b + (size_t)(v * 128 + (p >> 1) * 64) * 8192;
          glds<(p & 1) * 128>(ldsw + (v & 3) * 32768 + (p >> 1) * 16384 + (p & 1) * (1024 - 128), wq_v, sb);
        } else if constexpr (v < 16) {
          glds<p * 1024, true>(ldsk + (v & 3) * 32768, kv_v, kv_b + (size_t)(v - 8) * 32768);
        } else {
          const unsigned wo_v = (unsigned)((((wid >> 2) * 128 + (wid & 3) * 8 + ((l & 31) >> 2)) * DM + (l >> 5) * 32 + ((l & 3) ^ (2 * (wid & 1))) * 8) * 2);
          const char* sb = wo_b + (size_t)(((v & 3) * 4 + (p >> 1) * 2) * 128 + ((v - 16) >> 2) * 32) * (DM * 2) + (((p & 1) ^ kh ^ 1) << 7);
          glds<0>(ldsw + (v & 3) * 32768 + (p >> 1) * 16384 + (p & 1) * 1024, wo_v, sb);
        }
      }
      if constexpr (k == OF && i < 8)
        gld16<0>(fw[i], (unsigned)(((32 * wid + (l >> 3)) * FF + (l & 7) * 8) * 2), (const char*)(i < 4 ? wgate : wup) + ((size_t)((fj & 3) * 256 + 8 * (i & 3)) * FF + (fj >> 2) * 64) * 2);
      if constexpr (k == OF && i >= 8) {
        const int frho = l >> 2, fch = (l & 3) ^ (frho & 4 ? 2 : 0);
        glds<0>(ldsf + ((i - 8) % 3) * 1024, (unsigned)((frho * DM + wid * 128 + fch * 8) * 2), (const char*)wdown + (size_t)fj * 16 * DM * 2 + (i - 8) * 64);
      }
      if constexpr (k == OS && i < 2) {
        const int idx = SEQ - 1 - (int)(key & 2047u);
        gld8<i * 512, true>(fxs[i], (unsigned)((((idx >> 5) * 32 + (fj & 3) * 8 + wid) * 2 + ((idx >> 4) & 1)) * 1024 + ((g >> 1) * 16 + (idx & 15)) * 16 + (g & 1) * 8), ws + WS_XN);
      }
      if constexpr (k == OS && i == 2) gld16<0, true>(fm[0], gu_v, ws + WS_GU);
      if constexpr (k == OM && i > 0) gld8<0, true>(fg, fo_v, ws + WS_FO + (i - 1) * 16384);
      if constexpr (k == OM && i == 0) gld8<0, true>(fg, (unsigned)((t & 255) * 8), wid < 4 ? ws + WS_G0 : ws + WS_FK + (head >> 3) * 2048);
      if constexpr (k == OE && i < 4) gld8<32 * i>(xe[i], (unsigned)((erow * DM + ecol) * 2), x);
      if constexpr (k == OE && i == 4) gld8<0, true>(st, (unsigned)(erow * 8), ws + WS_ST);
    });
  };
  f32x4 fa0, fa1, fq[4];
  s16x4 fb0, fb1, fxa, ft[4];
  auto endslot = [&](auto Sl) AI {
    constexpr int sl = Sl, u = sl >> 3, i = sl & 7;
    if constexpr (sl == 2) {
      if (wid < 4 && l == 0) gst8((u32x2){tlo, thi}, (unsigned)((bid * 4 + wid) * 8), ws + WS_FK);
    }
    if constexpr (sl == 38) {
      waitv<SC.younger(OM, 0, sl - 1)>(fg);
      const unsigned lo = wid < 4 ? glo : tlo, hi = wid < 4 ? ghi : thi, mk = wid < 4 ? 0xf8000000u : 0xffffffffu;
      for (int spin = 0; spin < SPINS; ++spin) {
        if (__builtin_expect(__all(((fg[0] & mk) == lo) & (fg[1] == hi)), 1)) break;
        __builtin_amdgcn_s_sleep(1);
        gld8<0, true>(fg, (unsigned)((t & 255) * 8), wid < 4 ? ws + WS_G0 : ws + WS_FK + (head >> 3) * 2048);
        waitv<0>(fg);
      }
      const unsigned km = wave_max_u32(fg[0] & 0x07ffffffu);
      *(LAS unsigned*)(lds + HSC + wid * 4) = km;
    }
    if constexpr (sl == 47) {
      if (bid == 0 && t == 0) gst8((u32x2){tlo, thi}, 0u, ws + WS_EP);
      const u32x4 kk = *(LAS const u32x4*)(lds + HSC);
      key = __builtin_amdgcn_readfirstlane(umax(umax(kk[0], kk[1]), umax(kk[2], kk[3])));
    }
    if constexpr (sl == 61 || sl == 64 || sl == 67 || sl == 70) {
      if (wid == 4 + (sl - 61) / 3) touch(lds0 + DUMMY, (unsigned)(((wid & 3) * 64 + l) * 128), (const char*)wdown + (size_t)fj * 16 * DM * 2);
    }
    if constexpr (sl == 73 || sl == 76 || sl == 79 || sl == 82) {
      if (wid == (sl - 73) / 3)
        touch(lds0 + DUMMY, (unsigned)((kh * 2048 + (within >> 1) * 128 + wid * 32 + ((within & 1) * 2 + (xcd & 1)) * 8 + (l >> 3)) * (DM * 2) + ((xcd >> 1) & 1) * 1024 + (l & 7) * 128), wo);
    }
    if constexpr (sl >= 72 && sl < 80) {
      constexpr int r = (sl - 72) >> 1;
      if constexpr (sl == 72) { waitv<SC.younger(OS, 1, sl - 1)>(fxs[0], fxs[1]); }
      if constexpr (!(sl & 1)) {
        LAS char* wp = (LAS char*)(uintptr_t)(ldsf + (l >> 3) * 128 + (((l & 7) ^ ((l >> 3) & 6)) * 16));
        PIN(fw[2 * r]); PIN(fw[2 * r + 1]);
        *(LAS u32x4*)wp = fw[2 * r]; *(LAS u32x4*)(wp + 1024) = fw[2 * r + 1];
        const unsigned rp = ldsf + (4 * g + (c >> 2)) * 128 + (((2 * g + (c >> 3)) & 3) << 5) + (c & 3) * 8;
#pragma unroll
        for (int j = 0; j < 4; ++j) ft[j] = trd((LAS const char*)(uintptr_t)(rp ^ (j << 5)));
      } else {
#pragma unroll
        for (int j = 0; j < 4; ++j) { fq[j] = MFMA16(fxs[r & 1], ft[j], (r & 1) ? fq[j] : (f32x4){}); PIN(fq[j]); }
      }
    }
    if constexpr (sl == 76 || sl == 80) {
      const float v = sel2(g & 2, sel2(g & 1, fq[3][0], fq[2][0]), sel2(g & 1, fq[1][0], fq[0][0]));
      gst8((u32x2){__float_as_uint(v), tlo}, (unsigned)(((((fj & 3) * 8 + wid) * 2 + (sl - 76) / 4) * FF + (fj >> 2) * 64 + l) * 8), ws + WS_GU);
    }
    if constexpr (sl == 101) {
      waitv<SC.younger(OS, 2, sl - 1)>(fm[0]);
      for (int spin = 0; spin < SPINS; ++spin) {
        if (__builtin_expect(__all((fm[0][1] == tlo) & (fm[0][3] == tlo)), 1)) break;
        __builtin_amdgcn_s_sleep(1);
        gld16<0, true>(fm[0], gu_v, ws + WS_GU);
        waitv<0>(fm[0]);
      }
      *(LAS f32x2*)(lds + RED1 + (wid * 32 + 2 * c) * 4) = (f32x2){xg_sum(__uint_as_float(fm[0][0])), xg_sum(__uint_as_float(fm[0][2]))};
    }
    if constexpr (sl == 103) {
#pragma unroll
      for (int w = 0; w < 2; ++w) { fa0[w] = *(LAS const float*)(lds + RED1 + ((2 * g + w) * 32 + c) * 4); fa0[2 + w] = *(LAS const float*)(lds + RED1 + ((2 * g + w) * 32 + 16 + c) * 4); }
    }
    if constexpr (sl == 104) { fa0[0] = xg_sum(fa0[0] + fa0[1]); fa0[2] = xg_sum(fa0[2] + fa0[3]); PIN(fa0); }
    if constexpr (sl == 105) {
      const float hh = rbf(silu_f(rbf(fa0[0]))) * rbf(fa0[2]);
      *(LAS u16*)(lds + HSC + 32 + wid * 32 + c * 2) = (u16)pk2(hh, hh);
    }
    if constexpr (sl == 106) fxa = *(LAS const s16x4*)(lds + HSC + 32 + wid * 32 + g * 8);
    constexpr int pr = fpiece(sl), pm = fpiece(sl - 1), ps = fpiece(sl - 2);
    if constexpr (ps >= 0) gst4<ps * 128>(sel2(g & 1, fa1[0], fa0[0]), (unsigned)((wid * 128 + (l & 31)) * 4), ws + WS_YP + (size_t)yrow * 4096);
    if constexpr (pm >= 0) { fa0 = MFMA16(fxa, fb0, (f32x4){}); fa1 = MFMA16(fxa, fb1, (f32x4){}); PIN(fa0); PIN(fa1); }
    if constexpr (pr >= 0) {
      waitv<SC.younger(OF, 8 + pr, sl - 1)>();
      const unsigned fp = ldsf + (4 * g + (c >> 2)) * 64 + (g & 1) * 32 + (c & 3) * 8;
      fb0 = trd((LAS const char*)(uintptr_t)fp + (pr % 3) * 1024); fb1 = trd((LAS const char*)(uintptr_t)(fp ^ 32) + (pr % 3) * 1024);
    }
    if constexpr (sl == 110 || sl == 118 || sl == 126) {
      waitv<SC.younger(OW, (sl - 110) / 4 + 1, sl - 1)>();
      if (l == 0) gst8((u32x2){tlo, thi}, (unsigned)(((rb * 8 + wid) * 32 + head) * 8), ws + WS_FO + ((sl - 110) / 8) * 16384);
    }
    if constexpr (sl == 120) {
      waitv<SC.younger(OM, 1, sl - 1)>(fg);
      if (__builtin_expect(!__all((fg[0] == tlo) & (fg[1] == thi)), 0)) poll8(fo_v, ws + WS_FO, tlo, thi, 0xffffffffu);
    }
    issue(Sl);
    SBAR();
    if constexpr (i == 6) {
      if constexpr (u == 5 || u == 12) waitbar_l<SC.younger(OG, 4 * u + 7, sl)>();
      else waitbar<SC.younger(OG, 4 * u + 7, sl)>();
      if constexpr (u == 0 || u == 1 || u == 5 || u == 7 || u == 8 || u == 10 || u == 12 || u == 14)
        STAMP(u == 0 ? 4 : u == 1 ? 5 : u == 5 ? 6 : u == 7 ? 7 : u == 8 ? 8 : u == 10 ? 9 : u == 12 ? 10 : 11);
    }
  };

  issue(IC(-2));
  if (wid == 5) poll8((unsigned)((rb * 32 + (l & 31)) * 8), ws + WS_G0, glo, ghi, 0xf8000000u);
  waitbar<SC.younger(OG, 3, -2)>();
  issue(IC(-1));
  if (wid == 2 || wid == 3) touch(lds0 + DUMMY, (unsigned)((wid * 256 + within * 8 + (l >> 3)) * 8192 + xcd * 1024 + (l & 7) * 128), wq);
  STAMP(3);

  LBase trb[2], frb;
  trb[0].init(lds0 + g * 2048 + (c >> 2) * 64 + (g & 1) * 32 + (c & 3) * 8);
  trb[1].init(lds0 + g * 2048 + (c >> 2) * 64 + ((g & 1) ^ 1) * 32 + (c & 3) * 8);
  frb.init(lds0 + l * 16);
  f32x4 acc[8][2];
  bf16x8 wf[2][4];
  auto WRD1 = [&](auto Si, auto Base, auto Dd) AI {
    constexpr int s = Si, d = Dd, o = Base + (d >> 1) * 512;
    wf[s & 1][d] = cat8(trd(trb[d & 1].template at<o>()), trd(trb[d & 1].template at<o + 256>()));
  };
  auto WRD = [&](auto Si, auto Base) AI { sfor<4>([&](auto Dd) AI { WRD1(Si, Base, Dd); }); };
#define WA(s) IC((((s) >> 3) & 3) * 32768 + (((s) >> 1) & 3) * 8192 + ((s) & 1) * 1024)
#define WB(s) IC((((s) >> 2) & 3) * 32768 + (((s) >> 1) & 1) * 8192 + ((s) & 1) * 1024)
  bf16x8 fr[4];
  auto FRD = [&](auto Ni) AI { constexpr int n = Ni; if constexpr (n < 256) fr[n & 3] = *(LAS const bf16x8*)(frb.at<((n >> 5) & 3) * 32768 + (n & 31) * 1024>()); };

  WRD(IC(0), WA(0));
  SBAR();
  bf16x8 qf[4][2];
  auto QPACK = [&](auto Ds) AI {
    constexpr int ds = Ds;
#pragma unroll
    for (int qb = 0; qb < 2; ++qb) {
      const f32x4 a = acc[2 * ds][qb] * QSCALE, b = acc[2 * ds + 1][qb] * QSCALE;
      qf[ds][qb] = __builtin_bit_cast(bf16x8, (u32x4){pk2(a[0], a[1]), pk2(a[2], a[3]), pk2(b[0], b[1]), pk2(b[2], b[3])});
      PIN(qf[ds][qb]);
    }
  };
  sfor<64>([&](auto Si) AI {
    constexpr int s = Si, x0 = (s & ~1) % XD, x1 = (s | 1) % XD;
    if constexpr (!(s & 1)) waitv<SC.younger(OX, s + 1, s - 1)>(xf[x0], xf[x1]);
    sfor<4>([&](auto Dd) AI {
      constexpr int d = Dd, dt = (s & 1) * 4 + d;
      if constexpr (s + 1 < 64) WRD1(IC(s + 1), WA(s + 1), Dd);
      else if constexpr (d < 3) FRD(Dd);
      SBAR();
      if constexpr (s < 2) { MFZ(acc[dt][0], wf[s & 1][d], xf[x0]); MFZ(acc[dt][1], wf[s & 1][d], xf[x1]); }
      else { MF(acc[dt][0], wf[s & 1][d], xf[x0]); MF(acc[dt][1], wf[s & 1][d], xf[x1]); }
      if constexpr (s == 63 && (d & 1)) { QPACK(IC(d >> 1)); SBAR(); }
    });
    SBAR();
    endslot(Si);
  });

  f32x4 sc[2][2][2], negm[2], o[2][2][2];
  u32x4 pw[16][2];
  float lsum[2] = {0.f, 0.f}, inv[2];
#define MX3(a, b, c) __builtin_fmaxf(__builtin_fmaxf((a), (b)), (c))
#define EXP2(x) __builtin_amdgcn_exp2f(x)
  const char* const od_b = ws + WS_O + ((size_t)(rb * 8) * 128 + head * 4) * 2048 + 4096;
  const unsigned od_v = (unsigned)(wid * 262144 + l * 16);
  auto SM = [&](auto Hi, auto Pi) AI {
    constexpr int H = Hi, p = Pi, kt = p >> 2, qb = (p >> 1) & 1, r = 2 * (p & 1);
    const float e0 = EXP2(sc[H & 1][kt][qb][r]), e1 = EXP2(sc[H & 1][kt][qb][r + 1]);
    lsum[qb] += e0; lsum[qb] += e1;
    unsigned w = pk2(e0, e1);
    PIN(lsum[qb]); PIN(w);
    pw[H][qb][kt * 2 + (p & 1)] = w;
  };
  sfor<128>([&](auto Ni) AI {
    constexpr int n = Ni, H = n >> 3, i = n & 7, kt = i >> 2, ds = i & 3, sn = H & 1;
    FRD(IC(n + 3));
    if constexpr (n < 2) QPACK(IC(2 + n));
    sfor<2>([&](auto Qb) AI {
      constexpr int qb = Qb;
      if constexpr (ds == 0 && H == 0) MFZ(sc[sn][kt][qb], fr[n & 3], qf[0][qb]);
      else if constexpr (ds == 0) MFC(sc[sn][kt][qb], fr[n & 3], qf[0][qb], negm[qb]);
      else MF(sc[sn][kt][qb], fr[n & 3], qf[ds][qb]);
    });
    if constexpr (n == 5 || n == 6) {
      constexpr int qb = n - 5;
      NOPS(3); PIN(sc[0][0][qb]);
      const float m = -xg_max(__builtin_fmaxf(MX3(sc[0][0][qb][0], sc[0][0][qb][1], sc[0][0][qb][2]), sc[0][0][qb][3]));
      negm[qb] = (f32x4){m, m, m, m};
      sc[0][0][qb] += negm[qb];
      PIN(negm[qb]); PIN(sc[0][0][qb]);
    }
    if constexpr (n == 9 || n == 10) { NOPS(3); PIN(sc[0][1][n - 9]); sc[0][1][n - 9] += negm[n - 9]; PIN(sc[0][1][n - 9]); }
    if constexpr (H >= 1) SM(IC(H - 1), IC(i));
    if constexpr (n == 125) { PIN(negm[0]); PIN(negm[1]); }
    SBAR();
    if constexpr ((n & 3) == 3) endslot(IC(64 + (n >> 2)));
  });
  sfor<8>([&](auto Pi) AI { if constexpr (Pi == 4) { NOPS(3); PIN(sc[1][1][0]); PIN(sc[1][1][1]); } SM(IC(15), Pi); });
  if (__builtin_expect(__any(!(lsum[0] + lsum[1] <= LBOUND)), 0)) {
    auto tile = [&](unsigned off, f32x4& s0, f32x4& s1) AI {
      bf16x8 kf[4];
      SBAR(); NOPS(7);
      sfor<4>([&](auto Ds) AI { constexpr int ds = Ds; gld16<ds * 1024, true>(kf[ds], off, kv_b); });
      waitv<0>(kf[0], kf[1]);
      PIN(kf[2]); PIN(kf[3]);
#pragma unroll
      for (int ds = 0; ds < 4; ++ds) {
        s0 = __builtin_amdgcn_mfma_f32_16x16x32_bf16(kf[ds], qf[ds][0], s0, 0, 0, 0);
        s1 = __builtin_amdgcn_mfma_f32_16x16x32_bf16(kf[ds], qf[ds][1], s1, 0, 0, 0);
      }
    };
    float m0 = -__builtin_inff(), m1 = m0;
#pragma unroll 1
    for (int kt = 0; kt < 32; ++kt) {
      f32x4 s0 = {}, s1 = {};
      tile((unsigned)(kt * 4096 + l * 16), s0, s1);
      m0 = MX3(MX3(m0, s0[0], s0[1]), s0[2], s0[3]); m1 = MX3(MX3(m1, s1[0], s1[1]), s1[2], s1[3]);
    }
    m0 = -xg_max(m0); m1 = -xg_max(m1);
    lsum[0] = 0.f; lsum[1] = 0.f;
    sfor<16>([&](auto Hi) AI {
      constexpr int H = Hi;
#pragma unroll
      for (int kt = 0; kt < 2; ++kt) {
        sc[H & 1][kt][0] = (f32x4){m0, m0, m0, m0}; sc[H & 1][kt][1] = (f32x4){m1, m1, m1, m1};
        tile((unsigned)((2 * H + kt) * 4096 + l * 16), sc[H & 1][kt][0], sc[H & 1][kt][1]);
      }
      sfor<8>([&](auto Pi) AI { SM(Hi, Pi); });
      PIN(pw[H][0]); PIN(pw[H][1]);
    });
  }
  SBAR();
  NOPS(1);
  auto OST = [&](auto Os, auto Qb) AI {
    constexpr int os = Os, qb = Qb;
    PIN(o[os & 1][0][qb]); PIN(o[os & 1][1][qb]);
    const f32x4 a = o[os & 1][0][qb] * inv[qb], b = o[os & 1][1][qb] * inv[qb];
    gst16<(os * 2 + qb) * 1024 - 4096>((u32x4){pk2(a[0], a[1]), pk2(a[2], a[3]), pk2(b[0], b[1]), pk2(b[2], b[3])}, od_v, od_b);
  };
  sfor<128>([&](auto Mi) AI {
    constexpr int m = Mi, n = 128 + m, os = m >> 5, H = (m >> 1) & 15, par = m & 1;
    FRD(IC(n + 3));
    if constexpr (H == 0) { MFZ(o[os & 1][par][0], fr[n & 3], pw[H][0]); MFZ(o[os & 1][par][1], fr[n & 3], pw[H][1]); }
    else { MF(o[os & 1][par][0], fr[n & 3], pw[H][0]); MF(o[os & 1][par][1], fr[n & 3], pw[H][1]); }
    if constexpr (m == 2 || m == 3) { inv[m - 2] = __builtin_amdgcn_rcpf(xg_sum(lsum[m - 2])); PIN(inv[m - 2]); }
    if constexpr (os >= 1 && ((m & 31) == 2 || (m & 31) == 3)) OST(IC(os - 1), IC(m & 1));
    if constexpr (m == 123) {
      trb[0].init(lds0 + kg * 16384 + g * 2048 + (c >> 2) * 64 + (g & 1) * 32 + (c & 3) * 8);
      trb[1].init(lds0 + kg * 16384 + g * 2048 + (c >> 2) * 64 + ((g & 1) ^ 1) * 32 + (c & 3) * 8);
    }
    if constexpr (m >= 124) WRD1(IC(0), WB(0), IC(m - 124));
    SBAR();
    if constexpr ((n & 3) == 3) endslot(IC(64 + (n >> 2)));
  });
  STAMP(12); STAMP(13); STAMP(14); STAMP(15);

  const int idx = SEQ - 1 - (int)(key & 2047u);
  const bool owner = (idx >> 7) == mb;
  f32x4 yp[7];
  auto item = [](int s) { return s < 48 ? s : s < 56 ? 48 + 2 * (s - 48) : 49 + 2 * (s - 56); };
  u32x2 pf;
  f32x4 giv[4];
  sfor<64>([&](auto Si) AI {
    constexpr int s = Si, u = s >> 2, it = item(s), x0 = (it & ~1) % XD2, x1 = (it | 1) % XD2;
    if constexpr (!(it & 1)) waitv<SC.younger(OX, 64 + it + 1, B0 + s - 1)>(xf[x0], xf[x1]);
    sfor<4>([&](auto Dd) AI {
      constexpr int d = Dd, dt = (it & 1) * 4 + d;
      if constexpr (s + 1 < 64) WRD1(IC(s + 1), WB(item(s + 1)), Dd);
      SBAR();
      if constexpr (s < 2) { MFZ(acc[dt][0], wf[s & 1][d], xf[x0]); MFZ(acc[dt][1], wf[s & 1][d], xf[x1]); }
      else { MF(acc[dt][0], wf[s & 1][d], xf[x0]); MF(acc[dt][1], wf[s & 1][d], xf[x1]); }
      if constexpr (s == 0 && d >= 2) OST(IC(3), IC(d - 2));
    });
    SBAR();
    if constexpr (s == 7) {
      waitv<SC.younger(OW, 7, B0 + s - 1)>();
      if (l == 0) {
        gst8((u32x2){tlo, thi}, (unsigned)(((rb * 8 + wid) * 32 + head) * 8), ws + WS_FO + 3 * 16384);
        gst8((u32x2){tlo, thi}, (unsigned)((wid * 256 + yrow) * 8), ws + WS_FY);
      }
    }
    if constexpr (s == 1 || s == 17 || s == 33) {
      constexpr int j = s == 1 ? 1 : s == 17 ? 2 : 3;
      waitv<SC.younger(OM, j + 1, B0 + s - 1)>(fg);
      if (__builtin_expect(!__all((fg[0] == tlo) & (fg[1] == thi)), 0)) poll8(fo_v, ws + WS_FO + j * 16384, tlo, thi, 0xffffffffu);
    }
    if constexpr (s == 56) {
      LAS char* dst = lds + (wid ^ 4) * 4096 + l * 16;
      if (kg) {
#pragma unroll
        for (int d = 0; d < 4; ++d) { *(LAS f32x4*)(dst + (d & 1) * 2048 + (d >> 1) * 32768) = acc[d][0]; giv[d] = acc[d][1]; }
      } else {
#pragma unroll
        for (int d = 0; d < 4; ++d) { *(LAS f32x4*)(dst + (d & 1) * 2048 + (d >> 1) * 32768) = acc[d][1]; giv[d] = acc[d][0]; }
      }
      waitbar_l<63>();
    }
    if constexpr (s == 57) {
      LAS const char* src = lds + wid * 4096 + l * 16;
      sfor<4>([&](auto Dd) AI {
        constexpr int d = Dd;
        gst16<d * 1024>(giv[d] + *(LAS const f32x4*)(src + (d & 1) * 2048 + (d >> 1) * 32768), (unsigned)(wid * 4096 + l * 16), ws + WS_XC + (size_t)(bid ^ 4) * 32768);
      });
    }
    if constexpr (s == 62) {
      waitv<0>();
      if (l == 0) gst8((u32x2){tlo, thi}, (unsigned)(((bid ^ 4) * 8 + wid) * 64), ws + WS_FX);
      gld8<0, true>(pf, (unsigned)((bid * 8 + wid) * 64), ws + WS_FX);
    }
    if constexpr (s == 24) {
      if (owner) { gld16<0, true>(fm[0], (unsigned)(nb * 2048 + (l < 56 ? l : 55) * 32), ws + WS_FY); gld16<16, true>(fm[1], (unsigned)(nb * 2048 + (l < 56 ? l : 55) * 32), ws + WS_FY); }
    }
    if constexpr (s == 42) {
      if (owner) {
        PIN(fm[0]); PIN(fm[1]);
        for (int spin = 0; spin < SPINS; ++spin) {
          const bool ok = (fm[0][0] == tlo) & (fm[0][1] == thi) & (fm[0][2] == tlo) & (fm[0][3] == thi) & (fm[1][0] == tlo) & (fm[1][1] == thi) & (fm[1][2] == tlo) & (fm[1][3] == thi);
          if (__builtin_expect(__all(ok), 1)) break;
          __builtin_amdgcn_s_sleep(1);
          gld16<0, true>(fm[0], (unsigned)(nb * 2048 + (l < 56 ? l : 55) * 32), ws + WS_FY); gld16<16, true>(fm[1], (unsigned)(nb * 2048 + (l < 56 ? l : 55) * 32), ws + WS_FY);
          waitv<0>(fm[0], fm[1]);
        }
        const unsigned yv = (unsigned)(((t >> 4) * 1024 + nb * 128 + kh * 64 + (t & 15) * 4) * 4);
        sfor<7>([&](auto I) AI { constexpr int q = I; gld16<0, true>(yp[q], yv, ws + WS_YP + (size_t)q * 32 * 4096); });
      }
    }
    if constexpr (s == 55) {
      if (owner) {
        f32x4 a = {};
#pragma unroll
        for (int q = 0; q < 7; ++q) { PIN(yp[q]); a += yp[q]; }
#pragma unroll
        for (int j = 0; j < 4; ++j) a[j] = xg_sum(a[j]);
        *(LAS f32x4*)(lds + YS + (wid * 64 + c * 4) * 4) = a;
      }
    }
    issue(IC(B0 + s));
    SBAR();
    if constexpr (((s & 3) == 2 && u < 11) || s == 44) {
      waitbar<SC.younger(OG, 64 + 4 * u + 7, B0 + s)>();
      if constexpr (u % 4 == 1) STAMP(16 + u / 4);
    }
    if constexpr (s == 48) waitbar<SC.younger(OG, 64 + 55, B0 + s)>();
    if constexpr (s == 50) waitbar<SC.younger(OG, 64 + 59, B0 + s)>();
    if constexpr (s == 52) { waitbar_l<SC.younger(OG, 64 + 63, B0 + s)>(); STAMP(19); }
  });
  STAMP(20);

  f32x4 kep[4], oth[4];
  unsigned gate[8];
  {
    unsigned xnp[8];
    PIN(st);
#pragma unroll
    for (int j = 0; j < 8; ++j) {
      PIN(xe[j >> 1]);
      const unsigned xw = xe[j >> 1][j & 1];
      const unsigned dd = pk2(bflo(xw) - st[0], bfhi(xw) - st[0]);
      xnp[j] = pk2(bflo(dd) * st[1], bfhi(dd) * st[1]);
      gate[j] = pk2(silu_f(bflo(xnp[j])), silu_f(bfhi(xnp[j])));
      PIN(xnp[j]); PIN(gate[j]);
    }
    if (owner && erow == idx) {
      const unsigned ord = key >> 11, bits = (ord & 0x8000u) ? (ord & 0x7fffu) : (~ord & 0xffffu);
      const float sg = rbf(sigmoid_f(bflo(bits)));
#pragma unroll
      for (int q = 0; q < 4; ++q) {
        f32x4 a = {};
#pragma unroll
        for (int p = 0; p < 8; ++p) a += *(LAS const f32x4*)(lds + YS + (p * 64 + q * 16 + 4 * g) * 4);
#pragma unroll
        for (int j = 0; j < 2; ++j) {
          const float lo = rbf(bflo(xnp[2 * q + j]) + rbf(rbf(a[2 * j]) * sg)), hi = rbf(bfhi(xnp[2 * q + j]) + rbf(rbf(a[2 * j + 1]) * sg));
          gate[2 * q + j] = pk2(silu_f(lo), silu_f(hi));
        }
      }
    }
  }
  waitv<0>(pf);
  if (!__all((pf[0] == tlo) & (pf[1] == thi))) poll8((unsigned)((bid * 8 + wid) * 64), ws + WS_FX, tlo, thi, 0xffffffffu);
  STAMP(21);
  sfor<4>([&](auto Dd) AI { constexpr int d = Dd; gld16<d * 1024, true>(oth[d], (unsigned)(wid * 4096 + l * 16), ws + WS_XC + (size_t)bid * 32768); });
  {
    LAS char* dst = lds + 65536 + (wid ^ 4) * 4096 + l * 16;
    LAS const char* src = lds + 65536 + wid * 4096 + l * 16;
#pragma unroll
    for (int d = 4; d < 8; ++d) { PIN(acc[d][0]); PIN(acc[d][1]); }
    if (kg) {
#pragma unroll
      for (int d = 0; d < 4; ++d) { *(LAS f32x4*)(dst + (d & 1) * 2048 + (d >> 1) * 32768) = acc[4 + d][0]; kep[d] = acc[4 + d][1]; }
    } else {
#pragma unroll
      for (int d = 0; d < 4; ++d) { *(LAS f32x4*)(dst + (d & 1) * 2048 + (d >> 1) * 32768) = acc[4 + d][1]; kep[d] = acc[4 + d][0]; }
    }
    waitbar_l<63>();
    STAMP(22);
#pragma unroll
    for (int d = 0; d < 4; ++d) kep[d] += *(LAS const f32x4*)(src + (d & 1) * 2048 + (d >> 1) * 32768);
  }
  STAMP(23);
  waitv<0>(oth[0], oth[1]);
#pragma unroll
  for (int d = 2; d < 4; ++d) PIN(oth[d]);
  STAMP(24);
  u16* op = out + (size_t)erow * DM + ecol;
#pragma unroll
  for (int d = 0; d < 4; ++d) {
    const f32x4 a = kep[d] + oth[d];
    u32x2 res;
#pragma unroll
    for (int j = 0; j < 2; ++j) {
      const unsigned sp = pk2(a[2 * j], a[2 * j + 1]), gp = gate[2 * d + j], xw = xe[d][j];
      const unsigned pr = pk2(bflo(sp) * bflo(gp), bfhi(sp) * bfhi(gp));
      res[j] = pk2(bflo(xw) + bflo(pr), bfhi(xw) + bfhi(pr));
    }
    *(u32x2*)(op + d * 16) = res;
  }
  STAMP(25);
  STAMP_OUT(0, 26);
}

extern "C" void kernel_launch(void* const* d_in, const int* in_sizes, int n_in, void* d_out, int out_size, void* d_ws, size_t ws_size,
                              hipStream_t stream) {
  k_fused<<<256, 512, 0, stream>>>((const u16*)d_in[0], (const u16*)d_in[1], (const u16*)d_in[2], (const u16*)d_in[3], (const u16*)d_in[4],
                                   (const u16*)d_in[6], (const u16*)d_in[7], (const u16*)d_in[8], (const u16*)d_in[9], (char*)d_ws, (u16*)d_out);
}
```

```cpp
#include <hip/hip_runtime.h>
#include <stdint.h>
#include <type_traits>

typedef unsigned short u16;
typedef short bf16x8 __attribute__((ext_vector_type(8)));
typedef short s16x4 __attribute__((ext_vector_type(4)));
typedef float f32x4 __attribute__((ext_vector_type(4)));
typedef float f32x2 __attribute__((ext_vector_type(2)));
typedef unsigned u32x4 __attribute__((ext_vector_type(4)));
typedef unsigned u32x2 __attribute__((ext_vector_type(2)));
typedef __bf16 bf2_t __attribute__((ext_vector_type(2)));
#define LAS __attribute__((address_space(3)))
#define DI __device__ __forceinline__
#define AI __attribute__((always_inline))
#define SBAR() __builtin_amdgcn_sched_barrier(0)
template <class T> __device__ __forceinline__ void PIN(T& x) { asm volatile("" : "+v"(x)); }
template <class B> DI void MF(f32x4& acc, const bf16x8& a, const B& b) { asm volatile("v_mfma_f32_16x16x32_bf16 %0, %1, %2, %0" : "+v"(acc) : "v"(a), "v"(b)); }
template <class B> DI void MFC(f32x4& d, const bf16x8& a, const B& b, const f32x4& c) { asm volatile("v_mfma_f32_16x16x32_bf16 %0, %1, %2, %3" : "=&v"(d) : "v"(a), "v"(b), "v"(c)); }
template <class B> DI void MFZ(f32x4& d, const bf16x8& a, const B& b) { asm volatile("v_mfma_f32_16x16x32_bf16 %0, %1, %2, 0" : "=&v"(d) : "v"(a), "v"(b)); }
#define NOPS(n) asm volatile("s_nop " #n)
#define MFMA16(a, b, c) __builtin_amdgcn_mfma_f32_16x16x16bf16_1k((a), (b), (c), 0, 0, 0)
#define IC(n) std::integral_constant<int, (n)>{}

constexpr int SEQ = 2048, DM = 1024, DH = 128, SK = 2048, WIN = 512, FF = 3584;
constexpr size_t WS_XN = 0;
constexpr size_t WS_KV = 4u << 20;
constexpr size_t WS_ST = 5u << 20;
constexpr size_t WS_EP = WS_ST + 16384;
constexpr size_t WS_G0 = WS_ST + 32768;
constexpr size_t WS_FK = WS_ST + 49152;
constexpr size_t WS_YP = WS_ST + 65536;
constexpr size_t WS_FO = 7u << 20;
constexpr size_t WS_FY = WS_FO + 65536;
constexpr size_t WS_FX = WS_FO + 131072;
constexpr size_t WS_O = 8u << 20;
constexpr size_t WS_GU = 32u << 20;
constexpr size_t WS_XC = 24u << 20;

template <int I, int N, class F> DI void sfor_impl(F&& f) {
  if constexpr (I < N) { f(std::integral_constant<int, I>{}); sfor_impl<I + 1, N>(f); }
}
template <int N, class F> DI void sfor(F&& f) { sfor_impl<0, N>(f); }

DI unsigned pk2(float a, float b) { f32x2 v = {a, b}; return __builtin_bit_cast(unsigned, __builtin_convertvector(v, bf2_t)); }
DI float rbf(float a) { return (float)(__bf16)a; }
DI float bflo(unsigned u) { return __uint_as_float(u << 16); }
DI float bfhi(unsigned u) { return __uint_as_float(u & 0xffff0000u); }
DI float sigmoid_f(float v) { return __builtin_amdgcn_rcpf(1.f + __builtin_amdgcn_exp2f(v * -1.4426950408889634f)); }
DI float sel2(bool c, float a, float b) { PIN(a); PIN(b); return c ? a : b; }
DI float silu_f(float v) { return v * sigmoid_f(v); }
template <int CTRL> DI float dppf(float x) { return __builtin_bit_cast(float, __builtin_amdgcn_mov_dpp(__builtin_bit_cast(int, x), CTRL, 0xf, 0xf, true)); }
template <int CTRL> DI unsigned dppu(unsigned x) { return (unsigned)__builtin_amdgcn_mov_dpp((int)x, CTRL, 0xf, 0xf, true); }
constexpr int XOR1 = 0xB1, XOR2 = 0x4E, XOR7 = 0x141, XOR8 = 0x128;
DI float xg_sum(float x) {
  auto s = __builtin_amdgcn_permlane16_swap(__float_as_uint(x), __float_as_uint(x), false, false);
  x = __uint_as_float(s[0]) + __uint_as_float(s[1]);
  auto t = __builtin_amdgcn_permlane32_swap(__float_as_uint(x), __float_as_uint(x), false, false);
  return __uint_as_float(t[0]) + __uint_as_float(t[1]);
}
DI float xg_max(float x) {
  auto s = __builtin_amdgcn_permlane16_swap(__float_as_uint(x), __float_as_uint(x), false, false);
  x = __builtin_fmaxf(__uint_as_float(s[0]), __uint_as_float(s[1]));
  auto t = __builtin_amdgcn_permlane32_swap(__float_as_uint(x), __float_as_uint(x), false, false);
  return __builtin_fmaxf(__uint_as_float(t[0]), __uint_as_float(t[1]));
}
DI float wave_sum(float v) { v += dppf<XOR1>(v); v += dppf<XOR2>(v); v += dppf<XOR7>(v); v += dppf<XOR8>(v); return xg_sum(v); }
DI unsigned umax(unsigned a, unsigned b) { return a > b ? a : b; }
DI unsigned wave_max_u32(unsigned v) {
  v = umax(v, dppu<XOR1>(v)); v = umax(v, dppu<XOR2>(v)); v = umax(v, dppu<XOR7>(v)); v = umax(v, dppu<XOR8>(v));
  auto s = __builtin_amdgcn_permlane16_swap(v, v, false, false); v = umax(s[0], s[1]);
  auto t = __builtin_amdgcn_permlane32_swap(v, v, false, false); v = umax(t[0], t[1]);
  return __builtin_amdgcn_readfirstlane(v);
}

template <int OFF, bool DEV = false> DI void glds(unsigned ldsaddr, unsigned voff, const void* sbase) {
  if constexpr (DEV) asm volatile("s_mov_b32 m0, %0\n\ts_nop 0\n\tglobal_load_lds_dwordx4 %1, %2 offset:%3 sc1" ::"s"(ldsaddr), "v"(voff), "s"(sbase), "i"(OFF) : "memory");
  else asm volatile("s_mov_b32 m0, %0\n\ts_nop 0\n\tglobal_load_lds_dwordx4 %1, %2 offset:%3" ::"s"(ldsaddr), "v"(voff), "s"(sbase), "i"(OFF) : "memory");
}
DI void touch(unsigned& sink, unsigned voff, const void* sbase) {
  asm volatile("global_load_dword %0, %1, %2" : "+v"(sink) : "v"(voff), "s"(sbase) : "memory");
}
template <int OFF, bool DEV = false, class T> DI void gld16(T& dst, unsigned voff, const void* sbase) {
  if constexpr (DEV) asm volatile("global_load_dwordx4 %0, %1, %2 offset:%3 sc1" : "=v"(dst) : "v"(voff), "s"(sbase), "i"(OFF) : "memory");
  else asm volatile("global_load_dwordx4 %0, %1, %2 offset:%3" : "=v"(dst) : "v"(voff), "s"(sbase), "i"(OFF) : "memory");
}
template <int OFF, bool DEV = false, class T> DI void gld8(T& dst, unsigned voff, const void* sbase) {
  if constexpr (DEV) asm volatile("global_load_dwordx2 %0, %1, %2 offset:%3 sc1" : "=v"(dst) : "v"(voff), "s"(sbase), "i"(OFF) : "memory");
  else asm volatile("global_load_dwordx2 %0, %1, %2 offset:%3" : "=v"(dst) : "v"(voff), "s"(sbase), "i"(OFF) : "memory");
}
template <int OFF> DI void gld2(unsigned& dst, unsigned voff, const void* sbase) {
  asm volatile("global_load_ushort %0, %1, %2 offset:%3" : "=v"(dst) : "v"(voff), "s"(sbase), "i"(OFF) : "memory");
}
template <int OFF, class T> DI void gst16(const T& v, unsigned voff, const void* sbase) {
  asm volatile("global_store_dwordx4 %0, %1, %2 offset:%3 sc1\n\ts_nop 1" ::"v"(voff), "v"(v), "s"(sbase), "i"(OFF) : "memory");
}
template <class T> DI void gst8(const T& v, unsigned voff, const void* sbase) {
  asm volatile("global_store_dwordx2 %0, %1, %2 sc1" ::"v"(voff), "v"(v), "s"(sbase) : "memory");
}
template <int OFF> DI void gst4(float v, unsigned voff, const void* sbase) {
  asm volatile("global_store_dword %0, %1, %2 offset:%3 sc1" ::"v"(voff), "v"(v), "s"(sbase), "i"(OFF) : "memory");
}
constexpr int SPINS = 1 << 16;
DI void poll8(unsigned voff, const void* sbase, unsigned lo, unsigned hi, unsigned lomask) {
  for (int spin = 0; spin < SPINS; ++spin) {
    u32x2 v;
    asm volatile("global_load_dwordx2 %0, %1, %2 sc1\n\ts_waitcnt vmcnt(0)" : "=&v"(v) : "v"(voff), "s"(sbase) : "memory");
    if (__all(((v[0] & lomask) == lo) & (v[1] == hi))) break;
    __builtin_amdgcn_s_sleep(1);
  }
}
template <int N> DI void waitv() { static_assert(N >= 0, "count"); asm volatile("s_waitcnt vmcnt(%0)" ::"n"(N > 63 ? 63 : N) : "memory"); }
template <int N, class T> DI void waitv(T& x) { static_assert(N >= 0, "count"); asm volatile("s_waitcnt vmcnt(%1)" : "+v"(x) : "n"(N > 63 ? 63 : N) : "memory"); }
template <int N, class T> DI void waitv(T& x, T& y) { static_assert(N >= 0, "count"); asm volatile("s_waitcnt vmcnt(%2)" : "+v"(x), "+v"(y) : "n"(N > 63 ? 63 : N) : "memory"); }
template <int N> DI void waitbar() { static_assert(N >= 0, "count"); asm volatile("s_waitcnt vmcnt(%0)\n\ts_barrier" ::"n"(N > 63 ? 63 : N) : "memory"); }
template <int N> DI void waitbar_l() { static_assert(N >= 0, "count"); asm volatile("s_waitcnt vmcnt(%0) lgkmcnt(0)\n\ts_barrier" ::"n"(N > 63 ? 63 : N) : "memory"); }
typedef short v4i16_t __attribute__((ext_vector_type(4)));
DI s16x4 trd(LAS const char* p) { return __builtin_amdgcn_ds_read_tr16_b64_v4i16((LAS v4i16_t*)p); }
struct LBase {
  unsigned lo, hi;
  DI void init(unsigned a) { lo = a; hi = a + 65536; PIN(lo); PIN(hi); }
  template <int OFF> DI LAS const char* at() const { return (LAS const char*)(uintptr_t)(OFF < 65536 ? lo : hi) + (OFF & 65535); }
};
DI bf16x8 cat8(s16x4 lo, s16x4 hi) { return __builtin_shufflevector(lo, hi, 0, 1, 2, 3, 4, 5, 6, 7); }

enum { OX, OG, OF, OM, OS, OP, OE, OW };
struct Sched {
  int n; short kind[400], idx[400], slot[400];
  constexpr Sched() : n(0), kind{}, idx{}, slot{} {}
  constexpr void add(int s, int k, int i) { kind[n] = (short)k; idx[n] = (short)i; slot[n] = (short)s; ++n; }
  constexpr int younger(int k, int i, int s) const {
    int p = -1;
    for (int q = 0; q < n; ++q) if (kind[q] == k && idx[q] == i) p = q;
    if (p < 0 || slot[p] > s) return -1;
    int c = 0;
    for (int q = p + 1; q < n; ++q) if (slot[q] <= s) ++c;
    return c;
  }
  constexpr int first(int s) const { int q = 0; while (q < n && slot[q] < s) ++q; return q; }
  constexpr int count(int s) const { int c = 0; for (int q = 0; q < n; ++q) if (slot[q] == s) ++c; return c; }
};

#define STAMP_DECL
#define STAMP(i)
#define STAMP_OUT(kid, n)

constexpr int RING = 4 * 32768, FB = RING, RED1 = FB + 24576, HSC = RED1 + 1024, YS = HSC + 1024, LDS_BYTES = FB + 32768;
constexpr int STAGE = 3 * 32768;
constexpr int XD = 16, XD2 = 16;
constexpr float LBOUND = 65536.f;
constexpr float QSCALE = 0.08838834764831845f * 1.4426950408889634f;
constexpr int B0 = 128;
constexpr int FISS[4] = {96, 97, 98, 117}, FRDS[4] = {115, 118, 121, 124};
constexpr int fpiece(int sl) { for (int p = 0; p < 4; ++p) if (FRDS[p] == sl) return p; return -1; }
constexpr Sched make_sched() {
  Sched s;
  for (int i = 0; i < 12; ++i) s.add(-2, OG, i);
  for (int i = 0; i < 16; ++i) s.add(-1, OX, i);
  for (int t = 0; t < 128; ++t) {
    if (t == 104 || t == 112 || t == 120) { s.add(t, OW, (t - 104) / 4); s.add(t, OW, (t - 104) / 4 + 1); }
    for (int p = 0; p < 24; ++p) { const int a = 3 + p, b2 = 2 * p + 1; if ((a > b2 ? a : b2) == t) { s.add(t, OX, 16 + 2 * p); s.add(t, OX, 17 + 2 * p); } }
    if (t >= 120) { s.add(t, OX, 64 + 2 * (t - 120)); s.add(t, OX, 65 + 2 * (t - 120)); }
    for (int v = 3; v < 18; ++v) for (int p = 0; p < 4; ++p) if (8 * (v - 3) + 7 + p == t) s.add(t, OG, 4 * v + p);
    if (t == 127) { s.add(t, OG, 72); s.add(t, OG, 73); }
    if (t == 20) s.add(t, OM, 0);
    if (t == 115 || t == 123) s.add(t, OM, t == 115 ? 1 : 2);
    if (t >= 49 && t < 57 && (t & 1)) s.add(t, OF, (t - 49) / 2);
    if (t >= 71 && t < 79 && (t & 1)) s.add(t, OF, 4 + (t - 71) / 2);
    if (t == 48) s.add(t, OS, 0);
    if (t == 101) s.add(t, OS, 2);
    for (int p = 0; p < 4; ++p) if (FISS[p] == t) s.add(t, OF, 8 + p);
  }
  for (int t = 0; t < 64; ++t) {
    if (t == 0) { s.add(B0, OW, 6); s.add(B0, OW, 7); s.add(B0, OG, 74); s.add(B0, OG, 75); }
    if (t == 5 || t == 20) s.add(B0 + t, OM, t == 5 ? 3 : 4);
    if (t == 52) for (int i = 0; i < 5; ++i) s.add(B0 + t, OE, i);
    if ((t & 1) && t < 48) { s.add(B0 + t, OX, 79 + t); s.add(B0 + t, OX, 80 + t); }
    for (int v = 3; v < 16; ++v) for (int p = 0; p < 4; ++p) if ((v < 14 ? 4 * (v - 3) + 3 + (p >> 1) : v == 14 ? 45 : 49) == t) s.add(B0 + t, OG, 64 + 4 * v + p);
  }
  return s;
}
constexpr Sched SC = make_sched();

__global__ __launch_bounds__(512, 2) void k_fused(const u16* __restrict__ x, const u16* __restrict__ kc, const u16* __restrict__ vc,
                                                  const u16* __restrict__ wq, const u16* __restrict__ wo, const u16* __restrict__ wr,
                                                  const u16* __restrict__ wgate, const u16* __restrict__ wup, const u16* __restrict__ wdown,
                                                  char* __restrict__ ws, u16* __restrict__ out) {
  __shared__ __attribute__((aligned(16))) char lds_raw[LDS_BYTES];
  LAS char* const lds = (LAS char*)lds_raw;
  const int t = threadIdx.x, l = t & 63, bid = blockIdx.x, c = l & 15, g = l >> 4;
  const int wid = __builtin_amdgcn_readfirstlane(t >> 6);
  const int xcd = bid & 7, within = bid >> 3, head = xcd * 4 + (within & 3), rb = within >> 2;
  const int fj = xcd * 28 + (within < 28 ? within : within - 28);
  const int kh = xcd >> 2, mb = 2 * rb + (xcd & 1), nb = ((xcd >> 1) & 1) * 4 + (within & 3);
  const int rbw = wid & 3, kg = wid >> 2;
  const unsigned lds0 = (unsigned)(uintptr_t)lds;
  STAMP_DECL
  STAMP(0);

  unsigned tlo, thi, sink = 0;
  {
    const int row = 8 * bid + wid;
    u32x4 a0, a1, w0, w1, kvo;
    u32x2 ep, klo, khi;
    unsigned e[8];
    gld16<0>(a0, (unsigned)(row * DM * 2 + l * 16), x); gld16<1024>(a1, (unsigned)(row * DM * 2 + l * 16), x);
    gld16<0>(w0, (unsigned)(l * 16), wr); gld16<1024>(w1, (unsigned)(l * 16), wr);
    gld8<0, true>(ep, 0u, ws + WS_EP);
    __builtin_amdgcn_s_barrier();
    const int fn = (bid * 4 + wid) & 255, kvh = bid >> 6;
    if (wid < 4) {
      if (fn < 128) {
        const unsigned ko = (unsigned)(((kvh * SK + (SK - WIN) + (fn >> 2) * 16 + c) * DH + (fn & 3) * 32 + 4 * g) * 2);
        gld8<0>(klo, ko, kc); gld8<32>(khi, ko, kc);
      } else {
        const int d = 32 * ((fn - 128) >> 5) + 8 * (c >> 2) + 4 * (fn & 1) + (c & 3);
        const unsigned vo = (unsigned)(((kvh * SK + (SK - WIN) + ((fn >> 1) & 15) * 32 + 4 * g + 8) * DH + d) * 2);
        sfor<8>([&](auto J) AI { constexpr int j = J; gld2<(16 * (j >> 2) + (j & 3) - 8) * DH * 2>(e[j], vo, vc); });
      }
      if (wid < 2) touch(sink, (unsigned)((wid * 256 + within * 8 + (l >> 3)) * 8192 + xcd * 1024 + (l & 7) * 128), wq);
      if (fn < 128) waitv<2>(); else waitv<8>();
    } else {
      waitv<0>();
    }
    PIN(a0); PIN(a1); PIN(w0); PIN(w1); PIN(ep);
    STAMP(1);
    {
      const unsigned long long tg = (((unsigned long long)(unsigned)__builtin_amdgcn_readfirstlane(ep[1]) << 32) | (unsigned)__builtin_amdgcn_readfirstlane(ep[0])) + 1ull;
      tlo = (unsigned)tg; thi = (unsigned)(tg >> 32);
    }
    float v[16];
#pragma unroll
    for (int j = 0; j < 4; ++j) { v[2 * j] = bflo(a0[j]); v[2 * j + 1] = bfhi(a0[j]); v[8 + 2 * j] = bflo(a1[j]); v[9 + 2 * j] = bfhi(a1[j]); }
    float s = 0.f;
#pragma unroll
    for (int j = 0; j < 16; ++j) s += v[j];
    const float mean = wave_sum(s) * (1.f / DM);
    float q = 0.f;
#pragma unroll
    for (int j = 0; j < 16; ++j) { const float d = v[j] - mean; q += d * d; }
    const float var = wave_sum(q) * (1.f / DM);
    const float mbf = rbf(mean), rden = 1.f / rbf(sqrtf(rbf(rbf(var) + 1e-5f)));
    float rr = 0.f;
    u32x4 o0, o1;
#pragma unroll
    for (int j = 0; j < 8; ++j) {
      const unsigned dd = pk2(v[2 * j] - mbf, v[2 * j + 1] - mbf);
      const unsigned nn = pk2(bflo(dd) * rden, bfhi(dd) * rden);
      const unsigned ww = j < 4 ? w0[j] : w1[j - 4];
      rr += bflo(nn) * bflo(ww); rr += bfhi(nn) * bfhi(ww);
      if (j < 4) o0[j] = nn; else o1[j - 4] = nn;
    }
    *(LAS u32x4*)(lds + STAGE + (l * 8 + (wid ^ (l & 7))) * 16) = o0;
    *(LAS u32x4*)(lds + STAGE + 8192 + (l * 8 + (wid ^ (l & 7))) * 16) = o1;
    rr = wave_sum(rr);
    if (l == 0) {
      const unsigned bits = pk2(rr, 0.f) & 0xffffu;
      const unsigned ord = (bits & 0x8000u) ? (~bits & 0xffffu) : (bits | 0x8000u);
      *(LAS unsigned*)(lds + STAGE + 16384 + wid * 4) = (ord << 11) | (unsigned)(SEQ - 1 - row);
      *(LAS f32x2*)(lds + STAGE + 16384 + 32 + wid * 8) = (f32x2){mbf, rden};
    }
    waitbar_l<63>();
    if (wid >= 4) {
#pragma unroll
      for (int hh = 0; hh < 4; ++hh) {
        const int p = hh * 256 + (t - 256), ch = p >> 3, r = 8 * bid + ((p & 7) ^ (ch & 7));
        const u32x4 o = *(LAS const u32x4*)(lds + STAGE + p * 16);
        gst16<0>(o, (unsigned)(((((r >> 5) * 32 + (ch >> 2)) * 2 + ((r >> 4) & 1)) * 64 + (ch & 3) * 16 + (r & 15)) * 16), ws + WS_XN);
      }
      if (t < 256 + 8) gst8(*(LAS const f32x2*)(lds + STAGE + 16384 + 32 + (t - 256) * 8), (unsigned)((8 * bid + t - 256) * 8), ws + WS_ST);
      waitv<0>();
    } else {
      if (wid < 2) waitv<1>(); else waitv<0>();
      if (fn < 128) {
        PIN(klo); PIN(khi);
        kvo = (u32x4){klo[0], klo[1], khi[0], khi[1]};
      } else {
#pragma unroll
        for (int j = 0; j < 8; ++j) PIN(e[j]);
        kvo = (u32x4){e[0] | (e[1] << 16), e[2] | (e[3] << 16), e[4] | (e[5] << 16), e[6] | (e[7] << 16)};
      }
      gst16<0>(kvo, (unsigned)((bid * 4 + wid) * 1024 + l * 16), ws + WS_KV);
    }
    waitbar_l<63>();
    if (t == 256) {
      const u32x4 ka = *(LAS const u32x4*)(lds + STAGE + 16384), kb = *(LAS const u32x4*)(lds + STAGE + 16400);
      const unsigned key = umax(umax(umax(ka[0], ka[1]), umax(ka[2], ka[3])), umax(umax(kb[0], kb[1]), umax(kb[2], kb[3])));
      gst8((u32x2){(tlo << 27) | key, (tlo >> 5) | (thi << 27)}, (unsigned)(bid * 8), ws + WS_G0);
    }
    STAMP(2);
  }
  const unsigned glo = tlo << 27, ghi = (tlo >> 5) | (thi << 27);

  const char* const wq_b = (const char*)wq + head * 256;
  const unsigned wq_v = (unsigned)(((8 * wid + ((l & 31) >> 2)) * 4096 + (l >> 5) * 32 + ((l & 3) ^ (2 * (wid & 1))) * 8) * 2);
  const char* const kv_b = ws + WS_KV + (size_t)(head >> 3) * 262144;
  const unsigned kv_v = (unsigned)(wid * 4096 + l * 16);
  const char* const xn_b = ws + WS_XN + (size_t)rb * 8 * 65536 + 4096;
  const unsigned xn_v = (unsigned)(wid * 65536 + l * 16);
  const char* const wo_b = (const char*)wo + (size_t)kh * 2048 * (DM * 2) + nb * 256;
  const char* const o_b = ws + WS_O + (size_t)mb * 4 * 262144 + kh * 131072;
  const unsigned ldsw = lds0 + wid * 2048, ldsk = lds0 + wid * 4096, ldsf = lds0 + FB + wid * 1024;
  const int erow = mb * 128 + rbw * 32 + kg * 16 + c, ecol = nb * 128 + kh * 64 + 4 * g;
  bf16x8 xf[XD];
  u32x4 fm[2];
  bf16x8 fxs;
  const unsigned gu_v = (unsigned)((((4 * wid + g) * 2 + ((l >> 3) & 1)) * FF + fj * 16 + (l & 7) * 2) * 8);
  u32x2 fg;
  u32x2 xe[4];
  f32x2 st;
  unsigned key = 0;

  const unsigned fo_v = (unsigned)(((mb * 4 + rbw) * 32 + kh * 16 + c) * 8);
  const int yrow = within < 28 ? fj : 224 + xcd * 4 + within - 28;
  auto issue = [&](auto Sl) AI {
    constexpr int sl = Sl, q0 = SC.first(sl), nq = SC.count(sl);
    sfor<nq>([&](auto Qi) AI {
      constexpr int k = SC.kind[q0 + Qi], i = SC.idx[q0 + Qi];
      if constexpr (k == OX && i < 64) gld16<(i & 7) * 1024 - 4096, true>(xf[i % XD], xn_v, xn_b + (size_t)(i >> 3) * 8192);
      if constexpr (k == OX && i >= 64) {
        constexpr int q = i - 64, v = q >> 2;
        gld16<(q & 1) * 1024, true>(xf[q % XD2], (unsigned)(rbw * 262144 + kg * 16384 + l * 16), o_b + (size_t)(((v & 3) * 4 + ((q >> 1) & 1)) * 8192 + (v >> 2) * 2048));
      }
      if constexpr (k == OG) {
        constexpr int v = i >> 2, p = i & 3;
        if constexpr (v < 8) {
          const char* sb = wq_b + (size_t)(v * 128 + (p >> 1) * 64) * 8192;
          glds<(p & 1) * 128>(ldsw + (v & 3) * 32768 + (p >> 1) * 16384 + (p & 1) * (1024 - 128), wq_v, sb);
        } else if constexpr (v < 16) {
          glds<p * 1024, true>(ldsk + (v & 3) * 32768, kv_v, kv_b + (size_t)(v - 8) * 32768);
        } else {
          const unsigned wo_v = (unsigned)((((wid >> 2) * 128 + (wid & 3) * 8 + ((l & 31) >> 2)) * DM + (l >> 5) * 32 + ((l & 3) ^ (2 * (wid & 1))) * 8) * 2);
          const char* sb = wo_b + (size_t)(((v & 3) * 4 + (p >> 1) * 2) * 128 + ((v - 16) >> 2) * 32) * (DM * 2) + (((p & 1) ^ kh ^ 1) << 7);
          glds<0>(ldsw + (v & 3) * 32768 + (p >> 1) * 16384 + (p & 1) * 1024, wo_v, sb);
        }
      }
      if constexpr (k == OF && i < 8)
        glds<0>(ldsf + (i & 3) * 8192, (unsigned)(((32 * wid + (l >> 3)) * FF + ((l & 7) ^ ((i & 1) * 4 + ((l >> 3) & 2))) * 8) * 2),
                (const char*)(i < 4 ? wgate : wup) + ((size_t)((fj & 3) * 256 + 8 * (i & 3)) * FF + (fj >> 2) * 64) * 2);
      if constexpr (k == OF && i >= 8) {
        const int frho = l >> 2, fch = (l & 3) ^ (frho & 4 ? 2 : 0);
        glds<0>(ldsf + ((i - 8) % 3) * 8192, (unsigned)((frho * DM + wid * 128 + fch * 8) * 2), (const char*)wdown + (size_t)fj * 16 * DM * 2 + (i - 8) * 64);
      }
      if constexpr (k == OS && i == 0) {
        const int idx = SEQ - 1 - (int)(key & 2047u);
        gld16<0, true>(fxs, (unsigned)((((idx >> 5) * 32 + (fj & 3) * 8 + wid) * 2 + ((idx >> 4) & 1)) * 1024 + (g * 16 + (idx & 15)) * 16), ws + WS_XN);
      }
      if constexpr (k == OS && i == 2) gld16<0, true>(fm[0], gu_v, ws + WS_GU);
      if constexpr (k == OM && i > 0) gld8<0, true>(fg, fo_v, ws + WS_FO + (i - 1) * 16384);
      if constexpr (k == OM && i == 0) gld8<0, true>(fg, (unsigned)((t & 255) * 8), wid < 4 ? ws + WS_G0 : ws + WS_FK + (head >> 3) * 2048);
      if constexpr (k == OE && i < 4) gld8<32 * i>(xe[i], (unsigned)((erow * DM + ecol) * 2), x);
      if constexpr (k == OE && i == 4) gld8<0, true>(st, (unsigned)(erow * 8), ws + WS_ST);
    });
  };
  f32x4 fa0, fa1, fq[4];
  s16x4 fb0, fb1, fxa;
  bf16x8 ft[2];
  auto endslot = [&](auto Sl) AI {
    constexpr int sl = Sl, u = sl >> 3, i = sl & 7;
    if constexpr (sl == 2) {
      if (wid < 4 && l == 0) gst8((u32x2){tlo, thi}, (unsigned)((bid * 4 + wid) * 8), ws + WS_FK);
    }
    if constexpr (sl == 38) {
      waitv<SC.younger(OM, 0, sl - 1)>(fg);
      const unsigned lo = wid < 4 ? glo : tlo, hi = wid < 4 ? ghi : thi, mk = wid < 4 ? 0xf8000000u : 0xffffffffu;
      for (int spin = 0; spin < SPINS; ++spin) {
        if (__builtin_expect(__all(((fg[0] & mk) == lo) & (fg[1] == hi)), 1)) break;
        __builtin_amdgcn_s_sleep(1);
        gld8<0, true>(fg, (unsigned)((t & 255) * 8), wid < 4 ? ws + WS_G0 : ws + WS_FK + (head >> 3) * 2048);
        waitv<0>(fg);
      }
      const unsigned km = wave_max_u32(fg[0] & 0x07ffffffu);
      *(LAS unsigned*)(lds + HSC + wid * 4) = km;
    }
    if constexpr (sl == 47) {
      if (bid == 0 && t == 0) gst8((u32x2){tlo, thi}, 0u, ws + WS_EP);
      const u32x4 kk = *(LAS const u32x4*)(lds + HSC);
      key = __builtin_amdgcn_readfirstlane(umax(umax(kk[0], kk[1]), umax(kk[2], kk[3])));
    }
    if constexpr (sl == 58 || sl == 61 || sl == 64 || sl == 67) {
      if (wid == 4 + (sl - 58) / 3) touch(sink, (unsigned)(((wid & 3) * 64 + l) * 128), (const char*)wdown + (size_t)fj * 16 * DM * 2);
    }
    if constexpr (sl == 80 || sl == 82 || sl == 84 || sl == 86) {
      if (wid == (sl - 80) / 2)
        touch(sink, (unsigned)((kh * 2048 + (within >> 1) * 128 + wid * 32 + ((within & 1) * 2 + (xcd & 1)) * 8 + (l >> 3)) * (DM * 2) + ((xcd >> 1) & 1) * 1024 + (l & 7) * 128), wo);
    }
    if constexpr (sl == 110) PIN(sink);
    if constexpr ((sl >= 68 && sl <= 70) || (sl >= 90 && sl <= 92)) {
      constexpr int r = sl >= 90, q = sl - (r ? 90 : 68);
      if constexpr (q == 0) waitv<SC.younger(OF, 4 * r + 3, sl - 1)>(fxs);
      if constexpr (q >= 1) {
#pragma unroll
        for (int j = 0; j < 2; ++j) { fq[2 * q - 2 + j] = __builtin_amdgcn_mfma_f32_16x16x32_bf16(fxs, ft[j], (f32x4){}, 0, 0, 0); PIN(fq[2 * q - 2 + j]); }
      }
      if constexpr (q < 2) {
        const unsigned rp = ldsf + g * 8192 + (c >> 2) * 128 + ((((g & 1) << 1) | (c >> 3)) << 5) + (c & 3) * 8;
#pragma unroll
        for (int j = 0; j < 2; ++j) { LAS const char* a = (LAS const char*)(uintptr_t)(rp ^ ((2 * q + j) << 5)); ft[j] = cat8(trd(a), trd(a + 512)); }
      }
    }
    if constexpr (sl == 71 || sl == 93) {
      const float v = sel2(g & 2, sel2(g & 1, fq[3][0], fq[2][0]), sel2(g & 1, fq[1][0], fq[0][0]));
      gst8((u32x2){__float_as_uint(v), tlo}, (unsigned)(((((fj & 3) * 8 + wid) * 2 + (sl == 93)) * FF + (fj >> 2) * 64 + l) * 8), ws + WS_GU);
    }
    if constexpr (sl == 109) {
      waitv<SC.younger(OS, 2, sl - 1)>(fm[0]);
      for (int spin = 0; spin < SPINS; ++spin) {
        if (__builtin_expect(__all((fm[0][1] == tlo) & (fm[0][3] == tlo)), 1)) break;
        __builtin_amdgcn_s_sleep(1);
        gld16<0, true>(fm[0], gu_v, ws + WS_GU);
        waitv<0>(fm[0]);
      }
      *(LAS f32x2*)(lds + RED1 + (wid * 32 + 2 * c) * 4) = (f32x2){xg_sum(__uint_as_float(fm[0][0])), xg_sum(__uint_as_float(fm[0][2]))};
    }
    if constexpr (sl == 111) {
#pragma unroll
      for (int w = 0; w < 2; ++w) { fa0[w] = *(LAS const float*)(lds + RED1 + ((2 * g + w) * 32 + c) * 4); fa0[2 + w] = *(LAS const float*)(lds + RED1 + ((2 * g + w) * 32 + 16 + c) * 4); }
    }
    if constexpr (sl == 112) { fa0[0] = xg_sum(fa0[0] + fa0[1]); fa0[2] = xg_sum(fa0[2] + fa0[3]); PIN(fa0); }
    if constexpr (sl == 113) {
      const float hh = rbf(silu_f(rbf(fa0[0]))) * rbf(fa0[2]);
      *(LAS u16*)(lds + HSC + 32 + wid * 32 + c * 2) = (u16)pk2(hh, hh);
    }
    if constexpr (sl == 114) fxa = *(LAS const s16x4*)(lds + HSC + 32 + wid * 32 + g * 8);
    constexpr int pr = fpiece(sl), pm = fpiece(sl - 1), ps = fpiece(sl - 2);
    if constexpr (ps >= 0) gst4<ps * 128>(sel2(g & 1, fa1[0], fa0[0]), (unsigned)((wid * 128 + (l & 31)) * 4), ws + WS_YP + (size_t)yrow * 4096);
    if constexpr (pm >= 0) { fa0 = MFMA16(fxa, fb0, (f32x4){}); fa1 = MFMA16(fxa, fb1, (f32x4){}); PIN(fa0); PIN(fa1); }
    if constexpr (pr >= 0) {
      waitv<SC.younger(OF, 8 + pr, sl - 1)>();
      const unsigned fp = ldsf + (4 * g + (c >> 2)) * 64 + (g & 1) * 32 + (c & 3) * 8;
      fb0 = trd((LAS const char*)(uintptr_t)fp + (pr % 3) * 8192); fb1 = trd((LAS const char*)(uintptr_t)(fp ^ 32) + (pr % 3) * 8192);
    }
    if constexpr (sl == 110 || sl == 118 || sl == 126) {
      waitv<SC.younger(OW, (sl - 110) / 4 + 1, sl - 1)>();
      if (l == 0) gst8((u32x2){tlo, thi}, (unsigned)(((rb * 8 + wid) * 32 + head) * 8), ws + WS_FO + ((sl - 110) / 8) * 16384);
    }
    if constexpr (sl == 120) {
      waitv<SC.younger(OM, 1, sl - 1)>(fg);
      if (__builtin_expect(!__all((fg[0] == tlo) & (fg[1] == thi)), 0)) poll8(fo_v, ws + WS_FO, tlo, thi, 0xffffffffu);
    }
    issue(Sl);
    SBAR();
    if constexpr (i == 6) {
      if constexpr (u == 5 || u == 13) waitbar_l<SC.younger(OG, 4 * u + 7, sl)>();
      else waitbar<SC.younger(OG, 4 * u + 7, sl)>();
      if constexpr (u == 0 || u == 1 || u == 5 || u == 7 || u == 8 || u == 10 || u == 12 || u == 14)
        STAMP(u == 0 ? 4 : u == 1 ? 5 : u == 5 ? 6 : u == 7 ? 7 : u == 8 ? 8 : u == 10 ? 9 : u == 12 ? 10 : 11);
    }
  };

  issue(IC(-2));
  if (wid == 5) poll8((unsigned)((rb * 32 + (l & 31)) * 8), ws + WS_G0, glo, ghi, 0xf8000000u);
  waitbar<SC.younger(OG, 3, -2)>();
  issue(IC(-1));
  if (wid == 2 || wid == 3) touch(sink, (unsigned)((wid * 256 + within * 8 + (l >> 3)) * 8192 + xcd * 1024 + (l & 7) * 128), wq);
  STAMP(3);

  LBase trb[2], frb;
  trb[0].init(lds0 + g * 2048 + (c >> 2) * 64 + (g & 1) * 32 + (c & 3) * 8);
  trb[1].init(lds0 + g * 2048 + (c >> 2) * 64 + ((g & 1) ^ 1) * 32 + (c & 3) * 8);
  frb.init(lds0 + l * 16);
  f32x4 acc[8][2];
  bf16x8 wf[2][4];
  auto WRD1 = [&](auto Si, auto Base, auto Dd) AI {
    constexpr int s = Si, d = Dd, o = Base + (d >> 1) * 512;
    wf[s & 1][d] = cat8(trd(trb[d & 1].template at<o>()), trd(trb[d & 1].template at<o + 256>()));
  };
  auto WRD = [&](auto Si, auto Base) AI { sfor<4>([&](auto Dd) AI { WRD1(Si, Base, Dd); }); };
#define WA(s) IC((((s) >> 3) & 3) * 32768 + (((s) >> 1) & 3) * 8192 + ((s) & 1) * 1024)
#define WB(s) IC((((s) >> 2) & 3) * 32768 + (((s) >> 1) & 1) * 8192 + ((s) & 1) * 1024)
  bf16x8 fr[4];
  auto FRD = [&](auto Ni) AI { constexpr int n = Ni; if constexpr (n < 256) fr[n & 3] = *(LAS const bf16x8*)(frb.at<((n >> 5) & 3) * 32768 + (n & 31) * 1024>()); };

  WRD(IC(0), WA(0));
  SBAR();
  bf16x8 qf[4][2];
  auto QPACK = [&](auto Ds) AI {
    constexpr int ds = Ds;
#pragma unroll
    for (int qb = 0; qb < 2; ++qb) {
      const f32x4 a = acc[2 * ds][qb] * QSCALE, b = acc[2 * ds + 1][qb] * QSCALE;
      qf[ds][qb] = __builtin_bit_cast(bf16x8, (u32x4){pk2(a[0], a[1]), pk2(a[2], a[3]), pk2(b[0], b[1]), pk2(b[2], b[3])});
      PIN(qf[ds][qb]);
    }
  };
  sfor<64>([&](auto Si) AI {
    constexpr int s = Si, x0 = (s & ~1) % XD, x1 = (s | 1) % XD;
    if constexpr (!(s & 1)) waitv<SC.younger(OX, s + 1, s - 1)>(xf[x0], xf[x1]);
    sfor<4>([&](auto Dd) AI {
      constexpr int d = Dd, dt = (s & 1) * 4 + d;
      if constexpr (s + 1 < 64) WRD1(IC(s + 1), WA(s + 1), Dd);
      else if constexpr (d < 3) FRD(Dd);
      SBAR();
      if constexpr (s < 2) { MFZ(acc[dt][0], wf[s & 1][d], xf[x0]); MFZ(acc[dt][1], wf[s & 1][d], xf[x1]); }
      else { MF(acc[dt][0], wf[s & 1][d], xf[x0]); MF(acc[dt][1], wf[s & 1][d], xf[x1]); }
      if constexpr (s == 63 && (d & 1)) { QPACK(IC(d >> 1)); SBAR(); }
    });
    SBAR();
    endslot(Si);
  });

  f32x4 sc[2][2][2], negm[2], o[2][2][2];
  u32x4 pw[16][2];
  float lsum[2] = {0.f, 0.f}, inv[2];
#define MX3(a, b, c) __builtin_fmaxf(__builtin_fmaxf((a), (b)), (c))
#define EXP2(x) __builtin_amdgcn_exp2f(x)
  const char* const od_b = ws + WS_O + ((size_t)(rb * 8) * 128 + head * 4) * 2048 + 4096;
  const unsigned od_v = (unsigned)(wid * 262144 + l * 16);
  auto SM = [&](auto Hi, auto Pi) AI {
    constexpr int H = Hi, p = Pi, kt = p >> 2, qb = (p >> 1) & 1, r = 2 * (p & 1);
    const float e0 = EXP2(sc[H & 1][kt][qb][r]), e1 = EXP2(sc[H & 1][kt][qb][r + 1]);
    lsum[qb] += e0; lsum[qb] += e1;
    unsigned w = pk2(e0, e1);
    PIN(lsum[qb]); PIN(w);
    pw[H][qb][kt * 2 + (p & 1)] = w;
  };
  sfor<128>([&](auto Ni) AI {
    constexpr int n = Ni, H = n >> 3, i = n & 7, kt = i >> 2, ds = i & 3, sn = H & 1;
    FRD(IC(n + 3));
    if constexpr (n < 2) QPACK(IC(2 + n));
    sfor<2>([&](auto Qb) AI {
      constexpr int qb = Qb;
      if constexpr (ds == 0 && H == 0) MFZ(sc[sn][kt][qb], fr[n & 3], qf[0][qb]);
      else if constexpr (ds == 0) MFC(sc[sn][kt][qb], fr[n & 3], qf[0][qb], negm[qb]);
      else MF(sc[sn][kt][qb], fr[n & 3], qf[ds][qb]);
    });
    if constexpr (n == 5 || n == 6) {
      constexpr int qb = n - 5;
      NOPS(3); PIN(sc[0][0][qb]);
      const float m = -xg_max(__builtin_fmaxf(MX3(sc[0][0][qb][0], sc[0][0][qb][1], sc[0][0][qb][2]), sc[0][0][qb][3]));
      negm[qb] = (f32x4){m, m, m, m};
      sc[0][0][qb] += negm[qb];
      PIN(negm[qb]); PIN(sc[0][0][qb]);
    }
    if constexpr (n == 9 || n == 10) { NOPS(3); PIN(sc[0][1][n - 9]); sc[0][1][n - 9] += negm[n - 9]; PIN(sc[0][1][n - 9]); }
    if constexpr (H >= 1) SM(IC(H - 1), IC(i));
    if constexpr (n == 125) { PIN(negm[0]); PIN(negm[1]); }
    SBAR();
    if constexpr ((n & 3) == 3) endslot(IC(64 + (n >> 2)));
  });
  sfor<8>([&](auto Pi) AI { if constexpr (Pi == 4) { NOPS(3); PIN(sc[1][1][0]); PIN(sc[1][1][1]); } SM(IC(15), Pi); });
  if (__builtin_expect(__any(!(lsum[0] + lsum[1] <= LBOUND)), 0)) {
    auto tile = [&](unsigned off, f32x4& s0, f32x4& s1) AI {
      bf16x8 kf[4];
      SBAR(); NOPS(7);
      sfor<4>([&](auto Ds) AI { constexpr int ds = Ds; gld16<ds * 1024, true>(kf[ds], off, kv_b); });
      waitv<0>(kf[0], kf[1]);
      PIN(kf[2]); PIN(kf[3]);
#pragma unroll
      for (int ds = 0; ds < 4; ++ds) {
        s0 = __builtin_amdgcn_mfma_f32_16x16x32_bf16(kf[ds], qf[ds][0], s0, 0, 0, 0);
        s1 = __builtin_amdgcn_mfma_f32_16x16x32_bf16(kf[ds], qf[ds][1], s1, 0, 0, 0);
      }
    };
    float m0 = -__builtin_inff(), m1 = m0;
#pragma unroll 1
    for (int kt = 0; kt < 32; ++kt) {
      f32x4 s0 = {}, s1 = {};
      tile((unsigned)(kt * 4096 + l * 16), s0, s1);
      m0 = MX3(MX3(m0, s0[0], s0[1]), s0[2], s0[3]); m1 = MX3(MX3(m1, s1[0], s1[1]), s1[2], s1[3]);
    }
    m0 = -xg_max(m0); m1 = -xg_max(m1);
    lsum[0] = 0.f; lsum[1] = 0.f;
    sfor<16>([&](auto Hi) AI {
      constexpr int H = Hi;
#pragma unroll
      for (int kt = 0; kt < 2; ++kt) {
        sc[H & 1][kt][0] = (f32x4){m0, m0, m0, m0}; sc[H & 1][kt][1] = (f32x4){m1, m1, m1, m1};
        tile((unsigned)((2 * H + kt) * 4096 + l * 16), sc[H & 1][kt][0], sc[H & 1][kt][1]);
      }
      sfor<8>([&](auto Pi) AI { SM(Hi, Pi); });
      PIN(pw[H][0]); PIN(pw[H][1]);
    });
  }
  SBAR();
  NOPS(1);
  auto OST = [&](auto Os, auto Qb) AI {
    constexpr int os = Os, qb = Qb;
    PIN(o[os & 1][0][qb]); PIN(o[os & 1][1][qb]);
    const f32x4 a = o[os & 1][0][qb] * inv[qb], b = o[os & 1][1][qb] * inv[qb];
    gst16<(os * 2 + qb) * 1024 - 4096>((u32x4){pk2(a[0], a[1]), pk2(a[2], a[3]), pk2(b[0], b[1]), pk2(b[2], b[3])}, od_v, od_b);
  };
  sfor<128>([&](auto Mi) AI {
    constexpr int m = Mi, n = 128 + m, os = m >> 5, H = (m >> 1) & 15, par = m & 1;
    FRD(IC(n + 3));
    if constexpr (H == 0) { MFZ(o[os & 1][par][0], fr[n & 3], pw[H][0]); MFZ(o[os & 1][par][1], fr[n & 3], pw[H][1]); }
    else { MF(o[os & 1][par][0], fr[n & 3], pw[H][0]); MF(o[os & 1][par][1], fr[n & 3], pw[H][1]); }
    if constexpr (m == 2 || m == 3) { inv[m - 2] = __builtin_amdgcn_rcpf(xg_sum(lsum[m - 2])); PIN(inv[m - 2]); }
    if constexpr (os >= 1 && ((m & 31) == 2 || (m & 31) == 3)) OST(IC(os - 1), IC(m & 1));
    if constexpr (m == 123) {
      trb[0].init(lds0 + kg * 16384 + g * 2048 + (c >> 2) * 64 + (g & 1) * 32 + (c & 3) * 8);
      trb[1].init(lds0 + kg * 16384 + g * 2048 + (c >> 2) * 64 + ((g & 1) ^ 1) * 32 + (c & 3) * 8);
    }
    if constexpr (m >= 124) WRD1(IC(0), WB(0), IC(m - 124));
    SBAR();
    if constexpr ((n & 3) == 3) endslot(IC(64 + (n >> 2)));
  });
  STAMP(12); STAMP(13); STAMP(14); STAMP(15);

  const int idx = SEQ - 1 - (int)(key & 2047u);
  const bool owner = (idx >> 7) == mb;
  f32x4 yp[7];
  auto item = [](int s) { return s < 48 ? s : s < 56 ? 48 + 2 * (s - 48) : 49 + 2 * (s - 56); };
  u32x2 pf;
  f32x4 giv[4];
  sfor<64>([&](auto Si) AI {
    constexpr int s = Si, u = s >> 2, it = item(s), x0 = (it & ~1) % XD2, x1 = (it | 1) % XD2;
    if constexpr (!(it & 1)) waitv<SC.younger(OX, 64 + it + 1, B0 + s - 1)>(xf[x0], xf[x1]);
    sfor<4>([&](auto Dd) AI {
      constexpr int d = Dd, dt = (it & 1) * 4 + d;
      if constexpr (s + 1 < 64) WRD1(IC(s + 1), WB(item(s + 1)), Dd);
      SBAR();
      if constexpr (s < 2) { MFZ(acc[dt][0], wf[s & 1][d], xf[x0]); MFZ(acc[dt][1], wf[s & 1][d], xf[x1]); }
      else { MF(acc[dt][0], wf[s & 1][d], xf[x0]); MF(acc[dt][1], wf[s & 1][d], xf[x1]); }
      if constexpr (s == 0 && d >= 2) OST(IC(3), IC(d - 2));
    });
    SBAR();
    if constexpr (s == 7) {
      waitv<SC.younger(OW, 7, B0 + s - 1)>();
      if (l == 0) {
        gst8((u32x2){tlo, thi}, (unsigned)(((rb * 8 + wid) * 32 + head) * 8), ws + WS_FO + 3 * 16384);
        gst8((u32x2){tlo, thi}, (unsigned)((wid * 256 + yrow) * 8), ws + WS_FY);
      }
    }
    if constexpr (s == 1 || s == 17 || s == 33) {
      constexpr int j = s == 1 ? 1 : s == 17 ? 2 : 3;
      waitv<SC.younger(OM, j + 1, B0 + s - 1)>(fg);
      if (__builtin_expect(!__all((fg[0] == tlo) & (fg[1] == thi)), 0)) poll8(fo_v, ws + WS_FO + j * 16384, tlo, thi, 0xffffffffu);
    }
    if constexpr (s == 56) {
      LAS char* dst = lds + (wid ^ 4) * 4096 + l * 16;
      if (kg) {
#pragma unroll
        for (int d = 0; d < 4; ++d) { *(LAS f32x4*)(dst + (d & 1) * 2048 + (d >> 1) * 32768) = acc[d][0]; giv[d] = acc[d][1]; }
      } else {
#pragma unroll
        for (int d = 0; d < 4; ++d) { *(LAS f32x4*)(dst + (d & 1) * 2048 + (d >> 1) * 32768) = acc[d][1]; giv[d] = acc[d][0]; }
      }
      waitbar_l<63>();
    }
    if constexpr (s == 57) {
      LAS const char* src = lds + wid * 4096 + l * 16;
      sfor<4>([&](auto Dd) AI {
        constexpr int d = Dd;
        gst16<d * 1024>(giv[d] + *(LAS const f32x4*)(src + (d & 1) * 2048 + (d >> 1) * 32768), (unsigned)(wid * 4096 + l * 16), ws + WS_XC + (size_t)(bid ^ 4) * 32768);
      });
    }
    if constexpr (s == 62) {
      waitv<0>();
      if (l == 0) gst8((u32x2){tlo, thi}, (unsigned)(((bid ^ 4) * 8 + wid) * 64), ws + WS_FX);
      gld8<0, true>(pf, (unsigned)((bid * 8 + wid) * 64), ws + WS_FX);
    }
    if constexpr (s == 24) {
      if (owner) { gld16<0, true>(fm[0], (unsigned)(nb * 2048 + (l < 56 ? l : 55) * 32), ws + WS_FY); gld16<16, true>(fm[1], (unsigned)(nb * 2048 + (l < 56 ? l : 55) * 32), ws + WS_FY); }
    }
    if constexpr (s == 42) {
      if (owner) {
        PIN(fm[0]); PIN(fm[1]);
        for (int spin = 0; spin < SPINS; ++spin) {
          const bool ok = (fm[0][0] == tlo) & (fm[0][1] == thi) & (fm[0][2] == tlo) & (fm[0][3] == thi) & (fm[1][0] == tlo) & (fm[1][1] == thi) & (fm[1][2] == tlo) & (fm[1][3] == thi);
          if (__builtin_expect(__all(ok), 1)) break;
          __builtin_amdgcn_s_sleep(1);
          gld16<0, true>(fm[0], (unsigned)(nb * 2048 + (l < 56 ? l : 55) * 32), ws + WS_FY); gld16<16, true>(fm[1], (unsigned)(nb * 2048 + (l < 56 ? l : 55) * 32), ws + WS_FY);
          waitv<0>(fm[0], fm[1]);
        }
        const unsigned yv = (unsigned)(((t >> 4) * 1024 + nb * 128 + kh * 64 + (t & 15) * 4) * 4);
        sfor<7>([&](auto I) AI { constexpr int q = I; gld16<0, true>(yp[q], yv, ws + WS_YP + (size_t)q * 32 * 4096); });
      }
    }
    if constexpr (s == 55) {
      if (owner) {
        f32x4 a = {};
#pragma unroll
        for (int q = 0; q < 7; ++q) { PIN(yp[q]); a += yp[q]; }
#pragma unroll
        for (int j = 0; j < 4; ++j) a[j] = xg_sum(a[j]);
        *(LAS f32x4*)(lds + YS + (wid * 64 + c * 4) * 4) = a;
      }
    }
    issue(IC(B0 + s));
    SBAR();
    if constexpr (((s & 3) == 2 && u < 11) || s == 44) {
      waitbar<SC.younger(OG, 64 + 4 * u + 7, B0 + s)>();
      if constexpr (u % 4 == 1) STAMP(16 + u / 4);
    }
    if constexpr (s == 48) waitbar<SC.younger(OG, 64 + 55, B0 + s)>();
    if constexpr (s == 50) waitbar<SC.younger(OG, 64 + 59, B0 + s)>();
    if constexpr (s == 52) { waitbar_l<SC.younger(OG, 64 + 63, B0 + s)>(); STAMP(19); }
  });
  STAMP(20);

  f32x4 kep[4], oth[4];
  unsigned gate[8];
  {
    unsigned xnp[8];
    PIN(st);
#pragma unroll
    for (int j = 0; j < 8; ++j) {
      PIN(xe[j >> 1]);
      const unsigned xw = xe[j >> 1][j & 1];
      const unsigned dd = pk2(bflo(xw) - st[0], bfhi(xw) - st[0]);
      xnp[j] = pk2(bflo(dd) * st[1], bfhi(dd) * st[1]);
      gate[j] = pk2(silu_f(bflo(xnp[j])), silu_f(bfhi(xnp[j])));
      PIN(xnp[j]); PIN(gate[j]);
    }
    if (owner && erow == idx) {
      const unsigned ord = key >> 11, bits = (ord & 0x8000u) ? (ord & 0x7fffu) : (~ord & 0xffffu);
      const float sg = rbf(sigmoid_f(bflo(bits)));
#pragma unroll
      for (int q = 0; q < 4; ++q) {
        f32x4 a = {};
#pragma unroll
        for (int p = 0; p < 8; ++p) a += *(LAS const f32x4*)(lds + YS + (p * 64 + q * 16 + 4 * g) * 4);
#pragma unroll
        for (int j = 0; j < 2; ++j) {
          const float lo = rbf(bflo(xnp[2 * q + j]) + rbf(rbf(a[2 * j]) * sg)), hi = rbf(bfhi(xnp[2 * q + j]) + rbf(rbf(a[2 * j + 1]) * sg));
          gate[2 * q + j] = pk2(silu_f(lo), silu_f(hi));
        }
      }
    }
  }
  waitv<0>(pf);
  if (!__all((pf[0] == tlo) & (pf[1] == thi))) poll8((unsigned)((bid * 8 + wid) * 64), ws + WS_FX, tlo, thi, 0xffffffffu);
  STAMP(21);
  sfor<4>([&](auto Dd) AI { constexpr int d = Dd; gld16<d * 1024, true>(oth[d], (unsigned)(wid * 4096 + l * 16), ws + WS_XC + (size_t)bid * 32768); });
  {
    LAS char* dst = lds + 65536 + (wid ^ 4) * 4096 + l * 16;
    LAS const char* src = lds + 65536 + wid * 4096 + l * 16;
#pragma unroll
    for (int d = 4; d < 8; ++d) { PIN(acc[d][0]); PIN(acc[d][1]); }
    if (kg) {
#pragma unroll
      for (int d = 0; d < 4; ++d) { *(LAS f32x4*)(dst + (d & 1) * 2048 + (d >> 1) * 32768) = acc[4 + d][0]; kep[d] = acc[4 + d][1]; }
    } else {
#pragma unroll
      for (int d = 0; d < 4; ++d) { *(LAS f32x4*)(dst + (d & 1) * 2048 + (d >> 1) * 32768) = acc[4 + d][1]; kep[d] = acc[4 + d][0]; }
    }
    waitbar_l<63>();
    STAMP(22);
#pragma unroll
    for (int d = 0; d < 4; ++d) kep[d] += *(LAS const f32x4*)(src + (d & 1) * 2048 + (d >> 1) * 32768);
  }
  STAMP(23);
  waitv<0>(oth[0], oth[1]);
#pragma unroll
  for (int d = 2; d < 4; ++d) PIN(oth[d]);
  STAMP(24);
  u16* op = out + (size_t)erow * DM + ecol;
#pragma unroll
  for (int d = 0; d < 4; ++d) {
    const f32x4 a = kep[d] + oth[d];
    u32x2 res;
#pragma unroll
    for (int j = 0; j < 2; ++j) {
      const unsigned sp = pk2(a[2 * j], a[2 * j + 1]), gp = gate[2 * d + j], xw = xe[d][j];
      const unsigned pr = pk2(bflo(sp) * bflo(gp), bfhi(sp) * bfhi(gp));
      res[j] = pk2(bflo(xw) + bflo(pr), bfhi(xw) + bfhi(pr));
    }
    *(u32x2*)(op + d * 16) = res;
  }
  STAMP(25);
  STAMP_OUT(0, 26);
}

extern "C" void kernel_launch(void* const* d_in, const int* in_sizes, int n_in, void* d_out, int out_size, void* d_ws, size_t ws_size,
                              hipStream_t stream) {
  k_fused<<<256, 512, 0, stream>>>((const u16*)d_in[0], (const u16*)d_in[1], (const u16*)d_in[2], (const u16*)d_in[3], (const u16*)d_in[4],
                                   (const u16*)d_in[6], (const u16*)d_in[7], (const u16*)d_in[8], (const u16*)d_in[9], (char*)d_ws, (u16*)d_out);
}
```

```cpp
#include <hip/hip_runtime.h>
#include <stdint.h>
#include <type_traits>

typedef unsigned short u16;
typedef short bf16x8 __attribute__((ext_vector_type(8)));
typedef short s16x4 __attribute__((ext_vector_type(4)));
typedef float f32x4 __attribute__((ext_vector_type(4)));
typedef float f32x2 __attribute__((ext_vector_type(2)));
typedef unsigned u32x4 __attribute__((ext_vector_type(4)));
typedef unsigned u32x2 __attribute__((ext_vector_type(2)));
typedef __bf16 bf2_t __attribute__((ext_vector_type(2)));
#define LAS __attribute__((address_space(3)))
#define DI __device__ __forceinline__
#define AI __attribute__((always_inline))
#define SBAR() __builtin_amdgcn_sched_barrier(0)
template <class T> __device__ __forceinline__ void PIN(T& x) { asm volatile("" : "+v"(x)); }
template <class B> DI void MF(f32x4& acc, const bf16x8& a, const B& b) { asm volatile("v_mfma_f32_16x16x32_bf16 %0, %1, %2, %0" : "+v"(acc) : "v"(a), "v"(b)); }
template <class B> DI void MFC(f32x4& d, const bf16x8& a, const B& b, const f32x4& c) { asm volatile("v_mfma_f32_16x16x32_bf16 %0, %1, %2, %3" : "=&v"(d) : "v"(a), "v"(b), "v"(c)); }
template <class B> DI void MFZ(f32x4& d, const bf16x8& a, const B& b) { asm volatile("v_mfma_f32_16x16x32_bf16 %0, %1, %2, 0" : "=&v"(d) : "v"(a), "v"(b)); }
#define NOPS(n) asm volatile("s_nop " #n)
#define MFMA16(a, b, c) __builtin_amdgcn_mfma_f32_16x16x16bf16_1k((a), (b), (c), 0, 0, 0)
#define IC(n) std::integral_constant<int, (n)>{}

constexpr int SEQ = 2048, DM = 1024, DH = 128, SK = 2048, WIN = 512, FF = 3584;
constexpr size_t WS_XN = 0;
constexpr size_t WS_KV = 4u << 20;
constexpr size_t WS_ST = 5u << 20;
constexpr size_t WS_EP = WS_ST + 16384;
constexpr size_t WS_G0 = WS_ST + 32768;
constexpr size_t WS_FK = WS_ST + 49152;
constexpr size_t WS_YP = WS_ST + 65536;
constexpr size_t WS_FO = 7u << 20;
constexpr size_t WS_FY = WS_FO + 65536;
constexpr size_t WS_FX = WS_FO + 131072;
constexpr size_t WS_O = 8u << 20;
constexpr size_t WS_GU = 32u << 20;
constexpr size_t WS_XC = 24u << 20;

template <int I, int N, class F> DI void sfor_impl(F&& f) {
  if constexpr (I < N) { f(std::integral_constant<int, I>{}); sfor_impl<I + 1, N>(f); }
}
template <int N, class F> DI void sfor(F&& f) { sfor_impl<0, N>(f); }

DI unsigned pk2(float a, float b) { f32x2 v = {a, b}; return __builtin_bit_cast(unsigned, __builtin_convertvector(v, bf2_t)); }
DI float rbf(float a) { return (float)(__bf16)a; }
DI float bflo(unsigned u) { return __uint_as_float(u << 16); }
DI float bfhi(unsigned u) { return __uint_as_float(u & 0xffff0000u); }
DI float sigmoid_f(float v) { return __builtin_amdgcn_rcpf(1.f + __builtin_amdgcn_exp2f(v * -1.4426950408889634f)); }
DI float sel2(bool c, float a, float b) { PIN(a); PIN(b); return c ? a : b; }
DI float silu_f(float v) { return v * sigmoid_f(v); }
template <int CTRL> DI float dppf(float x) { return __builtin_bit_cast(float, __builtin_amdgcn_mov_dpp(__builtin_bit_cast(int, x), CTRL, 0xf, 0xf, true)); }
template <int CTRL> DI unsigned dppu(unsigned x) { return (unsigned)__builtin_amdgcn_mov_dpp((int)x, CTRL, 0xf, 0xf, true); }
constexpr int XOR1 = 0xB1, XOR2 = 0x4E, XOR7 = 0x141, XOR8 = 0x128;
DI float xg_sum(float x) {
  auto s = __builtin_amdgcn_permlane16_swap(__float_as_uint(x), __float_as_uint(x), false, false);
  x = __uint_as_float(s[0]) + __uint_as_float(s[1]);
  auto t = __builtin_amdgcn_permlane32_swap(__float_as_uint(x), __float_as_uint(x), false, false);
  return __uint_as_float(t[0]) + __uint_as_float(t[1]);
}
DI float xg_max(float x) {
  auto s = __builtin_amdgcn_permlane16_swap(__float_as_uint(x), __float_as_uint(x), false, false);
  x = __builtin_fmaxf(__uint_as_float(s[0]), __uint_as_float(s[1]));
  auto t = __builtin_amdgcn_permlane32_swap(__float_as_uint(x), __float_as_uint(x), false, false);
  return __builtin_fmaxf(__uint_as_float(t[0]), __uint_as_float(t[1]));
}
DI float wave_sum(float v) { v += dppf<XOR1>(v); v += dppf<XOR2>(v); v += dppf<XOR7>(v); v += dppf<XOR8>(v); return xg_sum(v); }
DI unsigned umax(unsigned a, unsigned b) { return a > b ? a : b; }
DI unsigned wave_max_u32(unsigned v) {
  v = umax(v, dppu<XOR1>(v)); v = umax(v, dppu<XOR2>(v)); v = umax(v, dppu<XOR7>(v)); v = umax(v, dppu<XOR8>(v));
  auto s = __builtin_amdgcn_permlane16_swap(v, v, false, false); v = umax(s[0], s[1]);
  auto t = __builtin_amdgcn_permlane32_swap(v, v, false, false); v = umax(t[0], t[1]);
  return __builtin_amdgcn_readfirstlane(v);
}

template <int OFF, bool DEV = false> DI void glds(unsigned ldsaddr, unsigned voff, const void* sbase) {
  if constexpr (DEV) asm volatile("s_mov_b32 m0, %0\n\ts_nop 0\n\tglobal_load_lds_dwordx4 %1, %2 offset:%3 sc1" ::"s"(ldsaddr), "v"(voff), "s"(sbase), "i"(OFF) : "memory");
  else asm volatile("s_mov_b32 m0, %0\n\ts_nop 0\n\tglobal_load_lds_dwordx4 %1, %2 offset:%3" ::"s"(ldsaddr), "v"(voff), "s"(sbase), "i"(OFF) : "memory");
}
DI void touch(unsigned& sink, unsigned voff, const void* sbase) {
  asm volatile("global_load_dword %0, %1, %2" : "+v"(sink) : "v"(voff), "s"(sbase) : "memory");
}
template <int OFF, bool DEV = false, class T> DI void gld16(T& dst, unsigned voff, const void* sbase) {
  if constexpr (DEV) asm volatile("global_load_dwordx4 %0, %1, %2 offset:%3 sc1" : "=v"(dst) : "v"(voff), "s"(sbase), "i"(OFF) : "memory");
  else asm volatile("global_load_dwordx4 %0, %1, %2 offset:%3" : "=v"(dst) : "v"(voff), "s"(sbase), "i"(OFF) : "memory");
}
template <int OFF, bool DEV = false, class T> DI void gld8(T& dst, unsigned voff, const void* sbase) {
  if constexpr (DEV) asm volatile("global_load_dwordx2 %0, %1, %2 offset:%3 sc1" : "=v"(dst) : "v"(voff), "s"(sbase), "i"(OFF) : "memory");
  else asm volatile("global_load_dwordx2 %0, %1, %2 offset:%3" : "=v"(dst) : "v"(voff), "s"(sbase), "i"(OFF) : "memory");
}
template <int OFF> DI void gld2(unsigned& dst, unsigned voff, const void* sbase) {
  asm volatile("global_load_ushort %0, %1, %2 offset:%3" : "=v"(dst) : "v"(voff), "s"(sbase), "i"(OFF) : "memory");
}
template <int OFF, class T> DI void gst16(const T& v, unsigned voff, const void* sbase) {
  asm volatile("global_store_dwordx4 %0, %1, %2 offset:%3 sc1\n\ts_nop 1" ::"v"(voff), "v"(v), "s"(sbase), "i"(OFF) : "memory");
}
template <class T> DI void gst8(const T& v, unsigned voff, const void* sbase) {
  asm volatile("global_store_dwordx2 %0, %1, %2 sc1" ::"v"(voff), "v"(v), "s"(sbase) : "memory");
}
template <int OFF> DI void gst4(float v, unsigned voff, const void* sbase) {
  asm volatile("global_store_dword %0, %1, %2 offset:%3 sc1" ::"v"(voff), "v"(v), "s"(sbase), "i"(OFF) : "memory");
}
constexpr int SPINS = 1 << 16;
DI void poll8(unsigned voff, const void* sbase, unsigned lo, unsigned hi, unsigned lomask) {
  for (int spin = 0; spin < SPINS; ++spin) {
    u32x2 v;
    asm volatile("global_load_dwordx2 %0, %1, %2 sc1\n\ts_waitcnt vmcnt(0)" : "=&v"(v) : "v"(voff), "s"(sbase) : "memory");
    if (__all(((v[0] & lomask) == lo) & (v[1] == hi))) break;
    __builtin_amdgcn_s_sleep(1);
  }
}
template <int N> DI void waitv() { static_assert(N >= 0, "count"); asm volatile("s_waitcnt vmcnt(%0)" ::"n"(N > 63 ? 63 : N) : "memory"); }
template <int N, class T> DI void waitv(T& x) { static_assert(N >= 0, "count"); asm volatile("s_waitcnt vmcnt(%1)" : "+v"(x) : "n"(N > 63 ? 63 : N) : "memory"); }
template <int N, class T> DI void waitv(T& x, T& y) { static_assert(N >= 0, "count"); asm volatile("s_waitcnt vmcnt(%2)" : "+v"(x), "+v"(y) : "n"(N > 63 ? 63 : N) : "memory"); }
template <int N> DI void waitbar() { static_assert(N >= 0, "count"); asm volatile("s_waitcnt vmcnt(%0)\n\ts_barrier" ::"n"(N > 63 ? 63 : N) : "memory"); }
template <int N> DI void waitbar_l() { static_assert(N >= 0, "count"); asm volatile("s_waitcnt vmcnt(%0) lgkmcnt(0)\n\ts_barrier" ::"n"(N > 63 ? 63 : N) : "memory"); }
typedef short v4i16_t __attribute__((ext_vector_type(4)));
DI s16x4 trd(LAS const char* p) { return __builtin_amdgcn_ds_read_tr16_b64_v4i16((LAS v4i16_t*)p); }
struct LBase {
  unsigned lo, hi;
  DI void init(unsigned a) { lo = a; hi = a + 65536; PIN(lo); PIN(hi); }
  template <int OFF> DI LAS const char* at() const { return (LAS const char*)(uintptr_t)(OFF < 65536 ? lo : hi) + (OFF & 65535); }
};
DI bf16x8 cat8(s16x4 lo, s16x4 hi) { return __builtin_shufflevector(lo, hi, 0, 1, 2, 3, 4, 5, 6, 7); }

enum { OX, OG, OF, OM, OS, OP, OE, OW };
struct Sched {
  int n; short kind[400], idx[400], slot[400];
  constexpr Sched() : n(0), kind{}, idx{}, slot{} {}
  constexpr void add(int s, int k, int i) { kind[n] = (short)k; idx[n] = (short)i; slot[n] = (short)s; ++n; }
  constexpr int younger(int k, int i, int s) const {
    int p = -1;
    for (int q = 0; q < n; ++q) if (kind[q] == k && idx[q] == i) p = q;
    if (p < 0 || slot[p] > s) return -1;
    int c = 0;
    for (int q = p + 1; q < n; ++q) if (slot[q] <= s) ++c;
    return c;
  }
  constexpr int first(int s) const { int q = 0; while (q < n && slot[q] < s) ++q; return q; }
  constexpr int count(int s) const { int c = 0; for (int q = 0; q < n; ++q) if (slot[q] == s) ++c; return c; }
};

#define STAMP_DECL
#define STAMP(i)
#define STAMP_OUT(kid, n)

constexpr int RING = 4 * 32768, FB = RING, RED1 = FB + 24576, HSC = RED1 + 1024, YS = HSC + 1024, LDS_BYTES = FB + 32768;
constexpr int STAGE = 3 * 32768;
constexpr int XD = 16, XD2 = 16;
constexpr float LBOUND = 65536.f;
constexpr float QSCALE = 0.08838834764831845f * 1.4426950408889634f;
constexpr int B0 = 128;
constexpr int FISS[4] = {96, 97, 98, 117}, FRDS[4] = {115, 118, 121, 124};
constexpr int fpiece(int sl) { for (int p = 0; p < 4; ++p) if (FRDS[p] == sl) return p; return -1; }
constexpr Sched make_sched() {
  Sched s;
  for (int i = 0; i < 12; ++i) s.add(-2, OG, i);
  for (int i = 0; i < 16; ++i) s.add(-1, OX, i);
  for (int t = 0; t < 128; ++t) {
    if (t >= 2 && t < 50) s.add(t, OX, t + 14);
    for (int v = 3; v < 18; ++v) for (int p = 0; p < 4; ++p) if (8 * (v - 3) + 7 + p == t) s.add(t, OG, 4 * v + p);
    if (t == 105 || t == 113 || t == 121) { s.add(t, OW, (t - 105) / 4); s.add(t, OW, (t - 105) / 4 + 1); }
    if (t >= 124) for (int i = 0; i < 3; ++i) s.add(t, OX, 64 + 3 * (t - 124) + i);
    if (t == 127) { s.add(t, OG, 72); s.add(t, OG, 73); }
    if (t == 20) s.add(t, OM, 0);
    if (t == 120) s.add(t, OM, 1);
    if (t >= 49 && t < 57 && (t & 1)) s.add(t, OF, (t - 49) / 2);
    if (t >= 71 && t < 79 && (t & 1)) s.add(t, OF, 4 + (t - 71) / 2);
    if (t == 48) s.add(t, OS, 0);
    if (t == 104) s.add(t, OS, 2);
    for (int p = 0; p < 4; ++p) if (FISS[p] == t) s.add(t, OF, 8 + p);
  }
  for (int t = 0; t < 64; ++t) {
    if (t == 0) { s.add(B0, OM, 2); s.add(B0, OG, 74); s.add(B0, OG, 75); s.add(B0, OW, 6); s.add(B0, OW, 7); }
    if (t == 1 || t == 2) { s.add(B0 + t, OX, 74 + 2 * t); s.add(B0 + t, OX, 75 + 2 * t); }
    if (t == 5 || t == 6 || t == 9) s.add(B0 + t, OX, 64 + (t == 5 ? 16 : t == 6 ? 18 : 22));
    if (t >= 5 && t < 50) s.add(B0 + t, OX, 64 + (t == 5 ? 17 : t < 9 ? t + 13 : t + 14));
    for (int v = 3; v < 16; ++v) for (int p = 0; p < 4; ++p) if ((v < 14 ? 4 * (v - 3) + 3 + (p >> 1) : v == 14 ? 45 + (p >> 1) : 49) == t) s.add(B0 + t, OG, 64 + 4 * v + p);
    if (t == 10 || t == 26) s.add(B0 + t, OM, t == 10 ? 3 : 4);
    if (t == 29 || t == 30 || t == 33 || t == 34 || t == 37) s.add(B0 + t, OE, t < 31 ? t - 29 : t < 35 ? t - 31 : 4);
  }
  return s;
}
constexpr Sched SC = make_sched();

__global__ __launch_bounds__(512, 2) void k_fused(const u16* __restrict__ x, const u16* __restrict__ kc, const u16* __restrict__ vc,
                                                  const u16* __restrict__ wq, const u16* __restrict__ wo, const u16* __restrict__ wr,
                                                  const u16* __restrict__ wgate, const u16* __restrict__ wup, const u16* __restrict__ wdown,
                                                  char* __restrict__ ws, u16* __restrict__ out) {
  __shared__ __attribute__((aligned(16))) char lds_raw[LDS_BYTES];
  LAS char* const lds = (LAS char*)lds_raw;
  const int t = threadIdx.x, l = t & 63, bid = blockIdx.x, c = l & 15, g = l >> 4;
  const int wid = __builtin_amdgcn_readfirstlane(t >> 6);
  const int xcd = bid & 7, within = bid >> 3, head = xcd * 4 + (within & 3), rb = within >> 2;
  const int fj = xcd * 28 + (within < 28 ? within : within - 28);
  const int fsl = fj / 7, fgr = fj - fsl * 7;
  const int kh = xcd >> 2, mb = 2 * rb + (xcd & 1), nb = ((xcd >> 1) & 1) * 4 + (within & 3);
  const int rbw = wid & 3, kg = wid >> 2;
  const unsigned lds0 = (unsigned)(uintptr_t)lds;
  STAMP_DECL
  STAMP(0);

  unsigned tlo, thi, sink = 0;
  {
    const int row = 8 * bid + wid;
    u32x4 a0, a1, w0, w1, kvo;
    u32x2 ep, klo, khi;
    unsigned e[8];
    gld16<0>(a0, (unsigned)(row * DM * 2 + l * 16), x); gld16<1024>(a1, (unsigned)(row * DM * 2 + l * 16), x);
    gld16<0>(w0, (unsigned)(l * 16), wr); gld16<1024>(w1, (unsigned)(l * 16), wr);
    gld8<0, true>(ep, 0u, ws + WS_EP);
    __builtin_amdgcn_s_barrier();
    const int fn = (bid * 4 + wid) & 255, kvh = bid >> 6;
    if (wid < 4) {
      if (fn < 128) {
        const unsigned ko = (unsigned)(((kvh * SK + (SK - WIN) + (fn >> 2) * 16 + c) * DH + (fn & 3) * 32 + 4 * g) * 2);
        gld8<0>(klo, ko, kc); gld8<32>(khi, ko, kc);
      } else {
        const int d = 32 * ((fn - 128) >> 5) + 8 * (c >> 2) + 4 * (fn & 1) + (c & 3);
        const unsigned vo = (unsigned)(((kvh * SK + (SK - WIN) + ((fn >> 1) & 15) * 32 + 4 * g + 8) * DH + d) * 2);
        sfor<8>([&](auto J) AI { constexpr int j = J; gld2<(16 * (j >> 2) + (j & 3) - 8) * DH * 2>(e[j], vo, vc); });
      }
      if (wid < 2) touch(sink, (unsigned)((wid * 256 + within * 8 + (l >> 3)) * 8192 + xcd * 1024 + (l & 7) * 128), wq);
      if (fn < 128) waitv<5>(); else waitv<11>();
    } else {
      waitv<3>();
    }
    PIN(a0); PIN(a1);
    STAMP(1);
    float v[16];
#pragma unroll
    for (int j = 0; j < 4; ++j) { v[2 * j] = bflo(a0[j]); v[2 * j + 1] = bfhi(a0[j]); v[8 + 2 * j] = bflo(a1[j]); v[9 + 2 * j] = bfhi(a1[j]); }
    float s = 0.f;
#pragma unroll
    for (int j = 0; j < 16; ++j) s += v[j];
    const float mean = wave_sum(s) * (1.f / DM);
    float q = 0.f;
#pragma unroll
    for (int j = 0; j < 16; ++j) { const float d = v[j] - mean; q += d * d; }
    const float var = wave_sum(q) * (1.f / DM);
    const float mbf = rbf(mean), rden = 1.f / rbf(sqrtf(rbf(rbf(var) + 1e-5f)));
    if (wid >= 4) waitv<0>(); else if (fn < 128) waitv<2>(); else waitv<8>();
    PIN(w0); PIN(w1); PIN(ep);
    {
      const unsigned long long tg = (((unsigned long long)(unsigned)__builtin_amdgcn_readfirstlane(ep[1]) << 32) | (unsigned)__builtin_amdgcn_readfirstlane(ep[0])) + 1ull;
      tlo = (unsigned)tg; thi = (unsigned)(tg >> 32);
    }
    float rr = 0.f;
    u32x4 o0, o1;
#pragma unroll
    for (int j = 0; j < 8; ++j) {
      const unsigned dd = pk2(v[2 * j] - mbf, v[2 * j + 1] - mbf);
      const unsigned nn = pk2(bflo(dd) * rden, bfhi(dd) * rden);
      const unsigned ww = j < 4 ? w0[j] : w1[j - 4];
      rr += bflo(nn) * bflo(ww); rr += bfhi(nn) * bfhi(ww);
      if (j < 4) o0[j] = nn; else o1[j - 4] = nn;
    }
    *(LAS u32x4*)(lds + STAGE + (l * 8 + (wid ^ (l & 7))) * 16) = o0;
    *(LAS u32x4*)(lds + STAGE + 8192 + (l * 8 + (wid ^ (l & 7))) * 16) = o1;
    rr = wave_sum(rr);
    if (l == 0) {
      const unsigned bits = pk2(rr, 0.f) & 0xffffu;
      const unsigned ord = (bits & 0x8000u) ? (~bits & 0xffffu) : (bits | 0x8000u);
      *(LAS unsigned*)(lds + STAGE + 16384 + wid * 4) = (ord << 11) | (unsigned)(SEQ - 1 - row);
      *(LAS f32x2*)(lds + STAGE + 16384 + 32 + wid * 8) = (f32x2){mbf, rden};
    }
    waitbar_l<63>();
    if (wid >= 4) {
#pragma unroll
      for (int hh = 0; hh < 4; ++hh) {
        const int p = hh * 256 + (t - 256), ch = p >> 3, r = 8 * bid + ((p & 7) ^ (ch & 7));
        const u32x4 o = *(LAS const u32x4*)(lds + STAGE + p * 16);
        gst16<0>(o, (unsigned)(((((r >> 5) * 32 + (ch >> 2)) * 2 + ((r >> 4) & 1)) * 64 + (ch & 3) * 16 + (r & 15)) * 16), ws + WS_XN);
      }
      if (t < 256 + 8) gst8(*(LAS const f32x2*)(lds + STAGE + 16384 + 32 + (t - 256) * 8), (unsigned)((8 * bid + t - 256) * 8), ws + WS_ST);
      waitv<0>();
    } else {
      if (wid < 2) waitv<1>(); else waitv<0>();
      if (fn < 128) {
        PIN(klo); PIN(khi);
        kvo = (u32x4){klo[0], klo[1], khi[0], khi[1]};
      } else {
#pragma unroll
        for (int j = 0; j < 8; ++j) PIN(e[j]);
        kvo = (u32x4){e[0] | (e[1] << 16), e[2] | (e[3] << 16), e[4] | (e[5] << 16), e[6] | (e[7] << 16)};
      }
      gst16<0>(kvo, (unsigned)((bid * 4 + wid) * 1024 + l * 16), ws + WS_KV);
    }
    waitbar_l<63>();
    if (t == 256) {
      const u32x4 ka = *(LAS const u32x4*)(lds + STAGE + 16384), kb = *(LAS const u32x4*)(lds + STAGE + 16400);
      const unsigned key = umax(umax(umax(ka[0], ka[1]), umax(ka[2], ka[3])), umax(umax(kb[0], kb[1]), umax(kb[2], kb[3])));
      gst8((u32x2){(tlo << 27) | key, (tlo >> 5) | (thi << 27)}, (unsigned)(bid * 8), ws + WS_G0);
    }
    STAMP(2);
  }
  const unsigned glo = tlo << 27, ghi = (tlo >> 5) | (thi << 27);

  const char* const wq_b = (const char*)wq + head * 256;
  const unsigned wq_v = (unsigned)(((8 * wid + ((l & 31) >> 2)) * 4096 + (l >> 5) * 32 + ((l & 3) ^ (2 * (wid & 1))) * 8) * 2);
  const char* const kv_b = ws + WS_KV + (size_t)(head >> 3) * 262144;
  const unsigned kv_v = (unsigned)(wid * 4096 + l * 16);
  const char* const xn_b = ws + WS_XN + (size_t)rb * 8 * 65536 + 4096;
  const unsigned xn_v = (unsigned)(wid * 65536 + l * 16);
  const char* const wo_b = (const char*)wo + (size_t)kh * 2048 * (DM * 2) + nb * 256;
  const char* const o_b = ws + WS_O + (size_t)mb * 4 * 262144 + kh * 131072;
  const unsigned ldsw = lds0 + wid * 2048, ldsk = lds0 + wid * 4096, ldsf = lds0 + FB + wid * 1024;
  const int erow = mb * 128 + rbw * 32 + kg * 16 + c, ecol = nb * 128 + kh * 64 + 4 * g;
  bf16x8 xf[XD];
  u32x4 fm[2];
  bf16x8 fxs;
  const unsigned gu_v = (unsigned)((((4 * wid + g) * 2 + ((l >> 3) & 1)) * FF + fj * 16 + (l & 7) * 2) * 8);
  u32x2 fg;
  u32x2 xe[4];
  f32x2 st;
  unsigned key = 0;

  const unsigned fo_v = (unsigned)(((mb * 4 + rbw) * 32 + kh * 16 + c) * 8);
  const int yrow = within < 28 ? fj : 224 + xcd * 4 + within - 28;
  f32x4 o[2][2][2];
  float inv[2];
  const char* const od_b = ws + WS_O + ((size_t)(rb * 8) * 128 + head * 4) * 2048 + 4096;
  auto issue = [&](auto Sl, auto Pos) AI {
    constexpr int sl = Sl, pos = Pos, cnt = SC.count(sl), q0 = SC.first(sl) + (pos < 0 ? 0 : pos);
    constexpr int nq = pos < 0 ? cnt : pos >= cnt ? 0 : pos < 3 ? 1 : cnt - 3;
    sfor<nq>([&](auto Qi) AI {
      constexpr int k = SC.kind[q0 + Qi], i = SC.idx[q0 + Qi];
      if constexpr (k == OX && i < 64) gld16<(i & 7) * 1024 - 4096, true>(xf[i % XD], xn_v, xn_b + (size_t)(i >> 3) * 8192);
      if constexpr (k == OX && i >= 64) {
        constexpr int q = i - 64, v = q >> 2;
        gld16<0, true>(xf[q % XD2], (unsigned)(rbw * 262144 + kg * 16384 + ((q & 1) ^ kg) * 1024 + l * 16), o_b + (size_t)(((v & 3) * 4 + ((q >> 1) & 1)) * 8192 + (v >> 2) * 2048));
      }
      if constexpr (k == OG) {
        constexpr int v = i >> 2, p = i & 3;
        if constexpr (v < 8) {
          const char* sb = wq_b + (size_t)(v * 128 + (p >> 1) * 64) * 8192;
          glds<(p & 1) * 128>(ldsw + (v & 3) * 32768 + (p >> 1) * 16384 + (p & 1) * (1024 - 128), wq_v, sb);
        } else if constexpr (v < 16) {
          glds<p * 1024, true>(ldsk + (v & 3) * 32768, kv_v, kv_b + (size_t)(v - 8) * 32768);
        } else {
          const unsigned wo_v = (unsigned)((((wid >> 2) * 128 + (wid & 3) * 8 + ((l & 31) >> 2)) * DM + (l >> 5) * 32 + ((l & 3) ^ (2 * (wid & 1))) * 8) * 2);
          const char* sb = wo_b + (size_t)(((v & 3) * 4 + (p >> 1) * 2) * 128 + ((v - 16) >> 2) * 32) * (DM * 2) + (((p & 1) ^ kh ^ 1) << 7);
          glds<0>(ldsw + (v & 3) * 32768 + (p >> 1) * 16384 + (p & 1) * 1024, wo_v, sb);
        }
      }
      if constexpr (k == OF && i < 8)
        glds<0>(ldsf + (i & 3) * 8192, (unsigned)(((l >> 3) * FF + wid * 64 + ((l & 7) ^ ((i & 1) * 4 + ((l >> 3) & 2))) * 8) * 2),
                (const char*)(i < 4 ? wgate : wup) + ((size_t)(fsl * 32 + 8 * (i & 3)) * FF + fgr * 512) * 2);
      if constexpr (k == OF && i >= 8) {
        const int frho = l >> 2, fch = (l & 3) ^ (frho & 4 ? 2 : 0);
        glds<0>(ldsf + ((i - 8) % 3) * 8192, (unsigned)((frho * DM + wid * 128 + fch * 8) * 2), (const char*)wdown + (size_t)fj * 16 * DM * 2 + (i - 8) * 64);
      }
      if constexpr (k == OS && i == 0) {
        const int idx = SEQ - 1 - (int)(key & 2047u);
        gld16<0, true>(fxs, (unsigned)((((idx >> 5) * 32 + fsl) * 2 + ((idx >> 4) & 1)) * 1024 + (g * 16 + (idx & 15)) * 16), ws + WS_XN);
      }
      if constexpr (k == OS && i == 2) gld16<0, true>(fm[0], gu_v, ws + WS_GU);
      if constexpr (k == OM && i > 0) gld8<0, true>(fg, fo_v, ws + WS_FO + (i - 1) * 16384);
      if constexpr (k == OM && i == 0) gld8<0, true>(fg, (unsigned)((t & 255) * 8), wid < 4 ? ws + WS_G0 : ws + WS_FK + (head >> 3) * 2048);
      if constexpr (k == OW) {
        constexpr int os = i >> 1, qb = i & 1;
        PIN(o[os & 1][0][qb]); PIN(o[os & 1][1][qb]);
        const f32x4 a = o[os & 1][0][qb] * inv[qb], b = o[os & 1][1][qb] * inv[qb];
        gst16<i * 1024 - 4096>((u32x4){pk2(a[0], a[1]), pk2(a[2], a[3]), pk2(b[0], b[1]), pk2(b[2], b[3])}, (unsigned)(wid * 262144 + l * 16), od_b);
      }
      if constexpr (k == OE && i < 4) gld8<32 * i>(xe[i], (unsigned)((erow * DM + ecol) * 2), x);
      if constexpr (k == OE && i == 4) gld8<0, true>(st, (unsigned)(erow * 8), ws + WS_ST);
    });
  };
  f32x4 fa0, fa1, fq[4];
  s16x4 fb0, fb1, fxa;
  bf16x8 ft[2];
  auto endslot = [&](auto Sl) AI {
    constexpr int sl = Sl, u = sl >> 3, i = sl & 7;
    if constexpr (sl == 3) {
      if (wid == 6 || wid == 7) touch(sink, (unsigned)(((wid - 4) * 256 + within * 8 + (l >> 3)) * 8192 + xcd * 1024 + (l & 7) * 128), wq);
    }
    if constexpr (sl == 2) {
      if (wid < 4 && l == 0) gst8((u32x2){tlo, thi}, (unsigned)((bid * 4 + wid) * 8), ws + WS_FK);
    }
    if constexpr (sl == 38) {
      waitv<SC.younger(OM, 0, sl)>(fg);
      const unsigned lo = wid < 4 ? glo : tlo, hi = wid < 4 ? ghi : thi, mk = wid < 4 ? 0xf8000000u : 0xffffffffu;
      for (int spin = 0; spin < SPINS; ++spin) {
        if (__builtin_expect(__all(((fg[0] & mk) == lo) & (fg[1] == hi)), 1)) break;
        __builtin_amdgcn_s_sleep(1);
        gld8<0, true>(fg, (unsigned)((t & 255) * 8), wid < 4 ? ws + WS_G0 : ws + WS_FK + (head >> 3) * 2048);
        waitv<0>(fg);
      }
      const unsigned km = wave_max_u32(fg[0] & 0x07ffffffu);
      *(LAS unsigned*)(lds + HSC + wid * 4) = km;
    }
    if constexpr (sl == 47) {
      if (bid == 0 && t == 0) gst8((u32x2){tlo, thi}, 0u, ws + WS_EP);
      const u32x4 kk = *(LAS const u32x4*)(lds + HSC);
      key = __builtin_amdgcn_readfirstlane(umax(umax(kk[0], kk[1]), umax(kk[2], kk[3])));
    }
    if constexpr (sl == 58 || sl == 61 || sl == 64 || sl == 67) {
      if (wid == 4 + (sl - 58) / 3) touch(sink, (unsigned)(((wid & 3) * 64 + l) * 128), (const char*)wdown + (size_t)fj * 16 * DM * 2);
    }
    if constexpr (sl == 80 || sl == 82 || sl == 84 || sl == 86) {
      if (wid == (sl - 80) / 2)
        touch(sink, (unsigned)((kh * 2048 + (within >> 1) * 128 + wid * 32 + ((within & 1) * 2 + (xcd & 1)) * 8 + (l >> 3)) * (DM * 2) + ((xcd >> 1) & 1) * 1024 + (l & 7) * 128), wo);
    }
    if constexpr (sl == 110) PIN(sink);
    if constexpr ((sl >= 68 && sl <= 70) || (sl >= 90 && sl <= 92)) {
      constexpr int r = sl >= 90, q = sl - (r ? 90 : 68);
      if constexpr (q == 0) waitv<SC.younger(OF, 4 * r + 3, sl)>(fxs);
      if constexpr (q >= 1) {
#pragma unroll
        for (int j = 0; j < 2; ++j) { fq[2 * q - 2 + j] = __builtin_amdgcn_mfma_f32_16x16x32_bf16(fxs, ft[j], (f32x4){}, 0, 0, 0); PIN(fq[2 * q - 2 + j]); }
      }
      if constexpr (q < 2) {
        const unsigned rp = ldsf + g * 8192 + (c >> 2) * 128 + ((((g & 1) << 1) | (c >> 3)) << 5) + (c & 3) * 8;
#pragma unroll
        for (int j = 0; j < 2; ++j) { LAS const char* a = (LAS const char*)(uintptr_t)(rp ^ ((2 * q + j) << 5)); ft[j] = cat8(trd(a), trd(a + 512)); }
      }
    }
    if constexpr (sl == 71 || sl == 93) {
      const float v = sel2(g & 2, sel2(g & 1, fq[3][0], fq[2][0]), sel2(g & 1, fq[1][0], fq[0][0]));
      gst8((u32x2){__float_as_uint(v), tlo}, (unsigned)(((fsl * 2 + (sl == 93)) * FF + fgr * 512 + t) * 8), ws + WS_GU);
    }
    if constexpr (sl == 109) {
      waitv<SC.younger(OS, 2, sl)>(fm[0]);
      for (int spin = 0; spin < SPINS; ++spin) {
        if (__builtin_expect(__all((fm[0][1] == tlo) & (fm[0][3] == tlo)), 1)) break;
        __builtin_amdgcn_s_sleep(1);
        gld16<0, true>(fm[0], gu_v, ws + WS_GU);
        waitv<0>(fm[0]);
      }
      *(LAS f32x2*)(lds + RED1 + (wid * 32 + 2 * c) * 4) = (f32x2){xg_sum(__uint_as_float(fm[0][0])), xg_sum(__uint_as_float(fm[0][2]))};
    }
    if constexpr (sl == 111) {
#pragma unroll
      for (int w = 0; w < 2; ++w) { fa0[w] = *(LAS const float*)(lds + RED1 + ((2 * g + w) * 32 + c) * 4); fa0[2 + w] = *(LAS const float*)(lds + RED1 + ((2 * g + w) * 32 + 16 + c) * 4); }
    }
    if constexpr (sl == 112) { fa0[0] = xg_sum(fa0[0] + fa0[1]); fa0[2] = xg_sum(fa0[2] + fa0[3]); PIN(fa0); }
    if constexpr (sl == 113) {
      const float hh = rbf(silu_f(rbf(fa0[0]))) * rbf(fa0[2]);
      *(LAS u16*)(lds + HSC + 32 + wid * 32 + c * 2) = (u16)pk2(hh, hh);
    }
    if constexpr (sl == 114) fxa = *(LAS const s16x4*)(lds + HSC + 32 + wid * 32 + g * 8);
    constexpr int pr = fpiece(sl), pm = fpiece(sl - 1), ps = fpiece(sl - 2);
    if constexpr (ps >= 0) gst4<ps * 128>(sel2(g & 1, fa1[0], fa0[0]), (unsigned)((wid * 128 + (l & 31)) * 4), ws + WS_YP + (size_t)yrow * 4096);
    if constexpr (pm >= 0) { fa0 = MFMA16(fxa, fb0, (f32x4){}); fa1 = MFMA16(fxa, fb1, (f32x4){}); PIN(fa0); PIN(fa1); }
    if constexpr (pr >= 0) {
      waitv<SC.younger(OF, 8 + pr, sl)>();
      const unsigned fp = ldsf + (4 * g + (c >> 2)) * 64 + (g & 1) * 32 + (c & 3) * 8;
      fb0 = trd((LAS const char*)(uintptr_t)fp + (pr % 3) * 8192); fb1 = trd((LAS const char*)(uintptr_t)(fp ^ 32) + (pr % 3) * 8192);
    }
    if constexpr (sl == 111 || sl == 119 || sl == 127) {
      waitv<SC.younger(OW, (sl - 111) / 4 + 1, sl)>();
      if (l == 0) gst8((u32x2){tlo, thi}, (unsigned)(((rb * 8 + wid) * 32 + head) * 8), ws + WS_FO + ((sl - 111) / 8) * 16384);
    }
    if constexpr (sl == 123) {
      waitv<SC.younger(OM, 1, sl)>(fg);
      if (__builtin_expect(!__all((fg[0] == tlo) & (fg[1] == thi)), 0)) poll8(fo_v, ws + WS_FO, tlo, thi, 0xffffffffu);
    }
    SBAR();
    if constexpr (i == 6) {
      if constexpr (u == 5 || u == 13) waitbar_l<SC.younger(OG, 4 * u + 7, sl)>();
      else waitbar<SC.younger(OG, 4 * u + 7, sl)>();
      if constexpr (u == 0 || u == 1 || u == 5 || u == 7 || u == 8 || u == 10 || u == 12 || u == 14)
        STAMP(u == 0 ? 4 : u == 1 ? 5 : u == 5 ? 6 : u == 7 ? 7 : u == 8 ? 8 : u == 10 ? 9 : u == 12 ? 10 : 11);
    }
  };

  issue(IC(-2), IC(-1));
  if (wid == 5) poll8((unsigned)((rb * 32 + (l & 31)) * 8), ws + WS_G0, glo, ghi, 0xf8000000u);
  waitbar<SC.younger(OG, 3, -2)>();
  issue(IC(-1), IC(-1));
  STAMP(3);

  LBase trb[2], frb;
  trb[0].init(lds0 + g * 2048 + (c >> 2) * 64 + (g & 1) * 32 + (c & 3) * 8);
  trb[1].init(lds0 + g * 2048 + (c >> 2) * 64 + ((g & 1) ^ 1) * 32 + (c & 3) * 8);
  frb.init(lds0 + l * 16);
  f32x4 acc[8][2];
  bf16x8 wf[2][4];
  auto WRD1 = [&](auto Si, auto Base, auto Dd) AI {
    constexpr int s = Si, d = Dd, o = Base + (d >> 1) * 512;
    wf[s & 1][d] = cat8(trd(trb[d & 1].template at<o>()), trd(trb[d & 1].template at<o + 256>()));
  };
  auto WRD = [&](auto Si, auto Base) AI { sfor<4>([&](auto Dd) AI { WRD1(Si, Base, Dd); }); };
#define WA(s) IC((((s) >> 3) & 3) * 32768 + (((s) >> 1) & 3) * 8192 + ((s) & 1) * 1024)
#define WB(s) IC((((s) >> 2) & 3) * 32768 + (((s) >> 1) & 1) * 8192 + ((s) & 1) * 1024)
  bf16x8 fr[4];
  auto FRD = [&](auto Ni) AI { constexpr int n = Ni; if constexpr (n < 256) fr[n & 3] = *(LAS const bf16x8*)(frb.at<((n >> 5) & 3) * 32768 + (n & 31) * 1024>()); };

  WRD(IC(0), WA(0));
  SBAR();
  bf16x8 qf[4][2];
  auto QPACK = [&](auto Ds) AI {
    constexpr int ds = Ds;
#pragma unroll
    for (int qb = 0; qb < 2; ++qb) {
      const f32x4 a = acc[2 * ds][qb] * QSCALE, b = acc[2 * ds + 1][qb] * QSCALE;
      qf[ds][qb] = __builtin_bit_cast(bf16x8, (u32x4){pk2(a[0], a[1]), pk2(a[2], a[3]), pk2(b[0], b[1]), pk2(b[2], b[3])});
      PIN(qf[ds][qb]);
    }
  };
  sfor<64>([&](auto Si) AI {
    constexpr int s = Si, x0 = (s & ~1) % XD, x1 = (s | 1) % XD;
    if constexpr (!(s & 1)) waitv<SC.younger(OX, s + 1, s - 1)>(xf[x0], xf[x1]);
    sfor<4>([&](auto Dd) AI {
      constexpr int d = Dd, dt = (s & 1) * 4 + d;
      if constexpr (s + 1 < 64) WRD1(IC(s + 1), WA(s + 1), Dd);
      else if constexpr (d < 3) FRD(Dd);
      SBAR();
      if constexpr (s < 2) { MFZ(acc[dt][0], wf[s & 1][d], xf[x0]); MFZ(acc[dt][1], wf[s & 1][d], xf[x1]); }
      else { MF(acc[dt][0], wf[s & 1][d], xf[x0]); MF(acc[dt][1], wf[s & 1][d], xf[x1]); }
      issue(Si, Dd);
      if constexpr (s == 63 && (d & 1)) { QPACK(IC(d >> 1)); SBAR(); }
    });
    SBAR();
    endslot(Si);
  });

  f32x4 sc[2][2][2], negm[2];
  u32x4 pw[16][2];
  float lsum[2] = {0.f, 0.f};
#define MX3(a, b, c) __builtin_fmaxf(__builtin_fmaxf((a), (b)), (c))
#define EXP2(x) __builtin_amdgcn_exp2f(x)
  auto SM = [&](auto Hi, auto Pi) AI {
    constexpr int H = Hi, p = Pi, kt = p >> 2, qb = (p >> 1) & 1, r = 2 * (p & 1);
    const float e0 = EXP2(sc[H & 1][kt][qb][r]), e1 = EXP2(sc[H & 1][kt][qb][r + 1]);
    lsum[qb] += e0; lsum[qb] += e1;
    unsigned w = pk2(e0, e1);
    PIN(lsum[qb]); PIN(w);
    pw[H][qb][kt * 2 + (p & 1)] = w;
  };
  sfor<128>([&](auto Ni) AI {
    constexpr int n = Ni, H = n >> 3, i = n & 7, kt = i >> 2, ds = i & 3, sn = H & 1;
    FRD(IC(n + 3));
    if constexpr (n < 2) QPACK(IC(2 + n));
    sfor<2>([&](auto Qb) AI {
      constexpr int qb = Qb;
      if constexpr (ds == 0 && H == 0) MFZ(sc[sn][kt][qb], fr[n & 3], qf[0][qb]);
      else if constexpr (ds == 0) MFC(sc[sn][kt][qb], fr[n & 3], qf[0][qb], negm[qb]);
      else MF(sc[sn][kt][qb], fr[n & 3], qf[ds][qb]);
    });
    if constexpr (n == 5 || n == 6) {
      constexpr int qb = n - 5;
      NOPS(3); PIN(sc[0][0][qb]);
      const float m = -xg_max(__builtin_fmaxf(MX3(sc[0][0][qb][0], sc[0][0][qb][1], sc[0][0][qb][2]), sc[0][0][qb][3]));
      negm[qb] = (f32x4){m, m, m, m};
      sc[0][0][qb] += negm[qb];
      PIN(negm[qb]); PIN(sc[0][0][qb]);
    }
    if constexpr (n == 9 || n == 10) { NOPS(3); PIN(sc[0][1][n - 9]); sc[0][1][n - 9] += negm[n - 9]; PIN(sc[0][1][n - 9]); }
    issue(IC(64 + (n >> 2)), IC(n & 3));
    if constexpr (H >= 1) SM(IC(H - 1), IC(i));
    if constexpr (n == 125) { PIN(negm[0]); PIN(negm[1]); }
    SBAR();
    if constexpr ((n & 3) == 3) endslot(IC(64 + (n >> 2)));
  });
  sfor<8>([&](auto Pi) AI { if constexpr (Pi == 4) { NOPS(3); PIN(sc[1][1][0]); PIN(sc[1][1][1]); } SM(IC(15), Pi); });
  if (__builtin_expect(__any(!(lsum[0] + lsum[1] <= LBOUND)), 0)) {
    auto tile = [&](unsigned off, f32x4& s0, f32x4& s1) AI {
      bf16x8 kf[4];
      SBAR(); NOPS(7);
      sfor<4>([&](auto Ds) AI { constexpr int ds = Ds; gld16<ds * 1024, true>(kf[ds], off, kv_b); });
      waitv<0>(kf[0], kf[1]);
      PIN(kf[2]); PIN(kf[3]);
#pragma unroll
      for (int ds = 0; ds < 4; ++ds) {
        s0 = __builtin_amdgcn_mfma_f32_16x16x32_bf16(kf[ds], qf[ds][0], s0, 0, 0, 0);
        s1 = __builtin_amdgcn_mfma_f32_16x16x32_bf16(kf[ds], qf[ds][1], s1, 0, 0, 0);
      }
    };
    float m0 = -__builtin_inff(), m1 = m0;
#pragma unroll 1
    for (int kt = 0; kt < 32; ++kt) {
      f32x4 s0 = {}, s1 = {};
      tile((unsigned)(kt * 4096 + l * 16), s0, s1);
      m0 = MX3(MX3(m0, s0[0], s0[1]), s0[2], s0[3]); m1 = MX3(MX3(m1, s1[0], s1[1]), s1[2], s1[3]);
    }
    m0 = -xg_max(m0); m1 = -xg_max(m1);
    lsum[0] = 0.f; lsum[1] = 0.f;
    sfor<16>([&](auto Hi) AI {
      constexpr int H = Hi;
#pragma unroll
      for (int kt = 0; kt < 2; ++kt) {
        sc[H & 1][kt][0] = (f32x4){m0, m0, m0, m0}; sc[H & 1][kt][1] = (f32x4){m1, m1, m1, m1};
        tile((unsigned)((2 * H + kt) * 4096 + l * 16), sc[H & 1][kt][0], sc[H & 1][kt][1]);
      }
      sfor<8>([&](auto Pi) AI { SM(Hi, Pi); });
      PIN(pw[H][0]); PIN(pw[H][1]);
    });
  }
  SBAR();
  NOPS(1);
  sfor<128>([&](auto Mi) AI {
    constexpr int m = Mi, n = 128 + m, os = m >> 5, H = (m >> 1) & 15, par = m & 1;
    FRD(IC(n + 3));
    if constexpr (H == 0) { MFZ(o[os & 1][par][0], fr[n & 3], pw[H][0]); MFZ(o[os & 1][par][1], fr[n & 3], pw[H][1]); }
    else { MF(o[os & 1][par][0], fr[n & 3], pw[H][0]); MF(o[os & 1][par][1], fr[n & 3], pw[H][1]); }
    if constexpr (m == 2 || m == 3) { inv[m - 2] = __builtin_amdgcn_rcpf(xg_sum(lsum[m - 2])); PIN(inv[m - 2]); }
    issue(IC(64 + (n >> 2)), IC(n & 3));
    if constexpr (m == 123) {
      trb[0].init(lds0 + kg * 16384 + g * 2048 + (c >> 2) * 64 + (g & 1) * 32 + (c & 3) * 8);
      trb[1].init(lds0 + kg * 16384 + g * 2048 + (c >> 2) * 64 + ((g & 1) ^ 1) * 32 + (c & 3) * 8);
    }
    if constexpr (m >= 124) WRD1(IC(0), WB(0), IC(m - 124));
    SBAR();
    if constexpr ((n & 3) == 3) endslot(IC(64 + (n >> 2)));
  });
  STAMP(12); STAMP(13); STAMP(14); STAMP(15);

  const int idx = SEQ - 1 - (int)(key & 2047u);
  const bool owner = (idx >> 7) == mb, ownw = (idx >> 4) == mb * 8 + rbw * 2 + kg;
  f32x4 yp[7], ya[4], oth[4];
  unsigned gate[8];
  auto item = [](int s) { return s < 48 ? s : s < 56 ? 48 + 2 * (s - 48) : 49 + 2 * (s - 56); };
  u32x2 pf;
  sfor<64>([&](auto Si) AI {
    constexpr int s = Si, u = s >> 2, it = item(s), x0 = (it & ~1) % XD2, x1 = (it | 1) % XD2;
    if constexpr (!(it & 1)) waitv<SC.younger(OX, 64 + it + 1, B0 + s - 1)>(xf[x0], xf[x1]);
    sfor<4>([&](auto Dd) AI {
      constexpr int d = Dd, dt = (it & 1) * 4 + d;
      if constexpr (s + 1 < 64) WRD1(IC(s + 1), WB(item(s + 1)), Dd);
      SBAR();
      if constexpr (s < 2) { MFZ(acc[dt][0], wf[s & 1][d], xf[x0]); MFZ(acc[dt][1], wf[s & 1][d], xf[x1]); }
      else { MF(acc[dt][0], wf[s & 1][d], xf[x0]); MF(acc[dt][1], wf[s & 1][d], xf[x1]); }
      issue(IC(B0 + s), Dd);
      if constexpr (s == 56) *(LAS f32x4*)(lds + (wid ^ 4) * 4096 + l * 16 + (d & 1) * 2048 + (d >> 1) * 32768) = acc[d][1];
      if constexpr (s == 57 && d == 0) {
#pragma unroll
        for (int e = 0; e < 4; ++e) oth[e] = *(LAS const f32x4*)(lds + wid * 4096 + l * 16 + (e & 1) * 2048 + (e >> 1) * 32768);
      }
      if constexpr (s == 57 && d >= 2) {
        gst16<(2 * d - 4) * 1024>(acc[2 * d - 4][0] + oth[2 * d - 4], (unsigned)(wid * 4096 + l * 16), ws + WS_XC + (size_t)(bid ^ 4) * 32768);
        gst16<(2 * d - 3) * 1024>(acc[2 * d - 3][0] + oth[2 * d - 3], (unsigned)(wid * 4096 + l * 16), ws + WS_XC + (size_t)(bid ^ 4) * 32768);
      }
      if constexpr (s == 54 || s == 55) {
        constexpr int j = (s - 54) * 4 + d;
        PIN(xe[j >> 1]); PIN(st);
        const unsigned xw = xe[j >> 1][j & 1];
        const unsigned dd = pk2(bflo(xw) - st[0], bfhi(xw) - st[0]);
        gate[j] = pk2(bflo(dd) * st[1], bfhi(dd) * st[1]);
        PIN(gate[j]);
      }
      if constexpr (s >= 58 && s < 62 && (d & 1)) {
        constexpr int j = (s - 58) * 2 + (d >> 1);
        gate[j] = pk2(silu_f(bflo(gate[j])), silu_f(bfhi(gate[j])));
        PIN(gate[j]);
      }
    });
    SBAR();
    if constexpr (s == 7) {
      waitv<SC.younger(OW, 7, B0 + s)>();
      if (l == 0) {
        gst8((u32x2){tlo, thi}, (unsigned)(((rb * 8 + wid) * 32 + head) * 8), ws + WS_FO + 3 * 16384);
        gst8((u32x2){tlo, thi}, (unsigned)((wid * 256 + yrow) * 8), ws + WS_FY);
      }
    }
    if constexpr (s == 4 || s == 17 || s == 33) {
      constexpr int j = s == 4 ? 1 : s == 17 ? 2 : 3;
      waitv<SC.younger(OM, j + 1, B0 + s)>(fg);
      if (__builtin_expect(!__all((fg[0] == tlo) & (fg[1] == thi)), 0)) poll8(fo_v, ws + WS_FO + j * 16384, tlo, thi, 0xffffffffu);
    }
    if constexpr (s == 60) {
      waitv<0>();
      if (l == 0) gst8((u32x2){tlo, thi}, (unsigned)(((bid ^ 4) * 8 + wid) * 64), ws + WS_FX);
    }
    if constexpr (s == 61) gld8<0, true>(pf, (unsigned)((bid * 8 + wid) * 64), ws + WS_FX);
    if constexpr (s == 19) {
      if (owner) { gld16<0, true>(fm[0], (unsigned)(nb * 2048 + (l < 56 ? l : 55) * 32), ws + WS_FY); gld16<16, true>(fm[1], (unsigned)(nb * 2048 + (l < 56 ? l : 55) * 32), ws + WS_FY); }
    }
    if constexpr (s == 34) {
      if (owner) {
        PIN(fm[0]); PIN(fm[1]);
        for (int spin = 0; spin < SPINS; ++spin) {
          const bool ok = (fm[0][0] == tlo) & (fm[0][1] == thi) & (fm[0][2] == tlo) & (fm[0][3] == thi) & (fm[1][0] == tlo) & (fm[1][1] == thi) & (fm[1][2] == tlo) & (fm[1][3] == thi);
          if (__builtin_expect(__all(ok), 1)) break;
          __builtin_amdgcn_s_sleep(1);
          gld16<0, true>(fm[0], (unsigned)(nb * 2048 + (l < 56 ? l : 55) * 32), ws + WS_FY); gld16<16, true>(fm[1], (unsigned)(nb * 2048 + (l < 56 ? l : 55) * 32), ws + WS_FY);
          waitv<0>(fm[0], fm[1]);
        }
        const unsigned yv = (unsigned)(((t >> 4) * 1024 + nb * 128 + kh * 64 + (t & 15) * 4) * 4);
        sfor<7>([&](auto I) AI { constexpr int q = I; gld16<0, true>(yp[q], yv, ws + WS_YP + (size_t)q * 32 * 4096); });
      }
    }
    if constexpr (s == 50) {
      if (owner) {
        f32x4 a = {};
#pragma unroll
        for (int q = 0; q < 7; ++q) { PIN(yp[q]); a += yp[q]; }
#pragma unroll
        for (int j = 0; j < 4; ++j) a[j] = xg_sum(a[j]);
        *(LAS f32x4*)(lds + YS + (wid * 64 + c * 4) * 4) = a;
      }
    }
    if constexpr (s == 53) {
      if (ownw) {
        float a = 0.f;
#pragma unroll
        for (int p = 0; p < 8; ++p) a += *(LAS const float*)(lds + YS + (p * 64 + l) * 4);
        const unsigned ord = key >> 11, bits = (ord & 0x8000u) ? (ord & 0x7fffu) : (~ord & 0xffffu);
        *(LAS float*)(lds + RED1 + l * 4) = rbf(rbf(a) * rbf(sigmoid_f(bflo(bits))));
#pragma unroll
        for (int q = 0; q < 4; ++q) ya[q] = *(LAS const f32x4*)(lds + RED1 + (q * 16 + 4 * g) * 4);
      }
    }
    if constexpr (s == 57) {
      if (ownw) {
#pragma unroll
        for (int j = 0; j < 8; ++j) {
          const unsigned w = pk2(bflo(gate[j]) + ya[j >> 1][2 * (j & 1)], bfhi(gate[j]) + ya[j >> 1][2 * (j & 1) + 1]);
          gate[j] = erow == idx ? w : gate[j];
          PIN(gate[j]);
        }
      }
    }
    SBAR();
    if constexpr (((s & 3) == 2 && u < 11) || s == 44) {
      waitbar<SC.younger(OG, 64 + 4 * u + 7, B0 + s)>();
      if constexpr (u % 4 == 1) STAMP(16 + u / 4);
    }
    if constexpr (s == 48) waitbar<SC.younger(OG, 64 + 55, B0 + s)>();
    if constexpr (s == 50) waitbar<SC.younger(OG, 64 + 59, B0 + s)>();
    if constexpr (s == 52) { waitbar_l<SC.younger(OG, 64 + 63, B0 + s)>(); STAMP(19); }
    if constexpr (s == 56) waitbar_l<63>();
  });
  STAMP(20);

  f32x4 kep[4];
  {
    waitv<0>(pf);
    const bool rdy = __all((pf[0] == tlo) & (pf[1] == thi));
    if (rdy) sfor<4>([&](auto Dd) AI { constexpr int d = Dd; gld16<d * 1024, true>(oth[d], (unsigned)(wid * 4096 + l * 16), ws + WS_XC + (size_t)bid * 32768); });
    NOPS(3);
#pragma unroll
    for (int d = 0; d < 4; ++d) { PIN(acc[4 + d][1]); *(LAS f32x4*)(lds + 65536 + (wid ^ 4) * 4096 + l * 16 + (d & 1) * 2048 + (d >> 1) * 32768) = acc[4 + d][1]; }
    waitbar_l<63>();
    STAMP(21);
#pragma unroll
    for (int d = 0; d < 4; ++d) kep[d] = acc[4 + d][0] + *(LAS const f32x4*)(lds + 65536 + wid * 4096 + l * 16 + (d & 1) * 2048 + (d >> 1) * 32768);
    STAMP(22);
    if (!rdy) {
      poll8((unsigned)((bid * 8 + wid) * 64), ws + WS_FX, tlo, thi, 0xffffffffu);
      sfor<4>([&](auto Dd) AI { constexpr int d = Dd; gld16<d * 1024, true>(oth[d], (unsigned)(wid * 4096 + l * 16), ws + WS_XC + (size_t)bid * 32768); });
    }
    STAMP(23);
    waitv<0>(oth[0], oth[1]);
    PIN(oth[2]); PIN(oth[3]);
    STAMP(24);
  }
  u16* op = out + (size_t)erow * DM + ecol;
#pragma unroll
  for (int d = 0; d < 4; ++d) {
    const f32x4 a = kep[d] + oth[d];
    u32x2 res;
#pragma unroll
    for (int j = 0; j < 2; ++j) {
      const unsigned sp = pk2(a[2 * j], a[2 * j + 1]), gp = gate[2 * d + j], xw = xe[d][j];
      const unsigned pr = pk2(bflo(sp) * bflo(gp), bfhi(sp) * bfhi(gp));
      res[j] = pk2(bflo(xw) + bflo(pr), bfhi(xw) + bfhi(pr));
    }
    *(u32x2*)(op + d * 16) = res;
  }
  STAMP(25);
  STAMP_OUT(0, 26);
}

extern "C" void kernel_launch(void* const* d_in, const int* in_sizes, int n_in, void* d_out, int out_size, void* d_ws, size_t ws_size,
                              hipStream_t stream) {
  k_fused<<<256, 512, 0, stream>>>((const u16*)d_in[0], (const u16*)d_in[1], (const u16*)d_in[2], (const u16*)d_in[3], (const u16*)d_in[4],
                                   (const u16*)d_in[6], (const u16*)d_in[7], (const u16*)d_in[8], (const u16*)d_in[9], (char*)d_ws, (u16*)d_out);
}
```

```cpp
#include <hip/hip_runtime.h>
#include <stdint.h>
#include <type_traits>

typedef unsigned short u16;
typedef short bf16x8 __attribute__((ext_vector_type(8)));
typedef short s16x4 __attribute__((ext_vector_type(4)));
typedef float f32x4 __attribute__((ext_vector_type(4)));
typedef float f32x2 __attribute__((ext_vector_type(2)));
typedef unsigned u32x4 __attribute__((ext_vector_type(4)));
typedef unsigned u32x2 __attribute__((ext_vector_type(2)));
typedef __bf16 bf2_t __attribute__((ext_vector_type(2)));
#define LAS __attribute__((address_space(3)))
#define DI __device__ __forceinline__
#define AI __attribute__((always_inline))
#define SBAR() __builtin_amdgcn_sched_barrier(0)
template <class T> __device__ __forceinline__ void PIN(T& x) { asm volatile("" : "+v"(x)); }
template <class B> DI void MF(f32x4& acc, const bf16x8& a, const B& b) { asm volatile("v_mfma_f32_16x16x32_bf16 %0, %1, %2, %0" : "+v"(acc) : "v"(a), "v"(b)); }
template <class B> DI void MFC(f32x4& d, const bf16x8& a, const B& b, const f32x4& c) { asm volatile("v_mfma_f32_16x16x32_bf16 %0, %1, %2, %3" : "=&v"(d) : "v"(a), "v"(b), "v"(c)); }
template <class B> DI void MFZ(f32x4& d, const bf16x8& a, const B& b) { asm volatile("v_mfma_f32_16x16x32_bf16 %0, %1, %2, 0" : "=&v"(d) : "v"(a), "v"(b)); }
#define NOPS(n) asm volatile("s_nop " #n)
#define MFMA16(a, b, c) __builtin_amdgcn_mfma_f32_16x16x16bf16_1k((a), (b), (c), 0, 0, 0)
#define IC(n) std::integral_constant<int, (n)>{}

constexpr int SEQ = 2048, DM = 1024, DH = 128, SK = 2048, WIN = 512, FF = 3584;
constexpr size_t WS_XN = 0;
constexpr size_t WS_KV = 4u << 20;
constexpr size_t WS_ST = 5u << 20;
constexpr size_t WS_EP = WS_ST + 16384;
constexpr size_t WS_G0 = WS_ST + 32768;
constexpr size_t WS_FK = WS_ST + 49152;
constexpr size_t WS_YP = WS_ST + 65536;
constexpr size_t WS_FO = 7u << 20;
constexpr size_t WS_FY = WS_FO + 65536;
constexpr size_t WS_FX = WS_FO + 131072;
constexpr size_t WS_O = 8u << 20;
constexpr size_t WS_GU = 32u << 20;
constexpr size_t WS_XC = 24u << 20;

template <int I, int N, class F> DI void sfor_impl(F&& f) {
  if constexpr (I < N) { f(std::integral_constant<int, I>{}); sfor_impl<I + 1, N>(f); }
}
template <int N, class F> DI void sfor(F&& f) { sfor_impl<0, N>(f); }

DI unsigned pk2(float a, float b) { f32x2 v = {a, b}; return __builtin_bit_cast(unsigned, __builtin_convertvector(v, bf2_t)); }
DI float rbf(float a) { return (float)(__bf16)a; }
DI float bflo(unsigned u) { return __uint_as_float(u << 16); }
DI float bfhi(unsigned u) { return __uint_as_float(u & 0xffff0000u); }
DI float sigmoid_f(float v) { return __builtin_amdgcn_rcpf(1.f + __builtin_amdgcn_exp2f(v * -1.4426950408889634f)); }
DI float sel2(bool c, float a, float b) { PIN(a); PIN(b); return c ? a : b; }
DI float silu_f(float v) { return v * sigmoid_f(v); }
template <int CTRL> DI float dppf(float x) { return __builtin_bit_cast(float, __builtin_amdgcn_mov_dpp(__builtin_bit_cast(int, x), CTRL, 0xf, 0xf, true)); }
template <int CTRL> DI unsigned dppu(unsigned x) { return (unsigned)__builtin_amdgcn_mov_dpp((int)x, CTRL, 0xf, 0xf, true); }
constexpr int XOR1 = 0xB1, XOR2 = 0x4E, XOR7 = 0x141, XOR8 = 0x128;
DI float xg_sum(float x) {
  auto s = __builtin_amdgcn_permlane16_swap(__float_as_uint(x), __float_as_uint(x), false, false);
  x = __uint_as_float(s[0]) + __uint_as_float(s[1]);
  auto t = __builtin_amdgcn_permlane32_swap(__float_as_uint(x), __float_as_uint(x), false, false);
  return __uint_as_float(t[0]) + __uint_as_float(t[1]);
}
DI float xg_max(float x) {
  auto s = __builtin_amdgcn_permlane16_swap(__float_as_uint(x), __float_as_uint(x), false, false);
  x = __builtin_fmaxf(__uint_as_float(s[0]), __uint_as_float(s[1]));
  auto t = __builtin_amdgcn_permlane32_swap(__float_as_uint(x), __float_as_uint(x), false, false);
  return __builtin_fmaxf(__uint_as_float(t[0]), __uint_as_float(t[1]));
}
DI float wave_sum(float v) { v += dppf<XOR1>(v); v += dppf<XOR2>(v); v += dppf<XOR7>(v); v += dppf<XOR8>(v); return xg_sum(v); }
DI unsigned umax(unsigned a, unsigned b) { return a > b ? a : b; }
DI unsigned wave_max_u32(unsigned v) {
  v = umax(v, dppu<XOR1>(v)); v = umax(v, dppu<XOR2>(v)); v = umax(v, dppu<XOR7>(v)); v = umax(v, dppu<XOR8>(v));
  auto s = __builtin_amdgcn_permlane16_swap(v, v, false, false); v = umax(s[0], s[1]);
  auto t = __builtin_amdgcn_permlane32_swap(v, v, false, false); v = umax(t[0], t[1]);
  return __builtin_amdgcn_readfirstlane(v);
}

template <int OFF, bool DEV = false> DI void glds(unsigned ldsaddr, unsigned voff, const void* sbase) {
  if constexpr (DEV) asm volatile("s_mov_b32 m0, %0\n\ts_nop 0\n\tglobal_load_lds_dwordx4 %1, %2 offset:%3 sc1" ::"s"(ldsaddr), "v"(voff), "s"(sbase), "i"(OFF) : "memory");
  else asm volatile("s_mov_b32 m0, %0\n\ts_nop 0\n\tglobal_load_lds_dwordx4 %1, %2 offset:%3" ::"s"(ldsaddr), "v"(voff), "s"(sbase), "i"(OFF) : "memory");
}
DI void touch(unsigned voff, const void* sbase) {
  asm volatile("global_load_dword v255, %0, %1" ::"v"(voff), "s"(sbase) : "memory", "v255");
}
template <int OFF, bool DEV = false, class T> DI void gld16(T& dst, unsigned voff, const void* sbase) {
  if constexpr (DEV) asm volatile("global_load_dwordx4 %0, %1, %2 offset:%3 sc1" : "=v"(dst) : "v"(voff), "s"(sbase), "i"(OFF) : "memory");
  else asm volatile("global_load_dwordx4 %0, %1, %2 offset:%3" : "=v"(dst) : "v"(voff), "s"(sbase), "i"(OFF) : "memory");
}
template <int OFF, bool DEV = false, class T> DI void gld8(T& dst, unsigned voff, const void* sbase) {
  if constexpr (DEV) asm volatile("global_load_dwordx2 %0, %1, %2 offset:%3 sc1" : "=v"(dst) : "v"(voff), "s"(sbase), "i"(OFF) : "memory");
  else asm volatile("global_load_dwordx2 %0, %1, %2 offset:%3" : "=v"(dst) : "v"(voff), "s"(sbase), "i"(OFF) : "memory");
}
template <int OFF> DI void gld2(unsigned& dst, unsigned voff, const void* sbase) {
  asm volatile("global_load_ushort %0, %1, %2 offset:%3" : "=v"(dst) : "v"(voff), "s"(sbase), "i"(OFF) : "memory");
}
template <int OFF, class T> DI void gst16(const T& v, unsigned voff, const void* sbase) {
  asm volatile("global_store_dwordx4 %0, %1, %2 offset:%3 sc1\n\ts_nop 1" ::"v"(voff), "v"(v), "s"(sbase), "i"(OFF) : "memory");
}
template <class T> DI void gst8(const T& v, unsigned voff, const void* sbase) {
  asm volatile("global_store_dwordx2 %0, %1, %2 sc1" ::"v"(voff), "v"(v), "s"(sbase) : "memory");
}
template <int OFF> DI void gst4(float v, unsigned voff, const void* sbase) {
  asm volatile("global_store_dword %0, %1, %2 offset:%3 sc1" ::"v"(voff), "v"(v), "s"(sbase), "i"(OFF) : "memory");
}
constexpr int SPINS = 1 << 16;
DI void poll8(unsigned voff, const void* sbase, unsigned lo, unsigned hi, unsigned lomask) {
  for (int spin = 0; spin < SPINS; ++spin) {
    u32x2 v;
    asm volatile("global_load_dwordx2 %0, %1, %2 sc1\n\ts_waitcnt vmcnt(0)" : "=&v"(v) : "v"(voff), "s"(sbase) : "memory");
    if (__all(((v[0] & lomask) == lo) & (v[1] == hi))) break;
    __builtin_amdgcn_s_sleep(1);
  }
}
template <int N> DI void waitv() { static_assert(N >= 0, "count"); asm volatile("s_waitcnt vmcnt(%0)" ::"n"(N > 63 ? 63 : N) : "memory"); }
template <int N, class T> DI void waitv(T& x) { static_assert(N >= 0, "count"); asm volatile("s_waitcnt vmcnt(%1)" : "+v"(x) : "n"(N > 63 ? 63 : N) : "memory"); }
template <int N, class T> DI void waitv(T& x, T& y) { static_assert(N >= 0, "count"); asm volatile("s_waitcnt vmcnt(%2)" : "+v"(x), "+v"(y) : "n"(N > 63 ? 63 : N) : "memory"); }
template <int N> DI void waitbar() { static_assert(N >= 0, "count"); asm volatile("s_waitcnt vmcnt(%0)\n\ts_barrier" ::"n"(N > 63 ? 63 : N) : "memory"); }
template <int N> DI void waitbar_l() { static_assert(N >= 0, "count"); asm volatile("s_waitcnt vmcnt(%0) lgkmcnt(0)\n\ts_barrier" ::"n"(N > 63 ? 63 : N) : "memory"); }
typedef short v4i16_t __attribute__((ext_vector_type(4)));
DI s16x4 trd(LAS const char* p) { return __builtin_amdgcn_ds_read_tr16_b64_v4i16((LAS v4i16_t*)p); }
struct LBase {
  unsigned lo, hi;
  DI void init(unsigned a) { lo = a; hi = a + 65536; PIN(lo); PIN(hi); }
  template <int OFF> DI LAS const char* at() const { return (LAS const char*)(uintptr_t)(OFF < 65536 ? lo : hi) + (OFF & 65535); }
};
DI bf16x8 cat8(s16x4 lo, s16x4 hi) { return __builtin_shufflevector(lo, hi, 0, 1, 2, 3, 4, 5, 6, 7); }

enum { OX, OG, OF, OM, OS, OP, OE, OW };
struct Sched {
  int n; short kind[400], idx[400], slot[400];
  constexpr Sched() : n(0), kind{}, idx{}, slot{} {}
  constexpr void add(int s, int k, int i) { kind[n] = (short)k; idx[n] = (short)i; slot[n] = (short)s; ++n; }
  constexpr int younger(int k, int i, int s) const {
    int p = -1;
    for (int q = 0; q < n; ++q) if (kind[q] == k && idx[q] == i) p = q;
    if (p < 0 || slot[p] > s) return -1;
    int c = 0;
    for (int q = p + 1; q < n; ++q) if (slot[q] <= s) ++c;
    return c;
  }
  constexpr int first(int s) const { int q = 0; while (q < n && slot[q] < s) ++q; return q; }
  constexpr int count(int s) const { int c = 0; for (int q = 0; q < n; ++q) if (slot[q] == s) ++c; return c; }
};

#define STAMP_DECL
#define STAMP(i)
#define STAMP_OUT(kid, n)

constexpr int RING = 4 * 32768, FB = RING, RED1 = FB + 24576, HSC = RED1 + 1024, YS = HSC + 1024, LDS_BYTES = FB + 32768;
constexpr int STAGE = 3 * 32768;
constexpr int XD = 16, XD2 = 16;
constexpr float LBOUND = 65536.f;
constexpr float QSCALE = 0.08838834764831845f * 1.4426950408889634f;
constexpr int B0 = 128;
constexpr int FISS[4] = {96, 97, 98, 117}, FRDS[4] = {115, 118, 121, 124};
constexpr int fpiece(int sl) { for (int p = 0; p < 4; ++p) if (FRDS[p] == sl) return p; return -1; }
constexpr Sched make_sched() {
  Sched s;
  for (int i = 0; i < 12; ++i) s.add(-2, OG, i);
  for (int i = 0; i < 16; ++i) s.add(-1, OX, i);
  for (int t = 0; t < 128; ++t) {
    if (t >= 2 && t < 50) s.add(t, OX, t + 14);
    for (int v = 3; v < 18; ++v) for (int p = 0; p < 4; ++p) if (8 * (v - 3) + 7 + p == t) s.add(t, OG, 4 * v + p);
    if (t == 105 || t == 113 || t == 121) { s.add(t, OW, (t - 105) / 4); s.add(t, OW, (t - 105) / 4 + 1); }
    if (t >= 124) for (int i = 0; i < 3; ++i) s.add(t, OX, 64 + 3 * (t - 124) + i);
    if (t == 127) { s.add(t, OG, 72); s.add(t, OG, 73); }
    if (t == 20) s.add(t, OM, 0);
    if (t == 120) s.add(t, OM, 1);
    if (t >= 49 && t < 57 && (t & 1)) s.add(t, OF, (t - 49) / 2);
    if (t >= 71 && t < 79 && (t & 1)) s.add(t, OF, 4 + (t - 71) / 2);
    if (t == 48) s.add(t, OS, 0);
    if (t == 104) s.add(t, OS, 2);
    for (int p = 0; p < 4; ++p) if (FISS[p] == t) s.add(t, OF, 8 + p);
  }
  for (int t = 0; t < 64; ++t) {
    if (t == 0) { s.add(B0, OM, 2); s.add(B0, OG, 74); s.add(B0, OG, 75); s.add(B0, OW, 6); s.add(B0, OW, 7); }
    if (t == 1 || t == 2) { s.add(B0 + t, OX, 74 + 2 * t); s.add(B0 + t, OX, 75 + 2 * t); }
    if (t == 5 || t == 6 || t == 9) s.add(B0 + t, OX, 64 + (t == 5 ? 16 : t == 6 ? 18 : 22));
    if (t >= 5 && t < 50) s.add(B0 + t, OX, 64 + (t == 5 ? 17 : t < 9 ? t + 13 : t + 14));
    for (int v = 3; v < 16; ++v) for (int p = 0; p < 4; ++p) if ((v < 14 ? 4 * (v - 3) + 3 + (p >> 1) : v == 14 ? 45 + (p >> 1) : 49) == t) s.add(B0 + t, OG, 64 + 4 * v + p);
    if (t == 10 || t == 26) s.add(B0 + t, OM, t == 10 ? 3 : 4);
    if (t == 50) for (int i = 0; i < 5; ++i) s.add(B0 + t, OE, i);
  }
  return s;
}
constexpr Sched SC = make_sched();

__global__ __launch_bounds__(512, 2) void k_fused(const u16* __restrict__ x, const u16* __restrict__ kc, const u16* __restrict__ vc,
                                                  const u16* __restrict__ wq, const u16* __restrict__ wo, const u16* __restrict__ wr,
                                                  const u16* __restrict__ wgate, const u16* __restrict__ wup, const u16* __restrict__ wdown,
                                                  char* __restrict__ ws, u16* __restrict__ out) {
  __shared__ __attribute__((aligned(16))) char lds_raw[LDS_BYTES];
  LAS char* const lds = (LAS char*)lds_raw;
  const int t = threadIdx.x, l = t & 63, bid = blockIdx.x, c = l & 15, g = l >> 4;
  const int wid = __builtin_amdgcn_readfirstlane(t >> 6);
  const int xcd = bid & 7, within = bid >> 3, head = xcd * 4 + (within & 3), rb = within >> 2;
  const int fj = xcd * 28 + (within < 28 ? within : within - 28);
  const int fsl = fj / 7, fgr = fj - fsl * 7;
  const int kh = xcd >> 2, mb = 2 * rb + (xcd & 1), nb = ((xcd >> 1) & 1) * 4 + (within & 3);
  const int rbw = wid & 3, kg = wid >> 2;
  const unsigned lds0 = (unsigned)(uintptr_t)lds;
  STAMP_DECL
  STAMP(0);

  unsigned tlo, thi;
  {
    const int row = 8 * bid + wid;
    u32x4 a0, a1, w0, w1, kvo;
    u32x2 ep, klo, khi;
    unsigned e[8];
    gld16<0>(a0, (unsigned)(row * DM * 2 + l * 16), x); gld16<1024>(a1, (unsigned)(row * DM * 2 + l * 16), x);
    gld16<0>(w0, (unsigned)(l * 16), wr); gld16<1024>(w1, (unsigned)(l * 16), wr);
    gld8<0, true>(ep, 0u, ws + WS_EP);
    if (wid == 7) {
      const char* pc;
      asm volatile("s_getpc_b64 %0" : "=s"(pc));
      touch((unsigned)((within * 12 + (l < 12 ? l : 11)) * 128), pc);
    }
    __builtin_amdgcn_s_barrier();
    const int fn = (bid * 4 + wid) & 255, kvh = bid >> 6;
    if (wid < 4) {
      if (fn < 128) {
        const unsigned ko = (unsigned)(((kvh * SK + (SK - WIN) + (fn >> 2) * 16 + c) * DH + (fn & 3) * 32 + 4 * g) * 2);
        gld8<0>(klo, ko, kc); gld8<32>(khi, ko, kc);
      } else {
        const int d = 32 * ((fn - 128) >> 5) + 8 * (c >> 2) + 4 * (fn & 1) + (c & 3);
        const unsigned vo = (unsigned)(((kvh * SK + (SK - WIN) + ((fn >> 1) & 15) * 32 + 4 * g + 8) * DH + d) * 2);
        sfor<8>([&](auto J) AI { constexpr int j = J; gld2<(16 * (j >> 2) + (j & 3) - 8) * DH * 2>(e[j], vo, vc); });
      }
      if (wid < 2) touch((unsigned)((wid * 256 + within * 8 + (l >> 3)) * 8192 + xcd * 1024 + (l & 7) * 128), wq);
      if (fn < 128) waitv<5>(); else waitv<11>();
    } else {
      waitv<3>();
    }
    PIN(a0); PIN(a1);
    STAMP(1);
    float v[16];
#pragma unroll
    for (int j = 0; j < 4; ++j) { v[2 * j] = bflo(a0[j]); v[2 * j + 1] = bfhi(a0[j]); v[8 + 2 * j] = bflo(a1[j]); v[9 + 2 * j] = bfhi(a1[j]); }
    float s = 0.f;
#pragma unroll
    for (int j = 0; j < 16; ++j) s += v[j];
    const float mean = wave_sum(s) * (1.f / DM);
    float q = 0.f;
#pragma unroll
    for (int j = 0; j < 16; ++j) { const float d = v[j] - mean; q += d * d; }
    const float var = wave_sum(q) * (1.f / DM);
    const float mbf = rbf(mean), rden = 1.f / rbf(sqrtf(rbf(rbf(var) + 1e-5f)));
    if (wid >= 4) waitv<0>(); else if (fn < 128) waitv<2>(); else waitv<8>();
    PIN(w0); PIN(w1); PIN(ep);
    {
      const unsigned long long tg = (((unsigned long long)(unsigned)__builtin_amdgcn_readfirstlane(ep[1]) << 32) | (unsigned)__builtin_amdgcn_readfirstlane(ep[0])) + 1ull;
      tlo = (unsigned)tg; thi = (unsigned)(tg >> 32);
    }
    float rr = 0.f;
    u32x4 o0, o1;
#pragma unroll
    for (int j = 0; j < 8; ++j) {
      const unsigned dd = pk2(v[2 * j] - mbf, v[2 * j + 1] - mbf);
      const unsigned nn = pk2(bflo(dd) * rden, bfhi(dd) * rden);
      const unsigned ww = j < 4 ? w0[j] : w1[j - 4];
      rr += bflo(nn) * bflo(ww); rr += bfhi(nn) * bfhi(ww);
      if (j < 4) o0[j] = nn; else o1[j - 4] = nn;
    }
    *(LAS u32x4*)(lds + STAGE + (l * 8 + (wid ^ (l & 7))) * 16) = o0;
    *(LAS u32x4*)(lds + STAGE + 8192 + (l * 8 + (wid ^ (l & 7))) * 16) = o1;
    rr = wave_sum(rr);
    if (l == 0) {
      const unsigned bits = pk2(rr, 0.f) & 0xffffu;
      const unsigned ord = (bits & 0x8000u) ? (~bits & 0xffffu) : (bits | 0x8000u);
      *(LAS unsigned*)(lds + STAGE + 16384 + wid * 4) = (ord << 11) | (unsigned)(SEQ - 1 - row);
      *(LAS f32x2*)(lds + STAGE + 16384 + 32 + wid * 8) = (f32x2){mbf, rden};
    }
    waitbar_l<63>();
    if (wid >= 4) {
#pragma unroll
      for (int hh = 0; hh < 4; ++hh) {
        const int p = hh * 256 + (t - 256), ch = p >> 3, r = 8 * bid + ((p & 7) ^ (ch & 7));
        const u32x4 o = *(LAS const u32x4*)(lds + STAGE + p * 16);
        gst16<0>(o, (unsigned)(((((r >> 5) * 32 + (ch >> 2)) * 2 + ((r >> 4) & 1)) * 64 + (ch & 3) * 16 + (r & 15)) * 16), ws + WS_XN);
      }
      if (t < 256 + 8) gst8(*(LAS const f32x2*)(lds + STAGE + 16384 + 32 + (t - 256) * 8), (unsigned)((8 * bid + t - 256) * 8), ws + WS_ST);
      waitv<0>();
    } else {
      if (wid < 2) waitv<1>(); else waitv<0>();
      if (fn < 128) {
        PIN(klo); PIN(khi);
        kvo = (u32x4){klo[0], klo[1], khi[0], khi[1]};
      } else {
#pragma unroll
        for (int j = 0; j < 8; ++j) PIN(e[j]);
        kvo = (u32x4){e[0] | (e[1] << 16), e[2] | (e[3] << 16), e[4] | (e[5] << 16), e[6] | (e[7] << 16)};
      }
      gst16<0>(kvo, (unsigned)((bid * 4 + wid) * 1024 + l * 16), ws + WS_KV);
    }
    waitbar_l<63>();
    if (t == 256) {
      const u32x4 ka = *(LAS const u32x4*)(lds + STAGE + 16384), kb = *(LAS const u32x4*)(lds + STAGE + 16400);
      const unsigned key = umax(umax(umax(ka[0], ka[1]), umax(ka[2], ka[3])), umax(umax(kb[0], kb[1]), umax(kb[2], kb[3])));
      gst8((u32x2){(tlo << 27) | key, (tlo >> 5) | (thi << 27)}, (unsigned)(bid * 8), ws + WS_G0);
    }
    STAMP(2);
  }
  const unsigned glo = tlo << 27, ghi = (tlo >> 5) | (thi << 27);

  const char* const wq_b = (const char*)wq + head * 256;
  const unsigned wq_v = (unsigned)(((8 * wid + ((l & 31) >> 2)) * 4096 + (l >> 5) * 32 + ((l & 3) ^ (2 * (wid & 1))) * 8) * 2);
  const char* const kv_b = ws + WS_KV + (size_t)(head >> 3) * 262144;
  const unsigned kv_v = (unsigned)(wid * 4096 + l * 16);
  const char* const xn_b = ws + WS_XN + (size_t)rb * 8 * 65536 + 4096;
  const unsigned xn_v = (unsigned)(wid * 65536 + l * 16);
  const char* const wo_b = (const char*)wo + (size_t)kh * 2048 * (DM * 2) + nb * 256;
  const char* const o_b = ws + WS_O + (size_t)mb * 4 * 262144 + kh * 131072;
  const unsigned ldsw = lds0 + wid * 2048, ldsk = lds0 + wid * 4096, ldsf = lds0 + FB + wid * 1024;
  const int erow = mb * 128 + rbw * 32 + kg * 16 + c, ecol = nb * 128 + kh * 64 + 4 * g;
  bf16x8 xf[XD];
  u32x4 fm[2];
  bf16x8 fxs;
  const unsigned gu_v = (unsigned)((((4 * wid + g) * 2 + ((l >> 3) & 1)) * FF + fj * 16 + (l & 7) * 2) * 8);
  u32x2 fg;
  u32x2 xe[4];
  f32x2 st;
  unsigned key = 0;

  const unsigned fo_v = (unsigned)(((mb * 4 + rbw) * 32 + kh * 16 + c) * 8);
  const int yrow = within < 28 ? fj : 224 + xcd * 4 + within - 28;
  f32x4 o[2][2][2];
  float inv[2];
  const char* const od_b = ws + WS_O + ((size_t)(rb * 8) * 128 + head * 4) * 2048 + 4096;
  auto issue = [&](auto Sl, auto Pos) AI {
    constexpr int sl = Sl, pos = Pos, cnt = SC.count(sl), q0 = SC.first(sl) + (pos < 0 ? 0 : pos);
    constexpr int nq = pos < 0 ? cnt : pos >= cnt ? 0 : pos < 3 ? 1 : cnt - 3;
    sfor<nq>([&](auto Qi) AI {
      constexpr int k = SC.kind[q0 + Qi], i = SC.idx[q0 + Qi];
      if constexpr (k == OX && i < 64) gld16<(i & 7) * 1024 - 4096, true>(xf[i % XD], xn_v, xn_b + (size_t)(i >> 3) * 8192);
      if constexpr (k == OX && i >= 64) {
        constexpr int q = i - 64, v = q >> 2;
        gld16<0, true>(xf[q % XD2], (unsigned)(rbw * 262144 + kg * 16384 + ((q & 1) ^ kg) * 1024 + l * 16), o_b + (size_t)(((v & 3) * 4 + ((q >> 1) & 1)) * 8192 + (v >> 2) * 2048));
      }
      if constexpr (k == OG) {
        constexpr int v = i >> 2, p = i & 3;
        if constexpr (v < 8) {
          const char* sb = wq_b + (size_t)(v * 128 + (p >> 1) * 64) * 8192;
          glds<(p & 1) * 128>(ldsw + (v & 3) * 32768 + (p >> 1) * 16384 + (p & 1) * (1024 - 128), wq_v, sb);
        } else if constexpr (v < 16) {
          glds<p * 1024, true>(ldsk + (v & 3) * 32768, kv_v, kv_b + (size_t)(v - 8) * 32768);
        } else {
          const unsigned wo_v = (unsigned)((((wid >> 2) * 128 + (wid & 3) * 8 + ((l & 31) >> 2)) * DM + (l >> 5) * 32 + ((l & 3) ^ (2 * (wid & 1))) * 8) * 2);
          const char* sb = wo_b + (size_t)(((v & 3) * 4 + (p >> 1) * 2) * 128 + ((v - 16) >> 2) * 32) * (DM * 2) + (((p & 1) ^ kh ^ 1) << 7);
          glds<0>(ldsw + (v & 3) * 32768 + (p >> 1) * 16384 + (p & 1) * 1024, wo_v, sb);
        }
      }
      if constexpr (k == OF && i < 8)
        glds<0>(ldsf + (i & 3) * 8192, (unsigned)(((l >> 3) * FF + wid * 64 + ((l & 7) ^ ((i & 1) * 4 + ((l >> 3) & 2))) * 8) * 2),
                (const char*)(i < 4 ? wgate : wup) + ((size_t)(fsl * 32 + 8 * (i & 3)) * FF + fgr * 512) * 2);
      if constexpr (k == OF && i >= 8) {
        const int frho = l >> 2, fch = (l & 3) ^ (frho & 4 ? 2 : 0);
        glds<0>(ldsf + ((i - 8) % 3) * 8192, (unsigned)((frho * DM + wid * 128 + fch * 8) * 2), (const char*)wdown + (size_t)fj * 16 * DM * 2 + (i - 8) * 64);
      }
      if constexpr (k == OS && i == 0) {
        const int idx = SEQ - 1 - (int)(key & 2047u);
        gld16<0, true>(fxs, (unsigned)((((idx >> 5) * 32 + fsl) * 2 + ((idx >> 4) & 1)) * 1024 + (g * 16 + (idx & 15)) * 16), ws + WS_XN);
      }
      if constexpr (k == OS && i == 2) gld16<0, true>(fm[0], gu_v, ws + WS_GU);
      if constexpr (k == OM && i > 0) gld8<0, true>(fg, fo_v, ws + WS_FO + (i - 1) * 16384);
      if constexpr (k == OM && i == 0) gld8<0, true>(fg, (unsigned)((t & 255) * 8), wid < 4 ? ws + WS_G0 : ws + WS_FK + (head >> 3) * 2048);
      if constexpr (k == OW) {
        constexpr int os = i >> 1, qb = i & 1;
        PIN(o[os & 1][0][qb]); PIN(o[os & 1][1][qb]);
        const f32x4 a = o[os & 1][0][qb] * inv[qb], b = o[os & 1][1][qb] * inv[qb];
        gst16<i * 1024 - 4096>((u32x4){pk2(a[0], a[1]), pk2(a[2], a[3]), pk2(b[0], b[1]), pk2(b[2], b[3])}, (unsigned)(wid * 262144 + l * 16), od_b);
      }
      if constexpr (k == OE && i < 4) gld8<32 * i>(xe[i], (unsigned)((erow * DM + ecol) * 2), x);
      if constexpr (k == OE && i == 4) gld8<0, true>(st, (unsigned)(erow * 8), ws + WS_ST);
    });
  };
  f32x4 fa0, fa1, fq[4];
  s16x4 fb0, fb1, fxa;
  bf16x8 ft[2];
  auto endslot = [&](auto Sl) AI {
    constexpr int sl = Sl, u = sl >> 3, i = sl & 7;
    if constexpr (sl == 3) {
      if (wid == 6 || wid == 7) touch((unsigned)(((wid - 4) * 256 + within * 8 + (l >> 3)) * 8192 + xcd * 1024 + (l & 7) * 128), wq);
    }
    if constexpr (sl == 2) {
      if (wid < 4 && l == 0) gst8((u32x2){tlo, thi}, (unsigned)((bid * 4 + wid) * 8), ws + WS_FK);
    }
    if constexpr (sl == 38) {
      waitv<SC.younger(OM, 0, sl)>(fg);
      const unsigned lo = wid < 4 ? glo : tlo, hi = wid < 4 ? ghi : thi, mk = wid < 4 ? 0xf8000000u : 0xffffffffu;
      for (int spin = 0; spin < SPINS; ++spin) {
        if (__builtin_expect(__all(((fg[0] & mk) == lo) & (fg[1] == hi)), 1)) break;
        __builtin_amdgcn_s_sleep(1);
        gld8<0, true>(fg, (unsigned)((t & 255) * 8), wid < 4 ? ws + WS_G0 : ws + WS_FK + (head >> 3) * 2048);
        waitv<0>(fg);
      }
      const unsigned km = wave_max_u32(fg[0] & 0x07ffffffu);
      *(LAS unsigned*)(lds + HSC + wid * 4) = km;
    }
    if constexpr (sl == 47) {
      if (bid == 0 && t == 0) gst8((u32x2){tlo, thi}, 0u, ws + WS_EP);
      const u32x4 kk = *(LAS const u32x4*)(lds + HSC);
      key = __builtin_amdgcn_readfirstlane(umax(umax(kk[0], kk[1]), umax(kk[2], kk[3])));
    }
    if constexpr (sl == 58 || sl == 61 || sl == 64 || sl == 67) {
      if (wid == 4 + (sl - 58) / 3) touch((unsigned)(((wid & 3) * 64 + l) * 128), (const char*)wdown + (size_t)fj * 16 * DM * 2);
    }
    if constexpr (sl == 80 || sl == 82 || sl == 84 || sl == 86) {
      if (wid == (sl - 80) / 2)
        touch((unsigned)((kh * 2048 + (within >> 1) * 128 + wid * 32 + ((within & 1) * 2 + (xcd & 1)) * 8 + (l >> 3)) * (DM * 2) + ((xcd >> 1) & 1) * 1024 + (l & 7) * 128), wo);
    }
    if constexpr ((sl >= 68 && sl <= 70) || (sl >= 90 && sl <= 92)) {
      constexpr int r = sl >= 90, q = sl - (r ? 90 : 68);
      if constexpr (q == 0) waitv<SC.younger(OF, 4 * r + 3, sl)>(fxs);
      if constexpr (q >= 1) {
#pragma unroll
        for (int j = 0; j < 2; ++j) { fq[2 * q - 2 + j] = __builtin_amdgcn_mfma_f32_16x16x32_bf16(fxs, ft[j], (f32x4){}, 0, 0, 0); PIN(fq[2 * q - 2 + j]); }
      }
      if constexpr (q < 2) {
        const unsigned rp = ldsf + g * 8192 + (c >> 2) * 128 + ((((g & 1) << 1) | (c >> 3)) << 5) + (c & 3) * 8;
#pragma unroll
        for (int j = 0; j < 2; ++j) { LAS const char* a = (LAS const char*)(uintptr_t)(rp ^ ((2 * q + j) << 5)); ft[j] = cat8(trd(a), trd(a + 512)); }
      }
    }
    if constexpr (sl == 71 || sl == 93) {
      const float v = sel2(g & 2, sel2(g & 1, fq[3][0], fq[2][0]), sel2(g & 1, fq[1][0], fq[0][0]));
      gst8((u32x2){__float_as_uint(v), tlo}, (unsigned)(((fsl * 2 + (sl == 93)) * FF + fgr * 512 + t) * 8), ws + WS_GU);
    }
    if constexpr (sl == 109) {
      waitv<SC.younger(OS, 2, sl)>(fm[0]);
      for (int spin = 0; spin < SPINS; ++spin) {
        if (__builtin_expect(__all((fm[0][1] == tlo) & (fm[0][3] == tlo)), 1)) break;
        __builtin_amdgcn_s_sleep(1);
        gld16<0, true>(fm[0], gu_v, ws + WS_GU);
        waitv<0>(fm[0]);
      }
      *(LAS f32x2*)(lds + RED1 + (wid * 32 + 2 * c) * 4) = (f32x2){xg_sum(__uint_as_float(fm[0][0])), xg_sum(__uint_as_float(fm[0][2]))};
    }
    if constexpr (sl == 111) {
#pragma unroll
      for (int w = 0; w < 2; ++w) { fa0[w] = *(LAS const float*)(lds + RED1 + ((2 * g + w) * 32 + c) * 4); fa0[2 + w] = *(LAS const float*)(lds + RED1 + ((2 * g + w) * 32 + 16 + c) * 4); }
    }
    if constexpr (sl == 112) { fa0[0] = xg_sum(fa0[0] + fa0[1]); fa0[2] = xg_sum(fa0[2] + fa0[3]); PIN(fa0); }
    if constexpr (sl == 113) {
      const float hh = rbf(silu_f(rbf(fa0[0]))) * rbf(fa0[2]);
      *(LAS u16*)(lds + HSC + 32 + wid * 32 + c * 2) = (u16)pk2(hh, hh);
    }
    if constexpr (sl == 114) fxa = *(LAS const s16x4*)(lds + HSC + 32 + wid * 32 + g * 8);
    constexpr int pr = fpiece(sl), pm = fpiece(sl - 1), ps = fpiece(sl - 2);
    if constexpr (ps >= 0) gst4<ps * 128>(sel2(g & 1, fa1[0], fa0[0]), (unsigned)((wid * 128 + (l & 31)) * 4), ws + WS_YP + (size_t)yrow * 4096);
    if constexpr (pm >= 0) { fa0 = MFMA16(fxa, fb0, (f32x4){}); fa1 = MFMA16(fxa, fb1, (f32x4){}); PIN(fa0); PIN(fa1); }
    if constexpr (pr >= 0) {
      waitv<SC.younger(OF, 8 + pr, sl)>();
      const unsigned fp = ldsf + (4 * g + (c >> 2)) * 64 + (g & 1) * 32 + (c & 3) * 8;
      fb0 = trd((LAS const char*)(uintptr_t)fp + (pr % 3) * 8192); fb1 = trd((LAS const char*)(uintptr_t)(fp ^ 32) + (pr % 3) * 8192);
    }
    if constexpr (sl == 111 || sl == 119 || sl == 127) {
      waitv<SC.younger(OW, (sl - 111) / 4 + 1, sl)>();
      if (l == 0) gst8((u32x2){tlo, thi}, (unsigned)(((rb * 8 + wid) * 32 + head) * 8), ws + WS_FO + ((sl - 111) / 8) * 16384);
    }
    if constexpr (sl == 123) {
      waitv<SC.younger(OM, 1, sl)>(fg);
      if (__builtin_expect(!__all((fg[0] == tlo) & (fg[1] == thi)), 0)) poll8(fo_v, ws + WS_FO, tlo, thi, 0xffffffffu);
    }
    SBAR();
    if constexpr (i == 6) {
      if constexpr (u == 5 || u == 13) waitbar_l<SC.younger(OG, 4 * u + 7, sl)>();
      else waitbar<SC.younger(OG, 4 * u + 7, sl)>();
      if constexpr (u == 0 || u == 1 || u == 5 || u == 7 || u == 8 || u == 10 || u == 12 || u == 14)
        STAMP(u == 0 ? 4 : u == 1 ? 5 : u == 5 ? 6 : u == 7 ? 7 : u == 8 ? 8 : u == 10 ? 9 : u == 12 ? 10 : 11);
    }
  };

  issue(IC(-2), IC(-1));
  if (wid == 5) poll8((unsigned)((rb * 32 + (l & 31)) * 8), ws + WS_G0, glo, ghi, 0xf8000000u);
  waitbar<SC.younger(OG, 3, -2)>();
  issue(IC(-1), IC(-1));
  STAMP(3);

  LBase trb[2], frb;
  trb[0].init(lds0 + g * 2048 + (c >> 2) * 64 + (g & 1) * 32 + (c & 3) * 8);
  trb[1].init(lds0 + g * 2048 + (c >> 2) * 64 + ((g & 1) ^ 1) * 32 + (c & 3) * 8);
  frb.init(lds0 + l * 16);
  f32x4 acc[8][2];
  bf16x8 wf[2][4];
  auto WRD1 = [&](auto Si, auto Base, auto Dd) AI {
    constexpr int s = Si, d = Dd, o = Base + (d >> 1) * 512;
    wf[s & 1][d] = cat8(trd(trb[d & 1].template at<o>()), trd(trb[d & 1].template at<o + 256>()));
  };
  auto WRD = [&](auto Si, auto Base) AI { sfor<4>([&](auto Dd) AI { WRD1(Si, Base, Dd); }); };
#define WA(s) IC((((s) >> 3) & 3) * 32768 + (((s) >> 1) & 3) * 8192 + ((s) & 1) * 1024)
#define WB(s) IC((((s) >> 2) & 3) * 32768 + (((s) >> 1) & 1) * 8192 + ((s) & 1) * 1024)
  bf16x8 fr[4];
  auto FRD = [&](auto Ni) AI { constexpr int n = Ni; if constexpr (n < 256) fr[n & 3] = *(LAS const bf16x8*)(frb.at<((n >> 5) & 3) * 32768 + (n & 31) * 1024>()); };

  WRD(IC(0), WA(0));
  SBAR();
  bf16x8 qf[4][2];
  auto QPACK = [&](auto Ds) AI {
    constexpr int ds = Ds;
#pragma unroll
    for (int qb = 0; qb < 2; ++qb) {
      const f32x4 a = acc[2 * ds][qb] * QSCALE, b = acc[2 * ds + 1][qb] * QSCALE;
      qf[ds][qb] = __builtin_bit_cast(bf16x8, (u32x4){pk2(a[0], a[1]), pk2(a[2], a[3]), pk2(b[0], b[1]), pk2(b[2], b[3])});
      PIN(qf[ds][qb]);
    }
  };
  sfor<64>([&](auto Si) AI {
    constexpr int s = Si, x0 = (s & ~1) % XD, x1 = (s | 1) % XD;
    if constexpr (!(s & 1)) waitv<SC.younger(OX, s + 1, s - 1)>(xf[x0], xf[x1]);
    sfor<4>([&](auto Dd) AI {
      constexpr int d = Dd, dt = (s & 1) * 4 + d;
      if constexpr (s + 1 < 64) WRD1(IC(s + 1), WA(s + 1), Dd);
      else if constexpr (d < 3) FRD(Dd);
      SBAR();
      if constexpr (s < 2) { MFZ(acc[dt][0], wf[s & 1][d], xf[x0]); MFZ(acc[dt][1], wf[s & 1][d], xf[x1]); }
      else { MF(acc[dt][0], wf[s & 1][d], xf[x0]); MF(acc[dt][1], wf[s & 1][d], xf[x1]); }
      issue(Si, Dd);
      if constexpr (s == 63 && (d & 1)) { QPACK(IC(d >> 1)); SBAR(); }
    });
    SBAR();
    endslot(Si);
  });

  f32x4 sc[2][2][2], negm[2];
  u32x4 pw[16][2];
  float lsum[2] = {0.f, 0.f};
#define MX3(a, b, c) __builtin_fmaxf(__builtin_fmaxf((a), (b)), (c))
#define EXP2(x) __builtin_amdgcn_exp2f(x)
  auto SM = [&](auto Hi, auto Pi) AI {
    constexpr int H = Hi, p = Pi, kt = p >> 2, qb = (p >> 1) & 1, r = 2 * (p & 1);
    const float e0 = EXP2(sc[H & 1][kt][qb][r]), e1 = EXP2(sc[H & 1][kt][qb][r + 1]);
    lsum[qb] += e0; lsum[qb] += e1;
    unsigned w = pk2(e0, e1);
    PIN(lsum[qb]); PIN(w);
    pw[H][qb][kt * 2 + (p & 1)] = w;
  };
  sfor<128>([&](auto Ni) AI {
    constexpr int n = Ni, H = n >> 3, i = n & 7, kt = i >> 2, ds = i & 3, sn = H & 1;
    FRD(IC(n + 3));
    if constexpr (n < 2) QPACK(IC(2 + n));
    sfor<2>([&](auto Qb) AI {
      constexpr int qb = Qb;
      if constexpr (ds == 0 && H == 0) MFZ(sc[sn][kt][qb], fr[n & 3], qf[0][qb]);
      else if constexpr (ds == 0) MFC(sc[sn][kt][qb], fr[n & 3], qf[0][qb], negm[qb]);
      else MF(sc[sn][kt][qb], fr[n & 3], qf[ds][qb]);
    });
    if constexpr (n == 5 || n == 6) {
      constexpr int qb = n - 5;
      NOPS(3); PIN(sc[0][0][qb]);
      const float m = -xg_max(__builtin_fmaxf(MX3(sc[0][0][qb][0], sc[0][0][qb][1], sc[0][0][qb][2]), sc[0][0][qb][3]));
      negm[qb] = (f32x4){m, m, m, m};
      sc[0][0][qb] += negm[qb];
      PIN(negm[qb]); PIN(sc[0][0][qb]);
    }
    if constexpr (n == 9 || n == 10) { NOPS(3); PIN(sc[0][1][n - 9]); sc[0][1][n - 9] += negm[n - 9]; PIN(sc[0][1][n - 9]); }
    issue(IC(64 + (n >> 2)), IC(n & 3));
    if constexpr (H >= 1) SM(IC(H - 1), IC(i));
    if constexpr (n == 125) { PIN(negm[0]); PIN(negm[1]); }
    SBAR();
    if constexpr ((n & 3) == 3) endslot(IC(64 + (n >> 2)));
  });
  sfor<8>([&](auto Pi) AI { if constexpr (Pi == 4) { NOPS(3); PIN(sc[1][1][0]); PIN(sc[1][1][1]); } SM(IC(15), Pi); });
  if (__builtin_expect(__any(!(lsum[0] + lsum[1] <= LBOUND)), 0)) {
    auto tile = [&](unsigned off, f32x4& s0, f32x4& s1) AI {
      bf16x8 kf[4];
      SBAR(); NOPS(7);
      sfor<4>([&](auto Ds) AI { constexpr int ds = Ds; gld16<ds * 1024, true>(kf[ds], off, kv_b); });
      waitv<0>(kf[0], kf[1]);
      PIN(kf[2]); PIN(kf[3]);
#pragma unroll
      for (int ds = 0; ds < 4; ++ds) {
        s0 = __builtin_amdgcn_mfma_f32_16x16x32_bf16(kf[ds], qf[ds][0], s0, 0, 0, 0);
        s1 = __builtin_amdgcn_mfma_f32_16x16x32_bf16(kf[ds], qf[ds][1], s1, 0, 0, 0);
      }
    };
    float m0 = -__builtin_inff(), m1 = m0;
#pragma unroll 1
    for (int kt = 0; kt < 32; ++kt) {
      f32x4 s0 = {}, s1 = {};
      tile((unsigned)(kt * 4096 + l * 16), s0, s1);
      m0 = MX3(MX3(m0, s0[0], s0[1]), s0[2], s0[3]); m1 = MX3(MX3(m1, s1[0], s1[1]), s1[2], s1[3]);
    }
    m0 = -xg_max(m0); m1 = -xg_max(m1);
    lsum[0] = 0.f; lsum[1] = 0.f;
    sfor<16>([&](auto Hi) AI {
      constexpr int H = Hi;
#pragma unroll
      for (int kt = 0; kt < 2; ++kt) {
        sc[H & 1][kt][0] = (f32x4){m0, m0, m0, m0}; sc[H & 1][kt][1] = (f32x4){m1, m1, m1, m1};
        tile((unsigned)((2 * H + kt) * 4096 + l * 16), sc[H & 1][kt][0], sc[H & 1][kt][1]);
      }
      sfor<8>([&](auto Pi) AI { SM(Hi, Pi); });
      PIN(pw[H][0]); PIN(pw[H][1]);
    });
  }
  SBAR();
  NOPS(1);
  sfor<128>([&](auto Mi) AI {
    constexpr int m = Mi, n = 128 + m, os = m >> 5, H = (m >> 1) & 15, par = m & 1;
    FRD(IC(n + 3));
    if constexpr (H == 0) { MFZ(o[os & 1][par][0], fr[n & 3], pw[H][0]); MFZ(o[os & 1][par][1], fr[n & 3], pw[H][1]); }
    else { MF(o[os & 1][par][0], fr[n & 3], pw[H][0]); MF(o[os & 1][par][1], fr[n & 3], pw[H][1]); }
    if constexpr (m == 2 || m == 3) { inv[m - 2] = __builtin_amdgcn_rcpf(xg_sum(lsum[m - 2])); PIN(inv[m - 2]); }
    issue(IC(64 + (n >> 2)), IC(n & 3));
    if constexpr (m == 123) {
      trb[0].init(lds0 + kg * 16384 + g * 2048 + (c >> 2) * 64 + (g & 1) * 32 + (c & 3) * 8);
      trb[1].init(lds0 + kg * 16384 + g * 2048 + (c >> 2) * 64 + ((g & 1) ^ 1) * 32 + (c & 3) * 8);
    }
    if constexpr (m >= 124) WRD1(IC(0), WB(0), IC(m - 124));
    SBAR();
    if constexpr ((n & 3) == 3) endslot(IC(64 + (n >> 2)));
  });
  STAMP(12); STAMP(13); STAMP(14); STAMP(15);

  const int idx = SEQ - 1 - (int)(key & 2047u);
  const bool owner = (idx >> 7) == mb, ownw = (idx >> 4) == mb * 8 + rbw * 2 + kg;
  f32x4 yp[7], ya[4], oth[4];
  unsigned gate[8];
  auto item = [](int s) { return s < 48 ? s : s < 56 ? 48 + 2 * (s - 48) : 49 + 2 * (s - 56); };
  u32x2 pf;
  sfor<64>([&](auto Si) AI {
    constexpr int s = Si, u = s >> 2, it = item(s), x0 = (it & ~1) % XD2, x1 = (it | 1) % XD2;
    if constexpr (!(it & 1)) waitv<SC.younger(OX, 64 + it + 1, B0 + s - 1)>(xf[x0], xf[x1]);
    sfor<4>([&](auto Dd) AI {
      constexpr int d = Dd, dt = (it & 1) * 4 + d;
      if constexpr (s + 1 < 64) WRD1(IC(s + 1), WB(item(s + 1)), Dd);
      SBAR();
      if constexpr (s < 2) { MFZ(acc[dt][0], wf[s & 1][d], xf[x0]); MFZ(acc[dt][1], wf[s & 1][d], xf[x1]); }
      else { MF(acc[dt][0], wf[s & 1][d], xf[x0]); MF(acc[dt][1], wf[s & 1][d], xf[x1]); }
      issue(IC(B0 + s), Dd);
      if constexpr (s == 56) *(LAS f32x4*)(lds + (wid ^ 4) * 4096 + l * 16 + (d & 1) * 2048 + (d >> 1) * 32768) = acc[d][1];
      if constexpr (s == 57 && d == 0) {
#pragma unroll
        for (int e = 0; e < 4; ++e) oth[e] = *(LAS const f32x4*)(lds + wid * 4096 + l * 16 + (e & 1) * 2048 + (e >> 1) * 32768);
      }
      if constexpr (s == 57 && d >= 2) {
        gst16<(2 * d - 4) * 1024>(acc[2 * d - 4][0] + oth[2 * d - 4], (unsigned)(wid * 4096 + l * 16), ws + WS_XC + (size_t)(bid ^ 4) * 32768);
        gst16<(2 * d - 3) * 1024>(acc[2 * d - 3][0] + oth[2 * d - 3], (unsigned)(wid * 4096 + l * 16), ws + WS_XC + (size_t)(bid ^ 4) * 32768);
      }
      if constexpr (s == 58 || s == 59) {
        constexpr int j = (s - 58) * 4 + d;
        if constexpr (j == 0) waitv<4>();
        PIN(xe[j >> 1]); PIN(st);
        const unsigned xw = xe[j >> 1][j & 1];
        const unsigned dd = pk2(bflo(xw) - st[0], bfhi(xw) - st[0]);
        gate[j] = pk2(bflo(dd) * st[1], bfhi(dd) * st[1]);
        PIN(gate[j]);
      }
      if constexpr (s >= 60 && !(d & 1)) {
        constexpr int j = (s - 60) * 2 + (d >> 1);
        gate[j] = pk2(silu_f(bflo(gate[j])), silu_f(bfhi(gate[j])));
        PIN(gate[j]);
      }
    });
    SBAR();
    if constexpr (s == 7) {
      waitv<SC.younger(OW, 7, B0 + s)>();
      if (l == 0) {
        gst8((u32x2){tlo, thi}, (unsigned)(((rb * 8 + wid) * 32 + head) * 8), ws + WS_FO + 3 * 16384);
        gst8((u32x2){tlo, thi}, (unsigned)((wid * 256 + yrow) * 8), ws + WS_FY);
      }
    }
    if constexpr (s == 4 || s == 17 || s == 33) {
      constexpr int j = s == 4 ? 1 : s == 17 ? 2 : 3;
      waitv<SC.younger(OM, j + 1, B0 + s)>(fg);
      if (__builtin_expect(!__all((fg[0] == tlo) & (fg[1] == thi)), 0)) poll8(fo_v, ws + WS_FO + j * 16384, tlo, thi, 0xffffffffu);
    }
    if constexpr (s == 60) {
      waitv<0>();
      if (l == 0) gst8((u32x2){tlo, thi}, (unsigned)(((bid ^ 4) * 8 + wid) * 64), ws + WS_FX);
    }
    if constexpr (s == 61) gld8<0, true>(pf, (unsigned)((bid * 8 + wid) * 64), ws + WS_FX);
    if constexpr (s == 19) {
      if (owner) { gld16<0, true>(fm[0], (unsigned)(nb * 2048 + (l < 56 ? l : 55) * 32), ws + WS_FY); gld16<16, true>(fm[1], (unsigned)(nb * 2048 + (l < 56 ? l : 55) * 32), ws + WS_FY); }
    }
    if constexpr (s == 34) {
      if (owner) {
        PIN(fm[0]); PIN(fm[1]);
        for (int spin = 0; spin < SPINS; ++spin) {
          const bool ok = (fm[0][0] == tlo) & (fm[0][1] == thi) & (fm[0][2] == tlo) & (fm[0][3] == thi) & (fm[1][0] == tlo) & (fm[1][1] == thi) & (fm[1][2] == tlo) & (fm[1][3] == thi);
          if (__builtin_expect(__all(ok), 1)) break;
          __builtin_amdgcn_s_sleep(1);
          gld16<0, true>(fm[0], (unsigned)(nb * 2048 + (l < 56 ? l : 55) * 32), ws + WS_FY); gld16<16, true>(fm[1], (unsigned)(nb * 2048 + (l < 56 ? l : 55) * 32), ws + WS_FY);
          waitv<0>(fm[0], fm[1]);
        }
        const unsigned yv = (unsigned)(((t >> 4) * 1024 + nb * 128 + kh * 64 + (t & 15) * 4) * 4);
        sfor<7>([&](auto I) AI { constexpr int q = I; gld16<0, true>(yp[q], yv, ws + WS_YP + (size_t)q * 32 * 4096); });
      }
    }
    if constexpr (s == 50) {
      if (owner) {
        f32x4 a = {};
#pragma unroll
        for (int q = 0; q < 7; ++q) { PIN(yp[q]); a += yp[q]; }
#pragma unroll
        for (int j = 0; j < 4; ++j) a[j] = xg_sum(a[j]);
        *(LAS f32x4*)(lds + YS + (wid * 64 + c * 4) * 4) = a;
      }
    }
    if constexpr (s == 53) {
      if (ownw) {
        float a = 0.f;
#pragma unroll
        for (int p = 0; p < 8; ++p) a += *(LAS const float*)(lds + YS + (p * 64 + l) * 4);
        const unsigned ord = key >> 11, bits = (ord & 0x8000u) ? (ord & 0x7fffu) : (~ord & 0xffffu);
        *(LAS float*)(lds + RED1 + l * 4) = rbf(rbf(a) * rbf(sigmoid_f(bflo(bits))));
#pragma unroll
        for (int q = 0; q < 4; ++q) ya[q] = *(LAS const f32x4*)(lds + RED1 + (q * 16 + 4 * g) * 4);
      }
    }
    if constexpr (s == 59) {
      if (ownw) {
#pragma unroll
        for (int j = 0; j < 8; ++j) {
          const unsigned w = pk2(bflo(gate[j]) + ya[j >> 1][2 * (j & 1)], bfhi(gate[j]) + ya[j >> 1][2 * (j & 1) + 1]);
          gate[j] = erow == idx ? w : gate[j];
          PIN(gate[j]);
        }
      }
    }
    SBAR();
    if constexpr (((s & 3) == 2 && u < 11) || s == 44) {
      waitbar<SC.younger(OG, 64 + 4 * u + 7, B0 + s)>();
      if constexpr (u % 4 == 1) STAMP(16 + u / 4);
    }
    if constexpr (s == 48) waitbar<SC.younger(OG, 64 + 55, B0 + s)>();
    if constexpr (s == 50) waitbar<SC.younger(OG, 64 + 59, B0 + s)>();
    if constexpr (s == 52) { waitbar_l<SC.younger(OG, 64 + 63, B0 + s)>(); STAMP(19); }
    if constexpr (s == 56) waitbar_l<63>();
  });
  STAMP(20);

  f32x4 kep[4];
  {
    waitv<0>(pf);
    const bool rdy = __all((pf[0] == tlo) & (pf[1] == thi));
    if (rdy) sfor<4>([&](auto Dd) AI { constexpr int d = Dd; gld16<d * 1024, true>(oth[d], (unsigned)(wid * 4096 + l * 16), ws + WS_XC + (size_t)bid * 32768); });
    NOPS(3);
#pragma unroll
    for (int d = 0; d < 4; ++d) { PIN(acc[4 + d][1]); *(LAS f32x4*)(lds + 65536 + (wid ^ 4) * 4096 + l * 16 + (d & 1) * 2048 + (d >> 1) * 32768) = acc[4 + d][1]; }
    waitbar_l<63>();
    STAMP(21);
#pragma unroll
    for (int d = 0; d < 4; ++d) kep[d] = acc[4 + d][0] + *(LAS const f32x4*)(lds + 65536 + wid * 4096 + l * 16 + (d & 1) * 2048 + (d >> 1) * 32768);
    STAMP(22);
    if (!rdy) {
      poll8((unsigned)((bid * 8 + wid) * 64), ws + WS_FX, tlo, thi, 0xffffffffu);
      sfor<4>([&](auto Dd) AI { constexpr int d = Dd; gld16<d * 1024, true>(oth[d], (unsigned)(wid * 4096 + l * 16), ws + WS_XC + (size_t)bid * 32768); });
    }
    STAMP(23);
    waitv<0>(oth[0], oth[1]);
    PIN(oth[2]); PIN(oth[3]);
    STAMP(24);
  }
  u16* op = out + (size_t)erow * DM + ecol;
#pragma unroll
  for (int d = 0; d < 4; ++d) {
    const f32x4 a = kep[d] + oth[d];
    u32x2 res;
#pragma unroll
    for (int j = 0; j < 2; ++j) {
      const unsigned sp = pk2(a[2 * j], a[2 * j + 1]), gp = gate[2 * d + j], xw = xe[d][j];
      const unsigned pr = pk2(bflo(sp) * bflo(gp), bfhi(sp) * bfhi(gp));
      res[j] = pk2(bflo(xw) + bflo(pr), bfhi(xw) + bfhi(pr));
    }
    *(u32x2*)(op + d * 16) = res;
  }
  STAMP(25);
  STAMP_OUT(0, 26);
}

extern "C" void kernel_launch(void* const* d_in, const int* in_sizes, int n_in, void* d_out, int out_size, void* d_ws, size_t ws_size,
                              hipStream_t stream) {
  k_fused<<<256, 512, 0, stream>>>((const u16*)d_in[0], (const u16*)d_in[1], (const u16*)d_in[2], (const u16*)d_in[3], (const u16*)d_in[4],
                                   (const u16*)d_in[6], (const u16*)d_in[7], (const u16*)d_in[8], (const u16*)d_in[9], (char*)d_ws, (u16*)d_out);
}
```

```cpp
#include <hip/hip_runtime.h>
#include <stdint.h>
#include <type_traits>

typedef unsigned short u16;
typedef short bf16x8 __attribute__((ext_vector_type(8)));
typedef short s16x4 __attribute__((ext_vector_type(4)));
typedef float f32x4 __attribute__((ext_vector_type(4)));
typedef float f32x2 __attribute__((ext_vector_type(2)));
typedef unsigned u32x4 __attribute__((ext_vector_type(4)));
typedef unsigned u32x2 __attribute__((ext_vector_type(2)));
typedef __bf16 bf2_t __attribute__((ext_vector_type(2)));
#define LAS __attribute__((address_space(3)))
#define DI __device__ __forceinline__
#define AI __attribute__((always_inline))
#define SBAR() __builtin_amdgcn_sched_barrier(0)
template <class T> __device__ __forceinline__ void PIN(T& x) { asm volatile("" : "+v"(x)); }
template <class B> DI void MF(f32x4& acc, const bf16x8& a, const B& b) { asm volatile("v_mfma_f32_16x16x32_bf16 %0, %1, %2, %0" : "+v"(acc) : "v"(a), "v"(b)); }
template <class B> DI void MFC(f32x4& d, const bf16x8& a, const B& b, const f32x4& c) { asm volatile("v_mfma_f32_16x16x32_bf16 %0, %1, %2, %3" : "=&v"(d) : "v"(a), "v"(b), "v"(c)); }
template <class B> DI void MFZ(f32x4& d, const bf16x8& a, const B& b) { asm volatile("v_mfma_f32_16x16x32_bf16 %0, %1, %2, 0" : "=&v"(d) : "v"(a), "v"(b)); }
#define NOPS(n) asm volatile("s_nop " #n)
#define MFMA16(a, b, c) __builtin_amdgcn_mfma_f32_16x16x16bf16_1k((a), (b), (c), 0, 0, 0)
#define IC(n) std::integral_constant<int, (n)>{}

constexpr int SEQ = 2048, DM = 1024, DH = 128, SK = 2048, WIN = 512, FF = 3584;
constexpr size_t WS_XN = 0;
constexpr size_t WS_KV = 4u << 20;
constexpr size_t WS_ST = 5u << 20;
constexpr size_t WS_EP = WS_ST + 16384;
constexpr size_t WS_G0 = WS_ST + 32768;
constexpr size_t WS_FK = WS_ST + 49152;
constexpr size_t WS_YP = WS_ST + 65536;
constexpr size_t WS_FO = 7u << 20;
constexpr size_t WS_FY = WS_FO + 65536;
constexpr size_t WS_FX = WS_FO + 131072;
constexpr size_t WS_O = 8u << 20;
constexpr size_t WS_GU = 32u << 20;
constexpr size_t WS_XC = 24u << 20;

template <int I, int N, class F> DI void sfor_impl(F&& f) {
  if constexpr (I < N) { f(std::integral_constant<int, I>{}); sfor_impl<I + 1, N>(f); }
}
template <int N, class F> DI void sfor(F&& f) { sfor_impl<0, N>(f); }

DI unsigned pk2(float a, float b) { f32x2 v = {a, b}; return __builtin_bit_cast(unsigned, __builtin_convertvector(v, bf2_t)); }
DI float rbf(float a) { return (float)(__bf16)a; }
DI float bflo(unsigned u) { return __uint_as_float(u << 16); }
DI float bfhi(unsigned u) { return __uint_as_float(u & 0xffff0000u); }
DI float sigmoid_f(float v) { return __builtin_amdgcn_rcpf(1.f + __builtin_amdgcn_exp2f(v * -1.4426950408889634f)); }
DI float sel2(bool c, float a, float b) { PIN(a); PIN(b); return c ? a : b; }
DI float silu_f(float v) { return v * sigmoid_f(v); }
template <int CTRL> DI float dppf(float x) { return __builtin_bit_cast(float, __builtin_amdgcn_mov_dpp(__builtin_bit_cast(int, x), CTRL, 0xf, 0xf, true)); }
template <int CTRL> DI unsigned dppu(unsigned x) { return (unsigned)__builtin_amdgcn_mov_dpp((int)x, CTRL, 0xf, 0xf, true); }
constexpr int XOR1 = 0xB1, XOR2 = 0x4E, XOR7 = 0x141, XOR8 = 0x128;
DI float xg_sum(float x) {
  auto s = __builtin_amdgcn_permlane16_swap(__float_as_uint(x), __float_as_uint(x), false, false);
  x = __uint_as_float(s[0]) + __uint_as_float(s[1]);
  auto t = __builtin_amdgcn_permlane32_swap(__float_as_uint(x), __float_as_uint(x), false, false);
  return __uint_as_float(t[0]) + __uint_as_float(t[1]);
}
DI float xg_max(float x) {
  auto s = __builtin_amdgcn_permlane16_swap(__float_as_uint(x), __float_as_uint(x), false, false);
  x = __builtin_fmaxf(__uint_as_float(s[0]), __uint_as_float(s[1]));
  auto t = __builtin_amdgcn_permlane32_swap(__float_as_uint(x), __float_as_uint(x), false, false);
  return __builtin_fmaxf(__uint_as_float(t[0]), __uint_as_float(t[1]));
}
DI float wave_sum(float v) { v += dppf<XOR1>(v); v += dppf<XOR2>(v); v += dppf<XOR7>(v); v += dppf<XOR8>(v); return xg_sum(v); }
DI unsigned umax(unsigned a, unsigned b) { return a > b ? a : b; }
DI unsigned wave_max_u32(unsigned v) {
  v = umax(v, dppu<XOR1>(v)); v = umax(v, dppu<XOR2>(v)); v = umax(v, dppu<XOR7>(v)); v = umax(v, dppu<XOR8>(v));
  auto s = __builtin_amdgcn_permlane16_swap(v, v, false, false); v = umax(s[0], s[1]);
  auto t = __builtin_amdgcn_permlane32_swap(v, v, false, false); v = umax(t[0], t[1]);
  return __builtin_amdgcn_readfirstlane(v);
}

template <int OFF, bool DEV = false> DI void glds(unsigned ldsaddr, unsigned voff, const void* sbase) {
  if constexpr (DEV) asm volatile("s_mov_b32 m0, %0\n\ts_nop 0\n\tglobal_load_lds_dwordx4 %1, %2 offset:%3 sc1" ::"s"(ldsaddr), "v"(voff), "s"(sbase), "i"(OFF) : "memory");
  else asm volatile("s_mov_b32 m0, %0\n\ts_nop 0\n\tglobal_load_lds_dwordx4 %1, %2 offset:%3" ::"s"(ldsaddr), "v"(voff), "s"(sbase), "i"(OFF) : "memory");
}
DI void touch(unsigned voff, const void* sbase) {
  asm volatile("global_load_dword v255, %0, %1" ::"v"(voff), "s"(sbase) : "memory", "v255");
}
template <int OFF, bool DEV = false, class T> DI void gld16(T& dst, unsigned voff, const void* sbase) {
  if constexpr (DEV) asm volatile("global_load_dwordx4 %0, %1, %2 offset:%3 sc1" : "=v"(dst) : "v"(voff), "s"(sbase), "i"(OFF) : "memory");
  else asm volatile("global_load_dwordx4 %0, %1, %2 offset:%3" : "=v"(dst) : "v"(voff), "s"(sbase), "i"(OFF) : "memory");
}
template <int OFF, bool DEV = false, class T> DI void gld8(T& dst, unsigned voff, const void* sbase) {
  if constexpr (DEV) asm volatile("global_load_dwordx2 %0, %1, %2 offset:%3 sc1" : "=v"(dst) : "v"(voff), "s"(sbase), "i"(OFF) : "memory");
  else asm volatile("global_load_dwordx2 %0, %1, %2 offset:%3" : "=v"(dst) : "v"(voff), "s"(sbase), "i"(OFF) : "memory");
}
template <int OFF> DI void gld2(unsigned& dst, unsigned voff, const void* sbase) {
  asm volatile("global_load_ushort %0, %1, %2 offset:%3" : "=v"(dst) : "v"(voff), "s"(sbase), "i"(OFF) : "memory");
}
template <int OFF, class T> DI void gst16(const T& v, unsigned voff, const void* sbase) {
  asm volatile("global_store_dwordx4 %0, %1, %2 offset:%3 sc1\n\ts_nop 1" ::"v"(voff), "v"(v), "s"(sbase), "i"(OFF) : "memory");
}
template <class T> DI void gst8(const T& v, unsigned voff, const void* sbase) {
  asm volatile("global_store_dwordx2 %0, %1, %2 sc1" ::"v"(voff), "v"(v), "s"(sbase) : "memory");
}
template <int OFF> DI void gst4(float v, unsigned voff, const void* sbase) {
  asm volatile("global_store_dword %0, %1, %2 offset:%3 sc1" ::"v"(voff), "v"(v), "s"(sbase), "i"(OFF) : "memory");
}
constexpr int SPINS = 1 << 16;
DI void poll8(unsigned voff, const void* sbase, unsigned lo, unsigned hi, unsigned lomask) {
  for (int spin = 0; spin < SPINS; ++spin) {
    u32x2 v;
    asm volatile("global_load_dwordx2 %0, %1, %2 sc1\n\ts_waitcnt vmcnt(0)" : "=&v"(v) : "v"(voff), "s"(sbase) : "memory");
    if (__all(((v[0] & lomask) == lo) & (v[1] == hi))) break;
    __builtin_amdgcn_s_sleep(1);
  }
}
template <int N> DI void waitv() { static_assert(N >= 0, "count"); asm volatile("s_waitcnt vmcnt(%0)" ::"n"(N > 63 ? 63 : N) : "memory"); }
template <int N, class T> DI void waitv(T& x) { static_assert(N >= 0, "count"); asm volatile("s_waitcnt vmcnt(%1)" : "+v"(x) : "n"(N > 63 ? 63 : N) : "memory"); }
template <int N, class T> DI void waitv(T& x, T& y) { static_assert(N >= 0, "count"); asm volatile("s_waitcnt vmcnt(%2)" : "+v"(x), "+v"(y) : "n"(N > 63 ? 63 : N) : "memory"); }
template <int N> DI void waitbar() { static_assert(N >= 0, "count"); asm volatile("s_waitcnt vmcnt(%0)\n\ts_barrier" ::"n"(N > 63 ? 63 : N) : "memory"); }
template <int N> DI void waitbar_l() { static_assert(N >= 0, "count"); asm volatile("s_waitcnt vmcnt(%0) lgkmcnt(0)\n\ts_barrier" ::"n"(N > 63 ? 63 : N) : "memory"); }
typedef short v4i16_t __attribute__((ext_vector_type(4)));
DI s16x4 trd(LAS const char* p) { return __builtin_amdgcn_ds_read_tr16_b64_v4i16((LAS v4i16_t*)p); }
struct LBase {
  unsigned lo, hi;
  DI void init(unsigned a) { lo = a; hi = a + 65536; PIN(lo); PIN(hi); }
  template <int OFF> DI LAS const char* at() const { return (LAS const char*)(uintptr_t)(OFF < 65536 ? lo : hi) + (OFF & 65535); }
};
DI bf16x8 cat8(s16x4 lo, s16x4 hi) { return __builtin_shufflevector(lo, hi, 0, 1, 2, 3, 4, 5, 6, 7); }

enum { OX, OG, OF, OM, OS, OP, OE, OW };
struct Sched {
  int n; short kind[400], idx[400], slot[400];
  constexpr Sched() : n(0), kind{}, idx{}, slot{} {}
  constexpr void add(int s, int k, int i) { kind[n] = (short)k; idx[n] = (short)i; slot[n] = (short)s; ++n; }
  constexpr int younger(int k, int i, int s) const {
    int p = -1;
    for (int q = 0; q < n; ++q) if (kind[q] == k && idx[q] == i) p = q;
    if (p < 0 || slot[p] > s) return -1;
    int c = 0;
    for (int q = p + 1; q < n; ++q) if (slot[q] <= s) ++c;
    return c;
  }
  constexpr int first(int s) const { int q = 0; while (q < n && slot[q] < s) ++q; return q; }
  constexpr int count(int s) const { int c = 0; for (int q = 0; q < n; ++q) if (slot[q] == s) ++c; return c; }
};

#define STAMP_DECL
#define STAMP(i)
#define STAMP_OUT(kid, n)

constexpr int RING = 4 * 32768, FB = RING, RED1 = FB + 24576, HSC = RED1 + 1024, YS = HSC + 1024, LDS_BYTES = FB + 32768;
constexpr int STAGE = 3 * 32768;
constexpr int XRAW = 24;
constexpr int XD = 16, XD2 = 16;
constexpr float LBOUND = 65536.f;
constexpr float QSCALE = 0.08838834764831845f * 1.4426950408889634f;
constexpr int B0 = 128;
#define PRIO 1
constexpr int FISS[4] = {96, 97, 98, 117}, FRDS[4] = {115, 118, 121, 124};
constexpr int fpiece(int sl) { for (int p = 0; p < 4; ++p) if (FRDS[p] == sl) return p; return -1; }
constexpr Sched make_sched() {
  Sched s;
  for (int i = 0; i < 4; ++i) s.add(-2, OX, i);
  s.add(-2, OG, 0); s.add(-2, OG, 1);
  s.add(-1, OG, 2); s.add(-1, OG, 3);
  for (int i = 4; i < 8; ++i) s.add(-1, OX, i);
  for (int i = 4; i < 8; ++i) s.add(-1, OG, i);
  for (int t = 0; t < 128; ++t) {
    if (t < 2) for (int i = 0; i < 4; ++i) s.add(t, OX, 8 + 4 * t + i);
    if (t >= 2 && t < 10) s.add(t, OX, t + 14);
    if (t >= 3 && t < 7) s.add(t, OG, 5 + t);
    if (t == 11) s.add(t, OP, 0);
    if (t == 16) { s.add(t, OP, 1); s.add(t, OP, 2); }
    if (t >= 16 && t < 22) { s.add(t, OX, 2 * t - 8); s.add(t, OX, 2 * t - 7); }
    if (t >= 22 && t < 50) s.add(t, OX, t + 14);
    for (int v = 3; v < 18; ++v) for (int p = 0; p < 4; ++p) if (8 * (v - 3) + 7 + p == t) s.add(t, OG, 4 * v + p);
    if (t == 105 || t == 113 || t == 121) { s.add(t, OW, (t - 105) / 4); s.add(t, OW, (t - 105) / 4 + 1); }
    if (t >= 124) for (int i = 0; i < 3; ++i) s.add(t, OX, 64 + 3 * (t - 124) + i);
    if (t == 127) { s.add(t, OG, 72); s.add(t, OG, 73); }
    if (t == 28) s.add(t, OM, 0);
    if (t == 120) s.add(t, OM, 1);
    if (t >= 49 && t < 57 && (t & 1)) s.add(t, OF, (t - 49) / 2);
    if (t >= 71 && t < 79 && (t & 1)) s.add(t, OF, 4 + (t - 71) / 2);
    if (t == 48) s.add(t, OS, 0);
    if (t == 104) s.add(t, OS, 2);
    for (int p = 0; p < 4; ++p) if (FISS[p] == t) s.add(t, OF, 8 + p);
  }
  for (int t = 0; t < 64; ++t) {
    if (t == 0) { s.add(B0, OM, 2); s.add(B0, OG, 74); s.add(B0, OG, 75); s.add(B0, OW, 6); s.add(B0, OW, 7); }
    if (t == 1 || t == 2) { s.add(B0 + t, OX, 74 + 2 * t); s.add(B0 + t, OX, 75 + 2 * t); }
    if (t == 5 || t == 6 || t == 9) s.add(B0 + t, OX, 64 + (t == 5 ? 16 : t == 6 ? 18 : 22));
    if (t >= 5 && t < 50) s.add(B0 + t, OX, 64 + (t == 5 ? 17 : t < 9 ? t + 13 : t + 14));
    for (int v = 3; v < 16; ++v) for (int p = 0; p < 4; ++p) if ((v < 14 ? 4 * (v - 3) + 3 + (p >> 1) : v == 14 ? 45 + (p >> 1) : 49) == t) s.add(B0 + t, OG, 64 + 4 * v + p);
    if (t == 10 || t == 26) s.add(B0 + t, OM, t == 10 ? 3 : 4);
    if (t == 50) for (int i = 0; i < 5; ++i) s.add(B0 + t, OE, i);
  }
  return s;
}
constexpr Sched SC = make_sched();

__global__ __launch_bounds__(512, 2) void k_fused(const u16* __restrict__ x, const u16* __restrict__ kc, const u16* __restrict__ vc,
                                                  const u16* __restrict__ wq, const u16* __restrict__ wo, const u16* __restrict__ wr,
                                                  const u16* __restrict__ wgate, const u16* __restrict__ wup, const u16* __restrict__ wdown,
                                                  char* __restrict__ ws, u16* __restrict__ out) {
  __shared__ __attribute__((aligned(16))) char lds_raw[LDS_BYTES];
  LAS char* const lds = (LAS char*)lds_raw;
  const int t = threadIdx.x, l = t & 63, bid = blockIdx.x, c = l & 15, g = l >> 4;
  const int wid = __builtin_amdgcn_readfirstlane(t >> 6);
  const int xcd = bid & 7, within = bid >> 3, head = xcd * 4 + (within & 3), rb = within >> 2;
  const int fj = xcd * 28 + (within < 28 ? within : within - 28);
  const int fsl = fj / 7, fgr = fj - fsl * 7;
  const int kh = xcd >> 2, mb = 2 * rb + (xcd & 1), nb = ((xcd >> 1) & 1) * 4 + (within & 3);
  const int rbw = wid & 3, kg = wid >> 2;
  const unsigned lds0 = (unsigned)(uintptr_t)lds;
  STAMP_DECL
  STAMP(0);

  const int row = 8 * bid + wid;
  u32x4 a0, a1, w0, w1;
  u32x2 ep, klo, khi;
  unsigned e[8], tlo, thi, glo, ghi;
  gld16<0>(a0, (unsigned)(row * DM * 2 + l * 16), x); gld16<1024>(a1, (unsigned)(row * DM * 2 + l * 16), x);
  gld16<0>(w0, (unsigned)(l * 16), wr); gld16<1024>(w1, (unsigned)(l * 16), wr);
  gld8<0, true>(ep, 0u, ws + WS_EP);
  const int fn = (bid * 4 + wid) & 255, kvh = bid >> 6;

  const char* const wq_b = (const char*)wq + head * 256;
  const unsigned wq_v = (unsigned)(((8 * wid + ((l & 31) >> 2)) * 4096 + (l >> 5) * 32 + ((l & 3) ^ (2 * (wid & 1))) * 8) * 2);
  const char* const kv_b = ws + WS_KV + (size_t)(head >> 3) * 262144;
  const unsigned kv_v = (unsigned)(wid * 4096 + l * 16);
  const char* const xn_b = ws + WS_XN + (size_t)rb * 8 * 65536 + 4096;
  const unsigned xn_v = (unsigned)(wid * 65536 + l * 16);
  const char* const wo_b = (const char*)wo + (size_t)kh * 2048 * (DM * 2) + nb * 256;
  const char* const o_b = ws + WS_O + (size_t)mb * 4 * 262144 + kh * 131072;
  const unsigned ldsw = lds0 + wid * 2048, ldsk = lds0 + wid * 4096, ldsf = lds0 + FB + wid * 1024;
  const int erow = mb * 128 + rbw * 32 + kg * 16 + c, ecol = nb * 128 + kh * 64 + 4 * g;
  bf16x8 xf[XD];
  u32x4 fm[2];
  bf16x8 fxs;
  const unsigned gu_v = (unsigned)((((4 * wid + g) * 2 + ((l >> 3) & 1)) * FF + fj * 16 + (l & 7) * 2) * 8);
  u32x2 fg;
  u32x2 xe[4];
  f32x2 st, qst[2];
  unsigned key = 0;

  const unsigned fo_v = (unsigned)(((mb * 4 + rbw) * 32 + kh * 16 + c) * 8);
  const int yrow = within < 28 ? fj : 224 + xcd * 4 + within - 28;
  f32x4 o[2][2][2];
  float inv[2];
  const char* const od_b = ws + WS_O + ((size_t)(rb * 8) * 128 + head * 4) * 2048 + 4096;
  auto issue = [&](auto Sl, auto Pos) AI {
    constexpr int sl = Sl, pos = Pos, cnt = SC.count(sl), q0 = SC.first(sl) + (pos < 0 ? 0 : pos);
    constexpr int nq = pos < 0 ? cnt : pos >= cnt ? 0 : pos < 3 ? 1 : cnt - 3;
    sfor<nq>([&](auto Qi) AI {
      constexpr int k = SC.kind[q0 + Qi], i = SC.idx[q0 + Qi];
      if constexpr (k == OX && i < XRAW)
        gld16<(i >> 2) * 128>(xf[i % XD], (unsigned)((wid * 32 + (l >> 3)) * (DM * 2) + (l & 7) * 16), (const char*)x + (size_t)(rb * 256 + (i & 3) * 8) * (DM * 2));
      if constexpr (k == OX && i >= XRAW && i < 64) gld16<(i & 7) * 1024 - 4096, true>(xf[i % XD], xn_v, xn_b + (size_t)(i >> 3) * 8192);
      if constexpr (k == OP && i == 0) gld8<0, true>(fg, (unsigned)(((rb * 8 + wid) * 4 + (l & 3)) * 8), ws + WS_G0);
      if constexpr (k == OP && i > 0) gld8<(i - 1) * 128, true>(qst[i - 1], (unsigned)((rb * 256 + wid * 32 + c) * 8), ws + WS_ST);
      if constexpr (k == OX && i >= 64) {
        constexpr int q = i - 64, v = q >> 2;
        gld16<0, true>(xf[q % XD2], (unsigned)(rbw * 262144 + kg * 16384 + ((q & 1) ^ kg) * 1024 + l * 16), o_b + (size_t)(((v & 3) * 4 + ((q >> 1) & 1)) * 8192 + (v >> 2) * 2048));
      }
      if constexpr (k == OG) {
        constexpr int v = i >> 2, p = i & 3;
        if constexpr (v < 8) {
          const char* sb = wq_b + (size_t)(v * 128 + (p >> 1) * 64) * 8192;
          glds<(p & 1) * 128>(ldsw + (v & 3) * 32768 + (p >> 1) * 16384 + (p & 1) * (1024 - 128), wq_v, sb);
        } else if constexpr (v < 16) {
          glds<p * 1024, true>(ldsk + (v & 3) * 32768, kv_v, kv_b + (size_t)(v - 8) * 32768);
        } else {
          const unsigned wo_v = (unsigned)((((wid >> 2) * 128 + (wid & 3) * 8 + ((l & 31) >> 2)) * DM + (l >> 5) * 32 + ((l & 3) ^ (2 * (wid & 1))) * 8) * 2);
          const char* sb = wo_b + (size_t)(((v & 3) * 4 + (p >> 1) * 2) * 128 + ((v - 16) >> 2) * 32) * (DM * 2) + (((p & 1) ^ kh ^ 1) << 7);
          glds<0>(ldsw + (v & 3) * 32768 + (p >> 1) * 16384 + (p & 1) * 1024, wo_v, sb);
        }
      }
      if constexpr (k == OF && i < 8)
        glds<0>(ldsf + (i & 3) * 8192, (unsigned)(((l >> 3) * FF + wid * 64 + ((l & 7) ^ ((i & 1) * 4 + ((l >> 3) & 2))) * 8) * 2),
                (const char*)(i < 4 ? wgate : wup) + ((size_t)(fsl * 32 + 8 * (i & 3)) * FF + fgr * 512) * 2);
      if constexpr (k == OF && i >= 8) {
        const int frho = l >> 2, fch = (l & 3) ^ (frho & 4 ? 2 : 0);
        glds<0>(ldsf + ((i - 8) % 3) * 8192, (unsigned)((frho * DM + wid * 128 + fch * 8) * 2), (const char*)wdown + (size_t)fj * 16 * DM * 2 + (i - 8) * 64);
      }
      if constexpr (k == OS && i == 0) {
        const int idx = SEQ - 1 - (int)(key & 2047u);
        gld16<0, true>(fxs, (unsigned)((((idx >> 5) * 32 + fsl) * 2 + ((idx >> 4) & 1)) * 1024 + (g * 16 + (idx & 15)) * 16), ws + WS_XN);
      }
      if constexpr (k == OS && i == 2) gld16<0, true>(fm[0], gu_v, ws + WS_GU);
      if constexpr (k == OM && i > 0) gld8<0, true>(fg, fo_v, ws + WS_FO + (i - 1) * 16384);
      if constexpr (k == OM && i == 0) gld8<0, true>(fg, (unsigned)((t & 255) * 8), wid < 4 ? ws + WS_G0 : ws + WS_FK + (head >> 3) * 2048);
      if constexpr (k == OW) {
        constexpr int os = i >> 1, qb = i & 1;
        PIN(o[os & 1][0][qb]); PIN(o[os & 1][1][qb]);
        const f32x4 a = o[os & 1][0][qb] * inv[qb], b = o[os & 1][1][qb] * inv[qb];
        gst16<i * 1024 - 4096>((u32x4){pk2(a[0], a[1]), pk2(a[2], a[3]), pk2(b[0], b[1]), pk2(b[2], b[3])}, (unsigned)(wid * 262144 + l * 16), od_b);
      }
      if constexpr (k == OE && i < 4) gld8<32 * i>(xe[i], (unsigned)((erow * DM + ecol) * 2), x);
      if constexpr (k == OE && i == 4) gld8<0, true>(st, (unsigned)(erow * 8), ws + WS_ST);
    });
  };
  f32x4 fa0, fa1, fq[4];
  s16x4 fb0, fb1, fxa;
  bf16x8 ft[2];
  auto endslot = [&](auto Sl) AI {
    constexpr int sl = Sl, u = sl >> 3, i = sl & 7;
    if constexpr (sl == 3) {
      if (t == 0) {
        const u32x4 ka = *(LAS const u32x4*)(lds + STAGE + 16384), kb = *(LAS const u32x4*)(lds + STAGE + 16400);
        gst8((u32x2){glo | umax(umax(umax(ka[0], ka[1]), umax(ka[2], ka[3])), umax(umax(kb[0], kb[1]), umax(kb[2], kb[3]))), ghi}, (unsigned)(bid * 8), ws + WS_G0);
      }
    }
    if constexpr (sl == 4) {
      if (wid < 4) {
        u32x4 kvo;
        if (fn < 128) {
          PIN(klo); PIN(khi);
          kvo = (u32x4){klo[0], klo[1], khi[0], khi[1]};
        } else {
#pragma unroll
          for (int j = 0; j < 8; ++j) PIN(e[j]);
          kvo = (u32x4){e[0] | (e[1] << 16), e[2] | (e[3] << 16), e[4] | (e[5] << 16), e[6] | (e[7] << 16)};
        }
        gst16<0>(kvo, (unsigned)((bid * 4 + wid) * 1024 + l * 16), ws + WS_KV);
      }
    }
    if constexpr (sl == 15) {
      waitv<SC.younger(OP, 0, sl)>(fg);
      if (wid < 4 && l == 0) gst8((u32x2){tlo, thi}, (unsigned)((bid * 4 + wid) * 8), ws + WS_FK);
      if (__builtin_expect(!__all(((fg[0] & 0xf8000000u) == glo) & (fg[1] == ghi)), 0))
        poll8((unsigned)(((rb * 8 + wid) * 4 + (l & 3)) * 8), ws + WS_G0, glo, ghi, 0xf8000000u);
    }
    if constexpr (sl == 38) {
      waitv<SC.younger(OM, 0, sl)>(fg);
      const unsigned lo = wid < 4 ? glo : tlo, hi = wid < 4 ? ghi : thi, mk = wid < 4 ? 0xf8000000u : 0xffffffffu;
      for (int spin = 0; spin < SPINS; ++spin) {
        if (__builtin_expect(__all(((fg[0] & mk) == lo) & (fg[1] == hi)), 1)) break;
        __builtin_amdgcn_s_sleep(1);
        gld8<0, true>(fg, (unsigned)((t & 255) * 8), wid < 4 ? ws + WS_G0 : ws + WS_FK + (head >> 3) * 2048);
        waitv<0>(fg);
      }
      const unsigned km = wave_max_u32(fg[0] & 0x07ffffffu);
      *(LAS unsigned*)(lds + HSC + wid * 4) = km;
    }
    if constexpr (sl == 47) {
      if (bid == 0 && t == 0) gst8((u32x2){tlo, thi}, 0u, ws + WS_EP);
      const u32x4 kk = *(LAS const u32x4*)(lds + HSC);
      key = __builtin_amdgcn_readfirstlane(umax(umax(kk[0], kk[1]), umax(kk[2], kk[3])));
    }
    if constexpr (sl == 58 || sl == 61 || sl == 64 || sl == 67) {
      if (wid == 4 + (sl - 58) / 3) touch((unsigned)(((wid & 3) * 64 + l) * 128), (const char*)wdown + (size_t)fj * 16 * DM * 2);
    }
    if constexpr (sl == 80 || sl == 82 || sl == 84 || sl == 86) {
      if (wid == (sl - 80) / 2)
        touch((unsigned)((kh * 2048 + (within >> 1) * 128 + wid * 32 + ((within & 1) * 2 + (xcd & 1)) * 8 + (l >> 3)) * (DM * 2) + ((xcd >> 1) & 1) * 1024 + (l & 7) * 128), wo);
    }
    if constexpr ((sl >= 68 && sl <= 70) || (sl >= 90 && sl <= 92)) {
      constexpr int r = sl >= 90, q = sl - (r ? 90 : 68);
      if constexpr (q == 0) waitv<SC.younger(OF, 4 * r + 3, sl)>(fxs);
      if constexpr (q >= 1) {
#pragma unroll
        for (int j = 0; j < 2; ++j) { fq[2 * q - 2 + j] = __builtin_amdgcn_mfma_f32_16x16x32_bf16(fxs, ft[j], (f32x4){}, 0, 0, 0); PIN(fq[2 * q - 2 + j]); }
      }
      if constexpr (q < 2) {
        const unsigned rp = ldsf + g * 8192 + (c >> 2) * 128 + ((((g & 1) << 1) | (c >> 3)) << 5) + (c & 3) * 8;
#pragma unroll
        for (int j = 0; j < 2; ++j) { LAS const char* a = (LAS const char*)(uintptr_t)(rp ^ ((2 * q + j) << 5)); ft[j] = cat8(trd(a), trd(a + 512)); }
      }
    }
    if constexpr (sl == 71 || sl == 93) {
      const float v = sel2(g & 2, sel2(g & 1, fq[3][0], fq[2][0]), sel2(g & 1, fq[1][0], fq[0][0]));
      gst8((u32x2){__float_as_uint(v), tlo}, (unsigned)(((fsl * 2 + (sl == 93)) * FF + fgr * 512 + t) * 8), ws + WS_GU);
    }
    if constexpr (sl == 109) {
      waitv<SC.younger(OS, 2, sl)>(fm[0]);
      for (int spin = 0; spin < SPINS; ++spin) {
        if (__builtin_expect(__all((fm[0][1] == tlo) & (fm[0][3] == tlo)), 1)) break;
        __builtin_amdgcn_s_sleep(1);
        gld16<0, true>(fm[0], gu_v, ws + WS_GU);
        waitv<0>(fm[0]);
      }
      *(LAS f32x2*)(lds + RED1 + (wid * 32 + 2 * c) * 4) = (f32x2){xg_sum(__uint_as_float(fm[0][0])), xg_sum(__uint_as_float(fm[0][2]))};
    }
    if constexpr (sl == 111) {
#pragma unroll
      for (int w = 0; w < 2; ++w) { fa0[w] = *(LAS const float*)(lds + RED1 + ((2 * g + w) * 32 + c) * 4); fa0[2 + w] = *(LAS const float*)(lds + RED1 + ((2 * g + w) * 32 + 16 + c) * 4); }
    }
    if constexpr (sl == 112) { fa0[0] = xg_sum(fa0[0] + fa0[1]); fa0[2] = xg_sum(fa0[2] + fa0[3]); PIN(fa0); }
    if constexpr (sl == 113) {
      const float hh = rbf(silu_f(rbf(fa0[0]))) * rbf(fa0[2]);
      *(LAS u16*)(lds + HSC + 32 + wid * 32 + c * 2) = (u16)pk2(hh, hh);
    }
    if constexpr (sl == 114) fxa = *(LAS const s16x4*)(lds + HSC + 32 + wid * 32 + g * 8);
    constexpr int pr = fpiece(sl), pm = fpiece(sl - 1), ps = fpiece(sl - 2);
    if constexpr (ps >= 0) gst4<ps * 128>(sel2(g & 1, fa1[0], fa0[0]), (unsigned)((wid * 128 + (l & 31)) * 4), ws + WS_YP + (size_t)yrow * 4096);
    if constexpr (pm >= 0) { fa0 = MFMA16(fxa, fb0, (f32x4){}); fa1 = MFMA16(fxa, fb1, (f32x4){}); PIN(fa0); PIN(fa1); }
    if constexpr (pr >= 0) {
      waitv<SC.younger(OF, 8 + pr, sl)>();
      const unsigned fp = ldsf + (4 * g + (c >> 2)) * 64 + (g & 1) * 32 + (c & 3) * 8;
      fb0 = trd((LAS const char*)(uintptr_t)fp + (pr % 3) * 8192); fb1 = trd((LAS const char*)(uintptr_t)(fp ^ 32) + (pr % 3) * 8192);
    }
    if constexpr (sl == 111 || sl == 119 || sl == 127) {
      waitv<SC.younger(OW, (sl - 111) / 4 + 1, sl)>();
      if (l == 0) gst8((u32x2){tlo, thi}, (unsigned)(((rb * 8 + wid) * 32 + head) * 8), ws + WS_FO + ((sl - 111) / 8) * 16384);
    }
    if constexpr (sl == 123) {
      waitv<SC.younger(OM, 1, sl)>(fg);
      if (__builtin_expect(!__all((fg[0] == tlo) & (fg[1] == thi)), 0)) poll8(fo_v, ws + WS_FO, tlo, thi, 0xffffffffu);
    }
    SBAR();
    if constexpr (sl == 2) waitbar_l<SC.younger(OG, 3, sl)>();
    if constexpr (i == 6) {
      if constexpr (u == 5 || u == 13) waitbar_l<SC.younger(OG, 4 * u + 7, sl)>();
      else waitbar<SC.younger(OG, 4 * u + 7, sl)>();
      if constexpr (u == 0 || u == 1 || u == 5 || u == 7 || u == 8 || u == 10 || u == 12 || u == 14)
        STAMP(u == 0 ? 4 : u == 1 ? 5 : u == 5 ? 6 : u == 7 ? 7 : u == 8 ? 8 : u == 10 ? 9 : u == 12 ? 10 : 11);
    }
    if constexpr (PRIO && !(i & 1)) __builtin_amdgcn_s_setprio(i == 6 ? 3 : 2 - i / 2);
  };

  __builtin_amdgcn_s_barrier();
  issue(IC(-2), IC(-1));
  if (wid < 4) {
    touch((unsigned)((wid * 256 + within * 8 + (l >> 3)) * 8192 + xcd * 1024 + (l & 7) * 128), wq);
    if (fn < 128) {
      const unsigned ko = (unsigned)(((kvh * SK + (SK - WIN) + (fn >> 2) * 16 + c) * DH + (fn & 3) * 32 + 4 * g) * 2);
      gld8<0>(klo, ko, kc); gld8<32>(khi, ko, kc);
      waitv<12>();
    } else {
      const int d = 32 * ((fn - 128) >> 5) + 8 * (c >> 2) + 4 * (fn & 1) + (c & 3);
      const unsigned vo = (unsigned)(((kvh * SK + (SK - WIN) + ((fn >> 1) & 15) * 32 + 4 * g + 8) * DH + d) * 2);
      sfor<8>([&](auto J) AI { constexpr int j = J; gld2<(16 * (j >> 2) + (j & 3) - 8) * DH * 2>(e[j], vo, vc); });
      waitv<18>();
    }
  } else {
    if (wid == 4) {
      const char* pc;
      asm volatile("s_getpc_b64 %0" : "=s"(pc));
      touch((unsigned)((within * 12 + (l < 12 ? l : 11)) * 128), pc);
    }
    waitv<9>();
  }
  PIN(a0); PIN(a1);
  STAMP(1);
  {
    float v[16];
#pragma unroll
    for (int j = 0; j < 4; ++j) { v[2 * j] = bflo(a0[j]); v[2 * j + 1] = bfhi(a0[j]); v[8 + 2 * j] = bflo(a1[j]); v[9 + 2 * j] = bfhi(a1[j]); }
    float sm = 0.f;
#pragma unroll
    for (int j = 0; j < 16; ++j) sm += v[j];
    const float mean = wave_sum(sm) * (1.f / DM);
    float q = 0.f;
#pragma unroll
    for (int j = 0; j < 16; ++j) { const float d = v[j] - mean; q += d * d; }
    const float var = wave_sum(q) * (1.f / DM);
    const float mbf = rbf(mean), rden = __builtin_amdgcn_rcpf(rbf(__builtin_amdgcn_sqrtf(rbf(rbf(var) + 1e-5f))));
    u32x4 o0, o1;
#pragma unroll
    for (int j = 0; j < 8; ++j) {
      const unsigned dd = pk2(v[2 * j] - mbf, v[2 * j + 1] - mbf);
      const unsigned nn = pk2(bflo(dd) * rden, bfhi(dd) * rden);
      if (j < 4) o0[j] = nn; else o1[j - 4] = nn;
    }
    *(LAS u32x4*)(lds + STAGE + (l * 8 + (wid ^ (l & 7))) * 16) = o0;
    *(LAS u32x4*)(lds + STAGE + 8192 + (l * 8 + (wid ^ (l & 7))) * 16) = o1;
    if (wid >= 4) waitv<6>(); else if (fn < 128) waitv<9>(); else waitv<15>();
    PIN(w0); PIN(w1); PIN(ep);
    const unsigned long long tg = (((unsigned long long)(unsigned)__builtin_amdgcn_readfirstlane(ep[1]) << 32) | (unsigned)__builtin_amdgcn_readfirstlane(ep[0])) + 1ull;
    tlo = (unsigned)tg; thi = (unsigned)(tg >> 32); glo = tlo << 27; ghi = (tlo >> 5) | (thi << 27);
    float rr = 0.f;
#pragma unroll
    for (int j = 0; j < 8; ++j) {
      const unsigned nn = j < 4 ? o0[j] : o1[j - 4], ww = j < 4 ? w0[j] : w1[j - 4];
      rr += bflo(nn) * bflo(ww); rr += bfhi(nn) * bfhi(ww);
    }
    rr = wave_sum(rr);
    const unsigned bits = pk2(rr, 0.f) & 0xffffu;
    const unsigned ord = (bits & 0x8000u) ? (~bits & 0xffffu) : (bits | 0x8000u);
    *(LAS unsigned*)(lds + STAGE + 16384 + wid * 4) = (ord << 11) | (unsigned)(SEQ - 1 - row);
    if (wid >= 4) waitv<1>(); else if (fn < 128) waitv<3>(); else waitv<9>();
    if (wid > 4) waitv<0>();
    waitbar_l<63>();
    if (l == 0) gst8((f32x2){mbf, rden}, (unsigned)(row * 8), ws + WS_ST);
#pragma unroll
    for (int hh = 0; hh < 2; ++hh) {
      const int p = hh * 512 + t, ch = p >> 3, r = 8 * bid + ((p & 7) ^ (ch & 7));
      const u32x4 o = *(LAS const u32x4*)(lds + STAGE + p * 16);
      gst16<0>(o, (unsigned)(((((r >> 5) * 32 + (ch >> 2)) * 2 + ((r >> 4) & 1)) * 64 + (ch & 3) * 16 + (r & 15)) * 16), ws + WS_XN);
    }
  }
  STAMP(2);
  issue(IC(-1), IC(-1));
  auto XW = [&](auto Ni) AI {
    constexpr int n = Ni, j = n & 3;
    PIN(xf[n % XD]);
    *(LAS bf16x8*)(uintptr_t)(ldsf + (((l >> 2) & 1) * 2 + (j >> 1)) * 8192 + ((l & 3) * 16 + (j & 1) * 8 + (((l >> 3) ^ l) & 7)) * 16) = xf[n % XD];
  };
  auto XR = [&](auto Ni) AI {
    constexpr int n = Ni, j = n & 3;
    xf[n % XD] = *(LAS const bf16x8*)(uintptr_t)(ldsf + j * 8192 + (g * 16 + (c & 8) + ((c ^ g ^ ((j >> 1) * 4)) & 7)) * 16);
  };
  sfor<4>([&](auto J) AI { XW(J); });
  sfor<4>([&](auto J) AI { XR(J); });
  STAMP(3);

  LBase trb[2], frb;
  trb[0].init(lds0 + g * 2048 + (c >> 2) * 64 + (g & 1) * 32 + (c & 3) * 8);
  trb[1].init(lds0 + g * 2048 + (c >> 2) * 64 + ((g & 1) ^ 1) * 32 + (c & 3) * 8);
  frb.init(lds0 + l * 16);
  f32x4 acc[8][2];
  bf16x8 wf[2][4];
  auto WRD1 = [&](auto Si, auto Base, auto Dd) AI {
    constexpr int s = Si, d = Dd, o = Base + (d >> 1) * 512;
    wf[s & 1][d] = cat8(trd(trb[d & 1].template at<o>()), trd(trb[d & 1].template at<o + 256>()));
  };
  auto WRD = [&](auto Si, auto Base) AI { sfor<4>([&](auto Dd) AI { WRD1(Si, Base, Dd); }); };
#define WA(s) IC((((s) >> 3) & 3) * 32768 + (((s) >> 1) & 3) * 8192 + ((s) & 1) * 1024)
#define WB(s) IC((((s) >> 2) & 3) * 32768 + (((s) >> 1) & 1) * 8192 + ((s) & 1) * 1024)
  bf16x8 fr[4];
  auto FRD = [&](auto Ni) AI { constexpr int n = Ni; if constexpr (n < 256) fr[n & 3] = *(LAS const bf16x8*)(frb.at<((n >> 5) & 3) * 32768 + (n & 31) * 1024>()); };

  WRD(IC(0), WA(0));
  SBAR();
  bf16x8 qf[4][2];
  auto QPACK = [&](auto Ds) AI {
    constexpr int ds = Ds;
#pragma unroll
    for (int qb = 0; qb < 2; ++qb) {
      const f32x4 a = acc[2 * ds][qb] * QSCALE, b = acc[2 * ds + 1][qb] * QSCALE;
      qf[ds][qb] = __builtin_bit_cast(bf16x8, (u32x4){pk2(a[0], a[1]), pk2(a[2], a[3]), pk2(b[0], b[1]), pk2(b[2], b[3])});
      PIN(qf[ds][qb]);
    }
  };
  f32x4 cs[8];
  bf16x8 ones = {0x3f80, 0x3f80, 0x3f80, 0x3f80, 0x3f80, 0x3f80, 0x3f80, 0x3f80};
  PIN(ones);
  NOPS(1);
  sfor<64>([&](auto Si) AI {
    constexpr int s = Si, x0 = (s & ~1) % XD, x1 = (s | 1) % XD;
    if constexpr (!(s & 1) && s >= XRAW) waitv<SC.younger(OX, s + 1, s - 1)>(xf[x0], xf[x1]);
    if constexpr ((s & 3) == 1 && s + 3 < XRAW) waitv<SC.younger(OX, s + 6, s - 1)>();
    sfor<4>([&](auto Dd) AI {
      constexpr int d = Dd, dt = (s & 1) * 4 + d;
      if constexpr (s + 1 < 64) WRD1(IC(s + 1), WA(s + 1), Dd);
      else if constexpr (d < 3) FRD(Dd);
      if constexpr ((s & 3) == 1 && s + 3 < XRAW) XW(IC(s + 3 + d));
      if constexpr ((s & 3) == 2 && s + 2 < XRAW) XR(IC(s + 2 + d));
      if constexpr (s == XRAW || s == XRAW + 1) {
#pragma unroll
        for (int qb = 0; qb < 2; ++qb) {
          PIN(qst[qb]); PIN(acc[dt][qb]); PIN(cs[dt]);
          acc[dt][qb] = (acc[dt][qb] - cs[dt] * qst[qb][0]) * qst[qb][1];
          PIN(acc[dt][qb]);
        }
        NOPS(1);
      }
      SBAR();
      if constexpr (s < 2) { MFZ(acc[dt][0], wf[s & 1][d], xf[x0]); MFZ(acc[dt][1], wf[s & 1][d], xf[x1]); MFZ(cs[dt], wf[s & 1][d], ones); }
      else { MF(acc[dt][0], wf[s & 1][d], xf[x0]); MF(acc[dt][1], wf[s & 1][d], xf[x1]); }
      if constexpr (s >= 2 && s < XRAW) MF(cs[dt], wf[s & 1][d], ones);
      issue(Si, Dd);
      if constexpr (s == 63 && (d & 1)) { QPACK(IC(d >> 1)); SBAR(); }
    });
    SBAR();
    endslot(Si);
  });

  f32x4 sc[2][2][2], negm[2];
  u32x4 pw[16][2];
  float lsum[2] = {0.f, 0.f};
#define MX3(a, b, c) __builtin_fmaxf(__builtin_fmaxf((a), (b)), (c))
#define EXP2(x) __builtin_amdgcn_exp2f(x)
  auto SM = [&](auto Hi, auto Pi) AI {
    constexpr int H = Hi, p = Pi, kt = p >> 2, qb = (p >> 1) & 1, r = 2 * (p & 1);
    const float e0 = EXP2(sc[H & 1][kt][qb][r]), e1 = EXP2(sc[H & 1][kt][qb][r + 1]);
    lsum[qb] += e0; lsum[qb] += e1;
    unsigned w = pk2(e0, e1);
    PIN(lsum[qb]); PIN(w);
    pw[H][qb][kt * 2 + (p & 1)] = w;
  };
  sfor<128>([&](auto Ni) AI {
    constexpr int n = Ni, H = n >> 3, i = n & 7, kt = i >> 2, ds = i & 3, sn = H & 1;
    FRD(IC(n + 3));
    if constexpr (n < 2) QPACK(IC(2 + n));
    sfor<2>([&](auto Qb) AI {
      constexpr int qb = Qb;
      if constexpr (ds == 0 && H == 0) MFZ(sc[sn][kt][qb], fr[n & 3], qf[0][qb]);
      else if constexpr (ds == 0) MFC(sc[sn][kt][qb], fr[n & 3], qf[0][qb], negm[qb]);
      else MF(sc[sn][kt][qb], fr[n & 3], qf[ds][qb]);
    });
    if constexpr (n == 5 || n == 6) {
      constexpr int qb = n - 5;
      NOPS(3); PIN(sc[0][0][qb]);
      const float m = -xg_max(__builtin_fmaxf(MX3(sc[0][0][qb][0], sc[0][0][qb][1], sc[0][0][qb][2]), sc[0][0][qb][3]));
      negm[qb] = (f32x4){m, m, m, m};
      sc[0][0][qb] += negm[qb];
      PIN(negm[qb]); PIN(sc[0][0][qb]);
    }
    if constexpr (n == 9 || n == 10) { NOPS(3); PIN(sc[0][1][n - 9]); sc[0][1][n - 9] += negm[n - 9]; PIN(sc[0][1][n - 9]); }
    issue(IC(64 + (n >> 2)), IC(n & 3));
    if constexpr (H >= 1) SM(IC(H - 1), IC(i));
    if constexpr (n == 125) { PIN(negm[0]); PIN(negm[1]); }
    SBAR();
    if constexpr ((n & 3) == 3) endslot(IC(64 + (n >> 2)));
  });
  sfor<8>([&](auto Pi) AI { if constexpr (Pi == 4) { NOPS(3); PIN(sc[1][1][0]); PIN(sc[1][1][1]); } SM(IC(15), Pi); });
  if (__builtin_expect(__any(!(lsum[0] + lsum[1] <= LBOUND)), 0)) {
    auto tile = [&](unsigned off, f32x4& s0, f32x4& s1) AI {
      bf16x8 kf[4];
      SBAR(); NOPS(7);
      sfor<4>([&](auto Ds) AI { constexpr int ds = Ds; gld16<ds * 1024, true>(kf[ds], off, kv_b); });
      waitv<0>(kf[0], kf[1]);
      PIN(kf[2]); PIN(kf[3]);
#pragma unroll
      for (int ds = 0; ds < 4; ++ds) {
        s0 = __builtin_amdgcn_mfma_f32_16x16x32_bf16(kf[ds], qf[ds][0], s0, 0, 0, 0);
        s1 = __builtin_amdgcn_mfma_f32_16x16x32_bf16(kf[ds], qf[ds][1], s1, 0, 0, 0);
      }
    };
    float m0 = -__builtin_inff(), m1 = m0;
#pragma unroll 1
    for (int kt = 0; kt < 32; ++kt) {
      f32x4 s0 = {}, s1 = {};
      tile((unsigned)(kt * 4096 + l * 16), s0, s1);
      m0 = MX3(MX3(m0, s0[0], s0[1]), s0[2], s0[3]); m1 = MX3(MX3(m1, s1[0], s1[1]), s1[2], s1[3]);
    }
    m0 = -xg_max(m0); m1 = -xg_max(m1);
    lsum[0] = 0.f; lsum[1] = 0.f;
    sfor<16>([&](auto Hi) AI {
      constexpr int H = Hi;
#pragma unroll
      for (int kt = 0; kt < 2; ++kt) {
        sc[H & 1][kt][0] = (f32x4){m0, m0, m0, m0}; sc[H & 1][kt][1] = (f32x4){m1, m1, m1, m1};
        tile((unsigned)((2 * H + kt) * 4096 + l * 16), sc[H & 1][kt][0], sc[H & 1][kt][1]);
      }
      sfor<8>([&](auto Pi) AI { SM(Hi, Pi); });
      PIN(pw[H][0]); PIN(pw[H][1]);
    });
  }
  SBAR();
  NOPS(1);
  sfor<128>([&](auto Mi) AI {
    constexpr int m = Mi, n = 128 + m, os = m >> 5, H = (m >> 1) & 15, par = m & 1;
    FRD(IC(n + 3));
    if constexpr (H == 0) { MFZ(o[os & 1][par][0], fr[n & 3], pw[H][0]); MFZ(o[os & 1][par][1], fr[n & 3], pw[H][1]); }
    else { MF(o[os & 1][par][0], fr[n & 3], pw[H][0]); MF(o[os & 1][par][1], fr[n & 3], pw[H][1]); }
    if constexpr (m == 2 || m == 3) { inv[m - 2] = __builtin_amdgcn_rcpf(xg_sum(lsum[m - 2])); PIN(inv[m - 2]); }
    issue(IC(64 + (n >> 2)), IC(n & 3));
    if constexpr (m == 123) {
      trb[0].init(lds0 + kg * 16384 + g * 2048 + (c >> 2) * 64 + (g & 1) * 32 + (c & 3) * 8);
      trb[1].init(lds0 + kg * 16384 + g * 2048 + (c >> 2) * 64 + ((g & 1) ^ 1) * 32 + (c & 3) * 8);
    }
    if constexpr (m >= 124) WRD1(IC(0), WB(0), IC(m - 124));
    SBAR();
    if constexpr ((n & 3) == 3) endslot(IC(64 + (n >> 2)));
  });
  STAMP(12); STAMP(13); STAMP(14); STAMP(15);

  const int idx = SEQ - 1 - (int)(key & 2047u);
  const bool owner = (idx >> 7) == mb, ownw = (idx >> 4) == mb * 8 + rbw * 2 + kg;
  f32x4 yp[7], ya[4], oth[4];
  unsigned gate[8];
  auto item = [](int s) { return s < 48 ? s : s < 56 ? 48 + 2 * (s - 48) : 49 + 2 * (s - 56); };
  u32x2 pf;
  sfor<64>([&](auto Si) AI {
    constexpr int s = Si, u = s >> 2, it = item(s), x0 = (it & ~1) % XD2, x1 = (it | 1) % XD2;
    if constexpr (!(it & 1)) waitv<SC.younger(OX, 64 + it + 1, B0 + s - 1)>(xf[x0], xf[x1]);
    sfor<4>([&](auto Dd) AI {
      constexpr int d = Dd, dt = (it & 1) * 4 + d;
      if constexpr (s + 1 < 64) WRD1(IC(s + 1), WB(item(s + 1)), Dd);
      SBAR();
      if constexpr (s < 2) { MFZ(acc[dt][0], wf[s & 1][d], xf[x0]); MFZ(acc[dt][1], wf[s & 1][d], xf[x1]); }
      else { MF(acc[dt][0], wf[s & 1][d], xf[x0]); MF(acc[dt][1], wf[s & 1][d], xf[x1]); }
      issue(IC(B0 + s), Dd);
      if constexpr (s == 56) *(LAS f32x4*)(lds + (wid ^ 4) * 4096 + l * 16 + (d & 1) * 2048 + (d >> 1) * 32768) = acc[d][1];
      if constexpr (s == 57 && d == 0) {
#pragma unroll
        for (int e = 0; e < 4; ++e) oth[e] = *(LAS const f32x4*)(lds + wid * 4096 + l * 16 + (e & 1) * 2048 + (e >> 1) * 32768);
      }
      if constexpr (s == 57 && d >= 2) {
        gst16<(2 * d - 4) * 1024>(acc[2 * d - 4][0] + oth[2 * d - 4], (unsigned)(wid * 4096 + l * 16), ws + WS_XC + (size_t)(bid ^ 4) * 32768);
        gst16<(2 * d - 3) * 1024>(acc[2 * d - 3][0] + oth[2 * d - 3], (unsigned)(wid * 4096 + l * 16), ws + WS_XC + (size_t)(bid ^ 4) * 32768);
      }
      if constexpr (s == 58 || s == 59) {
        constexpr int j = (s - 58) * 4 + d;
        if constexpr (j == 0) waitv<4>();
        PIN(xe[j >> 1]); PIN(st);
        const unsigned xw = xe[j >> 1][j & 1];
        const unsigned dd = pk2(bflo(xw) - st[0], bfhi(xw) - st[0]);
        gate[j] = pk2(bflo(dd) * st[1], bfhi(dd) * st[1]);
        PIN(gate[j]);
      }
      if constexpr (s >= 60 && !(d & 1)) {
        constexpr int j = (s - 60) * 2 + (d >> 1);
        gate[j] = pk2(silu_f(bflo(gate[j])), silu_f(bfhi(gate[j])));
        PIN(gate[j]);
      }
    });
    SBAR();
    if constexpr (s == 7) {
      waitv<SC.younger(OW, 7, B0 + s)>();
      if (l == 0) {
        gst8((u32x2){tlo, thi}, (unsigned)(((rb * 8 + wid) * 32 + head) * 8), ws + WS_FO + 3 * 16384);
        gst8((u32x2){tlo, thi}, (unsigned)((wid * 256 + yrow) * 8), ws + WS_FY);
      }
    }
    if constexpr (s == 4 || s == 17 || s == 33) {
      constexpr int j = s == 4 ? 1 : s == 17 ? 2 : 3;
      waitv<SC.younger(OM, j + 1, B0 + s)>(fg);
      if (__builtin_expect(!__all((fg[0] == tlo) & (fg[1] == thi)), 0)) poll8(fo_v, ws + WS_FO + j * 16384, tlo, thi, 0xffffffffu);
    }
    if constexpr (s == 60) {
      waitv<0>();
      if (l == 0) gst8((u32x2){tlo, thi}, (unsigned)(((bid ^ 4) * 8 + wid) * 64), ws + WS_FX);
    }
    if constexpr (s == 61) gld8<0, true>(pf, (unsigned)((bid * 8 + wid) * 64), ws + WS_FX);
    if constexpr (s == 19) {
      if (owner) { gld16<0, true>(fm[0], (unsigned)(nb * 2048 + (l < 56 ? l : 55) * 32), ws + WS_FY); gld16<16, true>(fm[1], (unsigned)(nb * 2048 + (l < 56 ? l : 55) * 32), ws + WS_FY); }
    }
    if constexpr (s == 34) {
      if (owner) {
        PIN(fm[0]); PIN(fm[1]);
        for (int spin = 0; spin < SPINS; ++spin) {
          const bool ok = (fm[0][0] == tlo) & (fm[0][1] == thi) & (fm[0][2] == tlo) & (fm[0][3] == thi) & (fm[1][0] == tlo) & (fm[1][1] == thi) & (fm[1][2] == tlo) & (fm[1][3] == thi);
          if (__builtin_expect(__all(ok), 1)) break;
          __builtin_amdgcn_s_sleep(1);
          gld16<0, true>(fm[0], (unsigned)(nb * 2048 + (l < 56 ? l : 55) * 32), ws + WS_FY); gld16<16, true>(fm[1], (unsigned)(nb * 2048 + (l < 56 ? l : 55) * 32), ws + WS_FY);
          waitv<0>(fm[0], fm[1]);
        }
        const unsigned yv = (unsigned)(((t >> 4) * 1024 + nb * 128 + kh * 64 + (t & 15) * 4) * 4);
        sfor<7>([&](auto I) AI { constexpr int q = I; gld16<0, true>(yp[q], yv, ws + WS_YP + (size_t)q * 32 * 4096); });
      }
    }
    if constexpr (s == 50) {
      if (owner) {
        f32x4 a = {};
#pragma unroll
        for (int q = 0; q < 7; ++q) { PIN(yp[q]); a += yp[q]; }
#pragma unroll
        for (int j = 0; j < 4; ++j) a[j] = xg_sum(a[j]);
        *(LAS f32x4*)(lds + YS + (wid * 64 + c * 4) * 4) = a;
      }
    }
    if constexpr (s == 53) {
      if (ownw) {
        float a = 0.f;
#pragma unroll
        for (int p = 0; p < 8; ++p) a += *(LAS const float*)(lds + YS + (p * 64 + l) * 4);
        const unsigned ord = key >> 11, bits = (ord & 0x8000u) ? (ord & 0x7fffu) : (~ord & 0xffffu);
        *(LAS float*)(lds + RED1 + l * 4) = rbf(rbf(a) * rbf(sigmoid_f(bflo(bits))));
#pragma unroll
        for (int q = 0; q < 4; ++q) ya[q] = *(LAS const f32x4*)(lds + RED1 + (q * 16 + 4 * g) * 4);
      }
    }
    if constexpr (s == 59) {
      if (ownw) {
#pragma unroll
        for (int j = 0; j < 8; ++j) {
          const unsigned w = pk2(bflo(gate[j]) + ya[j >> 1][2 * (j & 1)], bfhi(gate[j]) + ya[j >> 1][2 * (j & 1) + 1]);
          gate[j] = erow == idx ? w : gate[j];
          PIN(gate[j]);
        }
      }
    }
    SBAR();
    if constexpr (((s & 3) == 2 && u < 11) || s == 44) {
      waitbar<SC.younger(OG, 64 + 4 * u + 7, B0 + s)>();
      if constexpr (u % 4 == 1) STAMP(16 + u / 4);
    }
    if constexpr (s == 48) waitbar<SC.younger(OG, 64 + 55, B0 + s)>();
    if constexpr (s == 50) waitbar<SC.younger(OG, 64 + 59, B0 + s)>();
    if constexpr (s == 52) { waitbar_l<SC.younger(OG, 64 + 63, B0 + s)>(); STAMP(19); }
    if constexpr (s == 56) waitbar_l<63>();
    if constexpr (PRIO) __builtin_amdgcn_s_setprio(3 - ((s + 2) & 3));
  });
  STAMP(20);

  f32x4 kep[4];
  {
    waitv<0>(pf);
    const bool rdy = __all((pf[0] == tlo) & (pf[1] == thi));
    if (rdy) sfor<4>([&](auto Dd) AI { constexpr int d = Dd; gld16<d * 1024, true>(oth[d], (unsigned)(wid * 4096 + l * 16), ws + WS_XC + (size_t)bid * 32768); });
    NOPS(3);
#pragma unroll
    for (int d = 0; d < 4; ++d) { PIN(acc[4 + d][1]); *(LAS f32x4*)(lds + 65536 + (wid ^ 4) * 4096 + l * 16 + (d & 1) * 2048 + (d >> 1) * 32768) = acc[4 + d][1]; }
    waitbar_l<63>();
    STAMP(21);
#pragma unroll
    for (int d = 0; d < 4; ++d) kep[d] = acc[4 + d][0] + *(LAS const f32x4*)(lds + 65536 + wid * 4096 + l * 16 + (d & 1) * 2048 + (d >> 1) * 32768);
    STAMP(22);
    if (!rdy) {
      poll8((unsigned)((bid * 8 + wid) * 64), ws + WS_FX, tlo, thi, 0xffffffffu);
      sfor<4>([&](auto Dd) AI { constexpr int d = Dd; gld16<d * 1024, true>(oth[d], (unsigned)(wid * 4096 + l * 16), ws + WS_XC + (size_t)bid * 32768); });
    }
    STAMP(23);
    waitv<0>(oth[0], oth[1]);
    PIN(oth[2]); PIN(oth[3]);
    STAMP(24);
  }
  u16* op = out + (size_t)erow * DM + ecol;
#pragma unroll
  for (int d = 0; d < 4; ++d) {
    const f32x4 a = kep[d] + oth[d];
    u32x2 res;
#pragma unroll
    for (int j = 0; j < 2; ++j) {
      const unsigned sp = pk2(a[2 * j], a[2 * j + 1]), gp = gate[2 * d + j], xw = xe[d][j];
      const unsigned pr = pk2(bflo(sp) * bflo(gp), bfhi(sp) * bfhi(gp));
      res[j] = pk2(bflo(xw) + bflo(pr), bfhi(xw) + bfhi(pr));
    }
    *(u32x2*)(op + d * 16) = res;
  }
  STAMP(25);
  STAMP_OUT(0, 26);
}

extern "C" void kernel_launch(void* const* d_in, const int* in_sizes, int n_in, void* d_out, int out_size, void* d_ws, size_t ws_size,
                              hipStream_t stream) {
  k_fused<<<256, 512, 0, stream>>>((const u16*)d_in[0], (const u16*)d_in[1], (const u16*)d_in[2], (const u16*)d_in[3], (const u16*)d_in[4],
                                   (const u16*)d_in[6], (const u16*)d_in[7], (const u16*)d_in[8], (const u16*)d_in[9], (char*)d_ws, (u16*)d_out);
}
```

```cpp
#include <hip/hip_runtime.h>
#include <stdint.h>
#include <type_traits>

typedef unsigned short u16;
typedef short bf16x8 __attribute__((ext_vector_type(8)));
typedef short s16x4 __attribute__((ext_vector_type(4)));
typedef float f32x4 __attribute__((ext_vector_type(4)));
typedef float f32x2 __attribute__((ext_vector_type(2)));
typedef unsigned u32x4 __attribute__((ext_vector_type(4)));
typedef unsigned u32x2 __attribute__((ext_vector_type(2)));
typedef __bf16 bf2_t __attribute__((ext_vector_type(2)));
#define LAS __attribute__((address_space(3)))
#define DI __device__ __forceinline__
#define AI __attribute__((always_inline))
#define SBAR() __builtin_amdgcn_sched_barrier(0)
template <class T> __device__ __forceinline__ void PIN(T& x) { asm volatile("" : "+v"(x)); }
template <class B> DI void MF(f32x4& acc, const bf16x8& a, const B& b) { asm volatile("v_mfma_f32_16x16x32_bf16 %0, %1, %2, %0" : "+v"(acc) : "v"(a), "v"(b)); }
template <class B> DI void MFC(f32x4& d, const bf16x8& a, const B& b, const f32x4& c) { asm volatile("v_mfma_f32_16x16x32_bf16 %0, %1, %2, %3" : "=&v"(d) : "v"(a), "v"(b), "v"(c)); }
template <class B> DI void MFZ(f32x4& d, const bf16x8& a, const B& b) { asm volatile("v_mfma_f32_16x16x32_bf16 %0, %1, %2, 0" : "=&v"(d) : "v"(a), "v"(b)); }
#define NOPS(n) asm volatile("s_nop " #n)
#define MFMA16(a, b, c) __builtin_amdgcn_mfma_f32_16x16x16bf16_1k((a), (b), (c), 0, 0, 0)
#define IC(n) std::integral_constant<int, (n)>{}

constexpr int SEQ = 2048, DM = 1024, DH = 128, SK = 2048, WIN = 512, FF = 3584;
constexpr size_t WS_XN = 0;
constexpr size_t WS_KV = 4u << 20;
constexpr size_t WS_ST = 5u << 20;
constexpr size_t WS_EP = WS_ST + 16384;
constexpr size_t WS_G0 = WS_ST + 32768;
constexpr size_t WS_FK = WS_ST + 49152;
constexpr size_t WS_YP = WS_ST + 65536;
constexpr size_t WS_FO = 7u << 20;
constexpr size_t WS_FY = WS_FO + 65536;
constexpr size_t WS_FX = WS_FO + 131072;
constexpr size_t WS_O = 8u << 20;
constexpr size_t WS_GU = 32u << 20;
constexpr size_t WS_XC = 24u << 20;

template <int I, int N, class F> DI void sfor_impl(F&& f) {
  if constexpr (I < N) { f(std::integral_constant<int, I>{}); sfor_impl<I + 1, N>(f); }
}
template <int N, class F> DI void sfor(F&& f) { sfor_impl<0, N>(f); }

DI unsigned pk2(float a, float b) { f32x2 v = {a, b}; return __builtin_bit_cast(unsigned, __builtin_convertvector(v, bf2_t)); }
DI float rbf(float a) { return (float)(__bf16)a; }
DI float bflo(unsigned u) { return __uint_as_float(u << 16); }
DI float bfhi(unsigned u) { return __uint_as_float(u & 0xffff0000u); }
DI float sigmoid_f(float v) { return __builtin_amdgcn_rcpf(1.f + __builtin_amdgcn_exp2f(v * -1.4426950408889634f)); }
DI float sel2(bool c, float a, float b) { PIN(a); PIN(b); return c ? a : b; }
DI float silu_f(float v) { return v * sigmoid_f(v); }
template <int CTRL> DI float dppf(float x) { return __builtin_bit_cast(float, __builtin_amdgcn_mov_dpp(__builtin_bit_cast(int, x), CTRL, 0xf, 0xf, true)); }
template <int CTRL> DI unsigned dppu(unsigned x) { return (unsigned)__builtin_amdgcn_mov_dpp((int)x, CTRL, 0xf, 0xf, true); }
constexpr int XOR1 = 0xB1, XOR2 = 0x4E, XOR7 = 0x141, XOR8 = 0x128;
DI float xg_sum(float x) {
  auto s = __builtin_amdgcn_permlane16_swap(__float_as_uint(x), __float_as_uint(x), false, false);
  x = __uint_as_float(s[0]) + __uint_as_float(s[1]);
  auto t = __builtin_amdgcn_permlane32_swap(__float_as_uint(x), __float_as_uint(x), false, false);
  return __uint_as_float(t[0]) + __uint_as_float(t[1]);
}
DI float xg_max(float x) {
  auto s = __builtin_amdgcn_permlane16_swap(__float_as_uint(x), __float_as_uint(x), false, false);
  x = __builtin_fmaxf(__uint_as_float(s[0]), __uint_as_float(s[1]));
  auto t = __builtin_amdgcn_permlane32_swap(__float_as_uint(x), __float_as_uint(x), false, false);
  return __builtin_fmaxf(__uint_as_float(t[0]), __uint_as_float(t[1]));
}
DI float wave_sum(float v) { v += dppf<XOR1>(v); v += dppf<XOR2>(v); v += dppf<XOR7>(v); v += dppf<XOR8>(v); return xg_sum(v); }
DI unsigned umax(unsigned a, unsigned b) { return a > b ? a : b; }
DI unsigned wave_max_u32(unsigned v) {
  v = umax(v, dppu<XOR1>(v)); v = umax(v, dppu<XOR2>(v)); v = umax(v, dppu<XOR7>(v)); v = umax(v, dppu<XOR8>(v));
  auto s = __builtin_amdgcn_permlane16_swap(v, v, false, false); v = umax(s[0], s[1]);
  auto t = __builtin_amdgcn_permlane32_swap(v, v, false, false); v = umax(t[0], t[1]);
  return __builtin_amdgcn_readfirstlane(v);
}

template <int OFF, bool DEV = false> DI void glds(unsigned ldsaddr, unsigned voff, const void* sbase) {
  if constexpr (DEV) asm volatile("s_mov_b32 m0, %0\n\ts_nop 0\n\tglobal_load_lds_dwordx4 %1, %2 offset:%3 sc1" ::"s"(ldsaddr), "v"(voff), "s"(sbase), "i"(OFF) : "memory");
  else asm volatile("s_mov_b32 m0, %0\n\ts_nop 0\n\tglobal_load_lds_dwordx4 %1, %2 offset:%3" ::"s"(ldsaddr), "v"(voff), "s"(sbase), "i"(OFF) : "memory");
}
DI void touch(unsigned voff, const void* sbase) {
  asm volatile("global_load_dword v255, %0, %1" ::"v"(voff), "s"(sbase) : "memory", "v255");
}
template <int OFF, bool DEV = false, class T> DI void gld16(T& dst, unsigned voff, const void* sbase) {
  if constexpr (DEV) asm volatile("global_load_dwordx4 %0, %1, %2 offset:%3 sc1" : "=v"(dst) : "v"(voff), "s"(sbase), "i"(OFF) : "memory");
  else asm volatile("global_load_dwordx4 %0, %1, %2 offset:%3" : "=v"(dst) : "v"(voff), "s"(sbase), "i"(OFF) : "memory");
}
template <int OFF, bool DEV = false, class T> DI void gld8(T& dst, unsigned voff, const void* sbase) {
  if constexpr (DEV) asm volatile("global_load_dwordx2 %0, %1, %2 offset:%3 sc1" : "=v"(dst) : "v"(voff), "s"(sbase), "i"(OFF) : "memory");
  else asm volatile("global_load_dwordx2 %0, %1, %2 offset:%3" : "=v"(dst) : "v"(voff), "s"(sbase), "i"(OFF) : "memory");
}
template <int OFF> DI void gld2(unsigned& dst, unsigned voff, const void* sbase) {
  asm volatile("global_load_ushort %0, %1, %2 offset:%3" : "=v"(dst) : "v"(voff), "s"(sbase), "i"(OFF) : "memory");
}
template <int OFF, class T> DI void gst16(const T& v, unsigned voff, const void* sbase) {
  asm volatile("global_store_dwordx4 %0, %1, %2 offset:%3 sc1\n\ts_nop 1" ::"v"(voff), "v"(v), "s"(sbase), "i"(OFF) : "memory");
}
template <class T> DI void gst8(const T& v, unsigned voff, const void* sbase) {
  asm volatile("global_store_dwordx2 %0, %1, %2 sc1" ::"v"(voff), "v"(v), "s"(sbase) : "memory");
}
template <int OFF> DI void gst4(float v, unsigned voff, const void* sbase) {
  asm volatile("global_store_dword %0, %1, %2 offset:%3 sc1" ::"v"(voff), "v"(v), "s"(sbase), "i"(OFF) : "memory");
}
constexpr int SPINS = 1 << 16;
DI void poll8(unsigned voff, const void* sbase, unsigned lo, unsigned hi, unsigned lomask) {
  for (int spin = 0; spin < SPINS; ++spin) {
    u32x2 v;
    asm volatile("global_load_dwordx2 %0, %1, %2 sc1\n\ts_waitcnt vmcnt(0)" : "=&v"(v) : "v"(voff), "s"(sbase) : "memory");
    if (__all(((v[0] & lomask) == lo) & (v[1] == hi))) break;
    __builtin_amdgcn_s_sleep(1);
  }
}
template <int N> DI void waitv() { static_assert(N >= 0, "count"); asm volatile("s_waitcnt vmcnt(%0)" ::"n"(N > 63 ? 63 : N) : "memory"); }
template <int N, class T> DI void waitv(T& x) { static_assert(N >= 0, "count"); asm volatile("s_waitcnt vmcnt(%1)" : "+v"(x) : "n"(N > 63 ? 63 : N) : "memory"); }
template <int N, class T> DI void waitv(T& x, T& y) { static_assert(N >= 0, "count"); asm volatile("s_waitcnt vmcnt(%2)" : "+v"(x), "+v"(y) : "n"(N > 63 ? 63 : N) : "memory"); }
template <int N> DI void waitbar() { static_assert(N >= 0, "count"); asm volatile("s_waitcnt vmcnt(%0)\n\ts_barrier" ::"n"(N > 63 ? 63 : N) : "memory"); }
template <int N> DI void waitbar_l() { static_assert(N >= 0, "count"); asm volatile("s_waitcnt vmcnt(%0) lgkmcnt(0)\n\ts_barrier" ::"n"(N > 63 ? 63 : N) : "memory"); }
typedef short v4i16_t __attribute__((ext_vector_type(4)));
DI s16x4 trd(LAS const char* p) { return __builtin_amdgcn_ds_read_tr16_b64_v4i16((LAS v4i16_t*)p); }
struct LBase {
  unsigned lo, hi;
  DI void init(unsigned a) { lo = a; hi = a + 65536; PIN(lo); PIN(hi); }
  template <int OFF> DI LAS const char* at() const { return (LAS const char*)(uintptr_t)(OFF < 65536 ? lo : hi) + (OFF & 65535); }
};
DI bf16x8 cat8(s16x4 lo, s16x4 hi) { return __builtin_shufflevector(lo, hi, 0, 1, 2, 3, 4, 5, 6, 7); }

enum { OX, OG, OF, OM, OS, OP, OE, OW };
struct Sched {
  int n; short kind[400], idx[400], slot[400];
  constexpr Sched() : n(0), kind{}, idx{}, slot{} {}
  constexpr void add(int s, int k, int i) { kind[n] = (short)k; idx[n] = (short)i; slot[n] = (short)s; ++n; }
  constexpr int younger(int k, int i, int s) const {
    int p = -1;
    for (int q = 0; q < n; ++q) if (kind[q] == k && idx[q] == i) p = q;
    if (p < 0 || slot[p] > s) return -1;
    int c = 0;
    for (int q = p + 1; q < n; ++q) if (slot[q] <= s) ++c;
    return c;
  }
  constexpr int first(int s) const { int q = 0; while (q < n && slot[q] < s) ++q; return q; }
  constexpr int count(int s) const { int c = 0; for (int q = 0; q < n; ++q) if (slot[q] == s) ++c; return c; }
};

#define STAMP_DECL
#define STAMP(i)
#define STAMP_OUT(kid, n)

constexpr int RING = 4 * 32768, FB = RING, RED1 = FB + 24576, HSC = RED1 + 1024, YS = HSC + 1024, LDS_BYTES = FB + 32768;
constexpr int STAGE = 3 * 32768;
constexpr int XD = 16, XD2 = 16;
constexpr float LBOUND = 65536.f;
constexpr float QSCALE = 0.08838834764831845f * 1.4426950408889634f;
constexpr int B0 = 128;
#define PRIO 1
constexpr int FISS[4] = {96, 97, 98, 117}, FRDS[4] = {115, 118, 121, 124};
constexpr int fpiece(int sl) { for (int p = 0; p < 4; ++p) if (FRDS[p] == sl) return p; return -1; }
constexpr Sched make_sched() {
  Sched s;
#define XP 8
  for (int i = 0; i < 12; ++i) s.add(-2, OG, i);
  for (int i = 0; i < XP; ++i) s.add(-1, OX, i);
  for (int t = 0; t < 128; ++t) {
    if (t < 2) for (int i = XP + t * (16 - XP) / 2; i < XP + (t + 1) * (16 - XP) / 2; ++i) s.add(t, OX, i);
    if (t >= 2 && t < 50) s.add(t, OX, t + 14);
    for (int v = 3; v < 18; ++v) for (int p = 0; p < 4; ++p) if (8 * (v - 3) + 7 + p == t) s.add(t, OG, 4 * v + p);
    if (t == 105 || t == 113 || t == 121) { s.add(t, OW, (t - 105) / 4); s.add(t, OW, (t - 105) / 4 + 1); }
    if (t >= 124) for (int i = 0; i < 3; ++i) s.add(t, OX, 64 + 3 * (t - 124) + i);
    if (t == 127) { s.add(t, OG, 72); s.add(t, OG, 73); }
    if (t == 20) s.add(t, OM, 0);
    if (t == 120) s.add(t, OM, 1);
    if (t >= 49 && t < 57 && (t & 1)) s.add(t, OF, (t - 49) / 2);
    if (t >= 71 && t < 79 && (t & 1)) s.add(t, OF, 4 + (t - 71) / 2);
    if (t == 48) s.add(t, OS, 0);
    if (t == 104) s.add(t, OS, 2);
    for (int p = 0; p < 4; ++p) if (FISS[p] == t) s.add(t, OF, 8 + p);
  }
  for (int t = 0; t < 64; ++t) {
    if (t == 0) { s.add(B0, OM, 2); s.add(B0, OG, 74); s.add(B0, OG, 75); s.add(B0, OW, 6); s.add(B0, OW, 7); }
    if (t == 1 || t == 2) { s.add(B0 + t, OX, 74 + 2 * t); s.add(B0 + t, OX, 75 + 2 * t); }
    if (t == 5 || t == 6 || t == 9) s.add(B0 + t, OX, 64 + (t == 5 ? 16 : t == 6 ? 18 : 22));
    if (t >= 5 && t < 50) s.add(B0 + t, OX, 64 + (t == 5 ? 17 : t < 9 ? t + 13 : t + 14));
    for (int v = 3; v < 16; ++v) for (int p = 0; p < 4; ++p) if ((v < 14 ? 4 * (v - 3) + 3 + (p >> 1) : v == 14 ? 45 + (p >> 1) : 49) == t) s.add(B0 + t, OG, 64 + 4 * v + p);
    if (t == 10 || t == 26) s.add(B0 + t, OM, t == 10 ? 3 : 4);
    if (t == 50) for (int i = 0; i < 5; ++i) s.add(B0 + t, OE, i);
  }
  return s;
}
constexpr Sched SC = make_sched();

__global__ __launch_bounds__(512, 2) void k_fused(const u16* __restrict__ x, const u16* __restrict__ kc, const u16* __restrict__ vc,
                                                  const u16* __restrict__ wq, const u16* __restrict__ wo, const u16* __restrict__ wr,
                                                  const u16* __restrict__ wgate, const u16* __restrict__ wup, const u16* __restrict__ wdown,
                                                  char* __restrict__ ws, u16* __restrict__ out) {
  __shared__ __attribute__((aligned(16))) char lds_raw[LDS_BYTES];
  LAS char* const lds = (LAS char*)lds_raw;
  const int t = threadIdx.x, l = t & 63, bid = blockIdx.x, c = l & 15, g = l >> 4;
  const int wid = __builtin_amdgcn_readfirstlane(t >> 6);
  const int xcd = bid & 7, within = bid >> 3, head = xcd * 4 + (within & 3), rb = within >> 2;
  const int fj = xcd * 28 + (within < 28 ? within : within - 28);
  const int fsl = fj / 7, fgr = fj - fsl * 7;
  const int kh = xcd >> 2, mb = 2 * rb + (xcd & 1), nb = ((xcd >> 1) & 1) * 4 + (within & 3);
  const int rbw = wid & 3, kg = wid >> 2;
  const unsigned lds0 = (unsigned)(uintptr_t)lds;
  STAMP_DECL
  STAMP(0);

  unsigned tlo, thi;
  {
    const int row = 8 * bid + wid;
    u32x4 a0, a1, w0, w1, kvo;
    u32x2 ep, klo, khi;
    unsigned e[8];
    gld16<0>(a0, (unsigned)(row * DM * 2 + l * 16), x); gld16<1024>(a1, (unsigned)(row * DM * 2 + l * 16), x);
    gld16<0>(w0, (unsigned)(l * 16), wr); gld16<1024>(w1, (unsigned)(l * 16), wr);
    gld8<0, true>(ep, 0u, ws + WS_EP);
    if (wid == 3) {
      const char* pc;
      asm volatile("s_getpc_b64 %0" : "=s"(pc));
      touch((unsigned)((within * 12 + (l < 12 ? l : 11)) * 128), pc);
    }
    __builtin_amdgcn_s_barrier();
    const int fn = (bid * 4 + wid) & 255, kvh = bid >> 6;
    if (wid < 4) {
      if (fn < 128) {
        const unsigned ko = (unsigned)(((kvh * SK + (SK - WIN) + (fn >> 2) * 16 + c) * DH + (fn & 3) * 32 + 4 * g) * 2);
        gld8<0>(klo, ko, kc); gld8<32>(khi, ko, kc);
      } else {
        const int d = 32 * ((fn - 128) >> 5) + 8 * (c >> 2) + 4 * (fn & 1) + (c & 3);
        const unsigned vo = (unsigned)(((kvh * SK + (SK - WIN) + ((fn >> 1) & 15) * 32 + 4 * g + 8) * DH + d) * 2);
        sfor<8>([&](auto J) AI { constexpr int j = J; gld2<(16 * (j >> 2) + (j & 3) - 8) * DH * 2>(e[j], vo, vc); });
      }
      if (wid < 2) touch((unsigned)((wid * 256 + within * 8 + (l >> 3)) * 8192 + xcd * 1024 + (l & 7) * 128), wq);
      if (fn < 128) waitv<5>(); else waitv<11>();
    } else {
      waitv<3>();
    }
    PIN(a0); PIN(a1);
    STAMP(1);
    float v[16];
#pragma unroll
    for (int j = 0; j < 4; ++j) { v[2 * j] = bflo(a0[j]); v[2 * j + 1] = bfhi(a0[j]); v[8 + 2 * j] = bflo(a1[j]); v[9 + 2 * j] = bfhi(a1[j]); }
    float s = 0.f;
#pragma unroll
    for (int j = 0; j < 16; ++j) s += v[j];
    const float mean = wave_sum(s) * (1.f / DM);
    float q = 0.f;
#pragma unroll
    for (int j = 0; j < 16; ++j) { const float d = v[j] - mean; q += d * d; }
    const float var = wave_sum(q) * (1.f / DM);
    const float mbf = rbf(mean), rden = __builtin_amdgcn_rcpf(rbf(__builtin_amdgcn_sqrtf(rbf(rbf(var) + 1e-5f))));
    u32x4 o0, o1;
#pragma unroll
    for (int j = 0; j < 8; ++j) {
      const unsigned dd = pk2(v[2 * j] - mbf, v[2 * j + 1] - mbf);
      const unsigned nn = pk2(bflo(dd) * rden, bfhi(dd) * rden);
      if (j < 4) o0[j] = nn; else o1[j - 4] = nn;
    }
    *(LAS u32x4*)(lds + STAGE + (l * 8 + (wid ^ (l & 7))) * 16) = o0;
    *(LAS u32x4*)(lds + STAGE + 8192 + (l * 8 + (wid ^ (l & 7))) * 16) = o1;
    *(LAS f32x2*)(lds + STAGE + 16384 + 32 + wid * 8) = (f32x2){mbf, rden};
    waitbar_l<63>();
    if (wid >= 4) {
#pragma unroll
      for (int hh = 0; hh < 4; ++hh) {
        const int p = hh * 256 + (t - 256), ch = p >> 3, r = 8 * bid + ((p & 7) ^ (ch & 7));
        const u32x4 o = *(LAS const u32x4*)(lds + STAGE + p * 16);
        gst16<0>(o, (unsigned)(((((r >> 5) * 32 + (ch >> 2)) * 2 + ((r >> 4) & 1)) * 64 + (ch & 3) * 16 + (r & 15)) * 16), ws + WS_XN);
      }
      if (t < 256 + 8) gst8(*(LAS const f32x2*)(lds + STAGE + 16384 + 32 + (t - 256) * 8), (unsigned)((8 * bid + t - 256) * 8), ws + WS_ST);
      waitv<4>();
    } else if (fn < 128) waitv<2>(); else waitv<8>();
    PIN(w0); PIN(w1); PIN(ep);
    {
      const unsigned long long tg = (((unsigned long long)(unsigned)__builtin_amdgcn_readfirstlane(ep[1]) << 32) | (unsigned)__builtin_amdgcn_readfirstlane(ep[0])) + 1ull;
      tlo = (unsigned)tg; thi = (unsigned)(tg >> 32);
    }
    float rr = 0.f;
#pragma unroll
    for (int j = 0; j < 8; ++j) {
      const unsigned nn = j < 4 ? o0[j] : o1[j - 4], ww = j < 4 ? w0[j] : w1[j - 4];
      rr += bflo(nn) * bflo(ww); rr += bfhi(nn) * bfhi(ww);
    }
    rr = wave_sum(rr);
    {
      const unsigned bits = pk2(rr, 0.f) & 0xffffu;
      const unsigned ord = (bits & 0x8000u) ? (~bits & 0xffffu) : (bits | 0x8000u);
      *(LAS unsigned*)(lds + STAGE + 16384 + wid * 4) = (ord << 11) | (unsigned)(SEQ - 1 - row);
    }
    if (wid >= 4) waitv<0>();
    waitbar_l<63>();
    if (t == 256) {
      const u32x4 ka = *(LAS const u32x4*)(lds + STAGE + 16384), kb = *(LAS const u32x4*)(lds + STAGE + 16400);
      const unsigned key = umax(umax(umax(ka[0], ka[1]), umax(ka[2], ka[3])), umax(umax(kb[0], kb[1]), umax(kb[2], kb[3])));
      gst8((u32x2){(tlo << 27) | key, (tlo >> 5) | (thi << 27)}, (unsigned)(bid * 8), ws + WS_G0);
    }
    if (wid < 4) {
      if (wid < 2) waitv<1>(); else waitv<0>();
      if (fn < 128) {
        PIN(klo); PIN(khi);
        kvo = (u32x4){klo[0], klo[1], khi[0], khi[1]};
      } else {
#pragma unroll
        for (int j = 0; j < 8; ++j) PIN(e[j]);
        kvo = (u32x4){e[0] | (e[1] << 16), e[2] | (e[3] << 16), e[4] | (e[5] << 16), e[6] | (e[7] << 16)};
      }
      gst16<0>(kvo, (unsigned)((bid * 4 + wid) * 1024 + l * 16), ws + WS_KV);
    }
    STAMP(2);
  }
  const unsigned glo = tlo << 27, ghi = (tlo >> 5) | (thi << 27);

  const char* const wq_b = (const char*)wq + head * 256;
  const unsigned wq_v = (unsigned)(((8 * wid + ((l & 31) >> 2)) * 4096 + (l >> 5) * 32 + ((l & 3) ^ (2 * (wid & 1))) * 8) * 2);
  const char* const kv_b = ws + WS_KV + (size_t)(head >> 3) * 262144;
  const unsigned kv_v = (unsigned)(wid * 4096 + l * 16);
  const char* const xn_b = ws + WS_XN + (size_t)rb * 8 * 65536 + 4096;
  const unsigned xn_v = (unsigned)(wid * 65536 + l * 16);
  const char* const wo_b = (const char*)wo + (size_t)kh * 2048 * (DM * 2) + nb * 256;
  const char* const o_b = ws + WS_O + (size_t)mb * 4 * 262144 + kh * 131072;
  const unsigned ldsw = lds0 + wid * 2048, ldsk = lds0 + wid * 4096, ldsf = lds0 + FB + wid * 1024;
  const int erow = mb * 128 + rbw * 32 + kg * 16 + c, ecol = nb * 128 + kh * 64 + 4 * g;
  bf16x8 xf[XD];
  u32x4 fm[2];
  bf16x8 fxs;
  const unsigned gu_v = (unsigned)((((4 * wid + g) * 2 + ((l >> 3) & 1)) * FF + fj * 16 + (l & 7) * 2) * 8);
  u32x2 fg;
  u32x2 xe[4];
  f32x2 st;
  unsigned key = 0;

  const unsigned fo_v = (unsigned)(((mb * 4 + rbw) * 32 + kh * 16 + c) * 8);
  const int yrow = within < 28 ? fj : 224 + xcd * 4 + within - 28;
  f32x4 o[2][2][2];
  float inv[2];
  const char* const od_b = ws + WS_O + ((size_t)(rb * 8) * 128 + head * 4) * 2048 + 4096;
  auto issue = [&](auto Sl, auto Pos) AI {
    constexpr int sl = Sl, pos = Pos, cnt = SC.count(sl), q0 = SC.first(sl) + (pos < 0 ? 0 : pos);
    constexpr int nq = pos < 0 ? cnt : pos >= cnt ? 0 : pos < 3 ? 1 : cnt - 3;
    sfor<nq>([&](auto Qi) AI {
      constexpr int k = SC.kind[q0 + Qi], i = SC.idx[q0 + Qi];
      if constexpr (k == OX && i < 64) gld16<(i & 7) * 1024 - 4096, true>(xf[i % XD], xn_v, xn_b + (size_t)(i >> 3) * 8192);
      if constexpr (k == OX && i >= 64) {
        constexpr int q = i - 64, v = q >> 2;
        gld16<0, true>(xf[q % XD2], (unsigned)(rbw * 262144 + kg * 16384 + ((q & 1) ^ kg) * 1024 + l * 16), o_b + (size_t)(((v & 3) * 4 + ((q >> 1) & 1)) * 8192 + (v >> 2) * 2048));
      }
      if constexpr (k == OG) {
        constexpr int v = i >> 2, p = i & 3;
        if constexpr (v < 8) {
          const char* sb = wq_b + (size_t)(v * 128 + (p >> 1) * 64) * 8192;
          glds<(p & 1) * 128>(ldsw + (v & 3) * 32768 + (p >> 1) * 16384 + (p & 1) * (1024 - 128), wq_v, sb);
        } else if constexpr (v < 16) {
          glds<p * 1024, true>(ldsk + (v & 3) * 32768, kv_v, kv_b + (size_t)(v - 8) * 32768);
        } else {
          const unsigned wo_v = (unsigned)((((wid >> 2) * 128 + (wid & 3) * 8 + ((l & 31) >> 2)) * DM + (l >> 5) * 32 + ((l & 3) ^ (2 * (wid & 1))) * 8) * 2);
          const char* sb = wo_b + (size_t)(((v & 3) * 4 + (p >> 1) * 2) * 128 + ((v - 16) >> 2) * 32) * (DM * 2) + (((p & 1) ^ kh ^ 1) << 7);
          glds<0>(ldsw + (v & 3) * 32768 + (p >> 1) * 16384 + (p & 1) * 1024, wo_v, sb);
        }
      }
      if constexpr (k == OF && i < 8)
        glds<0>(ldsf + (i & 3) * 8192, (unsigned)(((l >> 3) * FF + wid * 64 + ((l & 7) ^ ((i & 1) * 4 + ((l >> 3) & 2))) * 8) * 2),
                (const char*)(i < 4 ? wgate : wup) + ((size_t)(fsl * 32 + 8 * (i & 3)) * FF + fgr * 512) * 2);
      if constexpr (k == OF && i >= 8) {
        const int frho = l >> 2, fch = (l & 3) ^ (frho & 4 ? 2 : 0);
        glds<0>(ldsf + ((i - 8) % 3) * 8192, (unsigned)((frho * DM + wid * 128 + fch * 8) * 2), (const char*)wdown + (size_t)fj * 16 * DM * 2 + (i - 8) * 64);
      }
      if constexpr (k == OS && i == 0) {
        const int idx = SEQ - 1 - (int)(key & 2047u);
        gld16<0, true>(fxs, (unsigned)((((idx >> 5) * 32 + fsl) * 2 + ((idx >> 4) & 1)) * 1024 + (g * 16 + (idx & 15)) * 16), ws + WS_XN);
      }
      if constexpr (k == OS && i == 2) gld16<0, true>(fm[0], gu_v, ws + WS_GU);
      if constexpr (k == OM && i > 0) gld8<0, true>(fg, fo_v, ws + WS_FO + (i - 1) * 16384);
      if constexpr (k == OM && i == 0) gld8<0, true>(fg, (unsigned)((t & 255) * 8), wid < 4 ? ws + WS_G0 : ws + WS_FK + (head >> 3) * 2048);
      if constexpr (k == OW) {
        constexpr int os = i >> 1, qb = i & 1;
        PIN(o[os & 1][0][qb]); PIN(o[os & 1][1][qb]);
        const f32x4 a = o[os & 1][0][qb] * inv[qb], b = o[os & 1][1][qb] * inv[qb];
        gst16<i * 1024 - 4096>((u32x4){pk2(a[0], a[1]), pk2(a[2], a[3]), pk2(b[0], b[1]), pk2(b[2], b[3])}, (unsigned)(wid * 262144 + l * 16), od_b);
      }
      if constexpr (k == OE && i < 4) gld8<32 * i>(xe[i], (unsigned)((erow * DM + ecol) * 2), x);
      if constexpr (k == OE && i == 4) gld8<0, true>(st, (unsigned)(erow * 8), ws + WS_ST);
    });
  };
  f32x4 fa0, fa1, fq[4];
  s16x4 fb0, fb1, fxa;
  bf16x8 ft[2];
  auto endslot = [&](auto Sl) AI {
    constexpr int sl = Sl, u = sl >> 3, i = sl & 7;
    if constexpr (sl == 3) {
      if (wid == 6 || wid == 7) touch((unsigned)(((wid - 4) * 256 + within * 8 + (l >> 3)) * 8192 + xcd * 1024 + (l & 7) * 128), wq);
    }
    if constexpr (sl == 2) {
      if (wid < 4 && l == 0) gst8((u32x2){tlo, thi}, (unsigned)((bid * 4 + wid) * 8), ws + WS_FK);
    }
    if constexpr (sl == 38) {
      waitv<SC.younger(OM, 0, sl)>(fg);
      const unsigned lo = wid < 4 ? glo : tlo, hi = wid < 4 ? ghi : thi, mk = wid < 4 ? 0xf8000000u : 0xffffffffu;
      for (int spin = 0; spin < SPINS; ++spin) {
        if (__builtin_expect(__all(((fg[0] & mk) == lo) & (fg[1] == hi)), 1)) break;
        __builtin_amdgcn_s_sleep(1);
        gld8<0, true>(fg, (unsigned)((t & 255) * 8), wid < 4 ? ws + WS_G0 : ws + WS_FK + (head >> 3) * 2048);
        waitv<0>(fg);
      }
      const unsigned km = wave_max_u32(fg[0] & 0x07ffffffu);
      *(LAS unsigned*)(lds + HSC + wid * 4) = km;
    }
    if constexpr (sl == 47) {
      if (bid == 0 && t == 0) gst8((u32x2){tlo, thi}, 0u, ws + WS_EP);
      const u32x4 kk = *(LAS const u32x4*)(lds + HSC);
      key = __builtin_amdgcn_readfirstlane(umax(umax(kk[0], kk[1]), umax(kk[2], kk[3])));
    }
    if constexpr (sl == 58 || sl == 61 || sl == 64 || sl == 67) {
      if (wid == 4 + (sl - 58) / 3) touch((unsigned)(((wid & 3) * 64 + l) * 128), (const char*)wdown + (size_t)fj * 16 * DM * 2);
    }
    if constexpr (sl == 80 || sl == 82 || sl == 84 || sl == 86) {
      if (wid == (sl - 80) / 2)
        touch((unsigned)((kh * 2048 + (within >> 1) * 128 + wid * 32 + ((within & 1) * 2 + (xcd & 1)) * 8 + (l >> 3)) * (DM * 2) + ((xcd >> 1) & 1) * 1024 + (l & 7) * 128), wo);
    }
    if constexpr ((sl >= 68 && sl <= 70) || (sl >= 90 && sl <= 92)) {
      constexpr int r = sl >= 90, q = sl - (r ? 90 : 68);
      if constexpr (q == 0) waitv<SC.younger(OF, 4 * r + 3, sl)>(fxs);
      if constexpr (q >= 1) {
#pragma unroll
        for (int j = 0; j < 2; ++j) { fq[2 * q - 2 + j] = __builtin_amdgcn_mfma_f32_16x16x32_bf16(fxs, ft[j], (f32x4){}, 0, 0, 0); PIN(fq[2 * q - 2 + j]); }
      }
      if constexpr (q < 2) {
        const unsigned rp = ldsf + g * 8192 + (c >> 2) * 128 + ((((g & 1) << 1) | (c >> 3)) << 5) + (c & 3) * 8;
#pragma unroll
        for (int j = 0; j < 2; ++j) { LAS const char* a = (LAS const char*)(uintptr_t)(rp ^ ((2 * q + j) << 5)); ft[j] = cat8(trd(a), trd(a + 512)); }
      }
    }
    if constexpr (sl == 71 || sl == 93) {
      const float v = sel2(g & 2, sel2(g & 1, fq[3][0], fq[2][0]), sel2(g & 1, fq[1][0], fq[0][0]));
      gst8((u32x2){__float_as_uint(v), tlo}, (unsigned)(((fsl * 2 + (sl == 93)) * FF + fgr * 512 + t) * 8), ws + WS_GU);
    }
    if constexpr (sl == 109) {
      waitv<SC.younger(OS, 2, sl)>(fm[0]);
      for (int spin = 0; spin < SPINS; ++spin) {
        if (__builtin_expect(__all((fm[0][1] == tlo) & (fm[0][3] == tlo)), 1)) break;
        __builtin_amdgcn_s_sleep(1);
        gld16<0, true>(fm[0], gu_v, ws + WS_GU);
        waitv<0>(fm[0]);
      }
      *(LAS f32x2*)(lds + RED1 + (wid * 32 + 2 * c) * 4) = (f32x2){xg_sum(__uint_as_float(fm[0][0])), xg_sum(__uint_as_float(fm[0][2]))};
    }
    if constexpr (sl == 111) {
#pragma unroll
      for (int w = 0; w < 2; ++w) { fa0[w] = *(LAS const float*)(lds + RED1 + ((2 * g + w) * 32 + c) * 4); fa0[2 + w] = *(LAS const float*)(lds + RED1 + ((2 * g + w) * 32 + 16 + c) * 4); }
    }
    if constexpr (sl == 112) { fa0[0] = xg_sum(fa0[0] + fa0[1]); fa0[2] = xg_sum(fa0[2] + fa0[3]); PIN(fa0); }
    if constexpr (sl == 113) {
      const float hh = rbf(silu_f(rbf(fa0[0]))) * rbf(fa0[2]);
      *(LAS u16*)(lds + HSC + 32 + wid * 32 + c * 2) = (u16)pk2(hh, hh);
    }
    if constexpr (sl == 114) fxa = *(LAS const s16x4*)(lds + HSC + 32 + wid * 32 + g * 8);
    constexpr int pr = fpiece(sl), pm = fpiece(sl - 1), ps = fpiece(sl - 2);
    if constexpr (ps >= 0) gst4<ps * 128>(sel2(g & 1, fa1[0], fa0[0]), (unsigned)((wid * 128 + (l & 31)) * 4), ws + WS_YP + (size_t)yrow * 4096);
    if constexpr (pm >= 0) { fa0 = MFMA16(fxa, fb0, (f32x4){}); fa1 = MFMA16(fxa, fb1, (f32x4){}); PIN(fa0); PIN(fa1); }
    if constexpr (pr >= 0) {
      waitv<SC.younger(OF, 8 + pr, sl)>();
      const unsigned fp = ldsf + (4 * g + (c >> 2)) * 64 + (g & 1) * 32 + (c & 3) * 8;
      fb0 = trd((LAS const char*)(uintptr_t)fp + (pr % 3) * 8192); fb1 = trd((LAS const char*)(uintptr_t)(fp ^ 32) + (pr % 3) * 8192);
    }
    if constexpr (sl == 111 || sl == 119 || sl == 127) {
      waitv<SC.younger(OW, (sl - 111) / 4 + 1, sl)>();
      if (l == 0) gst8((u32x2){tlo, thi}, (unsigned)(((rb * 8 + wid) * 32 + head) * 8), ws + WS_FO + ((sl - 111) / 8) * 16384);
    }
    if constexpr (sl == 123) {
      waitv<SC.younger(OM, 1, sl)>(fg);
      if (__builtin_expect(!__all((fg[0] == tlo) & (fg[1] == thi)), 0)) poll8(fo_v, ws + WS_FO, tlo, thi, 0xffffffffu);
    }
    SBAR();
    if constexpr (i == 6) {
      if constexpr (u == 5 || u == 13) waitbar_l<SC.younger(OG, 4 * u + 7, sl)>();
      else waitbar<SC.younger(OG, 4 * u + 7, sl)>();
      if constexpr (u == 0 || u == 1 || u == 5 || u == 7 || u == 8 || u == 10 || u == 12 || u == 14)
        STAMP(u == 0 ? 4 : u == 1 ? 5 : u == 5 ? 6 : u == 7 ? 7 : u == 8 ? 8 : u == 10 ? 9 : u == 12 ? 10 : 11);
    }
    if constexpr (PRIO && !(i & 1)) __builtin_amdgcn_s_setprio(i == 6 ? 3 : 2 - i / 2);
  };

  issue(IC(-2), IC(-1));
  if (wid == 5) poll8((unsigned)((rb * 32 + (l & 31)) * 8), ws + WS_G0, glo, ghi, 0xf8000000u);
  waitbar<SC.younger(OG, 3, -2)>();
  issue(IC(-1), IC(-1));
  STAMP(3);

  LBase trb[2], frb;
  trb[0].init(lds0 + g * 2048 + (c >> 2) * 64 + (g & 1) * 32 + (c & 3) * 8);
  trb[1].init(lds0 + g * 2048 + (c >> 2) * 64 + ((g & 1) ^ 1) * 32 + (c & 3) * 8);
  frb.init(lds0 + l * 16);
  f32x4 acc[8][2];
  bf16x8 wf[2][4];
  auto WRD1 = [&](auto Si, auto Base, auto Dd) AI {
    constexpr int s = Si, d = Dd, o = Base + (d >> 1) * 512;
    wf[s & 1][d] = cat8(trd(trb[d & 1].template at<o>()), trd(trb[d & 1].template at<o + 256>()));
  };
  auto WRD = [&](auto Si, auto Base) AI { sfor<4>([&](auto Dd) AI { WRD1(Si, Base, Dd); }); };
#define WA(s) IC((((s) >> 3) & 3) * 32768 + (((s) >> 1) & 3) * 8192 + ((s) & 1) * 1024)
#define WB(s) IC((((s) >> 2) & 3) * 32768 + (((s) >> 1) & 1) * 8192 + ((s) & 1) * 1024)
  bf16x8 fr[4];
  auto FRD = [&](auto Ni) AI { constexpr int n = Ni; if constexpr (n < 256) fr[n & 3] = *(LAS const bf16x8*)(frb.at<((n >> 5) & 3) * 32768 + (n & 31) * 1024>()); };

  WRD(IC(0), WA(0));
  SBAR();
  bf16x8 qf[4][2];
  auto QPACK = [&](auto Ds) AI {
    constexpr int ds = Ds;
#pragma unroll
    for (int qb = 0; qb < 2; ++qb) {
      const f32x4 a = acc[2 * ds][qb] * QSCALE, b = acc[2 * ds + 1][qb] * QSCALE;
      qf[ds][qb] = __builtin_bit_cast(bf16x8, (u32x4){pk2(a[0], a[1]), pk2(a[2], a[3]), pk2(b[0], b[1]), pk2(b[2], b[3])});
      PIN(qf[ds][qb]);
    }
  };
  sfor<64>([&](auto Si) AI {
    constexpr int s = Si, x0 = (s & ~1) % XD, x1 = (s | 1) % XD;
    if constexpr (!(s & 1)) waitv<SC.younger(OX, s + 1, s - 1)>(xf[x0], xf[x1]);
    sfor<4>([&](auto Dd) AI {
      constexpr int d = Dd, dt = (s & 1) * 4 + d;
      if constexpr (s + 1 < 64) WRD1(IC(s + 1), WA(s + 1), Dd);
      else if constexpr (d < 3) FRD(Dd);
      SBAR();
      if constexpr (s < 2) { MFZ(acc[dt][0], wf[s & 1][d], xf[x0]); MFZ(acc[dt][1], wf[s & 1][d], xf[x1]); }
      else { MF(acc[dt][0], wf[s & 1][d], xf[x0]); MF(acc[dt][1], wf[s & 1][d], xf[x1]); }
      issue(Si, Dd);
      if constexpr (s == 63 && (d & 1)) { QPACK(IC(d >> 1)); SBAR(); }
    });
    SBAR();
    endslot(Si);
  });

  f32x4 sc[2][2][2], negm[2];
  u32x4 pw[16][2];
  float lsum[2] = {0.f, 0.f};
#define MX3(a, b, c) __builtin_fmaxf(__builtin_fmaxf((a), (b)), (c))
#define EXP2(x) __builtin_amdgcn_exp2f(x)
  auto SM = [&](auto Hi, auto Pi) AI {
    constexpr int H = Hi, p = Pi, kt = p >> 2, qb = (p >> 1) & 1, r = 2 * (p & 1);
    const float e0 = EXP2(sc[H & 1][kt][qb][r]), e1 = EXP2(sc[H & 1][kt][qb][r + 1]);
    lsum[qb] += e0; lsum[qb] += e1;
    unsigned w = pk2(e0, e1);
    PIN(lsum[qb]); PIN(w);
    pw[H][qb][kt * 2 + (p & 1)] = w;
  };
  sfor<128>([&](auto Ni) AI {
    constexpr int n = Ni, H = n >> 3, i = n & 7, kt = i >> 2, ds = i & 3, sn = H & 1;
    FRD(IC(n + 3));
    if constexpr (n < 2) QPACK(IC(2 + n));
    sfor<2>([&](auto Qb) AI {
      constexpr int qb = Qb;
      if constexpr (ds == 0 && H == 0) MFZ(sc[sn][kt][qb], fr[n & 3], qf[0][qb]);
      else if constexpr (ds == 0) MFC(sc[sn][kt][qb], fr[n & 3], qf[0][qb], negm[qb]);
      else MF(sc[sn][kt][qb], fr[n & 3], qf[ds][qb]);
    });
    if constexpr (n == 5 || n == 6) {
      constexpr int qb = n - 5;
      NOPS(3); PIN(sc[0][0][qb]);
      const float m = -xg_max(__builtin_fmaxf(MX3(sc[0][0][qb][0], sc[0][0][qb][1], sc[0][0][qb][2]), sc[0][0][qb][3]));
      negm[qb] = (f32x4){m, m, m, m};
      sc[0][0][qb] += negm[qb];
      PIN(negm[qb]); PIN(sc[0][0][qb]);
    }
    if constexpr (n == 9 || n == 10) { NOPS(3); PIN(sc[0][1][n - 9]); sc[0][1][n - 9] += negm[n - 9]; PIN(sc[0][1][n - 9]); }
    issue(IC(64 + (n >> 2)), IC(n & 3));
    if constexpr (H >= 1) SM(IC(H - 1), IC(i));
    if constexpr (n == 125) { PIN(negm[0]); PIN(negm[1]); }
    SBAR();
    if constexpr ((n & 3) == 3) endslot(IC(64 + (n >> 2)));
  });
  sfor<8>([&](auto Pi) AI { if constexpr (Pi == 4) { NOPS(3); PIN(sc[1][1][0]); PIN(sc[1][1][1]); } SM(IC(15), Pi); });
  if (__builtin_expect(__any(!(lsum[0] + lsum[1] <= LBOUND)), 0)) {
    auto tile = [&](unsigned off, f32x4& s0, f32x4& s1) AI {
      bf16x8 kf[4];
      SBAR(); NOPS(7);
      sfor<4>([&](auto Ds) AI { constexpr int ds = Ds; gld16<ds * 1024, true>(kf[ds], off, kv_b); });
      waitv<0>(kf[0], kf[1]);
      PIN(kf[2]); PIN(kf[3]);
#pragma unroll
      for (int ds = 0; ds < 4; ++ds) {
        s0 = __builtin_amdgcn_mfma_f32_16x16x32_bf16(kf[ds], qf[ds][0], s0, 0, 0, 0);
        s1 = __builtin_amdgcn_mfma_f32_16x16x32_bf16(kf[ds], qf[ds][1], s1, 0, 0, 0);
      }
    };
    float m0 = -__builtin_inff(), m1 = m0;
#pragma unroll 1
    for (int kt = 0; kt < 32; ++kt) {
      f32x4 s0 = {}, s1 = {};
      tile((unsigned)(kt * 4096 + l * 16), s0, s1);
      m0 = MX3(MX3(m0, s0[0], s0[1]), s0[2], s0[3]); m1 = MX3(MX3(m1, s1[0], s1[1]), s1[2], s1[3]);
    }
    m0 = -xg_max(m0); m1 = -xg_max(m1);
    lsum[0] = 0.f; lsum[1] = 0.f;
    sfor<16>([&](auto Hi) AI {
      constexpr int H = Hi;
#pragma unroll
      for (int kt = 0; kt < 2; ++kt) {
        sc[H & 1][kt][0] = (f32x4){m0, m0, m0, m0}; sc[H & 1][kt][1] = (f32x4){m1, m1, m1, m1};
        tile((unsigned)((2 * H + kt) * 4096 + l * 16), sc[H & 1][kt][0], sc[H & 1][kt][1]);
      }
      sfor<8>([&](auto Pi) AI { SM(Hi, Pi); });
      PIN(pw[H][0]); PIN(pw[H][1]);
    });
  }
  SBAR();
  NOPS(1);
  sfor<128>([&](auto Mi) AI {
    constexpr int m = Mi, n = 128 + m, os = m >> 5, H = (m >> 1) & 15, par = m & 1;
    FRD(IC(n + 3));
    if constexpr (H == 0) { MFZ(o[os & 1][par][0], fr[n & 3], pw[H][0]); MFZ(o[os & 1][par][1], fr[n & 3], pw[H][1]); }
    else { MF(o[os & 1][par][0], fr[n & 3], pw[H][0]); MF(o[os & 1][par][1], fr[n & 3], pw[H][1]); }
    if constexpr (m == 2 || m == 3) { inv[m - 2] = __builtin_amdgcn_rcpf(xg_sum(lsum[m - 2])); PIN(inv[m - 2]); }
    issue(IC(64 + (n >> 2)), IC(n & 3));
    if constexpr (m == 123) {
      trb[0].init(lds0 + kg * 16384 + g * 2048 + (c >> 2) * 64 + (g & 1) * 32 + (c & 3) * 8);
      trb[1].init(lds0 + kg * 16384 + g * 2048 + (c >> 2) * 64 + ((g & 1) ^ 1) * 32 + (c & 3) * 8);
    }
    if constexpr (m >= 124) WRD1(IC(0), WB(0), IC(m - 124));
    SBAR();
    if constexpr ((n & 3) == 3) endslot(IC(64 + (n >> 2)));
  });
  STAMP(12); STAMP(13); STAMP(14); STAMP(15);

  const int idx = SEQ - 1 - (int)(key & 2047u);
  const bool owner = (idx >> 7) == mb, ownw = (idx >> 4) == mb * 8 + rbw * 2 + kg;
  f32x4 yp[7], ya[4], oth[4];
  unsigned gate[8];
  auto item = [](int s) { return s < 48 ? s : s < 56 ? 48 + 2 * (s - 48) : 49 + 2 * (s - 56); };
  u32x2 pf;
  sfor<64>([&](auto Si) AI {
    constexpr int s = Si, u = s >> 2, it = item(s), x0 = (it & ~1) % XD2, x1 = (it | 1) % XD2;
    if constexpr (!(it & 1)) waitv<SC.younger(OX, 64 + it + 1, B0 + s - 1)>(xf[x0], xf[x1]);
    sfor<4>([&](auto Dd) AI {
      constexpr int d = Dd, dt = (it & 1) * 4 + d;
      if constexpr (s + 1 < 64) WRD1(IC(s + 1), WB(item(s + 1)), Dd);
      SBAR();
      if constexpr (s < 2) { MFZ(acc[dt][0], wf[s & 1][d], xf[x0]); MFZ(acc[dt][1], wf[s & 1][d], xf[x1]); }
      else { MF(acc[dt][0], wf[s & 1][d], xf[x0]); MF(acc[dt][1], wf[s & 1][d], xf[x1]); }
      issue(IC(B0 + s), Dd);
      if constexpr (s == 56) *(LAS f32x4*)(lds + (wid ^ 4) * 4096 + l * 16 + (d & 1) * 2048 + (d >> 1) * 32768) = acc[d][1];
      if constexpr (s == 57 && d == 0) {
#pragma unroll
        for (int e = 0; e < 4; ++e) oth[e] = *(LAS const f32x4*)(lds + wid * 4096 + l * 16 + (e & 1) * 2048 + (e >> 1) * 32768);
      }
      if constexpr (s == 57 && d >= 2) {
        gst16<(2 * d - 4) * 1024>(acc[2 * d - 4][0] + oth[2 * d - 4], (unsigned)(wid * 4096 + l * 16), ws + WS_XC + (size_t)(bid ^ 4) * 32768);
        gst16<(2 * d - 3) * 1024>(acc[2 * d - 3][0] + oth[2 * d - 3], (unsigned)(wid * 4096 + l * 16), ws + WS_XC + (size_t)(bid ^ 4) * 32768);
      }
      if constexpr (s == 58 || s == 59) {
        constexpr int j = (s - 58) * 4 + d;
        if constexpr (j == 0) waitv<4>();
        PIN(xe[j >> 1]); PIN(st);
        const unsigned xw = xe[j >> 1][j & 1];
        const unsigned dd = pk2(bflo(xw) - st[0], bfhi(xw) - st[0]);
        gate[j] = pk2(bflo(dd) * st[1], bfhi(dd) * st[1]);
        PIN(gate[j]);
      }
      if constexpr (s >= 60 && !(d & 1)) {
        constexpr int j = (s - 60) * 2 + (d >> 1);
        gate[j] = pk2(silu_f(bflo(gate[j])), silu_f(bfhi(gate[j])));
        PIN(gate[j]);
      }
    });
    SBAR();
    if constexpr (s == 7) {
      waitv<SC.younger(OW, 7, B0 + s)>();
      if (l == 0) {
        gst8((u32x2){tlo, thi}, (unsigned)(((rb * 8 + wid) * 32 + head) * 8), ws + WS_FO + 3 * 16384);
        gst8((u32x2){tlo, thi}, (unsigned)((wid * 256 + yrow) * 8), ws + WS_FY);
      }
    }
    if constexpr (s == 4 || s == 17 || s == 33) {
      constexpr int j = s == 4 ? 1 : s == 17 ? 2 : 3;
      waitv<SC.younger(OM, j + 1, B0 + s)>(fg);
      if (__builtin_expect(!__all((fg[0] == tlo) & (fg[1] == thi)), 0)) poll8(fo_v, ws + WS_FO + j * 16384, tlo, thi, 0xffffffffu);
    }
    if constexpr (s == 60) {
      waitv<0>();
      if (l == 0) gst8((u32x2){tlo, thi}, (unsigned)(((bid ^ 4) * 8 + wid) * 64), ws + WS_FX);
    }
    if constexpr (s == 61) gld8<0, true>(pf, (unsigned)((bid * 8 + wid) * 64), ws + WS_FX);
    if constexpr (s == 19) {
      if (owner) { gld16<0, true>(fm[0], (unsigned)(nb * 2048 + (l < 56 ? l : 55) * 32), ws + WS_FY); gld16<16, true>(fm[1], (unsigned)(nb * 2048 + (l < 56 ? l : 55) * 32), ws + WS_FY); }
    }
    if constexpr (s == 34) {
      if (owner) {
        PIN(fm[0]); PIN(fm[1]);
        for (int spin = 0; spin < SPINS; ++spin) {
          const bool ok = (fm[0][0] == tlo) & (fm[0][1] == thi) & (fm[0][2] == tlo) & (fm[0][3] == thi) & (fm[1][0] == tlo) & (fm[1][1] == thi) & (fm[1][2] == tlo) & (fm[1][3] == thi);
          if (__builtin_expect(__all(ok), 1)) break;
          __builtin_amdgcn_s_sleep(1);
          gld16<0, true>(fm[0], (unsigned)(nb * 2048 + (l < 56 ? l : 55) * 32), ws + WS_FY); gld16<16, true>(fm[1], (unsigned)(nb * 2048 + (l < 56 ? l : 55) * 32), ws + WS_FY);
          waitv<0>(fm[0], fm[1]);
        }
        const unsigned yv = (unsigned)(((t >> 4) * 1024 + nb * 128 + kh * 64 + (t & 15) * 4) * 4);
        sfor<7>([&](auto I) AI { constexpr int q = I; gld16<0, true>(yp[q], yv, ws + WS_YP + (size_t)q * 32 * 4096); });
      }
    }
    if constexpr (s == 50) {
      if (owner) {
        f32x4 a = {};
#pragma unroll
        for (int q = 0; q < 7; ++q) { PIN(yp[q]); a += yp[q]; }
#pragma unroll
        for (int j = 0; j < 4; ++j) a[j] = xg_sum(a[j]);
        *(LAS f32x4*)(lds + YS + (wid * 64 + c * 4) * 4) = a;
      }
    }
    if constexpr (s == 53) {
      if (ownw) {
        float a = 0.f;
#pragma unroll
        for (int p = 0; p < 8; ++p) a += *(LAS const float*)(lds + YS + (p * 64 + l) * 4);
        const unsigned ord = key >> 11, bits = (ord & 0x8000u) ? (ord & 0x7fffu) : (~ord & 0xffffu);
        *(LAS float*)(lds + RED1 + l * 4) = rbf(rbf(a) * rbf(sigmoid_f(bflo(bits))));
#pragma unroll
        for (int q = 0; q < 4; ++q) ya[q] = *(LAS const f32x4*)(lds + RED1 + (q * 16 + 4 * g) * 4);
      }
    }
    if constexpr (s == 59) {
      if (ownw) {
#pragma unroll
        for (int j = 0; j < 8; ++j) {
          const unsigned w = pk2(bflo(gate[j]) + ya[j >> 1][2 * (j & 1)], bfhi(gate[j]) + ya[j >> 1][2 * (j & 1) + 1]);
          gate[j] = erow == idx ? w : gate[j];
          PIN(gate[j]);
        }
      }
    }
    SBAR();
    if constexpr (((s & 3) == 2 && u < 11) || s == 44) {
      waitbar<SC.younger(OG, 64 + 4 * u + 7, B0 + s)>();
      if constexpr (u % 4 == 1) STAMP(16 + u / 4);
    }
    if constexpr (s == 48) waitbar<SC.younger(OG, 64 + 55, B0 + s)>();
    if constexpr (s == 50) waitbar<SC.younger(OG, 64 + 59, B0 + s)>();
    if constexpr (s == 52) { waitbar_l<SC.younger(OG, 64 + 63, B0 + s)>(); STAMP(19); }
    if constexpr (s == 56) waitbar_l<63>();
    if constexpr (PRIO) __builtin_amdgcn_s_setprio(3 - ((s + 2) & 3));
  });
  STAMP(20);

  f32x4 kep[4];
  {
    waitv<0>(pf);
    const bool rdy = __all((pf[0] == tlo) & (pf[1] == thi));
    if (rdy) sfor<4>([&](auto Dd) AI { constexpr int d = Dd; gld16<d * 1024, true>(oth[d], (unsigned)(wid * 4096 + l * 16), ws + WS_XC + (size_t)bid * 32768); });
    NOPS(3);
#pragma unroll
    for (int d = 0; d < 4; ++d) { PIN(acc[4 + d][1]); *(LAS f32x4*)(lds + 65536 + (wid ^ 4) * 4096 + l * 16 + (d & 1) * 2048 + (d >> 1) * 32768) = acc[4 + d][1]; }
    waitbar_l<63>();
    STAMP(21);
#pragma unroll
    for (int d = 0; d < 4; ++d) kep[d] = acc[4 + d][0] + *(LAS const f32x4*)(lds + 65536 + wid * 4096 + l * 16 + (d & 1) * 2048 + (d >> 1) * 32768);
    STAMP(22);
    if (!rdy) {
      poll8((unsigned)((bid * 8 + wid) * 64), ws + WS_FX, tlo, thi, 0xffffffffu);
      sfor<4>([&](auto Dd) AI { constexpr int d = Dd; gld16<d * 1024, true>(oth[d], (unsigned)(wid * 4096 + l * 16), ws + WS_XC + (size_t)bid * 32768); });
    }
    STAMP(23);
    waitv<0>(oth[0], oth[1]);
    PIN(oth[2]); PIN(oth[3]);
    STAMP(24);
  }
  u16* op = out + (size_t)erow * DM + ecol;
#pragma unroll
  for (int d = 0; d < 4; ++d) {
    const f32x4 a = kep[d] + oth[d];
    u32x2 res;
#pragma unroll
    for (int j = 0; j < 2; ++j) {
      const unsigned sp = pk2(a[2 * j], a[2 * j + 1]), gp = gate[2 * d + j], xw = xe[d][j];
      const unsigned pr = pk2(bflo(sp) * bflo(gp), bfhi(sp) * bfhi(gp));
      res[j] = pk2(bflo(xw) + bflo(pr), bfhi(xw) + bfhi(pr));
    }
    *(u32x2*)(op + d * 16) = res;
  }
  STAMP(25);
  STAMP_OUT(0, 26);
}

extern "C" void kernel_launch(void* const* d_in, const int* in_sizes, int n_in, void* d_out, int out_size, void* d_ws, size_t ws_size,
                              hipStream_t stream) {
  k_fused<<<256, 512, 0, stream>>>((const u16*)d_in[0], (const u16*)d_in[1], (const u16*)d_in[2], (const u16*)d_in[3], (const u16*)d_in[4],
                                   (const u16*)d_in[6], (const u16*)d_in[7], (const u16*)d_in[8], (const u16*)d_in[9], (char*)d_ws, (u16*)d_out);
}
```

```cpp
#include <hip/hip_runtime.h>
#include <stdint.h>
#include <type_traits>

typedef unsigned short u16;
typedef short bf16x8 __attribute__((ext_vector_type(8)));
typedef short s16x4 __attribute__((ext_vector_type(4)));
typedef float f32x4 __attribute__((ext_vector_type(4)));
typedef float f32x2 __attribute__((ext_vector_type(2)));
typedef unsigned u32x4 __attribute__((ext_vector_type(4)));
typedef unsigned u32x2 __attribute__((ext_vector_type(2)));
typedef __bf16 bf2_t __attribute__((ext_vector_type(2)));
#define LAS __attribute__((address_space(3)))
#define DI __device__ __forceinline__
#define AI __attribute__((always_inline))
#define SBAR() __builtin_amdgcn_sched_barrier(0)
template <class T> __device__ __forceinline__ void PIN(T& x) { asm volatile("" : "+v"(x)); }
template <class B> DI void MF(f32x4& acc, const bf16x8& a, const B& b) { asm volatile("v_mfma_f32_16x16x32_bf16 %0, %1, %2, %0" : "+v"(acc) : "v"(a), "v"(b)); }
template <class B> DI void MFC(f32x4& d, const bf16x8& a, const B& b, const f32x4& c) { asm volatile("v_mfma_f32_16x16x32_bf16 %0, %1, %2, %3" : "=&v"(d) : "v"(a), "v"(b), "v"(c)); }
template <class B> DI void MFZ(f32x4& d, const bf16x8& a, const B& b) { asm volatile("v_mfma_f32_16x16x32_bf16 %0, %1, %2, 0" : "=&v"(d) : "v"(a), "v"(b)); }
#define NOPS(n) asm volatile("s_nop " #n)
#define MFMA16(a, b, c) __builtin_amdgcn_mfma_f32_16x16x16bf16_1k((a), (b), (c), 0, 0, 0)
#define IC(n) std::integral_constant<int, (n)>{}

constexpr int SEQ = 2048, DM = 1024, DH = 128, SK = 2048, WIN = 512, FF = 3584;
constexpr size_t WS_XN = 0;
constexpr size_t WS_KV = 4u << 20;
constexpr size_t WS_ST = 5u << 20;
constexpr size_t WS_EP = WS_ST + 16384;
constexpr size_t WS_G0 = WS_ST + 32768;
constexpr size_t WS_FK = WS_ST + 49152;
constexpr size_t WS_YP = WS_ST + 65536;
constexpr size_t WS_FO = 7u << 20;
constexpr size_t WS_FY = WS_FO + 65536;
constexpr size_t WS_FX = WS_FO + 131072;
constexpr size_t WS_O = 8u << 20;
constexpr size_t WS_GU = 32u << 20;
constexpr size_t WS_XC = 24u << 20;

template <int I, int N, class F> DI void sfor_impl(F&& f) {
  if constexpr (I < N) { f(std::integral_constant<int, I>{}); sfor_impl<I + 1, N>(f); }
}
template <int N, class F> DI void sfor(F&& f) { sfor_impl<0, N>(f); }

DI unsigned pk2(float a, float b) { f32x2 v = {a, b}; return __builtin_bit_cast(unsigned, __builtin_convertvector(v, bf2_t)); }
DI float rbf(float a) { return (float)(__bf16)a; }
DI float bflo(unsigned u) { return __uint_as_float(u << 16); }
DI float bfhi(unsigned u) { return __uint_as_float(u & 0xffff0000u); }
DI float sigmoid_f(float v) { return __builtin_amdgcn_rcpf(1.f + __builtin_amdgcn_exp2f(v * -1.4426950408889634f)); }
DI float sel2(bool c, float a, float b) { PIN(a); PIN(b); return c ? a : b; }
DI float silu_f(float v) { return v * sigmoid_f(v); }
template <int CTRL> DI float dppf(float x) { return __builtin_bit_cast(float, __builtin_amdgcn_mov_dpp(__builtin_bit_cast(int, x), CTRL, 0xf, 0xf, true)); }
template <int CTRL> DI unsigned dppu(unsigned x) { return (unsigned)__builtin_amdgcn_mov_dpp((int)x, CTRL, 0xf, 0xf, true); }
constexpr int XOR1 = 0xB1, XOR2 = 0x4E, XOR7 = 0x141, XOR8 = 0x128;
DI float xg_sum(float x) {
  auto s = __builtin_amdgcn_permlane16_swap(__float_as_uint(x), __float_as_uint(x), false, false);
  x = __uint_as_float(s[0]) + __uint_as_float(s[1]);
  auto t = __builtin_amdgcn_permlane32_swap(__float_as_uint(x), __float_as_uint(x), false, false);
  return __uint_as_float(t[0]) + __uint_as_float(t[1]);
}
DI float xg_max(float x) {
  auto s = __builtin_amdgcn_permlane16_swap(__float_as_uint(x), __float_as_uint(x), false, false);
  x = __builtin_fmaxf(__uint_as_float(s[0]), __uint_as_float(s[1]));
  auto t = __builtin_amdgcn_permlane32_swap(__float_as_uint(x), __float_as_uint(x), false, false);
  return __builtin_fmaxf(__uint_as_float(t[0]), __uint_as_float(t[1]));
}
DI float wave_sum(float v) { v += dppf<XOR1>(v); v += dppf<XOR2>(v); v += dppf<XOR7>(v); v += dppf<XOR8>(v); return xg_sum(v); }
DI unsigned umax(unsigned a, unsigned b) { return a > b ? a : b; }
DI unsigned wave_max_u32(unsigned v) {
  v = umax(v, dppu<XOR1>(v)); v = umax(v, dppu<XOR2>(v)); v = umax(v, dppu<XOR7>(v)); v = umax(v, dppu<XOR8>(v));
  auto s = __builtin_amdgcn_permlane16_swap(v, v, false, false); v = umax(s[0], s[1]);
  auto t = __builtin_amdgcn_permlane32_swap(v, v, false, false); v = umax(t[0], t[1]);
  return __builtin_amdgcn_readfirstlane(v);
}

template <int OFF, bool DEV = false> DI void glds(unsigned ldsaddr, unsigned voff, const void* sbase) {
  if constexpr (DEV) asm volatile("s_mov_b32 m0, %0\n\ts_nop 0\n\tglobal_load_lds_dwordx4 %1, %2 offset:%3 sc1" ::"s"(ldsaddr), "v"(voff), "s"(sbase), "i"(OFF) : "memory");
  else asm volatile("s_mov_b32 m0, %0\n\ts_nop 0\n\tglobal_load_lds_dwordx4 %1, %2 offset:%3" ::"s"(ldsaddr), "v"(voff), "s"(sbase), "i"(OFF) : "memory");
}
DI void touch(unsigned voff, const void* sbase) {
  asm volatile("global_load_dword v255, %0, %1" ::"v"(voff), "s"(sbase) : "memory", "v255");
}
template <int OFF, bool DEV = false, class T> DI void gld16(T& dst, unsigned voff, const void* sbase) {
  if constexpr (DEV) asm volatile("global_load_dwordx4 %0, %1, %2 offset:%3 sc1" : "=v"(dst) : "v"(voff), "s"(sbase), "i"(OFF) : "memory");
  else asm volatile("global_load_dwordx4 %0, %1, %2 offset:%3" : "=v"(dst) : "v"(voff), "s"(sbase), "i"(OFF) : "memory");
}
template <int OFF, bool DEV = false, class T> DI void gld8(T& dst, unsigned voff, const void* sbase) {
  if constexpr (DEV) asm volatile("global_load_dwordx2 %0, %1, %2 offset:%3 sc1" : "=v"(dst) : "v"(voff), "s"(sbase), "i"(OFF) : "memory");
  else asm volatile("global_load_dwordx2 %0, %1, %2 offset:%3" : "=v"(dst) : "v"(voff), "s"(sbase), "i"(OFF) : "memory");
}
template <int OFF> DI void gld2(unsigned& dst, unsigned voff, const void* sbase) {
  asm volatile("global_load_ushort %0, %1, %2 offset:%3" : "=v"(dst) : "v"(voff), "s"(sbase), "i"(OFF) : "memory");
}
template <int OFF, class T> DI void gst16(const T& v, unsigned voff, const void* sbase) {
  asm volatile("global_store_dwordx4 %0, %1, %2 offset:%3 sc1\n\ts_nop 1" ::"v"(voff), "v"(v), "s"(sbase), "i"(OFF) : "memory");
}
template <class T> DI void gst8(const T& v, unsigned voff, const void* sbase) {
  asm volatile("global_store_dwordx2 %0, %1, %2 sc1" ::"v"(voff), "v"(v), "s"(sbase) : "memory");
}
template <int OFF> DI void gst4(float v, unsigned voff, const void* sbase) {
  asm volatile("global_store_dword %0, %1, %2 offset:%3 sc1" ::"v"(voff), "v"(v), "s"(sbase), "i"(OFF) : "memory");
}
constexpr int SPINS = 1 << 16;
DI void poll8(unsigned voff, const void* sbase, unsigned lo, unsigned hi, unsigned lomask) {
  for (int spin = 0; spin < SPINS; ++spin) {
    u32x2 v;
    asm volatile("global_load_dwordx2 %0, %1, %2 sc1\n\ts_waitcnt vmcnt(0)" : "=&v"(v) : "v"(voff), "s"(sbase) : "memory");
    if (__all(((v[0] & lomask) == lo) & (v[1] == hi))) break;
    __builtin_amdgcn_s_sleep(1);
  }
}
template <int N> DI void waitv() { static_assert(N >= 0, "count"); asm volatile("s_waitcnt vmcnt(%0)" ::"n"(N > 63 ? 63 : N) : "memory"); }
template <int N, class T> DI void waitv(T& x) { static_assert(N >= 0, "count"); asm volatile("s_waitcnt vmcnt(%1)" : "+v"(x) : "n"(N > 63 ? 63 : N) : "memory"); }
template <int N, class T> DI void waitv(T& x, T& y) { static_assert(N >= 0, "count"); asm volatile("s_waitcnt vmcnt(%2)" : "+v"(x), "+v"(y) : "n"(N > 63 ? 63 : N) : "memory"); }
template <int N> DI void waitbar() { static_assert(N >= 0, "count"); asm volatile("s_waitcnt vmcnt(%0)\n\ts_barrier" ::"n"(N > 63 ? 63 : N) : "memory"); }
template <int N> DI void waitbar_l() { static_assert(N >= 0, "count"); asm volatile("s_waitcnt vmcnt(%0) lgkmcnt(0)\n\ts_barrier" ::"n"(N > 63 ? 63 : N) : "memory"); }
typedef short v4i16_t __attribute__((ext_vector_type(4)));
DI s16x4 trd(LAS const char* p) { return __builtin_amdgcn_ds_read_tr16_b64_v4i16((LAS v4i16_t*)p); }
struct LBase {
  unsigned lo, hi;
  DI void init(unsigned a) { lo = a; hi = a + 65536; PIN(lo); PIN(hi); }
  template <int OFF> DI LAS const char* at() const { return (LAS const char*)(uintptr_t)(OFF < 65536 ? lo : hi) + (OFF & 65535); }
};
DI bf16x8 cat8(s16x4 lo, s16x4 hi) { return __builtin_shufflevector(lo, hi, 0, 1, 2, 3, 4, 5, 6, 7); }

enum { OX, OG, OF, OM, OS, OP, OE, OW };
struct Sched {
  int n; short kind[400], idx[400], slot[400];
  constexpr Sched() : n(0), kind{}, idx{}, slot{} {}
  constexpr void add(int s, int k, int i) { kind[n] = (short)k; idx[n] = (short)i; slot[n] = (short)s; ++n; }
  constexpr int younger(int k, int i, int s) const {
    int p = -1;
    for (int q = 0; q < n; ++q) if (kind[q] == k && idx[q] == i) p = q;
    if (p < 0 || slot[p] > s) return -1;
    int c = 0;
    for (int q = p + 1; q < n; ++q) if (slot[q] <= s) ++c;
    return c;
  }
  constexpr int first(int s) const { int q = 0; while (q < n && slot[q] < s) ++q; return q; }
  constexpr int count(int s) const { int c = 0; for (int q = 0; q < n; ++q) if (slot[q] == s) ++c; return c; }
};

#define STAMP_DECL
#define STAMP(i)
#define STAMP_OUT(kid, n)

constexpr int RING = 4 * 32768, FB = RING, RED1 = FB + 24576, HSC = RED1 + 1024, YS = HSC + 1024, LDS_BYTES = FB + 32768;
constexpr int STAGE = 3 * 32768;
constexpr int XD = 16, XD2 = 16;
constexpr float LBOUND = 65536.f;
constexpr float QSCALE = 0.08838834764831845f * 1.4426950408889634f;
constexpr int B0 = 128;
#define PRIO 1
constexpr int FISS[4] = {96, 97, 98, 117}, FRDS[4] = {115, 118, 121, 124};
constexpr int fpiece(int sl) { for (int p = 0; p < 4; ++p) if (FRDS[p] == sl) return p; return -1; }
constexpr Sched make_sched() {
  Sched s;
#define XP 8
  for (int i = 0; i < 8; ++i) s.add(-2, OG, i);
  for (int i = 0; i < XP; ++i) s.add(-1, OX, i);
  for (int t = 0; t < 128; ++t) {
    if (t < 2) for (int i = XP + t * (16 - XP) / 2; i < XP + (t + 1) * (16 - XP) / 2; ++i) s.add(t, OX, i);
    if (t >= 2 && t < 50) s.add(t, OX, t + 14);
    if (t >= 2 && t < 6) s.add(t, OG, 6 + t);
    for (int v = 3; v < 18; ++v) for (int p = 0; p < 4; ++p) if (8 * (v - 3) + 7 + p == t) s.add(t, OG, 4 * v + p);
    if (t == 105 || t == 113 || t == 121) { s.add(t, OW, (t - 105) / 4); s.add(t, OW, (t - 105) / 4 + 1); }
    if (t >= 124) for (int i = 0; i < 3; ++i) s.add(t, OX, 64 + 3 * (t - 124) + i);
    if (t == 127) { s.add(t, OG, 72); s.add(t, OG, 73); }
    if (t == 30) s.add(t, OM, 0);
    if (t == 120) s.add(t, OM, 1);
    if (t >= 49 && t < 57 && (t & 1)) s.add(t, OF, (t - 49) / 2);
    if (t >= 71 && t < 79 && (t & 1)) s.add(t, OF, 4 + (t - 71) / 2);
    if (t == 48) s.add(t, OS, 0);
    if (t == 104) s.add(t, OS, 2);
    for (int p = 0; p < 4; ++p) if (FISS[p] == t) s.add(t, OF, 8 + p);
  }
  for (int t = 0; t < 64; ++t) {
    if (t == 0) { s.add(B0, OM, 2); s.add(B0, OG, 74); s.add(B0, OG, 75); s.add(B0, OW, 6); s.add(B0, OW, 7); }
    if (t == 1 || t == 2) { s.add(B0 + t, OX, 74 + 2 * t); s.add(B0 + t, OX, 75 + 2 * t); }
    if (t == 5 || t == 6 || t == 9) s.add(B0 + t, OX, 64 + (t == 5 ? 16 : t == 6 ? 18 : 22));
    if (t >= 5 && t < 50) s.add(B0 + t, OX, 64 + (t == 5 ? 17 : t < 9 ? t + 13 : t + 14));
    for (int v = 3; v < 16; ++v) for (int p = 0; p < 4; ++p) if ((v < 14 ? 4 * (v - 3) + 3 + (p >> 1) : v == 14 ? 45 + (p >> 1) : 49) == t) s.add(B0 + t, OG, 64 + 4 * v + p);
    if (t == 10 || t == 26) s.add(B0 + t, OM, t == 10 ? 3 : 4);
    if (t == 50) for (int i = 0; i < 5; ++i) s.add(B0 + t, OE, i);
  }
  return s;
}
constexpr Sched SC = make_sched();

__global__ __launch_bounds__(512, 2) void k_fused(const u16* __restrict__ x, const u16* __restrict__ kc, const u16* __restrict__ vc,
                                                  const u16* __restrict__ wq, const u16* __restrict__ wo, const u16* __restrict__ wr,
                                                  const u16* __restrict__ wgate, const u16* __restrict__ wup, const u16* __restrict__ wdown,
                                                  char* __restrict__ ws, u16* __restrict__ out) {
  __shared__ __attribute__((aligned(16))) char lds_raw[LDS_BYTES];
  LAS char* const lds = (LAS char*)lds_raw;
  const int t = threadIdx.x, l = t & 63, bid = blockIdx.x, c = l & 15, g = l >> 4;
  const int wid = __builtin_amdgcn_readfirstlane(t >> 6);
  const int xcd = bid & 7, within = bid >> 3, head = xcd * 4 + (within & 3), rb = within >> 2;
  const int fj = xcd * 28 + (within < 28 ? within : within - 28);
  const int fsl = fj / 7, fgr = fj - fsl * 7;
  const int kh = xcd >> 2, mb = 2 * rb + (xcd & 1), nb = ((xcd >> 1) & 1) * 4 + (within & 3);
  const int rbw = wid & 3, kg = wid >> 2;
  const unsigned lds0 = (unsigned)(uintptr_t)lds;
  STAMP_DECL
  STAMP(0);

  unsigned tlo, thi, e[8];
  u32x2 klo, khi;
  const int fn = (bid * 4 + wid) & 255, kvh = bid >> 6;
  {
    const int row = 8 * bid + wid;
    u32x4 a0, a1, w0, w1;
    u32x2 ep;
    gld16<0>(a0, (unsigned)(row * DM * 2 + l * 16), x); gld16<1024>(a1, (unsigned)(row * DM * 2 + l * 16), x);
    gld16<0>(w0, (unsigned)(l * 16), wr); gld16<1024>(w1, (unsigned)(l * 16), wr);
    gld8<0, true>(ep, 0u, ws + WS_EP);
    waitv<3>();
    if (wid == 0) touch((unsigned)((within * 8 + (l >> 3)) * 8192 + xcd * 1024 + (l & 7) * 128), wq);
    if (wid == 3) {
      const char* pc;
      asm volatile("s_getpc_b64 %0" : "=s"(pc));
      touch((unsigned)((within * 12 + (l < 12 ? l : 11)) * 128), pc);
    }
    PIN(a0); PIN(a1);
    STAMP(1);
    float v[16];
#pragma unroll
    for (int j = 0; j < 4; ++j) { v[2 * j] = bflo(a0[j]); v[2 * j + 1] = bfhi(a0[j]); v[8 + 2 * j] = bflo(a1[j]); v[9 + 2 * j] = bfhi(a1[j]); }
    float s = 0.f;
#pragma unroll
    for (int j = 0; j < 16; ++j) s += v[j];
    const float mean = wave_sum(s) * (1.f / DM);
    float q = 0.f;
#pragma unroll
    for (int j = 0; j < 16; ++j) { const float d = v[j] - mean; q += d * d; }
    const float var = wave_sum(q) * (1.f / DM);
    const float mbf = rbf(mean), rden = __builtin_amdgcn_rcpf(rbf(__builtin_amdgcn_sqrtf(rbf(rbf(var) + 1e-5f))));
    u32x4 o0, o1;
#pragma unroll
    for (int j = 0; j < 8; ++j) {
      const unsigned dd = pk2(v[2 * j] - mbf, v[2 * j + 1] - mbf);
      const unsigned nn = pk2(bflo(dd) * rden, bfhi(dd) * rden);
      if (j < 4) o0[j] = nn; else o1[j - 4] = nn;
    }
    *(LAS u32x4*)(lds + STAGE + (l * 8 + (wid ^ (l & 7))) * 16) = o0;
    *(LAS u32x4*)(lds + STAGE + 8192 + (l * 8 + (wid ^ (l & 7))) * 16) = o1;
    *(LAS f32x2*)(lds + STAGE + 16384 + 32 + wid * 8) = (f32x2){mbf, rden};
    waitbar_l<63>();
    if (wid >= 4) {
#pragma unroll
      for (int hh = 0; hh < 4; ++hh) {
        const int p = hh * 256 + (t - 256), ch = p >> 3, r = 8 * bid + ((p & 7) ^ (ch & 7));
        const u32x4 o = *(LAS const u32x4*)(lds + STAGE + p * 16);
        gst16<0>(o, (unsigned)(((((r >> 5) * 32 + (ch >> 2)) * 2 + ((r >> 4) & 1)) * 64 + (ch & 3) * 16 + (r & 15)) * 16), ws + WS_XN);
      }
      if (t < 256 + 8) gst8(*(LAS const f32x2*)(lds + STAGE + 16384 + 32 + (t - 256) * 8), (unsigned)((8 * bid + t - 256) * 8), ws + WS_ST);
      waitv<4>();
    } else if (wid == 0 || wid == 3) waitv<1>(); else waitv<0>();
    PIN(w0); PIN(w1); PIN(ep);
    {
      const unsigned long long tg = (((unsigned long long)(unsigned)__builtin_amdgcn_readfirstlane(ep[1]) << 32) | (unsigned)__builtin_amdgcn_readfirstlane(ep[0])) + 1ull;
      tlo = (unsigned)tg; thi = (unsigned)(tg >> 32);
    }
    float rr = 0.f;
#pragma unroll
    for (int j = 0; j < 8; ++j) {
      const unsigned nn = j < 4 ? o0[j] : o1[j - 4], ww = j < 4 ? w0[j] : w1[j - 4];
      rr += bflo(nn) * bflo(ww); rr += bfhi(nn) * bfhi(ww);
    }
    rr = wave_sum(rr);
    {
      const unsigned bits = pk2(rr, 0.f) & 0xffffu;
      const unsigned ord = (bits & 0x8000u) ? (~bits & 0xffffu) : (bits | 0x8000u);
      *(LAS unsigned*)(lds + STAGE + 16384 + wid * 4) = (ord << 11) | (unsigned)(SEQ - 1 - row);
    }
    if (wid >= 4) waitv<0>();
    waitbar_l<63>();
    if (t == 256) {
      const u32x4 ka = *(LAS const u32x4*)(lds + STAGE + 16384), kb = *(LAS const u32x4*)(lds + STAGE + 16400);
      const unsigned key = umax(umax(umax(ka[0], ka[1]), umax(ka[2], ka[3])), umax(umax(kb[0], kb[1]), umax(kb[2], kb[3])));
      gst8((u32x2){(tlo << 27) | key, (tlo >> 5) | (thi << 27)}, (unsigned)(bid * 8), ws + WS_G0);
    }
    STAMP(2);
  }
  const unsigned glo = tlo << 27, ghi = (tlo >> 5) | (thi << 27);

  const char* const wq_b = (const char*)wq + head * 256;
  const unsigned wq_v = (unsigned)(((8 * wid + ((l & 31) >> 2)) * 4096 + (l >> 5) * 32 + ((l & 3) ^ (2 * (wid & 1))) * 8) * 2);
  const char* const kv_b = ws + WS_KV + (size_t)(head >> 3) * 262144;
  const unsigned kv_v = (unsigned)(wid * 4096 + l * 16);
  const char* const xn_b = ws + WS_XN + (size_t)rb * 8 * 65536 + 4096;
  const unsigned xn_v = (unsigned)(wid * 65536 + l * 16);
  const char* const wo_b = (const char*)wo + (size_t)kh * 2048 * (DM * 2) + nb * 256;
  const char* const o_b = ws + WS_O + (size_t)mb * 4 * 262144 + kh * 131072;
  const unsigned ldsw = lds0 + wid * 2048, ldsk = lds0 + wid * 4096, ldsf = lds0 + FB + wid * 1024;
  const int erow = mb * 128 + rbw * 32 + kg * 16 + c, ecol = nb * 128 + kh * 64 + 4 * g;
  bf16x8 xf[XD];
  u32x4 fm[2];
  bf16x8 fxs;
  const unsigned gu_v = (unsigned)((((4 * wid + g) * 2 + ((l >> 3) & 1)) * FF + fj * 16 + (l & 7) * 2) * 8);
  u32x2 fg;
  u32x2 xe[4];
  f32x2 st;
  unsigned key = 0;

  const unsigned fo_v = (unsigned)(((mb * 4 + rbw) * 32 + kh * 16 + c) * 8);
  const int yrow = within < 28 ? fj : 224 + xcd * 4 + within - 28;
  f32x4 o[2][2][2];
  float inv[2];
  const char* const od_b = ws + WS_O + ((size_t)(rb * 8) * 128 + head * 4) * 2048 + 4096;
  auto issue = [&](auto Sl, auto Pos) AI {
    constexpr int sl = Sl, pos = Pos, cnt = SC.count(sl), q0 = SC.first(sl) + (pos < 0 ? 0 : pos);
    constexpr int nq = pos < 0 ? cnt : pos >= cnt ? 0 : pos < 3 ? 1 : cnt - 3;
    sfor<nq>([&](auto Qi) AI {
      constexpr int k = SC.kind[q0 + Qi], i = SC.idx[q0 + Qi];
      if constexpr (k == OX && i < 64) gld16<(i & 7) * 1024 - 4096, true>(xf[i % XD], xn_v, xn_b + (size_t)(i >> 3) * 8192);
      if constexpr (k == OX && i >= 64) {
        constexpr int q = i - 64, v = q >> 2;
        gld16<0, true>(xf[q % XD2], (unsigned)(rbw * 262144 + kg * 16384 + ((q & 1) ^ kg) * 1024 + l * 16), o_b + (size_t)(((v & 3) * 4 + ((q >> 1) & 1)) * 8192 + (v >> 2) * 2048));
      }
      if constexpr (k == OG) {
        constexpr int v = i >> 2, p = i & 3;
        if constexpr (v < 8) {
          const char* sb = wq_b + (size_t)(v * 128 + (p >> 1) * 64) * 8192;
          glds<(p & 1) * 128>(ldsw + (v & 3) * 32768 + (p >> 1) * 16384 + (p & 1) * (1024 - 128), wq_v, sb);
        } else if constexpr (v < 16) {
          glds<p * 1024, true>(ldsk + (v & 3) * 32768, kv_v, kv_b + (size_t)(v - 8) * 32768);
        } else {
          const unsigned wo_v = (unsigned)((((wid >> 2) * 128 + (wid & 3) * 8 + ((l & 31) >> 2)) * DM + (l >> 5) * 32 + ((l & 3) ^ (2 * (wid & 1))) * 8) * 2);
          const char* sb = wo_b + (size_t)(((v & 3) * 4 + (p >> 1) * 2) * 128 + ((v - 16) >> 2) * 32) * (DM * 2) + (((p & 1) ^ kh ^ 1) << 7);
          glds<0>(ldsw + (v & 3) * 32768 + (p >> 1) * 16384 + (p & 1) * 1024, wo_v, sb);
        }
      }
      if constexpr (k == OF && i < 8)
        glds<0>(ldsf + (i & 3) * 8192, (unsigned)(((l >> 3) * FF + wid * 64 + ((l & 7) ^ ((i & 1) * 4 + ((l >> 3) & 2))) * 8) * 2),
                (const char*)(i < 4 ? wgate : wup) + ((size_t)(fsl * 32 + 8 * (i & 3)) * FF + fgr * 512) * 2);
      if constexpr (k == OF && i >= 8) {
        const int frho = l >> 2, fch = (l & 3) ^ (frho & 4 ? 2 : 0);
        glds<0>(ldsf + ((i - 8) % 3) * 8192, (unsigned)((frho * DM + wid * 128 + fch * 8) * 2), (const char*)wdown + (size_t)fj * 16 * DM * 2 + (i - 8) * 64);
      }
      if constexpr (k == OS && i == 0) {
        const int idx = SEQ - 1 - (int)(key & 2047u);
        gld16<0, true>(fxs, (unsigned)((((idx >> 5) * 32 + fsl) * 2 + ((idx >> 4) & 1)) * 1024 + (g * 16 + (idx & 15)) * 16), ws + WS_XN);
      }
      if constexpr (k == OS && i == 2) gld16<0, true>(fm[0], gu_v, ws + WS_GU);
      if constexpr (k == OM && i > 0) gld8<0, true>(fg, fo_v, ws + WS_FO + (i - 1) * 16384);
      if constexpr (k == OM && i == 0) gld8<0, true>(fg, (unsigned)((t & 255) * 8), wid < 4 ? ws + WS_G0 : ws + WS_FK + (head >> 3) * 2048);
      if constexpr (k == OW) {
        constexpr int os = i >> 1, qb = i & 1;
        PIN(o[os & 1][0][qb]); PIN(o[os & 1][1][qb]);
        const f32x4 a = o[os & 1][0][qb] * inv[qb], b = o[os & 1][1][qb] * inv[qb];
        gst16<i * 1024 - 4096>((u32x4){pk2(a[0], a[1]), pk2(a[2], a[3]), pk2(b[0], b[1]), pk2(b[2], b[3])}, (unsigned)(wid * 262144 + l * 16), od_b);
      }
      if constexpr (k == OE && i < 4) gld8<32 * i>(xe[i], (unsigned)((erow * DM + ecol) * 2), x);
      if constexpr (k == OE && i == 4) gld8<0, true>(st, (unsigned)(erow * 8), ws + WS_ST);
    });
  };
  f32x4 fa0, fa1, fq[4];
  s16x4 fb0, fb1, fxa;
  bf16x8 ft[2];
  auto endslot = [&](auto Sl) AI {
    constexpr int sl = Sl, u = sl >> 3, i = sl & 7;
    if constexpr (sl == 3) {
      if (wid == 6 || wid == 7) touch((unsigned)(((wid - 4) * 256 + within * 8 + (l >> 3)) * 8192 + xcd * 1024 + (l & 7) * 128), wq);
    }
    if constexpr (sl == 4) {
      if (wid < 4) {
        u32x4 kvo;
        if (fn < 128) {
          PIN(klo); PIN(khi);
          kvo = (u32x4){klo[0], klo[1], khi[0], khi[1]};
        } else {
#pragma unroll
          for (int j = 0; j < 8; ++j) PIN(e[j]);
          kvo = (u32x4){e[0] | (e[1] << 16), e[2] | (e[3] << 16), e[4] | (e[5] << 16), e[6] | (e[7] << 16)};
        }
        gst16<0>(kvo, (unsigned)((bid * 4 + wid) * 1024 + l * 16), ws + WS_KV);
      }
    }
    if constexpr (sl == 20) {
      if (wid < 4 && l == 0) gst8((u32x2){tlo, thi}, (unsigned)((bid * 4 + wid) * 8), ws + WS_FK);
    }
    if constexpr (sl == 43) {
      waitv<SC.younger(OM, 0, sl)>(fg);
      const unsigned lo = wid < 4 ? glo : tlo, hi = wid < 4 ? ghi : thi, mk = wid < 4 ? 0xf8000000u : 0xffffffffu;
      for (int spin = 0; spin < SPINS; ++spin) {
        if (__builtin_expect(__all(((fg[0] & mk) == lo) & (fg[1] == hi)), 1)) break;
        __builtin_amdgcn_s_sleep(1);
        gld8<0, true>(fg, (unsigned)((t & 255) * 8), wid < 4 ? ws + WS_G0 : ws + WS_FK + (head >> 3) * 2048);
        waitv<0>(fg);
      }
      const unsigned km = wave_max_u32(fg[0] & 0x07ffffffu);
      *(LAS unsigned*)(lds + HSC + wid * 4) = km;
    }
    if constexpr (sl == 47) {
      if (bid == 0 && t == 0) gst8((u32x2){tlo, thi}, 0u, ws + WS_EP);
      const u32x4 kk = *(LAS const u32x4*)(lds + HSC);
      key = __builtin_amdgcn_readfirstlane(umax(umax(kk[0], kk[1]), umax(kk[2], kk[3])));
    }
    if constexpr (sl == 58 || sl == 61 || sl == 64 || sl == 67) {
      if (wid == 4 + (sl - 58) / 3) touch((unsigned)(((wid & 3) * 64 + l) * 128), (const char*)wdown + (size_t)fj * 16 * DM * 2);
    }
    if constexpr (sl == 81 || sl == 85 || sl == 89 || sl == 93) {
      if (wid == (sl - 81) / 4)
        touch((unsigned)((kh * 2048 + (within >> 1) * 128 + wid * 32 + ((within & 1) * 2 + (xcd & 1)) * 8 + (l >> 3)) * (DM * 2) + ((xcd >> 1) & 1) * 1024 + (l & 7) * 128), wo);
    }
    if constexpr ((sl >= 68 && sl <= 70) || (sl >= 90 && sl <= 92)) {
      constexpr int r = sl >= 90, q = sl - (r ? 90 : 68);
      if constexpr (q == 0) waitv<SC.younger(OF, 4 * r + 3, sl)>(fxs);
      if constexpr (q >= 1) {
#pragma unroll
        for (int j = 0; j < 2; ++j) { fq[2 * q - 2 + j] = __builtin_amdgcn_mfma_f32_16x16x32_bf16(fxs, ft[j], (f32x4){}, 0, 0, 0); PIN(fq[2 * q - 2 + j]); }
      }
      if constexpr (q < 2) {
        const unsigned rp = ldsf + g * 8192 + (c >> 2) * 128 + ((((g & 1) << 1) | (c >> 3)) << 5) + (c & 3) * 8;
#pragma unroll
        for (int j = 0; j < 2; ++j) { LAS const char* a = (LAS const char*)(uintptr_t)(rp ^ ((2 * q + j) << 5)); ft[j] = cat8(trd(a), trd(a + 512)); }
      }
    }
    if constexpr (sl == 71 || sl == 93) {
      const float v = sel2(g & 2, sel2(g & 1, fq[3][0], fq[2][0]), sel2(g & 1, fq[1][0], fq[0][0]));
      gst8((u32x2){__float_as_uint(v), tlo}, (unsigned)(((fsl * 2 + (sl == 93)) * FF + fgr * 512 + t) * 8), ws + WS_GU);
    }
    if constexpr (sl == 109) {
      waitv<SC.younger(OS, 2, sl)>(fm[0]);
      for (int spin = 0; spin < SPINS; ++spin) {
        if (__builtin_expect(__all((fm[0][1] == tlo) & (fm[0][3] == tlo)), 1)) break;
        __builtin_amdgcn_s_sleep(1);
        gld16<0, true>(fm[0], gu_v, ws + WS_GU);
        waitv<0>(fm[0]);
      }
      *(LAS f32x2*)(lds + RED1 + (wid * 32 + 2 * c) * 4) = (f32x2){xg_sum(__uint_as_float(fm[0][0])), xg_sum(__uint_as_float(fm[0][2]))};
    }
    if constexpr (sl == 111) {
#pragma unroll
      for (int w = 0; w < 2; ++w) { fa0[w] = *(LAS const float*)(lds + RED1 + ((2 * g + w) * 32 + c) * 4); fa0[2 + w] = *(LAS const float*)(lds + RED1 + ((2 * g + w) * 32 + 16 + c) * 4); }
    }
    if constexpr (sl == 112) { fa0[0] = xg_sum(fa0[0] + fa0[1]); fa0[2] = xg_sum(fa0[2] + fa0[3]); PIN(fa0); }
    if constexpr (sl == 113) {
      const float hh = rbf(silu_f(rbf(fa0[0]))) * rbf(fa0[2]);
      *(LAS u16*)(lds + HSC + 32 + wid * 32 + c * 2) = (u16)pk2(hh, hh);
    }
    if constexpr (sl == 114) fxa = *(LAS const s16x4*)(lds + HSC + 32 + wid * 32 + g * 8);
    constexpr int pr = fpiece(sl), pm = fpiece(sl - 1), ps = fpiece(sl - 2);
    if constexpr (ps >= 0) gst4<ps * 128>(sel2(g & 1, fa1[0], fa0[0]), (unsigned)((wid * 128 + (l & 31)) * 4), ws + WS_YP + (size_t)yrow * 4096);
    if constexpr (pm >= 0) { fa0 = MFMA16(fxa, fb0, (f32x4){}); fa1 = MFMA16(fxa, fb1, (f32x4){}); PIN(fa0); PIN(fa1); }
    if constexpr (pr >= 0) {
      waitv<SC.younger(OF, 8 + pr, sl)>();
      const unsigned fp = ldsf + (4 * g + (c >> 2)) * 64 + (g & 1) * 32 + (c & 3) * 8;
      fb0 = trd((LAS const char*)(uintptr_t)fp + (pr % 3) * 8192); fb1 = trd((LAS const char*)(uintptr_t)(fp ^ 32) + (pr % 3) * 8192);
    }
    if constexpr (sl == 111 || sl == 119 || sl == 127) {
      waitv<SC.younger(OW, (sl - 111) / 4 + 1, sl)>();
      if (l == 0) gst8((u32x2){tlo, thi}, (unsigned)(((rb * 8 + wid) * 32 + head) * 8), ws + WS_FO + ((sl - 111) / 8) * 16384);
    }
    if constexpr (sl == 123) {
      waitv<SC.younger(OM, 1, sl)>(fg);
      if (__builtin_expect(!__all((fg[0] == tlo) & (fg[1] == thi)), 0)) poll8(fo_v, ws + WS_FO, tlo, thi, 0xffffffffu);
    }
    SBAR();
    if constexpr (i == 6) {
      if constexpr (u == 5 || u == 13) waitbar_l<SC.younger(OG, 4 * u + 7, sl)>();
      else waitbar<SC.younger(OG, 4 * u + 7, sl)>();
      if constexpr (u == 0 || u == 1 || u == 5 || u == 7 || u == 8 || u == 10 || u == 12 || u == 14)
        STAMP(u == 0 ? 4 : u == 1 ? 5 : u == 5 ? 6 : u == 7 ? 7 : u == 8 ? 8 : u == 10 ? 9 : u == 12 ? 10 : 11);
    }
    if constexpr (PRIO && !(i & 1)) __builtin_amdgcn_s_setprio(i == 6 ? 3 : 2 - i / 2);
  };

  issue(IC(-2), IC(-1));
  if (wid < 4) {
    if (wid == 1) touch((unsigned)((256 + within * 8 + (l >> 3)) * 8192 + xcd * 1024 + (l & 7) * 128), wq);
    if (fn < 128) {
      const unsigned ko = (unsigned)(((kvh * SK + (SK - WIN) + (fn >> 2) * 16 + c) * DH + (fn & 3) * 32 + 4 * g) * 2);
      gld8<0>(klo, ko, kc); gld8<32>(khi, ko, kc);
      waitv<6>();
    } else {
      const int d = 32 * ((fn - 128) >> 5) + 8 * (c >> 2) + 4 * (fn & 1) + (c & 3);
      const unsigned vo = (unsigned)(((kvh * SK + (SK - WIN) + ((fn >> 1) & 15) * 32 + 4 * g + 8) * DH + d) * 2);
      sfor<8>([&](auto J) AI { constexpr int j = J; gld2<(16 * (j >> 2) + (j & 3) - 8) * DH * 2>(e[j], vo, vc); });
      waitv<12>();
    }
  } else waitv<4>();
  if (wid == 5) poll8((unsigned)((rb * 32 + (l & 31)) * 8), ws + WS_G0, glo, ghi, 0xf8000000u);
  waitbar<63>();
  issue(IC(-1), IC(-1));
  STAMP(3);

  LBase trb[2], frb;
  trb[0].init(lds0 + g * 2048 + (c >> 2) * 64 + (g & 1) * 32 + (c & 3) * 8);
  trb[1].init(lds0 + g * 2048 + (c >> 2) * 64 + ((g & 1) ^ 1) * 32 + (c & 3) * 8);
  frb.init(lds0 + l * 16);
  f32x4 acc[8][2];
  bf16x8 wf[2][4];
  auto WRD1 = [&](auto Si, auto Base, auto Dd) AI {
    constexpr int s = Si, d = Dd, o = Base + (d >> 1) * 512;
    wf[s & 1][d] = cat8(trd(trb[d & 1].template at<o>()), trd(trb[d & 1].template at<o + 256>()));
  };
  auto WRD = [&](auto Si, auto Base) AI { sfor<4>([&](auto Dd) AI { WRD1(Si, Base, Dd); }); };
#define WA(s) IC((((s) >> 3) & 3) * 32768 + (((s) >> 1) & 3) * 8192 + ((s) & 1) * 1024)
#define WB(s) IC((((s) >> 2) & 3) * 32768 + (((s) >> 1) & 1) * 8192 + ((s) & 1) * 1024)
  bf16x8 fr[4];
  auto FRD = [&](auto Ni) AI { constexpr int n = Ni; if constexpr (n < 256) fr[n & 3] = *(LAS const bf16x8*)(frb.at<((n >> 5) & 3) * 32768 + (n & 31) * 1024>()); };

  WRD(IC(0), WA(0));
  SBAR();
  bf16x8 qf[4][2];
  auto QPACK = [&](auto Ds) AI {
    constexpr int ds = Ds;
#pragma unroll
    for (int qb = 0; qb < 2; ++qb) {
      const f32x4 a = acc[2 * ds][qb] * QSCALE, b = acc[2 * ds + 1][qb] * QSCALE;
      qf[ds][qb] = __builtin_bit_cast(bf16x8, (u32x4){pk2(a[0], a[1]), pk2(a[2], a[3]), pk2(b[0], b[1]), pk2(b[2], b[3])});
      PIN(qf[ds][qb]);
    }
  };
  sfor<64>([&](auto Si) AI {
    constexpr int s = Si, x0 = (s & ~1) % XD, x1 = (s | 1) % XD;
    if constexpr (!(s & 1)) waitv<SC.younger(OX, s + 1, s - 1)>(xf[x0], xf[x1]);
    sfor<4>([&](auto Dd) AI {
      constexpr int d = Dd, dt = (s & 1) * 4 + d;
      if constexpr (s + 1 < 64) WRD1(IC(s + 1), WA(s + 1), Dd);
      else if constexpr (d < 3) FRD(Dd);
      SBAR();
      if constexpr (s < 2) { MFZ(acc[dt][0], wf[s & 1][d], xf[x0]); MFZ(acc[dt][1], wf[s & 1][d], xf[x1]); }
      else { MF(acc[dt][0], wf[s & 1][d], xf[x0]); MF(acc[dt][1], wf[s & 1][d], xf[x1]); }
      issue(Si, Dd);
      if constexpr (s == 63 && (d & 1)) { QPACK(IC(d >> 1)); SBAR(); }
    });
    SBAR();
    endslot(Si);
  });

  f32x4 sc[2][2][2], negm[2];
  u32x4 pw[16][2];
  f32x4 lsum[2];
  bf16x8 ones = {0x3f80, 0x3f80, 0x3f80, 0x3f80, 0x3f80, 0x3f80, 0x3f80, 0x3f80};
  PIN(ones);
#define MX3(a, b, c) __builtin_fmaxf(__builtin_fmaxf((a), (b)), (c))
#define EXP2(x) __builtin_amdgcn_exp2f(x)
  auto SM = [&](auto Hi, auto Pi) AI {
    constexpr int H = Hi, p = Pi, kt = p >> 2, qb = (p >> 1) & 1, r = 2 * (p & 1);
    const float e0 = EXP2(sc[H & 1][kt][qb][r]), e1 = EXP2(sc[H & 1][kt][qb][r + 1]);
    unsigned w = pk2(e0, e1);
    PIN(w);
    pw[H][qb][kt * 2 + (p & 1)] = w;
  };
  sfor<128>([&](auto Ni) AI {
    constexpr int n = Ni, H = n >> 3, i = n & 7, kt = i >> 2, ds = i & 3, sn = H & 1;
    FRD(IC(n + 3));
    if constexpr (n < 2) QPACK(IC(2 + n));
    sfor<2>([&](auto Qb) AI {
      constexpr int qb = Qb;
      if constexpr (ds == 0 && H == 0) MFZ(sc[sn][kt][qb], fr[n & 3], qf[0][qb]);
      else if constexpr (ds == 0) MFC(sc[sn][kt][qb], fr[n & 3], qf[0][qb], negm[qb]);
      else MF(sc[sn][kt][qb], fr[n & 3], qf[ds][qb]);
    });
    if constexpr (n == 5 || n == 6) {
      constexpr int qb = n - 5;
      NOPS(3); PIN(sc[0][0][qb]);
      const float m = -xg_max(__builtin_fmaxf(MX3(sc[0][0][qb][0], sc[0][0][qb][1], sc[0][0][qb][2]), sc[0][0][qb][3]));
      negm[qb] = (f32x4){m, m, m, m};
      sc[0][0][qb] += negm[qb];
      PIN(negm[qb]); PIN(sc[0][0][qb]);
    }
    if constexpr (n == 9 || n == 10) { NOPS(3); PIN(sc[0][1][n - 9]); sc[0][1][n - 9] += negm[n - 9]; PIN(sc[0][1][n - 9]); }
    issue(IC(64 + (n >> 2)), IC(n & 3));
    if constexpr (H == 2 && (i == 1 || i == 5)) MFZ(lsum[i >> 2], ones, pw[0][i >> 2]);
    if constexpr (H > 2 && (i == 1 || i == 5)) MF(lsum[i >> 2], ones, pw[H - 2][i >> 2]);
    if constexpr (H >= 1) SM(IC(H - 1), IC(i));
    if constexpr (n == 125) { PIN(negm[0]); PIN(negm[1]); }
    SBAR();
    if constexpr ((n & 3) == 3) endslot(IC(64 + (n >> 2)));
  });
  constexpr int CHK = 16;
  sfor<128>([&](auto Mi) AI {
    constexpr int m = Mi, n = 128 + m, os = m >> 5, H = (m >> 1) & 15, par = m & 1;
    FRD(IC(n + 3));
    if constexpr (H == 0) { MFZ(o[os & 1][par][0], fr[n & 3], pw[H][0]); MFZ(o[os & 1][par][1], fr[n & 3], pw[H][1]); }
    else { MF(o[os & 1][par][0], fr[n & 3], pw[H][0]); MF(o[os & 1][par][1], fr[n & 3], pw[H][1]); }
    if constexpr (m == 1 || m == 2) MF(lsum[m - 1], ones, pw[14][m - 1]);
    if constexpr (m < 8) SM(IC(15), Mi);
    if constexpr (m == 9 || m == 10) MF(lsum[m - 9], ones, pw[15][m - 9]);
    if constexpr (m == CHK + 1 || m == CHK + 2) { inv[m - CHK - 1] = __builtin_amdgcn_rcpf(lsum[m - CHK - 1][0]); PIN(inv[m - CHK - 1]); }
    issue(IC(64 + (n >> 2)), IC(n & 3));
    if constexpr (m == 123) {
      trb[0].init(lds0 + kg * 16384 + g * 2048 + (c >> 2) * 64 + (g & 1) * 32 + (c & 3) * 8);
      trb[1].init(lds0 + kg * 16384 + g * 2048 + (c >> 2) * 64 + ((g & 1) ^ 1) * 32 + (c & 3) * 8);
    }
    if constexpr (m >= 124) WRD1(IC(0), WB(0), IC(m - 124));
    SBAR();
    if constexpr ((n & 3) == 3) endslot(IC(64 + (n >> 2)));
    if constexpr (m == CHK - 1) {
      PIN(lsum[0]); PIN(lsum[1]);
      if (__builtin_expect(__any(!(lsum[0][0] + lsum[1][0] <= LBOUND)), 0)) {
        NOPS(7); SBAR();
        auto tile = [&](unsigned off, f32x4& s0, f32x4& s1) AI {
          bf16x8 kf[4];
          SBAR(); NOPS(7);
          sfor<4>([&](auto Ds) AI { constexpr int ds = Ds; gld16<ds * 1024, true>(kf[ds], off, kv_b); });
          waitv<0>(kf[0], kf[1]);
          PIN(kf[2]); PIN(kf[3]);
#pragma unroll
          for (int ds = 0; ds < 4; ++ds) {
            s0 = __builtin_amdgcn_mfma_f32_16x16x32_bf16(kf[ds], qf[ds][0], s0, 0, 0, 0);
            s1 = __builtin_amdgcn_mfma_f32_16x16x32_bf16(kf[ds], qf[ds][1], s1, 0, 0, 0);
          }
        };
        float m0 = -__builtin_inff(), m1 = m0;
#pragma unroll 1
        for (int kt = 0; kt < 32; ++kt) {
          f32x4 s0 = {}, s1 = {};
          tile((unsigned)(kt * 4096 + l * 16), s0, s1);
          m0 = MX3(MX3(m0, s0[0], s0[1]), s0[2], s0[3]); m1 = MX3(MX3(m1, s1[0], s1[1]), s1[2], s1[3]);
        }
        m0 = -xg_max(m0); m1 = -xg_max(m1);
        lsum[0] = (f32x4){}; lsum[1] = (f32x4){};
        sfor<16>([&](auto Hi) AI {
          constexpr int H = Hi;
#pragma unroll
          for (int kt = 0; kt < 2; ++kt) {
            sc[H & 1][kt][0] = (f32x4){m0, m0, m0, m0}; sc[H & 1][kt][1] = (f32x4){m1, m1, m1, m1};
            tile((unsigned)((2 * H + kt) * 4096 + l * 16), sc[H & 1][kt][0], sc[H & 1][kt][1]);
          }
          sfor<8>([&](auto Pi) AI { SM(Hi, Pi); });
          PIN(pw[H][0]); PIN(pw[H][1]);
          lsum[0] = __builtin_amdgcn_mfma_f32_16x16x32_bf16(ones, __builtin_bit_cast(bf16x8, pw[H][0]), lsum[0], 0, 0, 0);
          lsum[1] = __builtin_amdgcn_mfma_f32_16x16x32_bf16(ones, __builtin_bit_cast(bf16x8, pw[H][1]), lsum[1], 0, 0, 0);
        });
#pragma unroll
        for (int par = 0; par < 2; ++par) { o[0][par][0] = (f32x4){}; o[0][par][1] = (f32x4){}; }
        sfor<CHK / 4>([&](auto Qi) AI {
          constexpr int q = Qi;
          bf16x8 vf[4];
          SBAR(); NOPS(7);
          sfor<4>([&](auto J) AI { constexpr int j = J; gld16<j * 1024, true>(vf[j], (unsigned)((128 + 4 * q) * 1024 + l * 16), kv_b); });
          waitv<0>(vf[0], vf[1]);
          PIN(vf[2]); PIN(vf[3]);
#pragma unroll
          for (int j = 0; j < 4; ++j)
#pragma unroll
            for (int qb = 0; qb < 2; ++qb)
              o[0][j & 1][qb] = __builtin_amdgcn_mfma_f32_16x16x32_bf16(vf[j], __builtin_bit_cast(bf16x8, pw[2 * q + (j >> 1)][qb]), o[0][j & 1][qb], 0, 0, 0);
        });
#pragma unroll
        for (int par = 0; par < 2; ++par) { PIN(o[0][par][0]); PIN(o[0][par][1]); }
        PIN(lsum[0]); PIN(lsum[1]);
        SBAR(); NOPS(7);
      }
      SBAR();
    }
  });
  STAMP(12); STAMP(13); STAMP(14); STAMP(15);

  const int idx = SEQ - 1 - (int)(key & 2047u);
  const bool owner = (idx >> 7) == mb, ownw = (idx >> 4) == mb * 8 + rbw * 2 + kg;
  f32x4 yp[7], ya[4], oth[4];
  unsigned gate[8];
  auto item = [](int s) { return s < 48 ? s : s < 56 ? 48 + 2 * (s - 48) : 49 + 2 * (s - 56); };
  u32x2 pf;
  sfor<64>([&](auto Si) AI {
    constexpr int s = Si, u = s >> 2, it = item(s), x0 = (it & ~1) % XD2, x1 = (it | 1) % XD2;
    if constexpr (!(it & 1)) waitv<SC.younger(OX, 64 + it + 1, B0 + s - 1)>(xf[x0], xf[x1]);
    sfor<4>([&](auto Dd) AI {
      constexpr int d = Dd, dt = (it & 1) * 4 + d;
      if constexpr (s + 1 < 64) WRD1(IC(s + 1), WB(item(s + 1)), Dd);
      SBAR();
      if constexpr (s < 2) { MFZ(acc[dt][0], wf[s & 1][d], xf[x0]); MFZ(acc[dt][1], wf[s & 1][d], xf[x1]); }
      else { MF(acc[dt][0], wf[s & 1][d], xf[x0]); MF(acc[dt][1], wf[s & 1][d], xf[x1]); }
      issue(IC(B0 + s), Dd);
      if constexpr (s == 56) *(LAS f32x4*)(lds + (wid ^ 4) * 4096 + l * 16 + (d & 1) * 2048 + (d >> 1) * 32768) = acc[d][1];
      if constexpr (s == 57 && d == 0) {
#pragma unroll
        for (int e = 0; e < 4; ++e) oth[e] = *(LAS const f32x4*)(lds + wid * 4096 + l * 16 + (e & 1) * 2048 + (e >> 1) * 32768);
      }
      if constexpr (s == 57 && d >= 2) {
        gst16<(2 * d - 4) * 1024>(acc[2 * d - 4][0] + oth[2 * d - 4], (unsigned)(wid * 4096 + l * 16), ws + WS_XC + (size_t)(bid ^ 4) * 32768);
        gst16<(2 * d - 3) * 1024>(acc[2 * d - 3][0] + oth[2 * d - 3], (unsigned)(wid * 4096 + l * 16), ws + WS_XC + (size_t)(bid ^ 4) * 32768);
      }
      if constexpr (s == 58 || s == 59) {
        constexpr int j = (s - 58) * 4 + d;
        if constexpr (j == 0) waitv<4>();
        PIN(xe[j >> 1]); PIN(st);
        const unsigned xw = xe[j >> 1][j & 1];
        const unsigned dd = pk2(bflo(xw) - st[0], bfhi(xw) - st[0]);
        gate[j] = pk2(bflo(dd) * st[1], bfhi(dd) * st[1]);
        PIN(gate[j]);
      }
      if constexpr (s >= 60 && !(d & 1)) {
        constexpr int j = (s - 60) * 2 + (d >> 1);
        gate[j] = pk2(silu_f(bflo(gate[j])), silu_f(bfhi(gate[j])));
        PIN(gate[j]);
      }
    });
    SBAR();
    if constexpr (s == 7) {
      waitv<SC.younger(OW, 7, B0 + s)>();
      if (l == 0) {
        gst8((u32x2){tlo, thi}, (unsigned)(((rb * 8 + wid) * 32 + head) * 8), ws + WS_FO + 3 * 16384);
        gst8((u32x2){tlo, thi}, (unsigned)((wid * 256 + yrow) * 8), ws + WS_FY);
      }
    }
    if constexpr (s == 4 || s == 17 || s == 33) {
      constexpr int j = s == 4 ? 1 : s == 17 ? 2 : 3;
      waitv<SC.younger(OM, j + 1, B0 + s)>(fg);
      if (__builtin_expect(!__all((fg[0] == tlo) & (fg[1] == thi)), 0)) poll8(fo_v, ws + WS_FO + j * 16384, tlo, thi, 0xffffffffu);
    }
    if constexpr (s == 60) {
      waitv<0>();
      if (l == 0) gst8((u32x2){tlo, thi}, (unsigned)(((bid ^ 4) * 8 + wid) * 64), ws + WS_FX);
    }
    if constexpr (s == 61) gld8<0, true>(pf, (unsigned)((bid * 8 + wid) * 64), ws + WS_FX);
    if constexpr (s == 19) {
      if (owner) { gld16<0, true>(fm[0], (unsigned)(nb * 2048 + (l < 56 ? l : 55) * 32), ws + WS_FY); gld16<16, true>(fm[1], (unsigned)(nb * 2048 + (l < 56 ? l : 55) * 32), ws + WS_FY); }
    }
    if constexpr (s == 34) {
      if (owner) {
        PIN(fm[0]); PIN(fm[1]);
        for (int spin = 0; spin < SPINS; ++spin) {
          const bool ok = (fm[0][0] == tlo) & (fm[0][1] == thi) & (fm[0][2] == tlo) & (fm[0][3] == thi) & (fm[1][0] == tlo) & (fm[1][1] == thi) & (fm[1][2] == tlo) & (fm[1][3] == thi);
          if (__builtin_expect(__all(ok), 1)) break;
          __builtin_amdgcn_s_sleep(1);
          gld16<0, true>(fm[0], (unsigned)(nb * 2048 + (l < 56 ? l : 55) * 32), ws + WS_FY); gld16<16, true>(fm[1], (unsigned)(nb * 2048 + (l < 56 ? l : 55) * 32), ws + WS_FY);
          waitv<0>(fm[0], fm[1]);
        }
        const unsigned yv = (unsigned)(((t >> 4) * 1024 + nb * 128 + kh * 64 + (t & 15) * 4) * 4);
        sfor<7>([&](auto I) AI { constexpr int q = I; gld16<0, true>(yp[q], yv, ws + WS_YP + (size_t)q * 32 * 4096); });
      }
    }
    if constexpr (s == 50) {
      if (owner) {
        f32x4 a = {};
#pragma unroll
        for (int q = 0; q < 7; ++q) { PIN(yp[q]); a += yp[q]; }
#pragma unroll
        for (int j = 0; j < 4; ++j) a[j] = xg_sum(a[j]);
        *(LAS f32x4*)(lds + YS + (wid * 64 + c * 4) * 4) = a;
      }
    }
    if constexpr (s == 53) {
      if (ownw) {
        float a = 0.f;
#pragma unroll
        for (int p = 0; p < 8; ++p) a += *(LAS const float*)(lds + YS + (p * 64 + l) * 4);
        const unsigned ord = key >> 11, bits = (ord & 0x8000u) ? (ord & 0x7fffu) : (~ord & 0xffffu);
        *(LAS float*)(lds + RED1 + l * 4) = rbf(rbf(a) * rbf(sigmoid_f(bflo(bits))));
#pragma unroll
        for (int q = 0; q < 4; ++q) ya[q] = *(LAS const f32x4*)(lds + RED1 + (q * 16 + 4 * g) * 4);
      }
    }
    if constexpr (s == 59) {
      if (ownw) {
#pragma unroll
        for (int j = 0; j < 8; ++j) {
          const unsigned w = pk2(bflo(gate[j]) + ya[j >> 1][2 * (j & 1)], bfhi(gate[j]) + ya[j >> 1][2 * (j & 1) + 1]);
          gate[j] = erow == idx ? w : gate[j];
          PIN(gate[j]);
        }
      }
    }
    SBAR();
    if constexpr (((s & 3) == 2 && u < 11) || s == 44) {
      waitbar<SC.younger(OG, 64 + 4 * u + 7, B0 + s)>();
      if constexpr (u % 4 == 1) STAMP(16 + u / 4);
    }
    if constexpr (s == 48) waitbar<SC.younger(OG, 64 + 55, B0 + s)>();
    if constexpr (s == 50) waitbar<SC.younger(OG, 64 + 59, B0 + s)>();
    if constexpr (s == 52) { waitbar_l<SC.younger(OG, 64 + 63, B0 + s)>(); STAMP(19); }
    if constexpr (s == 56) waitbar_l<63>();
    if constexpr (PRIO) __builtin_amdgcn_s_setprio(3 - ((s + 2) & 3));
  });
  STAMP(20);

  f32x4 kep[4];
  {
    waitv<0>(pf);
    const bool rdy = __all((pf[0] == tlo) & (pf[1] == thi));
    if (rdy) sfor<4>([&](auto Dd) AI { constexpr int d = Dd; gld16<d * 1024, true>(oth[d], (unsigned)(wid * 4096 + l * 16), ws + WS_XC + (size_t)bid * 32768); });
    NOPS(3);
#pragma unroll
    for (int d = 0; d < 4; ++d) { PIN(acc[4 + d][1]); *(LAS f32x4*)(lds + 65536 + (wid ^ 4) * 4096 + l * 16 + (d & 1) * 2048 + (d >> 1) * 32768) = acc[4 + d][1]; }
    waitbar_l<63>();
    STAMP(21);
#pragma unroll
    for (int d = 0; d < 4; ++d) kep[d] = acc[4 + d][0] + *(LAS const f32x4*)(lds + 65536 + wid * 4096 + l * 16 + (d & 1) * 2048 + (d >> 1) * 32768);
    STAMP(22);
    if (!rdy) {
      poll8((unsigned)((bid * 8 + wid) * 64), ws + WS_FX, tlo, thi, 0xffffffffu);
      sfor<4>([&](auto Dd) AI { constexpr int d = Dd; gld16<d * 1024, true>(oth[d], (unsigned)(wid * 4096 + l * 16), ws + WS_XC + (size_t)bid * 32768); });
    }
    STAMP(23);
    waitv<0>(oth[0], oth[1]);
    PIN(oth[2]); PIN(oth[3]);
    STAMP(24);
  }
  u16* op = out + (size_t)erow * DM + ecol;
#pragma unroll
  for (int d = 0; d < 4; ++d) {
    const f32x4 a = kep[d] + oth[d];
    u32x2 res;
#pragma unroll
    for (int j = 0; j < 2; ++j) {
      const unsigned sp = pk2(a[2 * j], a[2 * j + 1]), gp = gate[2 * d + j], xw = xe[d][j];
      const unsigned pr = pk2(bflo(sp) * bflo(gp), bfhi(sp) * bfhi(gp));
      res[j] = pk2(bflo(xw) + bflo(pr), bfhi(xw) + bfhi(pr));
    }
    *(u32x2*)(op + d * 16) = res;
  }
  STAMP(25);
  STAMP_OUT(0, 26);
}

extern "C" void kernel_launch(void* const* d_in, const int* in_sizes, int n_in, void* d_out, int out_size, void* d_ws, size_t ws_size,
                              hipStream_t stream) {
  k_fused<<<256, 512, 0, stream>>>((const u16*)d_in[0], (const u16*)d_in[1], (const u16*)d_in[2], (const u16*)d_in[3], (const u16*)d_in[4],
                                   (const u16*)d_in[6], (const u16*)d_in[7], (const u16*)d_in[8], (const u16*)d_in[9], (char*)d_ws, (u16*)d_out);
}
```

```cpp
#include <hip/hip_runtime.h>
#include <stdint.h>
#include <type_traits>

typedef unsigned short u16;
typedef short bf16x8 __attribute__((ext_vector_type(8)));
typedef short s16x4 __attribute__((ext_vector_type(4)));
typedef float f32x4 __attribute__((ext_vector_type(4)));
typedef float f32x2 __attribute__((ext_vector_type(2)));
typedef unsigned u32x4 __attribute__((ext_vector_type(4)));
typedef unsigned u32x2 __attribute__((ext_vector_type(2)));
typedef __bf16 bf2_t __attribute__((ext_vector_type(2)));
#define LAS __attribute__((address_space(3)))
#define DI __device__ __forceinline__
#define AI __attribute__((always_inline))
#define SBAR() __builtin_amdgcn_sched_barrier(0)
template <class T> __device__ __forceinline__ void PIN(T& x) { asm volatile("" : "+v"(x)); }
template <class B> DI void MF(f32x4& acc, const bf16x8& a, const B& b) { asm volatile("v_mfma_f32_16x16x32_bf16 %0, %1, %2, %0" : "+v"(acc) : "v"(a), "v"(b)); }
template <class B> DI void MFC(f32x4& d, const bf16x8& a, const B& b, const f32x4& c) { asm volatile("v_mfma_f32_16x16x32_bf16 %0, %1, %2, %3" : "=&v"(d) : "v"(a), "v"(b), "v"(c)); }
template <class B> DI void MFZ(f32x4& d, const bf16x8& a, const B& b) { asm volatile("v_mfma_f32_16x16x32_bf16 %0, %1, %2, 0" : "=&v"(d) : "v"(a), "v"(b)); }
#define NOPS(n) asm volatile("s_nop " #n)
#define MFMA16(a, b, c) __builtin_amdgcn_mfma_f32_16x16x16bf16_1k((a), (b), (c), 0, 0, 0)
#define IC(n) std::integral_constant<int, (n)>{}

constexpr int SEQ = 2048, DM = 1024, DH = 128, SK = 2048, WIN = 512, FF = 3584;
constexpr size_t WS_XN = 0;
constexpr size_t WS_KV = 4u << 20;
constexpr size_t WS_ST = 5u << 20;
constexpr size_t WS_EP = WS_ST + 16384;
constexpr size_t WS_G0 = WS_ST + 32768;
constexpr size_t WS_FK = WS_ST + 49152;
constexpr size_t WS_YP = WS_ST + 65536;
constexpr size_t WS_FO = 7u << 20;
constexpr size_t WS_FY = WS_FO + 65536;
constexpr size_t WS_FX = WS_FO + 131072;
constexpr size_t WS_O = 8u << 20;
constexpr size_t WS_GU = 32u << 20;
constexpr size_t WS_XC = 24u << 20;

template <int I, int N, class F> DI void sfor_impl(F&& f) {
  if constexpr (I < N) { f(std::integral_constant<int, I>{}); sfor_impl<I + 1, N>(f); }
}
template <int N, class F> DI void sfor(F&& f) { sfor_impl<0, N>(f); }

DI unsigned pk2(float a, float b) { f32x2 v = {a, b}; return __builtin_bit_cast(unsigned, __builtin_convertvector(v, bf2_t)); }
DI float rbf(float a) { return (float)(__bf16)a; }
DI float bflo(unsigned u) { return __uint_as_float(u << 16); }
DI float bfhi(unsigned u) { return __uint_as_float(u & 0xffff0000u); }
DI float sigmoid_f(float v) { return __builtin_amdgcn_rcpf(1.f + __builtin_amdgcn_exp2f(v * -1.4426950408889634f)); }
DI float sel2(bool c, float a, float b) { PIN(a); PIN(b); return c ? a : b; }
DI float silu_f(float v) { return v * sigmoid_f(v); }
template <int CTRL> DI float dppf(float x) { return __builtin_bit_cast(float, __builtin_amdgcn_mov_dpp(__builtin_bit_cast(int, x), CTRL, 0xf, 0xf, true)); }
template <int CTRL> DI unsigned dppu(unsigned x) { return (unsigned)__builtin_amdgcn_mov_dpp((int)x, CTRL, 0xf, 0xf, true); }
constexpr int XOR1 = 0xB1, XOR2 = 0x4E, XOR7 = 0x141, XOR8 = 0x128;
DI float xg_sum(float x) {
  auto s = __builtin_amdgcn_permlane16_swap(__float_as_uint(x), __float_as_uint(x), false, false);
  x = __uint_as_float(s[0]) + __uint_as_float(s[1]);
  auto t = __builtin_amdgcn_permlane32_swap(__float_as_uint(x), __float_as_uint(x), false, false);
  return __uint_as_float(t[0]) + __uint_as_float(t[1]);
}
DI float xg_max(float x) {
  auto s = __builtin_amdgcn_permlane16_swap(__float_as_uint(x), __float_as_uint(x), false, false);
  x = __builtin_fmaxf(__uint_as_float(s[0]), __uint_as_float(s[1]));
  auto t = __builtin_amdgcn_permlane32_swap(__float_as_uint(x), __float_as_uint(x), false, false);
  return __builtin_fmaxf(__uint_as_float(t[0]), __uint_as_float(t[1]));
}
DI float wave_sum(float v) { v += dppf<XOR1>(v); v += dppf<XOR2>(v); v += dppf<XOR7>(v); v += dppf<XOR8>(v); return xg_sum(v); }
DI unsigned umax(unsigned a, unsigned b) { return a > b ? a : b; }
DI unsigned wave_max_u32(unsigned v) {
  v = umax(v, dppu<XOR1>(v)); v = umax(v, dppu<XOR2>(v)); v = umax(v, dppu<XOR7>(v)); v = umax(v, dppu<XOR8>(v));
  auto s = __builtin_amdgcn_permlane16_swap(v, v, false, false); v = umax(s[0], s[1]);
  auto t = __builtin_amdgcn_permlane32_swap(v, v, false, false); v = umax(t[0], t[1]);
  return __builtin_amdgcn_readfirstlane(v);
}

template <int OFF, bool DEV = false> DI void glds(unsigned ldsaddr, unsigned voff, const void* sbase) {
  if constexpr (DEV) asm volatile("s_mov_b32 m0, %0\n\ts_nop 0\n\tglobal_load_lds_dwordx4 %1, %2 offset:%3 sc1" ::"s"(ldsaddr), "v"(voff), "s"(sbase), "i"(OFF) : "memory");
  else asm volatile("s_mov_b32 m0, %0\n\ts_nop 0\n\tglobal_load_lds_dwordx4 %1, %2 offset:%3" ::"s"(ldsaddr), "v"(voff), "s"(sbase), "i"(OFF) : "memory");
}
DI void touch(unsigned voff, const void* sbase) {
  asm volatile("global_load_dword v255, %0, %1" ::"v"(voff), "s"(sbase) : "memory", "v255");
}
template <int OFF, bool DEV = false, class T> DI void gld16(T& dst, unsigned voff, const void* sbase) {
  if constexpr (DEV) asm volatile("global_load_dwordx4 %0, %1, %2 offset:%3 sc1" : "=v"(dst) : "v"(voff), "s"(sbase), "i"(OFF) : "memory");
  else asm volatile("global_load_dwordx4 %0, %1, %2 offset:%3" : "=v"(dst) : "v"(voff), "s"(sbase), "i"(OFF) : "memory");
}
template <int OFF, bool DEV = false, class T> DI void gld8(T& dst, unsigned voff, const void* sbase) {
  if constexpr (DEV) asm volatile("global_load_dwordx2 %0, %1, %2 offset:%3 sc1" : "=v"(dst) : "v"(voff), "s"(sbase), "i"(OFF) : "memory");
  else asm volatile("global_load_dwordx2 %0, %1, %2 offset:%3" : "=v"(dst) : "v"(voff), "s"(sbase), "i"(OFF) : "memory");
}
template <int OFF> DI void gld2(unsigned& dst, unsigned voff, const void* sbase) {
  asm volatile("global_load_ushort %0, %1, %2 offset:%3" : "=v"(dst) : "v"(voff), "s"(sbase), "i"(OFF) : "memory");
}
template <int OFF, class T> DI void gst16(const T& v, unsigned voff, const void* sbase) {
  asm volatile("global_store_dwordx4 %0, %1, %2 offset:%3 sc1\n\ts_nop 1" ::"v"(voff), "v"(v), "s"(sbase), "i"(OFF) : "memory");
}
template <class T> DI void gst8(const T& v, unsigned voff, const void* sbase) {
  asm volatile("global_store_dwordx2 %0, %1, %2 sc1" ::"v"(voff), "v"(v), "s"(sbase) : "memory");
}
template <int OFF> DI void gst4(float v, unsigned voff, const void* sbase) {
  asm volatile("global_store_dword %0, %1, %2 offset:%3 sc1" ::"v"(voff), "v"(v), "s"(sbase), "i"(OFF) : "memory");
}
constexpr int SPINS = 1 << 16;
DI void poll8(unsigned voff, const void* sbase, unsigned lo, unsigned hi, unsigned lomask) {
  for (int spin = 0; spin < SPINS; ++spin) {
    u32x2 v;
    asm volatile("global_load_dwordx2 %0, %1, %2 sc1\n\ts_waitcnt vmcnt(0)" : "=&v"(v) : "v"(voff), "s"(sbase) : "memory");
    if (__all(((v[0] & lomask) == lo) & (v[1] == hi))) break;
    __builtin_amdgcn_s_sleep(1);
  }
}
template <int N> DI void waitv() { static_assert(N >= 0, "count"); asm volatile("s_waitcnt vmcnt(%0)" ::"n"(N > 63 ? 63 : N) : "memory"); }
template <int N, class T> DI void waitv(T& x) { static_assert(N >= 0, "count"); asm volatile("s_waitcnt vmcnt(%1)" : "+v"(x) : "n"(N > 63 ? 63 : N) : "memory"); }
template <int N, class T> DI void waitv(T& x, T& y) { static_assert(N >= 0, "count"); asm volatile("s_waitcnt vmcnt(%2)" : "+v"(x), "+v"(y) : "n"(N > 63 ? 63 : N) : "memory"); }
template <int N> DI void waitbar() { static_assert(N >= 0, "count"); asm volatile("s_waitcnt vmcnt(%0)\n\ts_barrier" ::"n"(N > 63 ? 63 : N) : "memory"); }
template <int N> DI void waitbar_l() { static_assert(N >= 0, "count"); asm volatile("s_waitcnt vmcnt(%0) lgkmcnt(0)\n\ts_barrier" ::"n"(N > 63 ? 63 : N) : "memory"); }
typedef short v4i16_t __attribute__((ext_vector_type(4)));
DI s16x4 trd(LAS const char* p) { return __builtin_amdgcn_ds_read_tr16_b64_v4i16((LAS v4i16_t*)p); }
struct LBase {
  unsigned lo, hi;
  DI void init(unsigned a) { lo = a; hi = a + 65536; PIN(lo); PIN(hi); }
  template <int OFF> DI LAS const char* at() const { return (LAS const char*)(uintptr_t)(OFF < 65536 ? lo : hi) + (OFF & 65535); }
};
DI bf16x8 cat8(s16x4 lo, s16x4 hi) { return __builtin_shufflevector(lo, hi, 0, 1, 2, 3, 4, 5, 6, 7); }

enum { OX, OG, OF, OM, OS, OP, OE, OW };
struct Sched {
  int n; short kind[400], idx[400], slot[400];
  constexpr Sched() : n(0), kind{}, idx{}, slot{} {}
  constexpr void add(int s, int k, int i) { kind[n] = (short)k; idx[n] = (short)i; slot[n] = (short)s; ++n; }
  constexpr int younger(int k, int i, int s) const {
    int p = -1;
    for (int q = 0; q < n; ++q) if (kind[q] == k && idx[q] == i) p = q;
    if (p < 0 || slot[p] > s) return -1;
    int c = 0;
    for (int q = p + 1; q < n; ++q) if (slot[q] <= s) ++c;
    return c;
  }
  constexpr int first(int s) const { int q = 0; while (q < n && slot[q] < s) ++q; return q; }
  constexpr int count(int s) const { int c = 0; for (int q = 0; q < n; ++q) if (slot[q] == s) ++c; return c; }
};

#define STAMP_DECL
#define STAMP(i)
#define STAMP_OUT(kid, n)

constexpr int RING = 4 * 32768, FB = RING, RED1 = FB + 24576, HSC = RED1 + 1024, YS = HSC + 1024, LDS_BYTES = FB + 32768;
constexpr int STAGE = 3 * 32768;
constexpr int XD = 16, XD2 = 16;
constexpr float LBOUND = 65536.f;
constexpr float QSCALE = 0.08838834764831845f * 1.4426950408889634f;
constexpr int B0 = 128;
#define PRIO 1
constexpr int FISS[4] = {96, 97, 98, 117}, FRDS[4] = {115, 118, 121, 124};
constexpr int fpiece(int sl) { for (int p = 0; p < 4; ++p) if (FRDS[p] == sl) return p; return -1; }
constexpr Sched make_sched() {
  Sched s;
#define XP 8
  for (int i = 0; i < 8; ++i) s.add(-2, OG, i);
  for (int i = 0; i < XP; ++i) s.add(-1, OX, i);
  for (int t = 0; t < 128; ++t) {
    if (t < 2) for (int i = XP + t * (16 - XP) / 2; i < XP + (t + 1) * (16 - XP) / 2; ++i) s.add(t, OX, i);
    if (t >= 2 && t < 50) s.add(t, OX, t + 14);
    if (t >= 2 && t < 6) s.add(t, OG, 6 + t);
    for (int v = 3; v < 18; ++v) for (int p = 0; p < 4; ++p) if (8 * (v - 3) + 7 + p == t) s.add(t, OG, 4 * v + p);
    if (t == 105 || t == 113 || t == 121) { s.add(t, OW, (t - 105) / 4); s.add(t, OW, (t - 105) / 4 + 1); }
    if (t >= 124) for (int i = 0; i < 3; ++i) s.add(t, OX, 64 + 3 * (t - 124) + i);
    if (t == 127) { s.add(t, OG, 72); s.add(t, OG, 73); }
    if (t == 30) s.add(t, OM, 0);
    if (t == 120) s.add(t, OM, 1);
    if (t >= 49 && t < 57 && (t & 1)) s.add(t, OF, (t - 49) / 2);
    if (t >= 71 && t < 79 && (t & 1)) s.add(t, OF, 4 + (t - 71) / 2);
    if (t == 48) s.add(t, OS, 0);
    if (t == 104) s.add(t, OS, 2);
    for (int p = 0; p < 4; ++p) if (FISS[p] == t) s.add(t, OF, 8 + p);
  }
  for (int t = 0; t < 64; ++t) {
    if (t == 0) { s.add(B0, OM, 2); s.add(B0, OG, 74); s.add(B0, OG, 75); s.add(B0, OW, 6); s.add(B0, OW, 7); }
    if (t == 1 || t == 2) { s.add(B0 + t, OX, 74 + 2 * t); s.add(B0 + t, OX, 75 + 2 * t); }
    if (t == 5 || t == 6 || t == 9) s.add(B0 + t, OX, 64 + (t == 5 ? 16 : t == 6 ? 18 : 22));
    if (t >= 5 && t < 50) s.add(B0 + t, OX, 64 + (t == 5 ? 17 : t < 9 ? t + 13 : t + 14));
    for (int v = 3; v < 16; ++v) for (int p = 0; p < 4; ++p) if ((v < 14 ? 4 * (v - 3) + 3 + (p >> 1) : v == 14 ? 45 + (p >> 1) : 49) == t) s.add(B0 + t, OG, 64 + 4 * v + p);
    if (t == 10 || t == 26) s.add(B0 + t, OM, t == 10 ? 3 : 4);
    if (t == 13 || t == 14 || t == 18) s.add(B0 + t, OE, t == 18 ? 2 : t - 13);
  }
  return s;
}
constexpr Sched SC = make_sched();

__global__ __launch_bounds__(512, 2) void k_fused(const u16* __restrict__ x, const u16* __restrict__ kc, const u16* __restrict__ vc,
                                                  const u16* __restrict__ wq, const u16* __restrict__ wo, const u16* __restrict__ wr,
                                                  const u16* __restrict__ wgate, const u16* __restrict__ wup, const u16* __restrict__ wdown,
                                                  char* __restrict__ ws, u16* __restrict__ out) {
  __shared__ __attribute__((aligned(16))) char lds_raw[LDS_BYTES];
  LAS char* const lds = (LAS char*)lds_raw;
  const int t = threadIdx.x, l = t & 63, bid = blockIdx.x, c = l & 15, g = l >> 4;
  const int wid = __builtin_amdgcn_readfirstlane(t >> 6);
  const int xcd = bid & 7, within = bid >> 3, head = xcd * 4 + (within & 3), rb = within >> 2;
  const int fj = xcd * 28 + (within < 28 ? within : within - 28);
  const int fsl = fj / 7, fgr = fj - fsl * 7;
  const int kh = xcd >> 2, mb = 2 * rb + (xcd & 1), nb = ((xcd >> 1) & 1) * 4 + (within & 3);
  const int rbw = wid & 3, kg = wid >> 2;
  const unsigned lds0 = (unsigned)(uintptr_t)lds;
  STAMP_DECL
  STAMP(0);

  unsigned tlo, thi, e[8];
  u32x2 klo, khi;
  const int fn = (bid * 4 + wid) & 255, kvh = bid >> 6;
  {
    const int row = 8 * bid + wid;
    u32x4 a0, a1, w0, w1;
    u32x2 ep;
    gld16<0>(a0, (unsigned)(row * DM * 2 + l * 16), x); gld16<1024>(a1, (unsigned)(row * DM * 2 + l * 16), x);
    gld16<0>(w0, (unsigned)(l * 16), wr); gld16<1024>(w1, (unsigned)(l * 16), wr);
    gld8<0, true>(ep, 0u, ws + WS_EP);
    waitv<3>();
    if (wid == 0) touch((unsigned)((within * 8 + (l >> 3)) * 8192 + xcd * 1024 + (l & 7) * 128), wq);
    if (wid == 3) {
      const char* pc;
      asm volatile("s_getpc_b64 %0" : "=s"(pc));
      touch((unsigned)((within * 12 + (l < 12 ? l : 11)) * 128), pc);
    }
    PIN(a0); PIN(a1);
    STAMP(1);
    float v[16];
#pragma unroll
    for (int j = 0; j < 4; ++j) { v[2 * j] = bflo(a0[j]); v[2 * j + 1] = bfhi(a0[j]); v[8 + 2 * j] = bflo(a1[j]); v[9 + 2 * j] = bfhi(a1[j]); }
    float s = 0.f;
#pragma unroll
    for (int j = 0; j < 16; ++j) s += v[j];
    const float mean = wave_sum(s) * (1.f / DM);
    float q = 0.f;
#pragma unroll
    for (int j = 0; j < 16; ++j) { const float d = v[j] - mean; q += d * d; }
    const float var = wave_sum(q) * (1.f / DM);
    const float mbf = rbf(mean), rden = __builtin_amdgcn_rcpf(rbf(__builtin_amdgcn_sqrtf(rbf(rbf(var) + 1e-5f))));
    u32x4 o0, o1;
#pragma unroll
    for (int j = 0; j < 8; ++j) {
      const unsigned dd = pk2(v[2 * j] - mbf, v[2 * j + 1] - mbf);
      const unsigned nn = pk2(bflo(dd) * rden, bfhi(dd) * rden);
      if (j < 4) o0[j] = nn; else o1[j - 4] = nn;
    }
    *(LAS u32x4*)(lds + STAGE + (l * 8 + (wid ^ (l & 7))) * 16) = o0;
    *(LAS u32x4*)(lds + STAGE + 8192 + (l * 8 + (wid ^ (l & 7))) * 16) = o1;
    *(LAS f32x2*)(lds + STAGE + 16384 + 32 + wid * 8) = (f32x2){mbf, rden};
    waitbar_l<63>();
    if (wid >= 4) {
#pragma unroll
      for (int hh = 0; hh < 4; ++hh) {
        const int p = hh * 256 + (t - 256), ch = p >> 3, r = 8 * bid + ((p & 7) ^ (ch & 7));
        const u32x4 o = *(LAS const u32x4*)(lds + STAGE + p * 16);
        gst16<0>(o, (unsigned)(((((r >> 5) * 32 + (ch >> 2)) * 2 + ((r >> 4) & 1)) * 64 + (ch & 3) * 16 + (r & 15)) * 16), ws + WS_XN);
      }
      if (t < 256 + 8) gst8(*(LAS const f32x2*)(lds + STAGE + 16384 + 32 + (t - 256) * 8), (unsigned)((8 * bid + t - 256) * 8), ws + WS_ST);
      waitv<4>();
    } else if (wid == 0 || wid == 3) waitv<1>(); else waitv<0>();
    PIN(w0); PIN(w1); PIN(ep);
    {
      const unsigned long long tg = (((unsigned long long)(unsigned)__builtin_amdgcn_readfirstlane(ep[1]) << 32) | (unsigned)__builtin_amdgcn_readfirstlane(ep[0])) + 1ull;
      tlo = (unsigned)tg; thi = (unsigned)(tg >> 32);
    }
    float rr = 0.f;
#pragma unroll
    for (int j = 0; j < 8; ++j) {
      const unsigned nn = j < 4 ? o0[j] : o1[j - 4], ww = j < 4 ? w0[j] : w1[j - 4];
      rr += bflo(nn) * bflo(ww); rr += bfhi(nn) * bfhi(ww);
    }
    rr = wave_sum(rr);
    {
      const unsigned bits = pk2(rr, 0.f) & 0xffffu;
      const unsigned ord = (bits & 0x8000u) ? (~bits & 0xffffu) : (bits | 0x8000u);
      *(LAS unsigned*)(lds + STAGE + 16384 + wid * 4) = (ord << 11) | (unsigned)(SEQ - 1 - row);
    }
    if (wid >= 4) waitv<0>();
    waitbar_l<63>();
    if (t == 256) {
      const u32x4 ka = *(LAS const u32x4*)(lds + STAGE + 16384), kb = *(LAS const u32x4*)(lds + STAGE + 16400);
      const unsigned key = umax(umax(umax(ka[0], ka[1]), umax(ka[2], ka[3])), umax(umax(kb[0], kb[1]), umax(kb[2], kb[3])));
      gst8((u32x2){(tlo << 27) | key, (tlo >> 5) | (thi << 27)}, (unsigned)(bid * 8), ws + WS_G0);
    }
    STAMP(2);
  }
  const unsigned glo = tlo << 27, ghi = (tlo >> 5) | (thi << 27);

  const char* const wq_b = (const char*)wq + head * 256;
  const unsigned wq_v = (unsigned)(((8 * wid + ((l & 31) >> 2)) * 4096 + (l >> 5) * 32 + ((l & 3) ^ (2 * (wid & 1))) * 8) * 2);
  const char* const kv_b = ws + WS_KV + (size_t)(head >> 3) * 262144;
  const unsigned kv_v = (unsigned)(wid * 4096 + l * 16);
  const char* const xn_b = ws + WS_XN + (size_t)rb * 8 * 65536 + 4096;
  const unsigned xn_v = (unsigned)(wid * 65536 + l * 16);
  const char* const wo_b = (const char*)wo + (size_t)kh * 2048 * (DM * 2) + nb * 256;
  const char* const o_b = ws + WS_O + (size_t)mb * 4 * 262144 + kh * 131072;
  const unsigned ldsw = lds0 + wid * 2048, ldsk = lds0 + wid * 4096, ldsf = lds0 + FB + wid * 1024;
  const int erow = mb * 128 + rbw * 32 + kg * 16 + c;
  bf16x8 xf[XD];
  u32x4 fm[2];
  bf16x8 fxs;
  const unsigned gu_v = (unsigned)((((4 * wid + g) * 2 + ((l >> 3) & 1)) * FF + fj * 16 + (l & 7) * 2) * 8);
  u32x2 fg;
  u32x2 xe[4];
  f32x2 st;
  unsigned key = 0;

  const unsigned fo_v = (unsigned)(((mb * 4 + rbw) * 32 + kh * 16 + c) * 8);
  const int yrow = within < 28 ? fj : 224 + xcd * 4 + within - 28;
  f32x4 o[2][2][2];
  float inv[2];
  const char* const od_b = ws + WS_O + ((size_t)(rb * 8) * 128 + head * 4) * 2048 + 4096;
  auto issue = [&](auto Sl, auto Pos) AI {
    constexpr int sl = Sl, pos = Pos, cnt = SC.count(sl), q0 = SC.first(sl) + (pos < 0 ? 0 : pos);
    constexpr int nq = pos < 0 ? cnt : pos >= cnt ? 0 : pos < 3 ? 1 : cnt - 3;
    sfor<nq>([&](auto Qi) AI {
      constexpr int k = SC.kind[q0 + Qi], i = SC.idx[q0 + Qi];
      if constexpr (k == OX && i < 64) gld16<(i & 7) * 1024 - 4096, true>(xf[i % XD], xn_v, xn_b + (size_t)(i >> 3) * 8192);
      if constexpr (k == OX && i >= 64) {
        constexpr int q = i - 64, v = q >> 2;
        gld16<0, true>(xf[q % XD2], (unsigned)(rbw * 262144 + kg * 16384 + ((q & 1) ^ kg) * 1024 + l * 16), o_b + (size_t)(((v & 3) * 4 + ((q >> 1) & 1)) * 8192 + (v >> 2) * 2048));
      }
      if constexpr (k == OG) {
        constexpr int v = i >> 2, p = i & 3;
        if constexpr (v < 8) {
          const char* sb = wq_b + (size_t)(v * 128 + (p >> 1) * 64) * 8192;
          glds<(p & 1) * 128>(ldsw + (v & 3) * 32768 + (p >> 1) * 16384 + (p & 1) * (1024 - 128), wq_v, sb);
        } else if constexpr (v < 16) {
          glds<p * 1024, true>(ldsk + (v & 3) * 32768, kv_v, kv_b + (size_t)(v - 8) * 32768);
        } else {
          const unsigned wo_v = (unsigned)((((wid >> 2) * 128 + (wid & 3) * 8 + ((l & 31) >> 2)) * DM + (l >> 5) * 32 + ((l & 3) ^ (2 * (wid & 1))) * 8) * 2);
          const char* sb = wo_b + (size_t)(((v & 3) * 4 + (p >> 1) * 2) * 128 + ((v - 16) >> 2) * 32) * (DM * 2) + (((p & 1) ^ kh ^ 1) << 7);
          glds<0>(ldsw + (v & 3) * 32768 + (p >> 1) * 16384 + (p & 1) * 1024, wo_v, sb);
        }
      }
      if constexpr (k == OF && i < 8)
        glds<0>(ldsf + (i & 3) * 8192, (unsigned)(((l >> 3) * FF + wid * 64 + ((l & 7) ^ ((i & 1) * 4 + ((l >> 3) & 2))) * 8) * 2),
                (const char*)(i < 4 ? wgate : wup) + ((size_t)(fsl * 32 + 8 * (i & 3)) * FF + fgr * 512) * 2);
      if constexpr (k == OF && i >= 8) {
        const int frho = l >> 2, fch = (l & 3) ^ (frho & 4 ? 2 : 0);
        glds<0>(ldsf + ((i - 8) % 3) * 8192, (unsigned)((frho * DM + wid * 128 + fch * 8) * 2), (const char*)wdown + (size_t)fj * 16 * DM * 2 + (i - 8) * 64);
      }
      if constexpr (k == OS && i == 0) {
        const int idx = SEQ - 1 - (int)(key & 2047u);
        gld16<0, true>(fxs, (unsigned)((((idx >> 5) * 32 + fsl) * 2 + ((idx >> 4) & 1)) * 1024 + (g * 16 + (idx & 15)) * 16), ws + WS_XN);
      }
      if constexpr (k == OS && i == 2) gld16<0, true>(fm[0], gu_v, ws + WS_GU);
      if constexpr (k == OM && i > 0) gld8<0, true>(fg, fo_v, ws + WS_FO + (i - 1) * 16384);
      if constexpr (k == OM && i == 0) gld8<0, true>(fg, (unsigned)((t & 255) * 8), wid < 4 ? ws + WS_G0 : ws + WS_FK + (head >> 3) * 2048);
      if constexpr (k == OW) {
        constexpr int os = i >> 1, qb = i & 1;
        PIN(o[os & 1][0][qb]); PIN(o[os & 1][1][qb]);
        const f32x4 a = o[os & 1][0][qb] * inv[qb], b = o[os & 1][1][qb] * inv[qb];
        gst16<i * 1024 - 4096>((u32x4){pk2(a[0], a[1]), pk2(a[2], a[3]), pk2(b[0], b[1]), pk2(b[2], b[3])}, (unsigned)(wid * 262144 + l * 16), od_b);
      }
      if constexpr (k == OE && i < 2)
        glds<0>(ldsf + i * 8192, (unsigned)(((erow - c + i * 8 + (l >> 3)) * DM + nb * 128 + kh * 64 + ((l & 7) ^ (i * 4 + (l >> 4))) * 8) * 2), x);
      if constexpr (k == OE && i == 2) gld8<0, true>(st, (unsigned)(erow * 8), ws + WS_ST);
    });
  };
  f32x4 fa0, fa1, fq[4];
  s16x4 fb0, fb1, fxa;
  bf16x8 ft[2];
  auto endslot = [&](auto Sl) AI {
    constexpr int sl = Sl, u = sl >> 3, i = sl & 7;
    if constexpr (sl == 3) {
      if (wid == 6 || wid == 7) touch((unsigned)(((wid - 4) * 256 + within * 8 + (l >> 3)) * 8192 + xcd * 1024 + (l & 7) * 128), wq);
    }
    if constexpr (sl == 4) {
      if (wid < 4) {
        u32x4 kvo;
        if (fn < 128) {
          PIN(klo); PIN(khi);
          kvo = (u32x4){klo[0], klo[1], khi[0], khi[1]};
        } else {
#pragma unroll
          for (int j = 0; j < 8; ++j) PIN(e[j]);
          kvo = (u32x4){e[0] | (e[1] << 16), e[2] | (e[3] << 16), e[4] | (e[5] << 16), e[6] | (e[7] << 16)};
        }
        gst16<0>(kvo, (unsigned)((bid * 4 + wid) * 1024 + l * 16), ws + WS_KV);
      }
    }
    if constexpr (sl == 20) {
      if (wid < 4 && l == 0) gst8((u32x2){tlo, thi}, (unsigned)((bid * 4 + wid) * 8), ws + WS_FK);
    }
    if constexpr (sl == 43) {
      waitv<SC.younger(OM, 0, sl)>(fg);
      const unsigned lo = wid < 4 ? glo : tlo, hi = wid < 4 ? ghi : thi, mk = wid < 4 ? 0xf8000000u : 0xffffffffu;
      for (int spin = 0; spin < SPINS; ++spin) {
        if (__builtin_expect(__all(((fg[0] & mk) == lo) & (fg[1] == hi)), 1)) break;
        __builtin_amdgcn_s_sleep(1);
        gld8<0, true>(fg, (unsigned)((t & 255) * 8), wid < 4 ? ws + WS_G0 : ws + WS_FK + (head >> 3) * 2048);
        waitv<0>(fg);
      }
      const unsigned km = wave_max_u32(fg[0] & 0x07ffffffu);
      *(LAS unsigned*)(lds + HSC + wid * 4) = km;
    }
    if constexpr (sl == 47) {
      if (bid == 0 && t == 0) gst8((u32x2){tlo, thi}, 0u, ws + WS_EP);
      const u32x4 kk = *(LAS const u32x4*)(lds + HSC);
      key = __builtin_amdgcn_readfirstlane(umax(umax(kk[0], kk[1]), umax(kk[2], kk[3])));
    }
    if constexpr (sl == 58 || sl == 61 || sl == 64 || sl == 67) {
      if (wid == 4 + (sl - 58) / 3) touch((unsigned)(((wid & 3) * 64 + l) * 128), (const char*)wdown + (size_t)fj * 16 * DM * 2);
    }
    if constexpr (sl == 81 || sl == 85 || sl == 89 || sl == 93) {
      if (wid == (sl - 81) / 4)
        touch((unsigned)((kh * 2048 + (within >> 1) * 128 + wid * 32 + ((within & 1) * 2 + (xcd & 1)) * 8 + (l >> 3)) * (DM * 2) + ((xcd >> 1) & 1) * 1024 + (l & 7) * 128), wo);
    }
    if constexpr ((sl >= 68 && sl <= 70) || (sl >= 90 && sl <= 92)) {
      constexpr int r = sl >= 90, q = sl - (r ? 90 : 68);
      if constexpr (q == 0) waitv<SC.younger(OF, 4 * r + 3, sl)>(fxs);
      if constexpr (q >= 1) {
#pragma unroll
        for (int j = 0; j < 2; ++j) { fq[2 * q - 2 + j] = __builtin_amdgcn_mfma_f32_16x16x32_bf16(fxs, ft[j], (f32x4){}, 0, 0, 0); PIN(fq[2 * q - 2 + j]); }
      }
      if constexpr (q < 2) {
        const unsigned rp = ldsf + g * 8192 + (c >> 2) * 128 + ((((g & 1) << 1) | (c >> 3)) << 5) + (c & 3) * 8;
#pragma unroll
        for (int j = 0; j < 2; ++j) { LAS const char* a = (LAS const char*)(uintptr_t)(rp ^ ((2 * q + j) << 5)); ft[j] = cat8(trd(a), trd(a + 512)); }
      }
    }
    if constexpr (sl == 71 || sl == 93) {
      const float v = sel2(g & 2, sel2(g & 1, fq[3][0], fq[2][0]), sel2(g & 1, fq[1][0], fq[0][0]));
      gst8((u32x2){__float_as_uint(v), tlo}, (unsigned)(((fsl * 2 + (sl == 93)) * FF + fgr * 512 + t) * 8), ws + WS_GU);
    }
    if constexpr (sl == 109) {
      waitv<SC.younger(OS, 2, sl)>(fm[0]);
      for (int spin = 0; spin < SPINS; ++spin) {
        if (__builtin_expect(__all((fm[0][1] == tlo) & (fm[0][3] == tlo)), 1)) break;
        __builtin_amdgcn_s_sleep(1);
        gld16<0, true>(fm[0], gu_v, ws + WS_GU);
        waitv<0>(fm[0]);
      }
      *(LAS f32x2*)(lds + RED1 + (wid * 32 + 2 * c) * 4) = (f32x2){xg_sum(__uint_as_float(fm[0][0])), xg_sum(__uint_as_float(fm[0][2]))};
    }
    if constexpr (sl == 111) {
#pragma unroll
      for (int w = 0; w < 2; ++w) { fa0[w] = *(LAS const float*)(lds + RED1 + ((2 * g + w) * 32 + c) * 4); fa0[2 + w] = *(LAS const float*)(lds + RED1 + ((2 * g + w) * 32 + 16 + c) * 4); }
    }
    if constexpr (sl == 112) { fa0[0] = xg_sum(fa0[0] + fa0[1]); fa0[2] = xg_sum(fa0[2] + fa0[3]); PIN(fa0); }
    if constexpr (sl == 113) {
      const float hh = rbf(silu_f(rbf(fa0[0]))) * rbf(fa0[2]);
      *(LAS u16*)(lds + HSC + 32 + wid * 32 + c * 2) = (u16)pk2(hh, hh);
    }
    if constexpr (sl == 114) fxa = *(LAS const s16x4*)(lds + HSC + 32 + wid * 32 + g * 8);
    constexpr int pr = fpiece(sl), pm = fpiece(sl - 1), ps = fpiece(sl - 2);
    if constexpr (ps >= 0) gst4<ps * 128>(sel2(g & 1, fa1[0], fa0[0]), (unsigned)((wid * 128 + (l & 31)) * 4), ws + WS_YP + (size_t)yrow * 4096);
    if constexpr (pm >= 0) { fa0 = MFMA16(fxa, fb0, (f32x4){}); fa1 = MFMA16(fxa, fb1, (f32x4){}); PIN(fa0); PIN(fa1); }
    if constexpr (pr >= 0) {
      waitv<SC.younger(OF, 8 + pr, sl)>();
      const unsigned fp = ldsf + (4 * g + (c >> 2)) * 64 + (g & 1) * 32 + (c & 3) * 8;
      fb0 = trd((LAS const char*)(uintptr_t)fp + (pr % 3) * 8192); fb1 = trd((LAS const char*)(uintptr_t)(fp ^ 32) + (pr % 3) * 8192);
    }
    if constexpr (sl == 111 || sl == 119 || sl == 127) {
      waitv<SC.younger(OW, (sl - 111) / 4 + 1, sl)>();
      if (l == 0) gst8((u32x2){tlo, thi}, (unsigned)(((rb * 8 + wid) * 32 + head) * 8), ws + WS_FO + ((sl - 111) / 8) * 16384);
    }
    if constexpr (sl == 123) {
      waitv<SC.younger(OM, 1, sl)>(fg);
      if (__builtin_expect(!__all((fg[0] == tlo) & (fg[1] == thi)), 0)) poll8(fo_v, ws + WS_FO, tlo, thi, 0xffffffffu);
    }
    SBAR();
    if constexpr (i == 6) {
      if constexpr (u == 5 || u == 13) waitbar_l<SC.younger(OG, 4 * u + 7, sl)>();
      else waitbar<SC.younger(OG, 4 * u + 7, sl)>();
      if constexpr (u == 0 || u == 1 || u == 5 || u == 7 || u == 8 || u == 10 || u == 12 || u == 14)
        STAMP(u == 0 ? 4 : u == 1 ? 5 : u == 5 ? 6 : u == 7 ? 7 : u == 8 ? 8 : u == 10 ? 9 : u == 12 ? 10 : 11);
    }
    if constexpr (PRIO && !(i & 1)) __builtin_amdgcn_s_setprio(i == 6 ? 3 : 2 - i / 2);
  };

  issue(IC(-2), IC(-1));
  if (wid < 4) {
    if (wid == 1) touch((unsigned)((256 + within * 8 + (l >> 3)) * 8192 + xcd * 1024 + (l & 7) * 128), wq);
    if (fn < 128) {
      const unsigned ko = (unsigned)(((kvh * SK + (SK - WIN) + (fn >> 2) * 16 + c) * DH + (fn & 3) * 32 + 4 * g) * 2);
      gld8<0>(klo, ko, kc); gld8<32>(khi, ko, kc);
      waitv<6>();
    } else {
      const int d = 32 * ((fn - 128) >> 5) + 8 * (c >> 2) + 4 * (fn & 1) + (c & 3);
      const unsigned vo = (unsigned)(((kvh * SK + (SK - WIN) + ((fn >> 1) & 15) * 32 + 4 * g + 8) * DH + d) * 2);
      sfor<8>([&](auto J) AI { constexpr int j = J; gld2<(16 * (j >> 2) + (j & 3) - 8) * DH * 2>(e[j], vo, vc); });
      waitv<12>();
    }
  } else waitv<4>();
  if (wid == 5) poll8((unsigned)((rb * 32 + (l & 31)) * 8), ws + WS_G0, glo, ghi, 0xf8000000u);
  waitbar<63>();
  issue(IC(-1), IC(-1));
  STAMP(3);

  LBase trb[2], frb;
  trb[0].init(lds0 + g * 2048 + (c >> 2) * 64 + (g & 1) * 32 + (c & 3) * 8);
  trb[1].init(lds0 + g * 2048 + (c >> 2) * 64 + ((g & 1) ^ 1) * 32 + (c & 3) * 8);
  frb.init(lds0 + l * 16);
  f32x4 acc[8][2];
  bf16x8 wf[2][4];
  auto WRD1 = [&](auto Si, auto Base, auto Dd) AI {
    constexpr int s = Si, d = Dd, o = Base + (d >> 1) * 512;
    wf[s & 1][d] = cat8(trd(trb[d & 1].template at<o>()), trd(trb[d & 1].template at<o + 256>()));
  };
  auto WRD = [&](auto Si, auto Base) AI { sfor<4>([&](auto Dd) AI { WRD1(Si, Base, Dd); }); };
#define WA(s) IC((((s) >> 3) & 3) * 32768 + (((s) >> 1) & 3) * 8192 + ((s) & 1) * 1024)
#define WB(s) IC((((s) >> 2) & 3) * 32768 + (((s) >> 1) & 1) * 8192 + ((s) & 1) * 1024)
  bf16x8 fr[4];
  auto FRD = [&](auto Ni) AI { constexpr int n = Ni; if constexpr (n < 256) fr[n & 3] = *(LAS const bf16x8*)(frb.at<((n >> 5) & 3) * 32768 + (n & 31) * 1024>()); };

  WRD(IC(0), WA(0));
  SBAR();
  bf16x8 qf[4][2];
  auto QPACK = [&](auto Ds) AI {
    constexpr int ds = Ds;
#pragma unroll
    for (int qb = 0; qb < 2; ++qb) {
      const f32x4 a = acc[2 * ds][qb] * QSCALE, b = acc[2 * ds + 1][qb] * QSCALE;
      qf[ds][qb] = __builtin_bit_cast(bf16x8, (u32x4){pk2(a[0], a[1]), pk2(a[2], a[3]), pk2(b[0], b[1]), pk2(b[2], b[3])});
      PIN(qf[ds][qb]);
    }
  };
  sfor<64>([&](auto Si) AI {
    constexpr int s = Si, x0 = (s & ~1) % XD, x1 = (s | 1) % XD;
    if constexpr (!(s & 1)) waitv<SC.younger(OX, s + 1, s - 1)>(xf[x0], xf[x1]);
    sfor<4>([&](auto Dd) AI {
      constexpr int d = Dd, dt = (s & 1) * 4 + d;
      if constexpr (s + 1 < 64) WRD1(IC(s + 1), WA(s + 1), Dd);
      else if constexpr (d < 3) FRD(Dd);
      SBAR();
      if constexpr (s < 2) { MFZ(acc[dt][0], wf[s & 1][d], xf[x0]); MFZ(acc[dt][1], wf[s & 1][d], xf[x1]); }
      else { MF(acc[dt][0], wf[s & 1][d], xf[x0]); MF(acc[dt][1], wf[s & 1][d], xf[x1]); }
      issue(Si, Dd);
      if constexpr (s == 63 && (d & 1)) { QPACK(IC(d >> 1)); SBAR(); }
    });
    SBAR();
    endslot(Si);
  });

  f32x4 sc[2][2][2], negm[2];
  u32x4 pw[16][2];
  f32x4 lsum[2];
  bf16x8 ones = {0x3f80, 0x3f80, 0x3f80, 0x3f80, 0x3f80, 0x3f80, 0x3f80, 0x3f80};
  PIN(ones);
#define MX3(a, b, c) __builtin_fmaxf(__builtin_fmaxf((a), (b)), (c))
#define EXP2(x) __builtin_amdgcn_exp2f(x)
  auto SM = [&](auto Hi, auto Pi) AI {
    constexpr int H = Hi, p = Pi, kt = p >> 2, qb = (p >> 1) & 1, r = 2 * (p & 1);
    const float e0 = EXP2(sc[H & 1][kt][qb][r]), e1 = EXP2(sc[H & 1][kt][qb][r + 1]);
    unsigned w = pk2(e0, e1);
    PIN(w);
    pw[H][qb][kt * 2 + (p & 1)] = w;
  };
  sfor<128>([&](auto Ni) AI {
    constexpr int n = Ni, H = n >> 3, i = n & 7, kt = i >> 2, ds = i & 3, sn = H & 1;
    FRD(IC(n + 3));
    if constexpr (n < 2) QPACK(IC(2 + n));
    sfor<2>([&](auto Qb) AI {
      constexpr int qb = Qb;
      if constexpr (ds == 0 && H == 0) MFZ(sc[sn][kt][qb], fr[n & 3], qf[0][qb]);
      else if constexpr (ds == 0) MFC(sc[sn][kt][qb], fr[n & 3], qf[0][qb], negm[qb]);
      else MF(sc[sn][kt][qb], fr[n & 3], qf[ds][qb]);
    });
    if constexpr (n == 5 || n == 6) {
      constexpr int qb = n - 5;
      NOPS(3); PIN(sc[0][0][qb]);
      const float m = -xg_max(__builtin_fmaxf(MX3(sc[0][0][qb][0], sc[0][0][qb][1], sc[0][0][qb][2]), sc[0][0][qb][3]));
      negm[qb] = (f32x4){m, m, m, m};
      sc[0][0][qb] += negm[qb];
      PIN(negm[qb]); PIN(sc[0][0][qb]);
    }
    if constexpr (n == 9 || n == 10) { NOPS(3); PIN(sc[0][1][n - 9]); sc[0][1][n - 9] += negm[n - 9]; PIN(sc[0][1][n - 9]); }
    issue(IC(64 + (n >> 2)), IC(n & 3));
    if constexpr (H == 2 && (i == 1 || i == 5)) MFZ(lsum[i >> 2], ones, pw[0][i >> 2]);
    if constexpr (H > 2 && (i == 1 || i == 5)) MF(lsum[i >> 2], ones, pw[H - 2][i >> 2]);
    if constexpr (H >= 1) SM(IC(H - 1), IC(i));
    if constexpr (n == 125) { PIN(negm[0]); PIN(negm[1]); }
    SBAR();
    if constexpr ((n & 3) == 3) endslot(IC(64 + (n >> 2)));
  });
  constexpr int CHK = 16;
  sfor<128>([&](auto Mi) AI {
    constexpr int m = Mi, n = 128 + m, os = m >> 5, H = (m >> 1) & 15, par = m & 1;
    FRD(IC(n + 3));
    if constexpr (H == 0) { MFZ(o[os & 1][par][0], fr[n & 3], pw[H][0]); MFZ(o[os & 1][par][1], fr[n & 3], pw[H][1]); }
    else { MF(o[os & 1][par][0], fr[n & 3], pw[H][0]); MF(o[os & 1][par][1], fr[n & 3], pw[H][1]); }
    if constexpr (m == 1 || m == 2) MF(lsum[m - 1], ones, pw[14][m - 1]);
    if constexpr (m < 8) SM(IC(15), Mi);
    if constexpr (m == 9 || m == 10) MF(lsum[m - 9], ones, pw[15][m - 9]);
    if constexpr (m == CHK + 1 || m == CHK + 2) { inv[m - CHK - 1] = __builtin_amdgcn_rcpf(lsum[m - CHK - 1][0]); PIN(inv[m - CHK - 1]); }
    issue(IC(64 + (n >> 2)), IC(n & 3));
    if constexpr (m == 123) {
      trb[0].init(lds0 + kg * 16384 + g * 2048 + (c >> 2) * 64 + (g & 1) * 32 + (c & 3) * 8);
      trb[1].init(lds0 + kg * 16384 + g * 2048 + (c >> 2) * 64 + ((g & 1) ^ 1) * 32 + (c & 3) * 8);
    }
    if constexpr (m >= 124) WRD1(IC(0), WB(0), IC(m - 124));
    SBAR();
    if constexpr ((n & 3) == 3) endslot(IC(64 + (n >> 2)));
    if constexpr (m == CHK - 1) {
      PIN(lsum[0]); PIN(lsum[1]);
      if (__builtin_expect(__any(!(lsum[0][0] + lsum[1][0] <= LBOUND)), 0)) {
        NOPS(7); SBAR();
        auto tile = [&](unsigned off, f32x4& s0, f32x4& s1) AI {
          bf16x8 kf[4];
          SBAR(); NOPS(7);
          sfor<4>([&](auto Ds) AI { constexpr int ds = Ds; gld16<ds * 1024, true>(kf[ds], off, kv_b); });
          waitv<0>(kf[0], kf[1]);
          PIN(kf[2]); PIN(kf[3]);
#pragma unroll
          for (int ds = 0; ds < 4; ++ds) {
            s0 = __builtin_amdgcn_mfma_f32_16x16x32_bf16(kf[ds], qf[ds][0], s0, 0, 0, 0);
            s1 = __builtin_amdgcn_mfma_f32_16x16x32_bf16(kf[ds], qf[ds][1], s1, 0, 0, 0);
          }
        };
        float m0 = -__builtin_inff(), m1 = m0;
#pragma unroll 1
        for (int kt = 0; kt < 32; ++kt) {
          f32x4 s0 = {}, s1 = {};
          tile((unsigned)(kt * 4096 + l * 16), s0, s1);
          m0 = MX3(MX3(m0, s0[0], s0[1]), s0[2], s0[3]); m1 = MX3(MX3(m1, s1[0], s1[1]), s1[2], s1[3]);
        }
        m0 = -xg_max(m0); m1 = -xg_max(m1);
        lsum[0] = (f32x4){}; lsum[1] = (f32x4){};
        sfor<16>([&](auto Hi) AI {
          constexpr int H = Hi;
#pragma unroll
          for (int kt = 0; kt < 2; ++kt) {
            sc[H & 1][kt][0] = (f32x4){m0, m0, m0, m0}; sc[H & 1][kt][1] = (f32x4){m1, m1, m1, m1};
            tile((unsigned)((2 * H + kt) * 4096 + l * 16), sc[H & 1][kt][0], sc[H & 1][kt][1]);
          }
          sfor<8>([&](auto Pi) AI { SM(Hi, Pi); });
          PIN(pw[H][0]); PIN(pw[H][1]);
          lsum[0] = __builtin_amdgcn_mfma_f32_16x16x32_bf16(ones, __builtin_bit_cast(bf16x8, pw[H][0]), lsum[0], 0, 0, 0);
          lsum[1] = __builtin_amdgcn_mfma_f32_16x16x32_bf16(ones, __builtin_bit_cast(bf16x8, pw[H][1]), lsum[1], 0, 0, 0);
        });
#pragma unroll
        for (int par = 0; par < 2; ++par) { o[0][par][0] = (f32x4){}; o[0][par][1] = (f32x4){}; }
        sfor<CHK / 4>([&](auto Qi) AI {
          constexpr int q = Qi;
          bf16x8 vf[4];
          SBAR(); NOPS(7);
          sfor<4>([&](auto J) AI { constexpr int j = J; gld16<j * 1024, true>(vf[j], (unsigned)((128 + 4 * q) * 1024 + l * 16), kv_b); });
          waitv<0>(vf[0], vf[1]);
          PIN(vf[2]); PIN(vf[3]);
#pragma unroll
          for (int j = 0; j < 4; ++j)
#pragma unroll
            for (int qb = 0; qb < 2; ++qb)
              o[0][j & 1][qb] = __builtin_amdgcn_mfma_f32_16x16x32_bf16(vf[j], __builtin_bit_cast(bf16x8, pw[2 * q + (j >> 1)][qb]), o[0][j & 1][qb], 0, 0, 0);
        });
#pragma unroll
        for (int par = 0; par < 2; ++par) { PIN(o[0][par][0]); PIN(o[0][par][1]); }
        PIN(lsum[0]); PIN(lsum[1]);
        SBAR(); NOPS(7);
      }
      SBAR();
    }
  });
  STAMP(12); STAMP(13); STAMP(14); STAMP(15);

  const int idx = SEQ - 1 - (int)(key & 2047u);
  const bool owner = (idx >> 7) == mb, ownw = (idx >> 4) == mb * 8 + rbw * 2 + kg;
  f32x4 yp[7], ya[4], oth[4];
  unsigned gate[8];
  float glow;
  const unsigned ex_a = ldsf + (c >> 3) * 8192 + (c & 7) * 128 + (((g >> 1) ^ (c >> 1)) << 4) + (g & 1) * 8;
  auto item = [](int s) { return s < 48 ? s : s < 56 ? 48 + 2 * (s - 48) : 49 + 2 * (s - 56); };
  u32x2 pf;
  sfor<64>([&](auto Si) AI {
    constexpr int s = Si, u = s >> 2, it = item(s), x0 = (it & ~1) % XD2, x1 = (it | 1) % XD2;
    if constexpr (!(it & 1)) waitv<SC.younger(OX, 64 + it + 1, B0 + s - 1)>(xf[x0], xf[x1]);
    sfor<4>([&](auto Dd) AI {
      constexpr int d = Dd, dt = (it & 1) * 4 + d;
      if constexpr (s + 1 < 64) WRD1(IC(s + 1), WB(item(s + 1)), Dd);
      SBAR();
      if constexpr (s < 2) { MFZ(acc[dt][0], wf[s & 1][d], xf[x0]); MFZ(acc[dt][1], wf[s & 1][d], xf[x1]); }
      else { MF(acc[dt][0], wf[s & 1][d], xf[x0]); MF(acc[dt][1], wf[s & 1][d], xf[x1]); }
      issue(IC(B0 + s), Dd);
      if constexpr (s == 56) *(LAS f32x4*)(lds + (wid ^ 4) * 4096 + l * 16 + (d & 1) * 2048 + (d >> 1) * 32768) = acc[d][1];
      if constexpr (s == 57 && d == 0) {
#pragma unroll
        for (int e = 0; e < 4; ++e) oth[e] = *(LAS const f32x4*)(lds + wid * 4096 + l * 16 + (e & 1) * 2048 + (e >> 1) * 32768);
      }
      if constexpr (s == 57 && d >= 2) {
        gst16<(2 * d - 4) * 1024>(acc[2 * d - 4][0] + oth[2 * d - 4], (unsigned)(wid * 4096 + l * 16), ws + WS_XC + (size_t)(bid ^ 4) * 32768);
        gst16<(2 * d - 3) * 1024>(acc[2 * d - 3][0] + oth[2 * d - 3], (unsigned)(wid * 4096 + l * 16), ws + WS_XC + (size_t)(bid ^ 4) * 32768);
      }
      if constexpr (s == 34) xe[d] = *(LAS const u32x2*)(uintptr_t)(ex_a ^ (d << 5));
      if constexpr (s >= 36 && s < 44 && d == 1) {
        constexpr int j = s - 36;
        PIN(st);
        const unsigned xw = xe[j >> 1][j & 1];
        const unsigned dd = pk2(bflo(xw) - st[0], bfhi(xw) - st[0]);
        gate[j] = pk2(bflo(dd) * st[1], bfhi(dd) * st[1]);
        PIN(gate[j]);
      }
      if constexpr (s >= 47 && s < 63 && d == 1) {
        constexpr int j = (s - 47) >> 1;
        if constexpr ((s - 47) & 1) { gate[j] = pk2(glow, silu_f(bfhi(gate[j]))); PIN(gate[j]); }
        else { glow = silu_f(bflo(gate[j])); PIN(glow); }
      }
    });
    SBAR();
    if constexpr (s == 7) {
      waitv<SC.younger(OW, 7, B0 + s)>();
      if (l == 0) {
        gst8((u32x2){tlo, thi}, (unsigned)(((rb * 8 + wid) * 32 + head) * 8), ws + WS_FO + 3 * 16384);
        gst8((u32x2){tlo, thi}, (unsigned)((wid * 256 + yrow) * 8), ws + WS_FY);
      }
    }
    if constexpr (s == 4 || s == 17 || s == 33) {
      constexpr int j = s == 4 ? 1 : s == 17 ? 2 : 3;
      waitv<SC.younger(OM, j + 1, B0 + s)>(fg);
      if (__builtin_expect(!__all((fg[0] == tlo) & (fg[1] == thi)), 0)) poll8(fo_v, ws + WS_FO + j * 16384, tlo, thi, 0xffffffffu);
    }
    if constexpr (s == 60) {
      waitv<0>();
      if (l == 0) gst8((u32x2){tlo, thi}, (unsigned)(((bid ^ 4) * 8 + wid) * 64), ws + WS_FX);
    }
    if constexpr (s == 61) gld8<0, true>(pf, (unsigned)((bid * 8 + wid) * 64), ws + WS_FX);
    if constexpr (s == 13) {
      if (owner) { gld16<0, true>(fm[0], (unsigned)(nb * 2048 + (l < 56 ? l : 55) * 32), ws + WS_FY); gld16<16, true>(fm[1], (unsigned)(nb * 2048 + (l < 56 ? l : 55) * 32), ws + WS_FY); }
    }
    if constexpr (s == 28) {
      if (owner) {
        PIN(fm[0]); PIN(fm[1]);
        for (int spin = 0; spin < SPINS; ++spin) {
          const bool ok = (fm[0][0] == tlo) & (fm[0][1] == thi) & (fm[0][2] == tlo) & (fm[0][3] == thi) & (fm[1][0] == tlo) & (fm[1][1] == thi) & (fm[1][2] == tlo) & (fm[1][3] == thi);
          if (__builtin_expect(__all(ok), 1)) break;
          __builtin_amdgcn_s_sleep(1);
          gld16<0, true>(fm[0], (unsigned)(nb * 2048 + (l < 56 ? l : 55) * 32), ws + WS_FY); gld16<16, true>(fm[1], (unsigned)(nb * 2048 + (l < 56 ? l : 55) * 32), ws + WS_FY);
          waitv<0>(fm[0], fm[1]);
        }
        const unsigned yv = (unsigned)(((t >> 4) * 1024 + nb * 128 + kh * 64 + (t & 15) * 4) * 4);
        sfor<7>([&](auto I) AI { constexpr int q = I; gld16<0, true>(yp[q], yv, ws + WS_YP + (size_t)q * 32 * 4096); });
      }
    }
    if constexpr (s == 43) {
      if (owner) {
        f32x4 a = {};
#pragma unroll
        for (int q = 0; q < 7; ++q) { PIN(yp[q]); a += yp[q]; }
#pragma unroll
        for (int j = 0; j < 4; ++j) a[j] = xg_sum(a[j]);
        *(LAS f32x4*)(lds + YS + (wid * 64 + c * 4) * 4) = a;
      }
    }
    if constexpr (s == 45) {
      if (ownw) {
        float a = 0.f;
#pragma unroll
        for (int p = 0; p < 8; ++p) a += *(LAS const float*)(lds + YS + (p * 64 + l) * 4);
        const unsigned ord = key >> 11, bits = (ord & 0x8000u) ? (ord & 0x7fffu) : (~ord & 0xffffu);
        *(LAS float*)(lds + RED1 + l * 4) = rbf(rbf(a) * rbf(sigmoid_f(bflo(bits))));
#pragma unroll
        for (int q = 0; q < 4; ++q) ya[q] = *(LAS const f32x4*)(lds + RED1 + (q * 16 + 4 * g) * 4);
      }
    }
    if constexpr (s == 46) {
      if (ownw) {
#pragma unroll
        for (int j = 0; j < 8; ++j) {
          const unsigned w = pk2(bflo(gate[j]) + ya[j >> 1][2 * (j & 1)], bfhi(gate[j]) + ya[j >> 1][2 * (j & 1) + 1]);
          gate[j] = erow == idx ? w : gate[j];
          PIN(gate[j]);
        }
      }
    }
    SBAR();
    if constexpr (((s & 3) == 2 && u < 11) || s == 44) {
      if constexpr (s == 44) waitbar_l<SC.younger(OG, 64 + 4 * u + 7, B0 + s)>();
      else waitbar<SC.younger(OG, 64 + 4 * u + 7, B0 + s)>();
      if constexpr (u % 4 == 1) STAMP(16 + u / 4);
    }
    if constexpr (s == 48) waitbar<SC.younger(OG, 64 + 55, B0 + s)>();
    if constexpr (s == 50) waitbar<SC.younger(OG, 64 + 59, B0 + s)>();
    if constexpr (s == 52) { waitbar<SC.younger(OG, 64 + 63, B0 + s)>(); STAMP(19); }
    if constexpr (s == 56) waitbar_l<63>();
    if constexpr (PRIO) __builtin_amdgcn_s_setprio(3 - ((s + 2) & 3));
  });
  STAMP(20);

  f32x4 kep[4];
  {
    waitv<0>(pf);
    const bool rdy = __all((pf[0] == tlo) & (pf[1] == thi));
    if (rdy) sfor<4>([&](auto Dd) AI { constexpr int d = Dd; gld16<d * 1024, true>(oth[d], (unsigned)(wid * 4096 + l * 16), ws + WS_XC + (size_t)bid * 32768); });
    NOPS(3);
#pragma unroll
    for (int d = 0; d < 4; ++d) { PIN(acc[4 + d][1]); *(LAS f32x4*)(lds + 65536 + (wid ^ 4) * 4096 + l * 16 + (d & 1) * 2048 + (d >> 1) * 32768) = acc[4 + d][1]; }
    waitbar_l<63>();
    STAMP(21);
#pragma unroll
    for (int d = 0; d < 4; ++d) kep[d] = acc[4 + d][0] + *(LAS const f32x4*)(lds + 65536 + wid * 4096 + l * 16 + (d & 1) * 2048 + (d >> 1) * 32768);
    STAMP(22);
    if (!rdy) {
      poll8((unsigned)((bid * 8 + wid) * 64), ws + WS_FX, tlo, thi, 0xffffffffu);
      sfor<4>([&](auto Dd) AI { constexpr int d = Dd; gld16<d * 1024, true>(oth[d], (unsigned)(wid * 4096 + l * 16), ws + WS_XC + (size_t)bid * 32768); });
    }
    STAMP(23);
    waitv<0>(oth[0], oth[1]);
    PIN(oth[2]); PIN(oth[3]);
    STAMP(24);
  }
#pragma unroll
  for (int d = 0; d < 4; ++d) {
    const f32x4 a = kep[d] + oth[d];
    u32x2 res;
#pragma unroll
    for (int j = 0; j < 2; ++j) {
      const unsigned sp = pk2(a[2 * j], a[2 * j + 1]), gp = gate[2 * d + j], xw = xe[d][j];
      const unsigned pr = pk2(bflo(sp) * bflo(gp), bfhi(sp) * bfhi(gp));
      res[j] = pk2(bflo(xw) + bflo(pr), bfhi(xw) + bfhi(pr));
    }
    *(LAS u32x2*)(uintptr_t)(ex_a ^ (d << 5)) = res;
  }
#pragma unroll
  for (int i = 0; i < 2; ++i)
    *(u32x4*)(out + (size_t)(erow - c + i * 8 + (l >> 3)) * DM + nb * 128 + kh * 64 + ((l & 7) ^ (i * 4 + (l >> 4))) * 8) = *(LAS const u32x4*)(uintptr_t)(ldsf + i * 8192 + l * 16);
  STAMP(25);
  STAMP_OUT(0, 26);
}

extern "C" void kernel_launch(void* const* d_in, const int* in_sizes, int n_in, void* d_out, int out_size, void* d_ws, size_t ws_size,
                              hipStream_t stream) {
  k_fused<<<256, 512, 0, stream>>>((const u16*)d_in[0], (const u16*)d_in[1], (const u16*)d_in[2], (const u16*)d_in[3], (const u16*)d_in[4],
                                   (const u16*)d_in[6], (const u16*)d_in[7], (const u16*)d_in[8], (const u16*)d_in[9], (char*)d_ws, (u16*)d_out);
}
```

```cpp
#include <hip/hip_runtime.h>
#include <stdint.h>
#include <type_traits>

typedef unsigned short u16;
typedef short bf16x8 __attribute__((ext_vector_type(8)));
typedef short s16x4 __attribute__((ext_vector_type(4)));
typedef float f32x4 __attribute__((ext_vector_type(4)));
typedef float f32x2 __attribute__((ext_vector_type(2)));
typedef unsigned u32x4 __attribute__((ext_vector_type(4)));
typedef unsigned u32x2 __attribute__((ext_vector_type(2)));
typedef __bf16 bf2_t __attribute__((ext_vector_type(2)));
#define LAS __attribute__((address_space(3)))
#define DI __device__ __forceinline__
#define AI __attribute__((always_inline))
#define SBAR() __builtin_amdgcn_sched_barrier(0)
template <class T> __device__ __forceinline__ void PIN(T& x) { asm volatile("" : "+v"(x)); }
template <class B> DI void MF(f32x4& acc, const bf16x8& a, const B& b) { asm volatile("v_mfma_f32_16x16x32_bf16 %0, %1, %2, %0" : "+v"(acc) : "v"(a), "v"(b)); }
template <class B> DI void MFC(f32x4& d, const bf16x8& a, const B& b, const f32x4& c) { asm volatile("v_mfma_f32_16x16x32_bf16 %0, %1, %2, %3" : "=&v"(d) : "v"(a), "v"(b), "v"(c)); }
template <class B> DI void MFZ(f32x4& d, const bf16x8& a, const B& b) { asm volatile("v_mfma_f32_16x16x32_bf16 %0, %1, %2, 0" : "=&v"(d) : "v"(a), "v"(b)); }
#define NOPS(n) asm volatile("s_nop " #n)
#define MFMA16(a, b, c) __builtin_amdgcn_mfma_f32_16x16x16bf16_1k((a), (b), (c), 0, 0, 0)
#define IC(n) std::integral_constant<int, (n)>{}

constexpr int SEQ = 2048, DM = 1024, DH = 128, SK = 2048, WIN = 512, FF = 3584;
constexpr size_t WS_XN = 0;
constexpr size_t WS_KV = 4u << 20;
constexpr size_t WS_ST = 5u << 20;
constexpr size_t WS_EP = WS_ST + 16384;
constexpr size_t WS_G0 = WS_ST + 32768;
constexpr size_t WS_FK = WS_ST + 49152;
constexpr size_t WS_YP = WS_ST + 65536;
constexpr size_t WS_FO = 7u << 20;
constexpr size_t WS_FY = WS_FO + 65536;
constexpr size_t WS_FX = WS_FO + 131072;
constexpr size_t WS_O = 8u << 20;
constexpr size_t WS_GU = 32u << 20;
constexpr size_t WS_XC = 24u << 20;

template <int I, int N, class F> DI void sfor_impl(F&& f) {
  if constexpr (I < N) { f(std::integral_constant<int, I>{}); sfor_impl<I + 1, N>(f); }
}
template <int N, class F> DI void sfor(F&& f) { sfor_impl<0, N>(f); }

DI unsigned pk2(float a, float b) { f32x2 v = {a, b}; return __builtin_bit_cast(unsigned, __builtin_convertvector(v, bf2_t)); }
DI float rbf(float a) { return (float)(__bf16)a; }
DI float bflo(unsigned u) { return __uint_as_float(u << 16); }
DI float bfhi(unsigned u) { return __uint_as_float(u & 0xffff0000u); }
DI float sigmoid_f(float v) { return __builtin_amdgcn_rcpf(1.f + __builtin_amdgcn_exp2f(v * -1.4426950408889634f)); }
DI float sel2(bool c, float a, float b) { PIN(a); PIN(b); return c ? a : b; }
DI float silu_f(float v) { return v * sigmoid_f(v); }
template <int CTRL> DI float dppf(float x) { return __builtin_bit_cast(float, __builtin_amdgcn_mov_dpp(__builtin_bit_cast(int, x), CTRL, 0xf, 0xf, true)); }
template <int CTRL> DI unsigned dppu(unsigned x) { return (unsigned)__builtin_amdgcn_mov_dpp((int)x, CTRL, 0xf, 0xf, true); }
constexpr int XOR1 = 0xB1, XOR2 = 0x4E, XOR7 = 0x141, XOR8 = 0x128;
DI float xg_sum(float x) {
  auto s = __builtin_amdgcn_permlane16_swap(__float_as_uint(x), __float_as_uint(x), false, false);
  x = __uint_as_float(s[0]) + __uint_as_float(s[1]);
  auto t = __builtin_amdgcn_permlane32_swap(__float_as_uint(x), __float_as_uint(x), false, false);
  return __uint_as_float(t[0]) + __uint_as_float(t[1]);
}
DI float xg_max(float x) {
  auto s = __builtin_amdgcn_permlane16_swap(__float_as_uint(x), __float_as_uint(x), false, false);
  x = __builtin_fmaxf(__uint_as_float(s[0]), __uint_as_float(s[1]));
  auto t = __builtin_amdgcn_permlane32_swap(__float_as_uint(x), __float_as_uint(x), false, false);
  return __builtin_fmaxf(__uint_as_float(t[0]), __uint_as_float(t[1]));
}
DI float wave_sum(float v) { v += dppf<XOR1>(v); v += dppf<XOR2>(v); v += dppf<XOR7>(v); v += dppf<XOR8>(v); return xg_sum(v); }
DI unsigned umax(unsigned a, unsigned b) { return a > b ? a : b; }
DI unsigned wave_max_u32(unsigned v) {
  v = umax(v, dppu<XOR1>(v)); v = umax(v, dppu<XOR2>(v)); v = umax(v, dppu<XOR7>(v)); v = umax(v, dppu<XOR8>(v));
  auto s = __builtin_amdgcn_permlane16_swap(v, v, false, false); v = umax(s[0], s[1]);
  auto t = __builtin_amdgcn_permlane32_swap(v, v, false, false); v = umax(t[0], t[1]);
  return __builtin_amdgcn_readfirstlane(v);
}

template <int OFF, bool DEV = false> DI void glds(unsigned ldsaddr, unsigned voff, const void* sbase) {
  if constexpr (DEV) asm volatile("s_mov_b32 m0, %0\n\ts_nop 0\n\tglobal_load_lds_dwordx4 %1, %2 offset:%3 sc1" ::"s"(ldsaddr), "v"(voff), "s"(sbase), "i"(OFF) : "memory");
  else asm volatile("s_mov_b32 m0, %0\n\ts_nop 0\n\tglobal_load_lds_dwordx4 %1, %2 offset:%3" ::"s"(ldsaddr), "v"(voff), "s"(sbase), "i"(OFF) : "memory");
}
DI void touch(unsigned voff, const void* sbase) {
  asm volatile("global_load_dword v255, %0, %1" ::"v"(voff), "s"(sbase) : "memory", "v255");
}
template <int OFF, bool DEV = false, class T> DI void gld16(T& dst, unsigned voff, const void* sbase) {
  if constexpr (DEV) asm volatile("global_load_dwordx4 %0, %1, %2 offset:%3 sc1" : "=v"(dst) : "v"(voff), "s"(sbase), "i"(OFF) : "memory");
  else asm volatile("global_load_dwordx4 %0, %1, %2 offset:%3" : "=v"(dst) : "v"(voff), "s"(sbase), "i"(OFF) : "memory");
}
template <int OFF, bool DEV = false, class T> DI void gld8(T& dst, unsigned voff, const void* sbase) {
  if constexpr (DEV) asm volatile("global_load_dwordx2 %0, %1, %2 offset:%3 sc1" : "=v"(dst) : "v"(voff), "s"(sbase), "i"(OFF) : "memory");
  else asm volatile("global_load_dwordx2 %0, %1, %2 offset:%3" : "=v"(dst) : "v"(voff), "s"(sbase), "i"(OFF) : "memory");
}
template <int OFF> DI void gld2(unsigned& dst, unsigned voff, const void* sbase) {
  asm volatile("global_load_ushort %0, %1, %2 offset:%3" : "=v"(dst) : "v"(voff), "s"(sbase), "i"(OFF) : "memory");
}
template <int OFF, class T> DI void gst16(const T& v, unsigned voff, const void* sbase) {
  asm volatile("global_store_dwordx4 %0, %1, %2 offset:%3 sc1\n\ts_nop 1" ::"v"(voff), "v"(v), "s"(sbase), "i"(OFF) : "memory");
}
template <class T> DI void gst8(const T& v, unsigned voff, const void* sbase) {
  asm volatile("global_store_dwordx2 %0, %1, %2 sc1" ::"v"(voff), "v"(v), "s"(sbase) : "memory");
}
template <int OFF> DI void gst4(float v, unsigned voff, const void* sbase) {
  asm volatile("global_store_dword %0, %1, %2 offset:%3 sc1" ::"v"(voff), "v"(v), "s"(sbase), "i"(OFF) : "memory");
}
constexpr int SPINS = 1 << 16;
DI void poll8(unsigned voff, const void* sbase, unsigned lo, unsigned hi, unsigned lomask) {
  for (int spin = 0; spin < SPINS; ++spin) {
    u32x2 v;
    asm volatile("global_load_dwordx2 %0, %1, %2 sc1\n\ts_waitcnt vmcnt(0)" : "=&v"(v) : "v"(voff), "s"(sbase) : "memory");
    if (__all(((v[0] & lomask) == lo) & (v[1] == hi))) break;
    __builtin_amdgcn_s_sleep(1);
  }
}
template <int N> DI void waitv() { static_assert(N >= 0, "count"); asm volatile("s_waitcnt vmcnt(%0)" ::"n"(N > 63 ? 63 : N) : "memory"); }
template <int N, class T> DI void waitv(T& x) { static_assert(N >= 0, "count"); asm volatile("s_waitcnt vmcnt(%1)" : "+v"(x) : "n"(N > 63 ? 63 : N) : "memory"); }
template <int N, class T> DI void waitv(T& x, T& y) { static_assert(N >= 0, "count"); asm volatile("s_waitcnt vmcnt(%2)" : "+v"(x), "+v"(y) : "n"(N > 63 ? 63 : N) : "memory"); }
template <int N> DI void waitbar() { static_assert(N >= 0, "count"); asm volatile("s_waitcnt vmcnt(%0)\n\ts_barrier" ::"n"(N > 63 ? 63 : N) : "memory"); }
template <int N> DI void waitbar_l() { static_assert(N >= 0, "count"); asm volatile("s_waitcnt vmcnt(%0) lgkmcnt(0)\n\ts_barrier" ::"n"(N > 63 ? 63 : N) : "memory"); }
typedef short v4i16_t __attribute__((ext_vector_type(4)));
DI s16x4 trd(LAS const char* p) { return __builtin_amdgcn_ds_read_tr16_b64_v4i16((LAS v4i16_t*)p); }
struct LBase {
  unsigned lo, hi;
  DI void init(unsigned a) { lo = a; hi = a + 65536; PIN(lo); PIN(hi); }
  template <int OFF> DI LAS const char* at() const { return (LAS const char*)(uintptr_t)(OFF < 65536 ? lo : hi) + (OFF & 65535); }
};
DI bf16x8 cat8(s16x4 lo, s16x4 hi) { return __builtin_shufflevector(lo, hi, 0, 1, 2, 3, 4, 5, 6, 7); }

enum { OX, OG, OF, OM, OS, OP, OE, OW };
struct Sched {
  int n; short kind[400], idx[400], slot[400];
  constexpr Sched() : n(0), kind{}, idx{}, slot{} {}
  constexpr void add(int s, int k, int i) { kind[n] = (short)k; idx[n] = (short)i; slot[n] = (short)s; ++n; }
  constexpr int younger(int k, int i, int s) const {
    int p = -1;
    for (int q = 0; q < n; ++q) if (kind[q] == k && idx[q] == i) p = q;
    if (p < 0 || slot[p] > s) return -1;
    int c = 0;
    for (int q = p + 1; q < n; ++q) if (slot[q] <= s) ++c;
    return c;
  }
  constexpr int first(int s) const { int q = 0; while (q < n && slot[q] < s) ++q; return q; }
  constexpr int count(int s) const { int c = 0; for (int q = 0; q < n; ++q) if (slot[q] == s) ++c; return c; }
};

#define STAMP_DECL
#define STAMP(i)
#define STAMP_OUT(kid, n)

constexpr int RING = 4 * 32768, FB = RING, RED1 = FB + 24576, HSC = RED1 + 1024, YS = HSC + 1024, LDS_BYTES = FB + 32768;
constexpr int STAGE = 3 * 32768;
constexpr int XD = 16, XD2 = 16;
constexpr float LBOUND = 65536.f;
constexpr float QSCALE = 0.08838834764831845f * 1.4426950408889634f;
constexpr int B0 = 128;
#define PRIO 1
constexpr int FISS[4] = {96, 97, 98, 117}, FRDS[4] = {115, 118, 121, 124};
constexpr int fpiece(int sl) { for (int p = 0; p < 4; ++p) if (FRDS[p] == sl) return p; return -1; }
constexpr Sched make_sched() {
  Sched s;
#define XP 8
  for (int i = 0; i < 12; ++i) s.add(-2, OG, i);
  for (int i = 0; i < XP; ++i) s.add(-1, OX, i);
  for (int t = 0; t < 128; ++t) {
    if (t < 2) for (int i = XP + t * (16 - XP) / 2; i < XP + (t + 1) * (16 - XP) / 2; ++i) s.add(t, OX, i);
    if (t >= 2 && t < 50) s.add(t, OX, t + 14);
    for (int v = 3; v < 18; ++v) for (int p = 0; p < 4; ++p) if (8 * (v - 3) + 7 + p == t) s.add(t, OG, 4 * v + p);
    if (t == 105 || t == 113 || t == 121) { s.add(t, OW, (t - 105) / 4); s.add(t, OW, (t - 105) / 4 + 1); }
    if (t >= 124) for (int i = 0; i < 3; ++i) s.add(t, OX, 64 + 3 * (t - 124) + i);
    if (t == 127) { s.add(t, OG, 72); s.add(t, OG, 73); }
    if (t == 20) s.add(t, OM, 0);
    if (t == 120) s.add(t, OM, 1);
    if (t >= 49 && t < 57 && (t & 1)) s.add(t, OF, (t - 49) / 2);
    if (t >= 71 && t < 79 && (t & 1)) s.add(t, OF, 4 + (t - 71) / 2);
    if (t == 48) s.add(t, OS, 0);
    if (t == 104) s.add(t, OS, 2);
    for (int p = 0; p < 4; ++p) if (FISS[p] == t) s.add(t, OF, 8 + p);
  }
  for (int t = 0; t < 64; ++t) {
    if (t == 0) { s.add(B0, OM, 2); s.add(B0, OG, 74); s.add(B0, OG, 75); s.add(B0, OW, 6); s.add(B0, OW, 7); }
    if (t == 1 || t == 2) { s.add(B0 + t, OX, 74 + 2 * t); s.add(B0 + t, OX, 75 + 2 * t); }
    if (t == 5 || t == 6 || t == 9) s.add(B0 + t, OX, 64 + (t == 5 ? 16 : t == 6 ? 18 : 22));
    if (t >= 5 && t < 50) s.add(B0 + t, OX, 64 + (t == 5 ? 17 : t < 9 ? t + 13 : t + 14));
    for (int v = 3; v < 16; ++v) for (int p = 0; p < 4; ++p) if ((v < 14 ? 4 * (v - 3) + 3 + (p >> 1) : v == 14 ? 45 + (p >> 1) : 49) == t) s.add(B0 + t, OG, 64 + 4 * v + p);
    if (t == 10 || t == 26) s.add(B0 + t, OM, t == 10 ? 3 : 4);
    if (t == 13 || t == 14 || t == 18) s.add(B0 + t, OE, t == 18 ? 2 : t - 13);
  }
  return s;
}
constexpr Sched SC = make_sched();

__global__ __launch_bounds__(512, 2) void k_fused(const u16* __restrict__ x, const u16* __restrict__ kc, const u16* __restrict__ vc,
                                                  const u16* __restrict__ wq, const u16* __restrict__ wo, const u16* __restrict__ wr,
                                                  const u16* __restrict__ wgate, const u16* __restrict__ wup, const u16* __restrict__ wdown,
                                                  char* __restrict__ ws, u16* __restrict__ out) {
  __shared__ __attribute__((aligned(16))) char lds_raw[LDS_BYTES];
  LAS char* const lds = (LAS char*)lds_raw;
  const int t = threadIdx.x, l = t & 63, bid = blockIdx.x, c = l & 15, g = l >> 4;
  const int wid = __builtin_amdgcn_readfirstlane(t >> 6);
  const int xcd = bid & 7, within = bid >> 3, head = xcd * 4 + (within & 3), rb = within >> 2;
  const int fj = xcd * 28 + (within < 28 ? within : within - 28);
  const int fsl = fj / 7, fgr = fj - fsl * 7;
  const int kh = xcd >> 2, mb = 2 * rb + (xcd & 1), nb = ((xcd >> 1) & 1) * 4 + (within & 3);
  const int rbw = wid & 3, kg = wid >> 2;
  const unsigned lds0 = (unsigned)(uintptr_t)lds;
  STAMP_DECL
  STAMP(0);

  unsigned tlo, thi;
  {
    const int row = 8 * bid + wid;
    u32x4 a0, a1, w0, w1, kvo;
    u32x2 ep, klo, khi;
    unsigned e[8];
    gld16<0>(a0, (unsigned)(row * DM * 2 + l * 16), x); gld16<1024>(a1, (unsigned)(row * DM * 2 + l * 16), x);
    gld16<0>(w0, (unsigned)(l * 16), wr); gld16<1024>(w1, (unsigned)(l * 16), wr);
    gld8<0, true>(ep, 0u, ws + WS_EP);
    if (wid == 3) {
      const char* pc;
      asm volatile("s_getpc_b64 %0" : "=s"(pc));
      touch((unsigned)((within * 12 + (l < 12 ? l : 11)) * 128), pc);
    }
    __builtin_amdgcn_s_barrier();
    const int fn = (bid * 4 + wid) & 255, kvh = bid >> 6;
    if (wid < 4) {
      if (fn < 128) {
        const unsigned ko = (unsigned)(((kvh * SK + (SK - WIN) + (fn >> 2) * 16 + c) * DH + (fn & 3) * 32 + 4 * g) * 2);
        gld8<0>(klo, ko, kc); gld8<32>(khi, ko, kc);
      } else {
        const int d = 32 * ((fn - 128) >> 5) + 8 * (c >> 2) + 4 * (fn & 1) + (c & 3);
        const unsigned vo = (unsigned)(((kvh * SK + (SK - WIN) + ((fn >> 1) & 15) * 32 + 4 * g + 8) * DH + d) * 2);
        sfor<8>([&](auto J) AI { constexpr int j = J; gld2<(16 * (j >> 2) + (j & 3) - 8) * DH * 2>(e[j], vo, vc); });
      }
      if (wid < 2) touch((unsigned)((wid * 256 + within * 8 + (l >> 3)) * 8192 + xcd * 1024 + (l & 7) * 128), wq);
      if (fn < 128) waitv<5>(); else waitv<11>();
    } else {
      waitv<3>();
    }
    PIN(a0); PIN(a1);
    STAMP(1);
    float v[16];
#pragma unroll
    for (int j = 0; j < 4; ++j) { v[2 * j] = bflo(a0[j]); v[2 * j + 1] = bfhi(a0[j]); v[8 + 2 * j] = bflo(a1[j]); v[9 + 2 * j] = bfhi(a1[j]); }
    float s = 0.f;
#pragma unroll
    for (int j = 0; j < 16; ++j) s += v[j];
    const float mean = wave_sum(s) * (1.f / DM);
    float q = 0.f;
#pragma unroll
    for (int j = 0; j < 16; ++j) { const float d = v[j] - mean; q += d * d; }
    const float var = wave_sum(q) * (1.f / DM);
    const float mbf = rbf(mean), rden = __builtin_amdgcn_rcpf(rbf(__builtin_amdgcn_sqrtf(rbf(rbf(var) + 1e-5f))));
    u32x4 o0, o1;
#pragma unroll
    for (int j = 0; j < 8; ++j) {
      const unsigned dd = pk2(v[2 * j] - mbf, v[2 * j + 1] - mbf);
      const unsigned nn = pk2(bflo(dd) * rden, bfhi(dd) * rden);
      if (j < 4) o0[j] = nn; else o1[j - 4] = nn;
    }
    *(LAS u32x4*)(lds + STAGE + (l * 8 + (wid ^ (l & 7))) * 16) = o0;
    *(LAS u32x4*)(lds + STAGE + 8192 + (l * 8 + (wid ^ (l & 7))) * 16) = o1;
    *(LAS f32x2*)(lds + STAGE + 16384 + 32 + wid * 8) = (f32x2){mbf, rden};
    waitbar_l<63>();
    if (wid >= 4) {
#pragma unroll
      for (int hh = 0; hh < 4; ++hh) {
        const int p = hh * 256 + (t - 256), ch = p >> 3, r = 8 * bid + ((p & 7) ^ (ch & 7));
        const u32x4 o = *(LAS const u32x4*)(lds + STAGE + p * 16);
        gst16<0>(o, (unsigned)(((((r >> 5) * 32 + (ch >> 2)) * 2 + ((r >> 4) & 1)) * 64 + (ch & 3) * 16 + (r & 15)) * 16), ws + WS_XN);
      }
      if (t < 256 + 8) gst8(*(LAS const f32x2*)(lds + STAGE + 16384 + 32 + (t - 256) * 8), (unsigned)((8 * bid + t - 256) * 8), ws + WS_ST);
      waitv<4>();
    } else if (fn < 128) waitv<2>(); else waitv<8>();
    PIN(w0); PIN(w1); PIN(ep);
    {
      const unsigned long long tg = (((unsigned long long)(unsigned)__builtin_amdgcn_readfirstlane(ep[1]) << 32) | (unsigned)__builtin_amdgcn_readfirstlane(ep[0])) + 1ull;
      tlo = (unsigned)tg; thi = (unsigned)(tg >> 32);
    }
    float rr = 0.f;
#pragma unroll
    for (int j = 0; j < 8; ++j) {
      const unsigned nn = j < 4 ? o0[j] : o1[j - 4], ww = j < 4 ? w0[j] : w1[j - 4];
      rr += bflo(nn) * bflo(ww); rr += bfhi(nn) * bfhi(ww);
    }
    rr = wave_sum(rr);
    {
      const unsigned bits = pk2(rr, 0.f) & 0xffffu;
      const unsigned ord = (bits & 0x8000u) ? (~bits & 0xffffu) : (bits | 0x8000u);
      *(LAS unsigned*)(lds + STAGE + 16384 + wid * 4) = (ord << 11) | (unsigned)(SEQ - 1 - row);
    }
    if (wid >= 4) waitv<0>();
    waitbar_l<63>();
    if (t == 256) {
      const u32x4 ka = *(LAS const u32x4*)(lds + STAGE + 16384), kb = *(LAS const u32x4*)(lds + STAGE + 16400);
      const unsigned key = umax(umax(umax(ka[0], ka[1]), umax(ka[2], ka[3])), umax(umax(kb[0], kb[1]), umax(kb[2], kb[3])));
      gst8((u32x2){(tlo << 27) | key, (tlo >> 5) | (thi << 27)}, (unsigned)(bid * 8), ws + WS_G0);
    }
    if (wid < 4) {
      if (wid < 2) waitv<1>(); else waitv<0>();
      if (fn < 128) {
        PIN(klo); PIN(khi);
        kvo = (u32x4){klo[0], klo[1], khi[0], khi[1]};
      } else {
#pragma unroll
        for (int j = 0; j < 8; ++j) PIN(e[j]);
        kvo = (u32x4){e[0] | (e[1] << 16), e[2] | (e[3] << 16), e[4] | (e[5] << 16), e[6] | (e[7] << 16)};
      }
      gst16<0>(kvo, (unsigned)((bid * 4 + wid) * 1024 + l * 16), ws + WS_KV);
    }
    STAMP(2);
  }
  const unsigned glo = tlo << 27, ghi = (tlo >> 5) | (thi << 27);

  const char* const wq_b = (const char*)wq + head * 256;
  const unsigned wq_v = (unsigned)(((8 * wid + ((l & 31) >> 2)) * 4096 + (l >> 5) * 32 + ((l & 3) ^ (2 * (wid & 1))) * 8) * 2);
  const char* const kv_b = ws + WS_KV + (size_t)(head >> 3) * 262144;
  const unsigned kv_v = (unsigned)(wid * 4096 + l * 16);
  const char* const xn_b = ws + WS_XN + (size_t)rb * 8 * 65536 + 4096;
  const unsigned xn_v = (unsigned)(wid * 65536 + l * 16);
  const char* const wo_b = (const char*)wo + (size_t)kh * 2048 * (DM * 2) + nb * 256;
  const char* const o_b = ws + WS_O + (size_t)mb * 4 * 262144 + kh * 131072;
  const unsigned ldsw = lds0 + wid * 2048, ldsk = lds0 + wid * 4096, ldsf = lds0 + FB + wid * 1024;
  const int erow = mb * 128 + rbw * 32 + kg * 16 + c;
  bf16x8 xf[XD];
  u32x4 fm[2];
  bf16x8 fxs;
  const unsigned gu_v = (unsigned)((((4 * wid + g) * 2 + ((l >> 3) & 1)) * FF + fj * 16 + (l & 7) * 2) * 8);
  u32x2 fg;
  u32x2 xe[4];
  f32x2 st;
  unsigned key = 0;

  const unsigned fo_v = (unsigned)(((mb * 4 + rbw) * 32 + kh * 16 + c) * 8);
  const int yrow = within < 28 ? fj : 224 + xcd * 4 + within - 28;
  f32x4 o[2][2][2];
  float inv[2];
  const char* const od_b = ws + WS_O + ((size_t)(rb * 8) * 128 + head * 4) * 2048 + 4096;
  auto issue = [&](auto Sl, auto Pos) AI {
    constexpr int sl = Sl, pos = Pos, cnt = SC.count(sl), q0 = SC.first(sl) + (pos < 0 ? 0 : pos);
    constexpr int nq = pos < 0 ? cnt : pos >= cnt ? 0 : pos < 3 ? 1 : cnt - 3;
    sfor<nq>([&](auto Qi) AI {
      constexpr int k = SC.kind[q0 + Qi], i = SC.idx[q0 + Qi];
      if constexpr (k == OX && i < 64) gld16<(i & 7) * 1024 - 4096, true>(xf[i % XD], xn_v, xn_b + (size_t)(i >> 3) * 8192);
      if constexpr (k == OX && i >= 64) {
        constexpr int q = i - 64, v = q >> 2;
        gld16<0, true>(xf[q % XD2], (unsigned)(rbw * 262144 + kg * 16384 + ((q & 1) ^ kg) * 1024 + l * 16), o_b + (size_t)(((v & 3) * 4 + ((q >> 1) & 1)) * 8192 + (v >> 2) * 2048));
      }
      if constexpr (k == OG) {
        constexpr int v = i >> 2, p = i & 3;
        if constexpr (v < 8) {
          const char* sb = wq_b + (size_t)(v * 128 + (p >> 1) * 64) * 8192;
          glds<(p & 1) * 128>(ldsw + (v & 3) * 32768 + (p >> 1) * 16384 + (p & 1) * (1024 - 128), wq_v, sb);
        } else if constexpr (v < 16) {
          glds<p * 1024, true>(ldsk + (v & 3) * 32768, kv_v, kv_b + (size_t)(v - 8) * 32768);
        } else {
          const unsigned wo_v = (unsigned)((((wid >> 2) * 128 + (wid & 3) * 8 + ((l & 31) >> 2)) * DM + (l >> 5) * 32 + ((l & 3) ^ (2 * (wid & 1))) * 8) * 2);
          const char* sb = wo_b + (size_t)(((v & 3) * 4 + (p >> 1) * 2) * 128 + ((v - 16) >> 2) * 32) * (DM * 2) + (((p & 1) ^ kh ^ 1) << 7);
          glds<0>(ldsw + (v & 3) * 32768 + (p >> 1) * 16384 + (p & 1) * 1024, wo_v, sb);
        }
      }
      if constexpr (k == OF && i < 8)
        glds<0>(ldsf + (i & 3) * 8192, (unsigned)(((l >> 3) * FF + wid * 64 + ((l & 7) ^ ((i & 1) * 4 + ((l >> 3) & 2))) * 8) * 2),
                (const char*)(i < 4 ? wgate : wup) + ((size_t)(fsl * 32 + 8 * (i & 3)) * FF + fgr * 512) * 2);
      if constexpr (k == OF && i >= 8) {
        const int frho = l >> 2, fch = (l & 3) ^ (frho & 4 ? 2 : 0);
        glds<0>(ldsf + ((i - 8) % 3) * 8192, (unsigned)((frho * DM + wid * 128 + fch * 8) * 2), (const char*)wdown + (size_t)fj * 16 * DM * 2 + (i - 8) * 64);
      }
      if constexpr (k == OS && i == 0) {
        const int idx = SEQ - 1 - (int)(key & 2047u);
        gld16<0, true>(fxs, (unsigned)((((idx >> 5) * 32 + fsl) * 2 + ((idx >> 4) & 1)) * 1024 + (g * 16 + (idx & 15)) * 16), ws + WS_XN);
      }
      if constexpr (k == OS && i == 2) gld16<0, true>(fm[0], gu_v, ws + WS_GU);
      if constexpr (k == OM && i > 0) gld8<0, true>(fg, fo_v, ws + WS_FO + (i - 1) * 16384);
      if constexpr (k == OM && i == 0) gld8<0, true>(fg, (unsigned)((t & 255) * 8), wid < 4 ? ws + WS_G0 : ws + WS_FK + (head >> 3) * 2048);
      if constexpr (k == OW) {
        constexpr int os = i >> 1, qb = i & 1;
        PIN(o[os & 1][0][qb]); PIN(o[os & 1][1][qb]);
        const f32x4 a = o[os & 1][0][qb] * inv[qb], b = o[os & 1][1][qb] * inv[qb];
        gst16<i * 1024 - 4096>((u32x4){pk2(a[0], a[1]), pk2(a[2], a[3]), pk2(b[0], b[1]), pk2(b[2], b[3])}, (unsigned)(wid * 262144 + l * 16), od_b);
      }
      if constexpr (k == OE && i < 2)
        glds<0>(ldsf + i * 8192, (unsigned)(((erow - c + i * 8 + (l >> 3)) * DM + nb * 128 + kh * 64 + ((l & 7) ^ (i * 4 + (l >> 4))) * 8) * 2), x);
      if constexpr (k == OE && i == 2) gld8<0, true>(st, (unsigned)(erow * 8), ws + WS_ST);
    });
  };
  f32x4 fa0, fa1, fq[4];
  s16x4 fb0, fb1, fxa;
  bf16x8 ft[2];
  auto endslot = [&](auto Sl) AI {
    constexpr int sl = Sl, u = sl >> 3, i = sl & 7;
    if constexpr (sl == 3) {
      if (wid == 6 || wid == 7) touch((unsigned)(((wid - 4) * 256 + within * 8 + (l >> 3)) * 8192 + xcd * 1024 + (l & 7) * 128), wq);
    }
    if constexpr (sl == 2) {
      if (wid < 4 && l == 0) gst8((u32x2){tlo, thi}, (unsigned)((bid * 4 + wid) * 8), ws + WS_FK);
    }
    if constexpr (sl == 38) {
      waitv<SC.younger(OM, 0, sl)>(fg);
      const unsigned lo = wid < 4 ? glo : tlo, hi = wid < 4 ? ghi : thi, mk = wid < 4 ? 0xf8000000u : 0xffffffffu;
      for (int spin = 0; spin < SPINS; ++spin) {
        if (__builtin_expect(__all(((fg[0] & mk) == lo) & (fg[1] == hi)), 1)) break;
        __builtin_amdgcn_s_sleep(1);
        gld8<0, true>(fg, (unsigned)((t & 255) * 8), wid < 4 ? ws + WS_G0 : ws + WS_FK + (head >> 3) * 2048);
        waitv<0>(fg);
      }
      const unsigned km = wave_max_u32(fg[0] & 0x07ffffffu);
      *(LAS unsigned*)(lds + HSC + wid * 4) = km;
    }
    if constexpr (sl == 47) {
      if (bid == 0 && t == 0) gst8((u32x2){tlo, thi}, 0u, ws + WS_EP);
      const u32x4 kk = *(LAS const u32x4*)(lds + HSC);
      key = __builtin_amdgcn_readfirstlane(umax(umax(kk[0], kk[1]), umax(kk[2], kk[3])));
    }
    if constexpr (sl == 58 || sl == 61 || sl == 64 || sl == 67) {
      if (wid == 4 + (sl - 58) / 3) touch((unsigned)(((wid & 3) * 64 + l) * 128), (const char*)wdown + (size_t)fj * 16 * DM * 2);
    }
    if constexpr (sl == 81 || sl == 85 || sl == 89 || sl == 93) {
      if (wid == (sl - 81) / 4)
        touch((unsigned)((kh * 2048 + (within >> 1) * 128 + wid * 32 + ((within & 1) * 2 + (xcd & 1)) * 8 + (l >> 3)) * (DM * 2) + ((xcd >> 1) & 1) * 1024 + (l & 7) * 128), wo);
    }
    if constexpr ((sl >= 68 && sl <= 70) || (sl >= 90 && sl <= 92)) {
      constexpr int r = sl >= 90, q = sl - (r ? 90 : 68);
      if constexpr (q == 0) waitv<SC.younger(OF, 4 * r + 3, sl)>(fxs);
      if constexpr (q >= 1) {
#pragma unroll
        for (int j = 0; j < 2; ++j) { fq[2 * q - 2 + j] = __builtin_amdgcn_mfma_f32_16x16x32_bf16(fxs, ft[j], (f32x4){}, 0, 0, 0); PIN(fq[2 * q - 2 + j]); }
      }
      if constexpr (q < 2) {
        const unsigned rp = ldsf + g * 8192 + (c >> 2) * 128 + ((((g & 1) << 1) | (c >> 3)) << 5) + (c & 3) * 8;
#pragma unroll
        for (int j = 0; j < 2; ++j) { LAS const char* a = (LAS const char*)(uintptr_t)(rp ^ ((2 * q + j) << 5)); ft[j] = cat8(trd(a), trd(a + 512)); }
      }
    }
    if constexpr (sl == 71 || sl == 93) {
      const float v = sel2(g & 2, sel2(g & 1, fq[3][0], fq[2][0]), sel2(g & 1, fq[1][0], fq[0][0]));
      gst8((u32x2){__float_as_uint(v), tlo}, (unsigned)(((fsl * 2 + (sl == 93)) * FF + fgr * 512 + t) * 8), ws + WS_GU);
    }
    if constexpr (sl == 109) {
      waitv<SC.younger(OS, 2, sl)>(fm[0]);
      for (int spin = 0; spin < SPINS; ++spin) {
        if (__builtin_expect(__all((fm[0][1] == tlo) & (fm[0][3] == tlo)), 1)) break;
        __builtin_amdgcn_s_sleep(1);
        gld16<0, true>(fm[0], gu_v, ws + WS_GU);
        waitv<0>(fm[0]);
      }
      *(LAS f32x2*)(lds + RED1 + (wid * 32 + 2 * c) * 4) = (f32x2){xg_sum(__uint_as_float(fm[0][0])), xg_sum(__uint_as_float(fm[0][2]))};
    }
    if constexpr (sl == 111) {
#pragma unroll
      for (int w = 0; w < 2; ++w) { fa0[w] = *(LAS const float*)(lds + RED1 + ((2 * g + w) * 32 + c) * 4); fa0[2 + w] = *(LAS const float*)(lds + RED1 + ((2 * g + w) * 32 + 16 + c) * 4); }
    }
    if constexpr (sl == 112) { fa0[0] = xg_sum(fa0[0] + fa0[1]); fa0[2] = xg_sum(fa0[2] + fa0[3]); PIN(fa0); }
    if constexpr (sl == 113) {
      const float hh = rbf(silu_f(rbf(fa0[0]))) * rbf(fa0[2]);
      *(LAS u16*)(lds + HSC + 32 + wid * 32 + c * 2) = (u16)pk2(hh, hh);
    }
    if constexpr (sl == 114) fxa = *(LAS const s16x4*)(lds + HSC + 32 + wid * 32 + g * 8);
    constexpr int pr = fpiece(sl), pm = fpiece(sl - 1), ps = fpiece(sl - 2);
    if constexpr (ps >= 0) gst4<ps * 128>(sel2(g & 1, fa1[0], fa0[0]), (unsigned)((wid * 128 + (l & 31)) * 4), ws + WS_YP + (size_t)yrow * 4096);
    if constexpr (pm >= 0) { fa0 = MFMA16(fxa, fb0, (f32x4){}); fa1 = MFMA16(fxa, fb1, (f32x4){}); PIN(fa0); PIN(fa1); }
    if constexpr (pr >= 0) {
      waitv<SC.younger(OF, 8 + pr, sl)>();
      const unsigned fp = ldsf + (4 * g + (c >> 2)) * 64 + (g & 1) * 32 + (c & 3) * 8;
      fb0 = trd((LAS const char*)(uintptr_t)fp + (pr % 3) * 8192); fb1 = trd((LAS const char*)(uintptr_t)(fp ^ 32) + (pr % 3) * 8192);
    }
    if constexpr (sl == 111 || sl == 119 || sl == 127) {
      waitv<SC.younger(OW, (sl - 111) / 4 + 1, sl)>();
      if (l == 0) gst8((u32x2){tlo, thi}, (unsigned)(((rb * 8 + wid) * 32 + head) * 8), ws + WS_FO + ((sl - 111) / 8) * 16384);
    }
    if constexpr (sl == 123) {
      waitv<SC.younger(OM, 1, sl)>(fg);
      if (__builtin_expect(!__all((fg[0] == tlo) & (fg[1] == thi)), 0)) poll8(fo_v, ws + WS_FO, tlo, thi, 0xffffffffu);
    }
    SBAR();
    if constexpr (i == 6) {
      if constexpr (u == 5 || u == 13) waitbar_l<SC.younger(OG, 4 * u + 7, sl)>();
      else waitbar<SC.younger(OG, 4 * u + 7, sl)>();
      if constexpr (u == 0 || u == 1 || u == 5 || u == 7 || u == 8 || u == 10 || u == 12 || u == 14)
        STAMP(u == 0 ? 4 : u == 1 ? 5 : u == 5 ? 6 : u == 7 ? 7 : u == 8 ? 8 : u == 10 ? 9 : u == 12 ? 10 : 11);
    }
    if constexpr (PRIO && !(i & 1)) __builtin_amdgcn_s_setprio(i == 6 ? 3 : 2 - i / 2);
  };

  issue(IC(-2), IC(-1));
  if (wid == 5) poll8((unsigned)((rb * 32 + (l & 31)) * 8), ws + WS_G0, glo, ghi, 0xf8000000u);
  waitbar<SC.younger(OG, 3, -2)>();
  issue(IC(-1), IC(-1));
  STAMP(3);

  LBase trb[2], frb;
  trb[0].init(lds0 + g * 2048 + (c >> 2) * 64 + (g & 1) * 32 + (c & 3) * 8);
  trb[1].init(lds0 + g * 2048 + (c >> 2) * 64 + ((g & 1) ^ 1) * 32 + (c & 3) * 8);
  frb.init(lds0 + l * 16);
  f32x4 acc[8][2];
  bf16x8 wf[2][4];
  auto WRD1 = [&](auto Si, auto Base, auto Dd) AI {
    constexpr int s = Si, d = Dd, o = Base + (d >> 1) * 512;
    wf[s & 1][d] = cat8(trd(trb[d & 1].template at<o>()), trd(trb[d & 1].template at<o + 256>()));
  };
  auto WRD = [&](auto Si, auto Base) AI { sfor<4>([&](auto Dd) AI { WRD1(Si, Base, Dd); }); };
#define WA(s) IC((((s) >> 3) & 3) * 32768 + (((s) >> 1) & 3) * 8192 + ((s) & 1) * 1024)
#define WB(s) IC((((s) >> 2) & 3) * 32768 + (((s) >> 1) & 1) * 8192 + ((s) & 1) * 1024)
  bf16x8 fr[4];
  auto FRD = [&](auto Ni) AI { constexpr int n = Ni; if constexpr (n < 256) fr[n & 3] = *(LAS const bf16x8*)(frb.at<((n >> 5) & 3) * 32768 + (n & 31) * 1024>()); };

  WRD(IC(0), WA(0));
  SBAR();
  bf16x8 qf[4][2];
  auto QPACK = [&](auto Ds) AI {
    constexpr int ds = Ds;
#pragma unroll
    for (int qb = 0; qb < 2; ++qb) {
      const f32x4 a = acc[2 * ds][qb] * QSCALE, b = acc[2 * ds + 1][qb] * QSCALE;
      qf[ds][qb] = __builtin_bit_cast(bf16x8, (u32x4){pk2(a[0], a[1]), pk2(a[2], a[3]), pk2(b[0], b[1]), pk2(b[2], b[3])});
      PIN(qf[ds][qb]);
    }
  };
  sfor<64>([&](auto Si) AI {
    constexpr int s = Si, x0 = (s & ~1) % XD, x1 = (s | 1) % XD;
    if constexpr (!(s & 1)) waitv<SC.younger(OX, s + 1, s - 1)>(xf[x0], xf[x1]);
    sfor<4>([&](auto Dd) AI {
      constexpr int d = Dd, dt = (s & 1) * 4 + d;
      if constexpr (s + 1 < 64) WRD1(IC(s + 1), WA(s + 1), Dd);
      else if constexpr (d < 3) FRD(Dd);
      SBAR();
      if constexpr (s < 2) { MFZ(acc[dt][0], wf[s & 1][d], xf[x0]); MFZ(acc[dt][1], wf[s & 1][d], xf[x1]); }
      else { MF(acc[dt][0], wf[s & 1][d], xf[x0]); MF(acc[dt][1], wf[s & 1][d], xf[x1]); }
      issue(Si, Dd);
      if constexpr (s == 63 && (d & 1)) { QPACK(IC(d >> 1)); SBAR(); }
    });
    SBAR();
    endslot(Si);
  });

  f32x4 sc[2][2][2], negm[2];
  u32x4 pw[16][2];
  f32x4 lsum[2];
  bf16x8 ones = {0x3f80, 0x3f80, 0x3f80, 0x3f80, 0x3f80, 0x3f80, 0x3f80, 0x3f80};
  PIN(ones);
#define MX3(a, b, c) __builtin_fmaxf(__builtin_fmaxf((a), (b)), (c))
#define EXP2(x) __builtin_amdgcn_exp2f(x)
  auto SM = [&](auto Hi, auto Pi) AI {
    constexpr int H = Hi, p = Pi, kt = p >> 2, qb = (p >> 1) & 1, r = 2 * (p & 1);
    const float e0 = EXP2(sc[H & 1][kt][qb][r]), e1 = EXP2(sc[H & 1][kt][qb][r + 1]);
    unsigned w = pk2(e0, e1);
    PIN(w);
    pw[H][qb][kt * 2 + (p & 1)] = w;
  };
  sfor<128>([&](auto Ni) AI {
    constexpr int n = Ni, H = n >> 3, i = n & 7, kt = i >> 2, ds = i & 3, sn = H & 1;
    FRD(IC(n + 3));
    if constexpr (n < 2) QPACK(IC(2 + n));
    sfor<2>([&](auto Qb) AI {
      constexpr int qb = Qb;
      if constexpr (ds == 0 && H == 0) MFZ(sc[sn][kt][qb], fr[n & 3], qf[0][qb]);
      else if constexpr (ds == 0) MFC(sc[sn][kt][qb], fr[n & 3], qf[0][qb], negm[qb]);
      else MF(sc[sn][kt][qb], fr[n & 3], qf[ds][qb]);
    });
    if constexpr (n == 5 || n == 6) {
      constexpr int qb = n - 5;
      NOPS(3); PIN(sc[0][0][qb]);
      const float m = -xg_max(__builtin_fmaxf(MX3(sc[0][0][qb][0], sc[0][0][qb][1], sc[0][0][qb][2]), sc[0][0][qb][3]));
      negm[qb] = (f32x4){m, m, m, m};
      sc[0][0][qb] += negm[qb];
      PIN(negm[qb]); PIN(sc[0][0][qb]);
    }
    if constexpr (n == 9 || n == 10) { NOPS(3); PIN(sc[0][1][n - 9]); sc[0][1][n - 9] += negm[n - 9]; PIN(sc[0][1][n - 9]); }
    issue(IC(64 + (n >> 2)), IC(n & 3));
    if constexpr (H == 2 && (i == 1 || i == 5)) MFZ(lsum[i >> 2], ones, pw[0][i >> 2]);
    if constexpr (H > 2 && (i == 1 || i == 5)) MF(lsum[i >> 2], ones, pw[H - 2][i >> 2]);
    if constexpr (H >= 1) SM(IC(H - 1), IC(i));
    if constexpr (n == 125) { PIN(negm[0]); PIN(negm[1]); }
    SBAR();
    if constexpr ((n & 3) == 3) endslot(IC(64 + (n >> 2)));
  });
  constexpr int CHK = 16;
  sfor<128>([&](auto Mi) AI {
    constexpr int m = Mi, n = 128 + m, os = m >> 5, H = (m >> 1) & 15, par = m & 1;
    FRD(IC(n + 3));
    if constexpr (H == 0) { MFZ(o[os & 1][par][0], fr[n & 3], pw[H][0]); MFZ(o[os & 1][par][1], fr[n & 3], pw[H][1]); }
    else { MF(o[os & 1][par][0], fr[n & 3], pw[H][0]); MF(o[os & 1][par][1], fr[n & 3], pw[H][1]); }
    if constexpr (m == 1 || m == 2) MF(lsum[m - 1], ones, pw[14][m - 1]);
    if constexpr (m < 8) SM(IC(15), Mi);
    if constexpr (m == 9 || m == 10) MF(lsum[m - 9], ones, pw[15][m - 9]);
    if constexpr (m == CHK + 1 || m == CHK + 2) { inv[m - CHK - 1] = __builtin_amdgcn_rcpf(lsum[m - CHK - 1][0]); PIN(inv[m - CHK - 1]); }
    issue(IC(64 + (n >> 2)), IC(n & 3));
    if constexpr (m == 123) {
      trb[0].init(lds0 + kg * 16384 + g * 2048 + (c >> 2) * 64 + (g & 1) * 32 + (c & 3) * 8);
      trb[1].init(lds0 + kg * 16384 + g * 2048 + (c >> 2) * 64 + ((g & 1) ^ 1) * 32 + (c & 3) * 8);
    }
    if constexpr (m >= 124) WRD1(IC(0), WB(0), IC(m - 124));
    SBAR();
    if constexpr ((n & 3) == 3) endslot(IC(64 + (n >> 2)));
    if constexpr (m == CHK - 1) {
      PIN(lsum[0]); PIN(lsum[1]);
      if (__builtin_expect(__any(!(lsum[0][0] + lsum[1][0] <= LBOUND)), 0)) {
        NOPS(7); SBAR();
        auto tile = [&](unsigned off, f32x4& s0, f32x4& s1) AI {
          bf16x8 kf[4];
          SBAR(); NOPS(7);
          sfor<4>([&](auto Ds) AI { constexpr int ds = Ds; gld16<ds * 1024, true>(kf[ds], off, kv_b); });
          waitv<0>(kf[0], kf[1]);
          PIN(kf[2]); PIN(kf[3]);
#pragma unroll
          for (int ds = 0; ds < 4; ++ds) {
            s0 = __builtin_amdgcn_mfma_f32_16x16x32_bf16(kf[ds], qf[ds][0], s0, 0, 0, 0);
            s1 = __builtin_amdgcn_mfma_f32_16x16x32_bf16(kf[ds], qf[ds][1], s1, 0, 0, 0);
          }
        };
        float m0 = -__builtin_inff(), m1 = m0;
#pragma unroll 1
        for (int kt = 0; kt < 32; ++kt) {
          f32x4 s0 = {}, s1 = {};
          tile((unsigned)(kt * 4096 + l * 16), s0, s1);
          m0 = MX3(MX3(m0, s0[0], s0[1]), s0[2], s0[3]); m1 = MX3(MX3(m1, s1[0], s1[1]), s1[2], s1[3]);
        }
        m0 = -xg_max(m0); m1 = -xg_max(m1);
        lsum[0] = (f32x4){}; lsum[1] = (f32x4){};
        sfor<16>([&](auto Hi) AI {
          constexpr int H = Hi;
#pragma unroll
          for (int kt = 0; kt < 2; ++kt) {
            sc[H & 1][kt][0] = (f32x4){m0, m0, m0, m0}; sc[H & 1][kt][1] = (f32x4){m1, m1, m1, m1};
            tile((unsigned)((2 * H + kt) * 4096 + l * 16), sc[H & 1][kt][0], sc[H & 1][kt][1]);
          }
          sfor<8>([&](auto Pi) AI { SM(Hi, Pi); });
          PIN(pw[H][0]); PIN(pw[H][1]);
          lsum[0] = __builtin_amdgcn_mfma_f32_16x16x32_bf16(ones, __builtin_bit_cast(bf16x8, pw[H][0]), lsum[0], 0, 0, 0);
          lsum[1] = __builtin_amdgcn_mfma_f32_16x16x32_bf16(ones, __builtin_bit_cast(bf16x8, pw[H][1]), lsum[1], 0, 0, 0);
        });
#pragma unroll
        for (int par = 0; par < 2; ++par) { o[0][par][0] = (f32x4){}; o[0][par][1] = (f32x4){}; }
        sfor<CHK / 4>([&](auto Qi) AI {
          constexpr int q = Qi;
          bf16x8 vf[4];
          SBAR(); NOPS(7);
          sfor<4>([&](auto J) AI { constexpr int j = J; gld16<j * 1024, true>(vf[j], (unsigned)((128 + 4 * q) * 1024 + l * 16), kv_b); });
          waitv<0>(vf[0], vf[1]);
          PIN(vf[2]); PIN(vf[3]);
#pragma unroll
          for (int j = 0; j < 4; ++j)
#pragma unroll
            for (int qb = 0; qb < 2; ++qb)
              o[0][j & 1][qb] = __builtin_amdgcn_mfma_f32_16x16x32_bf16(vf[j], __builtin_bit_cast(bf16x8, pw[2 * q + (j >> 1)][qb]), o[0][j & 1][qb], 0, 0, 0);
        });
#pragma unroll
        for (int par = 0; par < 2; ++par) { PIN(o[0][par][0]); PIN(o[0][par][1]); }
        PIN(lsum[0]); PIN(lsum[1]);
        SBAR(); NOPS(7);
      }
      SBAR();
    }
  });
  STAMP(12); STAMP(13); STAMP(14); STAMP(15);

  const int idx = SEQ - 1 - (int)(key & 2047u);
  const bool owner = (idx >> 7) == mb, ownw = (idx >> 4) == mb * 8 + rbw * 2 + kg;
  f32x4 yp[7], ya[4], oth[4];
  unsigned gate[8];
  float glow;
  const unsigned ex_a = ldsf + (c >> 3) * 8192 + (c & 7) * 128 + (((g >> 1) ^ (c >> 1)) << 4) + (g & 1) * 8;
  auto item = [](int s) { return s < 48 ? s : s < 56 ? 48 + 2 * (s - 48) : 49 + 2 * (s - 56); };
  u32x2 pf;
  sfor<64>([&](auto Si) AI {
    constexpr int s = Si, u = s >> 2, it = item(s), x0 = (it & ~1) % XD2, x1 = (it | 1) % XD2;
    if constexpr (!(it & 1)) waitv<SC.younger(OX, 64 + it + 1, B0 + s - 1)>(xf[x0], xf[x1]);
    sfor<4>([&](auto Dd) AI {
      constexpr int d = Dd, dt = (it & 1) * 4 + d;
      if constexpr (s + 1 < 64) WRD1(IC(s + 1), WB(item(s + 1)), Dd);
      SBAR();
      if constexpr (s < 2) { MFZ(acc[dt][0], wf[s & 1][d], xf[x0]); MFZ(acc[dt][1], wf[s & 1][d], xf[x1]); }
      else { MF(acc[dt][0], wf[s & 1][d], xf[x0]); MF(acc[dt][1], wf[s & 1][d], xf[x1]); }
      issue(IC(B0 + s), Dd);
      if constexpr (s == 56) *(LAS f32x4*)(lds + (wid ^ 4) * 4096 + l * 16 + (d & 1) * 2048 + (d >> 1) * 32768) = acc[d][1];
      if constexpr (s == 57 && d == 0) {
#pragma unroll
        for (int e = 0; e < 4; ++e) oth[e] = *(LAS const f32x4*)(lds + wid * 4096 + l * 16 + (e & 1) * 2048 + (e >> 1) * 32768);
      }
      if constexpr (s == 57 && d >= 2) {
        gst16<(2 * d - 4) * 1024>(acc[2 * d - 4][0] + oth[2 * d - 4], (unsigned)(wid * 4096 + l * 16), ws + WS_XC + (size_t)(bid ^ 4) * 32768);
        gst16<(2 * d - 3) * 1024>(acc[2 * d - 3][0] + oth[2 * d - 3], (unsigned)(wid * 4096 + l * 16), ws + WS_XC + (size_t)(bid ^ 4) * 32768);
      }
      if constexpr (s == 34) xe[d] = *(LAS const u32x2*)(uintptr_t)(ex_a ^ (d << 5));
      if constexpr (s >= 36 && s < 44 && d == 1) {
        constexpr int j = s - 36;
        PIN(st);
        const unsigned xw = xe[j >> 1][j & 1];
        const unsigned dd = pk2(bflo(xw) - st[0], bfhi(xw) - st[0]);
        gate[j] = pk2(bflo(dd) * st[1], bfhi(dd) * st[1]);
        PIN(gate[j]);
      }
      if constexpr (s >= 47 && s < 63 && d == 1) {
        constexpr int j = (s - 47) >> 1;
        if constexpr ((s - 47) & 1) { gate[j] = pk2(glow, silu_f(bfhi(gate[j]))); PIN(gate[j]); }
        else { glow = silu_f(bflo(gate[j])); PIN(glow); }
      }
    });
    SBAR();
    if constexpr (s == 7) {
      waitv<SC.younger(OW, 7, B0 + s)>();
      if (l == 0) {
        gst8((u32x2){tlo, thi}, (unsigned)(((rb * 8 + wid) * 32 + head) * 8), ws + WS_FO + 3 * 16384);
        gst8((u32x2){tlo, thi}, (unsigned)((wid * 256 + yrow) * 8), ws + WS_FY);
      }
    }
    if constexpr (s == 4 || s == 17 || s == 33) {
      constexpr int j = s == 4 ? 1 : s == 17 ? 2 : 3;
      waitv<SC.younger(OM, j + 1, B0 + s)>(fg);
      if (__builtin_expect(!__all((fg[0] == tlo) & (fg[1] == thi)), 0)) poll8(fo_v, ws + WS_FO + j * 16384, tlo, thi, 0xffffffffu);
    }
    if constexpr (s == 60) {
      waitv<0>();
      if (l == 0) gst8((u32x2){tlo, thi}, (unsigned)(((bid ^ 4) * 8 + wid) * 64), ws + WS_FX);
    }
    if constexpr (s == 61) gld8<0, true>(pf, (unsigned)((bid * 8 + wid) * 64), ws + WS_FX);
    if constexpr (s == 13) {
      if (owner) { gld16<0, true>(fm[0], (unsigned)(nb * 2048 + (l < 56 ? l : 55) * 32), ws + WS_FY); gld16<16, true>(fm[1], (unsigned)(nb * 2048 + (l < 56 ? l : 55) * 32), ws + WS_FY); }
    }
    if constexpr (s == 28) {
      if (owner) {
        PIN(fm[0]); PIN(fm[1]);
        for (int spin = 0; spin < SPINS; ++spin) {
          const bool ok = (fm[0][0] == tlo) & (fm[0][1] == thi) & (fm[0][2] == tlo) & (fm[0][3] == thi) & (fm[1][0] == tlo) & (fm[1][1] == thi) & (fm[1][2] == tlo) & (fm[1][3] == thi);
          if (__builtin_expect(__all(ok), 1)) break;
          __builtin_amdgcn_s_sleep(1);
          gld16<0, true>(fm[0], (unsigned)(nb * 2048 + (l < 56 ? l : 55) * 32), ws + WS_FY); gld16<16, true>(fm[1], (unsigned)(nb * 2048 + (l < 56 ? l : 55) * 32), ws + WS_FY);
          waitv<0>(fm[0], fm[1]);
        }
        const unsigned yv = (unsigned)(((t >> 4) * 1024 + nb * 128 + kh * 64 + (t & 15) * 4) * 4);
        sfor<7>([&](auto I) AI { constexpr int q = I; gld16<0, true>(yp[q], yv, ws + WS_YP + (size_t)q * 32 * 4096); });
      }
    }
    if constexpr (s == 43) {
      if (owner) {
        f32x4 a = {};
#pragma unroll
        for (int q = 0; q < 7; ++q) { PIN(yp[q]); a += yp[q]; }
#pragma unroll
        for (int j = 0; j < 4; ++j) a[j] = xg_sum(a[j]);
        *(LAS f32x4*)(lds + YS + (wid * 64 + c * 4) * 4) = a;
      }
    }
    if constexpr (s == 45) {
      if (ownw) {
        float a = 0.f;
#pragma unroll
        for (int p = 0; p < 8; ++p) a += *(LAS const float*)(lds + YS + (p * 64 + l) * 4);
        const unsigned ord = key >> 11, bits = (ord & 0x8000u) ? (ord & 0x7fffu) : (~ord & 0xffffu);
        *(LAS float*)(lds + RED1 + l * 4) = rbf(rbf(a) * rbf(sigmoid_f(bflo(bits))));
#pragma unroll
        for (int q = 0; q < 4; ++q) ya[q] = *(LAS const f32x4*)(lds + RED1 + (q * 16 + 4 * g) * 4);
      }
    }
    if constexpr (s == 46) {
      if (ownw) {
#pragma unroll
        for (int j = 0; j < 8; ++j) {
          const unsigned w = pk2(bflo(gate[j]) + ya[j >> 1][2 * (j & 1)], bfhi(gate[j]) + ya[j >> 1][2 * (j & 1) + 1]);
          gate[j] = erow == idx ? w : gate[j];
          PIN(gate[j]);
        }
      }
    }
    SBAR();
    if constexpr (((s & 3) == 2 && u < 11) || s == 44) {
      if constexpr (s == 44) waitbar_l<SC.younger(OG, 64 + 4 * u + 7, B0 + s)>();
      else waitbar<SC.younger(OG, 64 + 4 * u + 7, B0 + s)>();
      if constexpr (u % 4 == 1) STAMP(16 + u / 4);
    }
    if constexpr (s == 48) waitbar<SC.younger(OG, 64 + 55, B0 + s)>();
    if constexpr (s == 50) waitbar<SC.younger(OG, 64 + 59, B0 + s)>();
    if constexpr (s == 52) { waitbar<SC.younger(OG, 64 + 63, B0 + s)>(); STAMP(19); }
    if constexpr (s == 56) waitbar_l<63>();
    if constexpr (PRIO) __builtin_amdgcn_s_setprio(3 - ((s + 2) & 3));
  });
  STAMP(20);

  f32x4 kep[4];
  {
    waitv<0>(pf);
    const bool rdy = __all((pf[0] == tlo) & (pf[1] == thi));
    if (rdy) sfor<4>([&](auto Dd) AI { constexpr int d = Dd; gld16<d * 1024, true>(oth[d], (unsigned)(wid * 4096 + l * 16), ws + WS_XC + (size_t)bid * 32768); });
    NOPS(3);
#pragma unroll
    for (int d = 0; d < 4; ++d) { PIN(acc[4 + d][1]); *(LAS f32x4*)(lds + 65536 + (wid ^ 4) * 4096 + l * 16 + (d & 1) * 2048 + (d >> 1) * 32768) = acc[4 + d][1]; }
    waitbar_l<63>();
    STAMP(21);
#pragma unroll
    for (int d = 0; d < 4; ++d) kep[d] = acc[4 + d][0] + *(LAS const f32x4*)(lds + 65536 + wid * 4096 + l * 16 + (d & 1) * 2048 + (d >> 1) * 32768);
    STAMP(22);
    if (!rdy) {
      poll8((unsigned)((bid * 8 + wid) * 64), ws + WS_FX, tlo, thi, 0xffffffffu);
      sfor<4>([&](auto Dd) AI { constexpr int d = Dd; gld16<d * 1024, true>(oth[d], (unsigned)(wid * 4096 + l * 16), ws + WS_XC + (size_t)bid * 32768); });
    }
    STAMP(23);
    waitv<0>(oth[0], oth[1]);
    PIN(oth[2]); PIN(oth[3]);
    STAMP(24);
  }
#pragma unroll
  for (int d = 0; d < 4; ++d) {
    const f32x4 a = kep[d] + oth[d];
    u32x2 res;
#pragma unroll
    for (int j = 0; j < 2; ++j) {
      const unsigned sp = pk2(a[2 * j], a[2 * j + 1]), gp = gate[2 * d + j], xw = xe[d][j];
      const unsigned pr = pk2(bflo(sp) * bflo(gp), bfhi(sp) * bfhi(gp));
      res[j] = pk2(bflo(xw) + bflo(pr), bfhi(xw) + bfhi(pr));
    }
    *(LAS u32x2*)(uintptr_t)(ex_a ^ (d << 5)) = res;
  }
#pragma unroll
  for (int i = 0; i < 2; ++i)
    *(u32x4*)(out + (size_t)(erow - c + i * 8 + (l >> 3)) * DM + nb * 128 + kh * 64 + ((l & 7) ^ (i * 4 + (l >> 4))) * 8) = *(LAS const u32x4*)(uintptr_t)(ldsf + i * 8192 + l * 16);
  STAMP(25);
  STAMP_OUT(0, 26);
}

extern "C" void kernel_launch(void* const* d_in, const int* in_sizes, int n_in, void* d_out, int out_size, void* d_ws, size_t ws_size,
                              hipStream_t stream) {
  k_fused<<<256, 512, 0, stream>>>((const u16*)d_in[0], (const u16*)d_in[1], (const u16*)d_in[2], (const u16*)d_in[3], (const u16*)d_in[4],
                                   (const u16*)d_in[6], (const u16*)d_in[7], (const u16*)d_in[8], (const u16*)d_in[9], (char*)d_ws, (u16*)d_out);
}
```

```cpp
#include <hip/hip_runtime.h>
#include <stdint.h>
#include <type_traits>

typedef unsigned short u16;
typedef short bf16x8 __attribute__((ext_vector_type(8)));
typedef short s16x4 __attribute__((ext_vector_type(4)));
typedef float f32x4 __attribute__((ext_vector_type(4)));
typedef float f32x2 __attribute__((ext_vector_type(2)));
typedef unsigned u32x4 __attribute__((ext_vector_type(4)));
typedef unsigned u32x2 __attribute__((ext_vector_type(2)));
typedef __bf16 bf2_t __attribute__((ext_vector_type(2)));
#define LAS __attribute__((address_space(3)))
#define DI __device__ __forceinline__
#define AI __attribute__((always_inline))
#define SBAR() __builtin_amdgcn_sched_barrier(0)
template <class T> __device__ __forceinline__ void PIN(T& x) { asm volatile("" : "+v"(x)); }
template <class B> DI void MF(f32x4& acc, const bf16x8& a, const B& b) { asm volatile("v_mfma_f32_16x16x32_bf16 %0, %1, %2, %0" : "+v"(acc) : "v"(a), "v"(b)); }
template <class B> DI void MFC(f32x4& d, const bf16x8& a, const B& b, const f32x4& c) { asm volatile("v_mfma_f32_16x16x32_bf16 %0, %1, %2, %3" : "=&v"(d) : "v"(a), "v"(b), "v"(c)); }
template <class B> DI void MFZ(f32x4& d, const bf16x8& a, const B& b) { asm volatile("v_mfma_f32_16x16x32_bf16 %0, %1, %2, 0" : "=&v"(d) : "v"(a), "v"(b)); }
#define NOPS(n) asm volatile("s_nop " #n)
#define MFMA16(a, b, c) __builtin_amdgcn_mfma_f32_16x16x16bf16_1k((a), (b), (c), 0, 0, 0)
#define IC(n) std::integral_constant<int, (n)>{}

constexpr int SEQ = 2048, DM = 1024, DH = 128, SK = 2048, WIN = 512, FF = 3584;
constexpr size_t WS_XN = 0;
constexpr size_t WS_KV = 4u << 20;
constexpr size_t WS_ST = 5u << 20;
constexpr size_t WS_EP = WS_ST + 16384;
constexpr size_t WS_G0 = WS_ST + 32768;
constexpr size_t WS_FK = WS_ST + 49152;
constexpr size_t WS_YP = WS_ST + 65536;
constexpr size_t WS_FO = 7u << 20;
constexpr size_t WS_FY = WS_FO + 65536;
constexpr size_t WS_FX = WS_FO + 131072;
constexpr size_t WS_O = 8u << 20;
constexpr size_t WS_GU = 32u << 20;
constexpr size_t WS_XC = 24u << 20;

template <int I, int N, class F> DI void sfor_impl(F&& f) {
  if constexpr (I < N) { f(std::integral_constant<int, I>{}); sfor_impl<I + 1, N>(f); }
}
template <int N, class F> DI void sfor(F&& f) { sfor_impl<0, N>(f); }

DI unsigned pk2(float a, float b) { f32x2 v = {a, b}; return __builtin_bit_cast(unsigned, __builtin_convertvector(v, bf2_t)); }
DI float rbf(float a) { return (float)(__bf16)a; }
DI float bflo(unsigned u) { return __uint_as_float(u << 16); }
DI float bfhi(unsigned u) { return __uint_as_float(u & 0xffff0000u); }
DI float sigmoid_f(float v) { return __builtin_amdgcn_rcpf(1.f + __builtin_amdgcn_exp2f(v * -1.4426950408889634f)); }
DI float sel2(bool c, float a, float b) { PIN(a); PIN(b); return c ? a : b; }
DI float silu_f(float v) { return v * sigmoid_f(v); }
template <int CTRL> DI float dppf(float x) { return __builtin_bit_cast(float, __builtin_amdgcn_mov_dpp(__builtin_bit_cast(int, x), CTRL, 0xf, 0xf, true)); }
template <int CTRL> DI unsigned dppu(unsigned x) { return (unsigned)__builtin_amdgcn_mov_dpp((int)x, CTRL, 0xf, 0xf, true); }
constexpr int XOR1 = 0xB1, XOR2 = 0x4E, XOR7 = 0x141, XOR8 = 0x128;
DI float xg_sum(float x) {
  auto s = __builtin_amdgcn_permlane16_swap(__float_as_uint(x), __float_as_uint(x), false, false);
  x = __uint_as_float(s[0]) + __uint_as_float(s[1]);
  auto t = __builtin_amdgcn_permlane32_swap(__float_as_uint(x), __float_as_uint(x), false, false);
  return __uint_as_float(t[0]) + __uint_as_float(t[1]);
}
DI float xg_max(float x) {
  auto s = __builtin_amdgcn_permlane16_swap(__float_as_uint(x), __float_as_uint(x), false, false);
  x = __builtin_fmaxf(__uint_as_float(s[0]), __uint_as_float(s[1]));
  auto t = __builtin_amdgcn_permlane32_swap(__float_as_uint(x), __float_as_uint(x), false, false);
  return __builtin_fmaxf(__uint_as_float(t[0]), __uint_as_float(t[1]));
}
DI float wave_sum(float v) { v += dppf<XOR1>(v); v += dppf<XOR2>(v); v += dppf<XOR7>(v); v += dppf<XOR8>(v); return xg_sum(v); }
DI unsigned umax(unsigned a, unsigned b) { return a > b ? a : b; }
DI unsigned wave_max_u32(unsigned v) {
  v = umax(v, dppu<XOR1>(v)); v = umax(v, dppu<XOR2>(v)); v = umax(v, dppu<XOR7>(v)); v = umax(v, dppu<XOR8>(v));
  auto s = __builtin_amdgcn_permlane16_swap(v, v, false, false); v = umax(s[0], s[1]);
  auto t = __builtin_amdgcn_permlane32_swap(v, v, false, false); v = umax(t[0], t[1]);
  return __builtin_amdgcn_readfirstlane(v);
}

template <int OFF, bool DEV = false> DI void glds(unsigned ldsaddr, unsigned voff, const void* sbase) {
  if constexpr (DEV) asm volatile("s_mov_b32 m0, %0\n\ts_nop 0\n\tglobal_load_lds_dwordx4 %1, %2 offset:%3 sc1" ::"s"(ldsaddr), "v"(voff), "s"(sbase), "i"(OFF) : "memory");
  else asm volatile("s_mov_b32 m0, %0\n\ts_nop 0\n\tglobal_load_lds_dwordx4 %1, %2 offset:%3" ::"s"(ldsaddr), "v"(voff), "s"(sbase), "i"(OFF) : "memory");
}
DI void touch(unsigned voff, const void* sbase) {
  asm volatile("global_load_dword v255, %0, %1" ::"v"(voff), "s"(sbase) : "memory", "v255");
}
template <int OFF, bool DEV = false, class T> DI void gld16(T& dst, unsigned voff, const void* sbase) {
  if constexpr (DEV) asm volatile("global_load_dwordx4 %0, %1, %2 offset:%3 sc1" : "=v"(dst) : "v"(voff), "s"(sbase), "i"(OFF) : "memory");
  else asm volatile("global_load_dwordx4 %0, %1, %2 offset:%3" : "=v"(dst) : "v"(voff), "s"(sbase), "i"(OFF) : "memory");
}
template <int OFF, bool DEV = false, class T> DI void gld8(T& dst, unsigned voff, const void* sbase) {
  if constexpr (DEV) asm volatile("global_load_dwordx2 %0, %1, %2 offset:%3 sc1" : "=v"(dst) : "v"(voff), "s"(sbase), "i"(OFF) : "memory");
  else asm volatile("global_load_dwordx2 %0, %1, %2 offset:%3" : "=v"(dst) : "v"(voff), "s"(sbase), "i"(OFF) : "memory");
}
template <int OFF> DI void gld2(unsigned& dst, unsigned voff, const void* sbase) {
  asm volatile("global_load_ushort %0, %1, %2 offset:%3" : "=v"(dst) : "v"(voff), "s"(sbase), "i"(OFF) : "memory");
}
template <int OFF, class T> DI void gst16(const T& v, unsigned voff, const void* sbase) {
  asm volatile("global_store_dwordx4 %0, %1, %2 offset:%3 sc1\n\ts_nop 1" ::"v"(voff), "v"(v), "s"(sbase), "i"(OFF) : "memory");
}
template <class T> DI void gst8(const T& v, unsigned voff, const void* sbase) {
  asm volatile("global_store_dwordx2 %0, %1, %2 sc1" ::"v"(voff), "v"(v), "s"(sbase) : "memory");
}
template <int OFF> DI void gst4(float v, unsigned voff, const void* sbase) {
  asm volatile("global_store_dword %0, %1, %2 offset:%3 sc1" ::"v"(voff), "v"(v), "s"(sbase), "i"(OFF) : "memory");
}
constexpr int SPINS = 1 << 16;
DI void poll8(unsigned voff, const void* sbase, unsigned lo, unsigned hi, unsigned lomask) {
  for (int spin = 0; spin < SPINS; ++spin) {
    u32x2 v;
    asm volatile("global_load_dwordx2 %0, %1, %2 sc1\n\ts_waitcnt vmcnt(0)" : "=&v"(v) : "v"(voff), "s"(sbase) : "memory");
    if (__all(((v[0] & lomask) == lo) & (v[1] == hi))) break;
    __builtin_amdgcn_s_sleep(1);
  }
}
template <int N> DI void waitv() { static_assert(N >= 0, "count"); asm volatile("s_waitcnt vmcnt(%0)" ::"n"(N > 63 ? 63 : N) : "memory"); }
template <int N, class T> DI void waitv(T& x) { static_assert(N >= 0, "count"); asm volatile("s_waitcnt vmcnt(%1)" : "+v"(x) : "n"(N > 63 ? 63 : N) : "memory"); }
template <int N, class T> DI void waitv(T& x, T& y) { static_assert(N >= 0, "count"); asm volatile("s_waitcnt vmcnt(%2)" : "+v"(x), "+v"(y) : "n"(N > 63 ? 63 : N) : "memory"); }
template <int N> DI void waitbar() { static_assert(N >= 0, "count"); asm volatile("s_waitcnt vmcnt(%0)\n\ts_barrier" ::"n"(N > 63 ? 63 : N) : "memory"); }
template <int N> DI void waitbar_l() { static_assert(N >= 0, "count"); asm volatile("s_waitcnt vmcnt(%0) lgkmcnt(0)\n\ts_barrier" ::"n"(N > 63 ? 63 : N) : "memory"); }
typedef short v4i16_t __attribute__((ext_vector_type(4)));
DI s16x4 trd(LAS const char* p) { return __builtin_amdgcn_ds_read_tr16_b64_v4i16((LAS v4i16_t*)p); }
struct LBase {
  unsigned lo, hi;
  DI void init(unsigned a) { lo = a; hi = a + 65536; PIN(lo); PIN(hi); }
  template <int OFF> DI LAS const char* at() const { return (LAS const char*)(uintptr_t)(OFF < 65536 ? lo : hi) + (OFF & 65535); }
};
DI bf16x8 cat8(s16x4 lo, s16x4 hi) { return __builtin_shufflevector(lo, hi, 0, 1, 2, 3, 4, 5, 6, 7); }

enum { OX, OG, OF, OM, OS, OP, OE, OW };
struct Sched {
  int n; short kind[400], idx[400], slot[400];
  constexpr Sched() : n(0), kind{}, idx{}, slot{} {}
  constexpr void add(int s, int k, int i) { kind[n] = (short)k; idx[n] = (short)i; slot[n] = (short)s; ++n; }
  constexpr int younger(int k, int i, int s) const {
    int p = -1;
    for (int q = 0; q < n; ++q) if (kind[q] == k && idx[q] == i) p = q;
    if (p < 0 || slot[p] > s) return -1;
    int c = 0;
    for (int q = p + 1; q < n; ++q) if (slot[q] <= s) ++c;
    return c;
  }
  constexpr int first(int s) const { int q = 0; while (q < n && slot[q] < s) ++q; return q; }
  constexpr int count(int s) const { int c = 0; for (int q = 0; q < n; ++q) if (slot[q] == s) ++c; return c; }
};

#define STAMP_DECL
#define STAMP(i)
#define STAMP_OUT(kid, n)

constexpr int RING = 4 * 32768, FB = RING, RED1 = FB + 24576, HSC = RED1 + 1024, YS = HSC + 1024, LDS_BYTES = FB + 32768;
constexpr int STAGE = 3 * 32768;
constexpr int XD = 16, XD2 = 16;
constexpr float LBOUND = 65536.f;
constexpr float QSCALE = 0.08838834764831845f * 1.4426950408889634f;
constexpr int B0 = 128;
#define PRIO 1
constexpr int FISS[4] = {96, 97, 98, 117}, FRDS[4] = {115, 118, 121, 124};
constexpr int fpiece(int sl) { for (int p = 0; p < 4; ++p) if (FRDS[p] == sl) return p; return -1; }
constexpr Sched make_sched() {
  Sched s;
#define XP 8
  for (int i = 0; i < 12; ++i) s.add(-2, OG, i);
  for (int i = 0; i < XP; ++i) s.add(-1, OX, i);
  for (int t = 0; t < 128; ++t) {
    if (t < 2) for (int i = XP + t * (16 - XP) / 2; i < XP + (t + 1) * (16 - XP) / 2; ++i) s.add(t, OX, i);
    if (t >= 2 && t < 50) s.add(t, OX, t + 14);
    for (int v = 3; v < 18; ++v) for (int p = 0; p < 4; ++p) if (8 * (v - 3) + 7 + p == t) s.add(t, OG, 4 * v + p);
    if (t == 105 || t == 113 || t == 121) { s.add(t, OW, (t - 105) / 4); s.add(t, OW, (t - 105) / 4 + 1); }
    if (t >= 124) for (int i = 0; i < 3; ++i) s.add(t, OX, 64 + 3 * (t - 124) + i);
    if (t == 127) { s.add(t, OG, 72); s.add(t, OG, 73); }
    if (t == 20) s.add(t, OM, 0);
    if (t == 120) s.add(t, OM, 1);
    if (t >= 49 && t < 57 && (t & 1)) s.add(t, OF, (t - 49) / 2);
    if (t >= 71 && t < 79 && (t & 1)) s.add(t, OF, 4 + (t - 71) / 2);
    if (t == 48) s.add(t, OS, 0);
    if (t == 104) s.add(t, OS, 2);
    for (int p = 0; p < 4; ++p) if (FISS[p] == t) s.add(t, OF, 8 + p);
  }
  for (int t = 0; t < 64; ++t) {
    if (t == 0) { s.add(B0, OM, 2); s.add(B0, OG, 74); s.add(B0, OG, 75); s.add(B0, OW, 6); s.add(B0, OW, 7); }
    if (t == 1 || t == 2) { s.add(B0 + t, OX, 74 + 2 * t); s.add(B0 + t, OX, 75 + 2 * t); }
    if (t == 5 || t == 6 || t == 9) s.add(B0 + t, OX, 64 + (t == 5 ? 16 : t == 6 ? 18 : 22));
    if (t >= 5 && t < 50) s.add(B0 + t, OX, 64 + (t == 5 ? 17 : t < 9 ? t + 13 : t + 14));
    for (int v = 3; v < 16; ++v) for (int p = 0; p < 4; ++p) if ((v < 14 ? 4 * (v - 3) + 3 + (p >> 1) : v == 14 ? 45 + (p >> 1) : 49) == t) s.add(B0 + t, OG, 64 + 4 * v + p);
    if (t == 10 || t == 26) s.add(B0 + t, OM, t == 10 ? 3 : 4);
    if (t == 13 || t == 14 || t == 18) s.add(B0 + t, OE, t == 18 ? 2 : t - 13);
  }
  return s;
}
constexpr Sched SC = make_sched();

__global__ __launch_bounds__(512, 2) void k_fused(const u16* __restrict__ x, const u16* __restrict__ kc, const u16* __restrict__ vc,
                                                  const u16* __restrict__ wq, const u16* __restrict__ wo, const u16* __restrict__ wr,
                                                  const u16* __restrict__ wgate, const u16* __restrict__ wup, const u16* __restrict__ wdown,
                                                  char* __restrict__ ws, u16* __restrict__ out) {
  __shared__ __attribute__((aligned(16))) char lds_raw[LDS_BYTES];
  LAS char* const lds = (LAS char*)lds_raw;
  const int t = threadIdx.x, l = t & 63, bid = blockIdx.x, c = l & 15, g = l >> 4;
  const int wid = __builtin_amdgcn_readfirstlane(t >> 6);
  const int xcd = bid & 7, within = bid >> 3, head = xcd * 4 + (within & 3), rb = within >> 2;
  const int fj = xcd * 28 + (within < 28 ? within : within - 28);
  const int fsl = fj / 7, fgr = fj - fsl * 7;
  const int kh = xcd >> 2, mb = 2 * rb + (xcd & 1), nb = ((xcd >> 1) & 1) * 4 + (within & 3);
  const int rbw = wid & 3, kg = wid >> 2;
  const unsigned lds0 = (unsigned)(uintptr_t)lds;
  STAMP_DECL
  STAMP(0);

  unsigned tlo, thi;
  {
    const int row = 8 * bid + wid;
    u32x4 a0, a1, w0, w1, kvo;
    u32x2 ep, klo, khi;
    unsigned e[8];
    gld16<0>(a0, (unsigned)(row * DM * 2 + l * 16), x); gld16<1024>(a1, (unsigned)(row * DM * 2 + l * 16), x);
    gld16<0>(w0, (unsigned)(l * 16), wr); gld16<1024>(w1, (unsigned)(l * 16), wr);
    gld8<0, true>(ep, 0u, ws + WS_EP);
    if (wid == 3) {
      const char* pc;
      asm volatile("s_getpc_b64 %0" : "=s"(pc));
      touch((unsigned)((within * 12 + (l < 12 ? l : 11)) * 128), pc);
    }
    __builtin_amdgcn_s_barrier();
    if (wid == 2) {
      asm volatile("global_load_dword v255, %0, %1 sc1" ::"v"((unsigned)((l < 40 ? l : 39) << 20) + 64 * 1024 - 128), "s"(ws) : "memory", "v255");
      const char* pb = l < 8 ? (const char*)wo : l < 16 ? (const char*)wgate : l < 24 ? (const char*)wup : l < 32 ? (const char*)wdown : (const char*)out;
      pb += (size_t)(l & 7) * (l < 8 ? 1048576 : l < 32 ? 917504 : 524288);
      asm volatile("global_load_dword v255, %0, off" ::"v"(pb) : "memory", "v255");
    }
    const int fn = (bid * 4 + wid) & 255, kvh = bid >> 6;
    if (wid < 4) {
      if (fn < 128) {
        const unsigned ko = (unsigned)(((kvh * SK + (SK - WIN) + (fn >> 2) * 16 + c) * DH + (fn & 3) * 32 + 4 * g) * 2);
        gld8<0>(klo, ko, kc); gld8<32>(khi, ko, kc);
      } else {
        const int d = 32 * ((fn - 128) >> 5) + 8 * (c >> 2) + 4 * (fn & 1) + (c & 3);
        const unsigned vo = (unsigned)(((kvh * SK + (SK - WIN) + ((fn >> 1) & 15) * 32 + 4 * g + 8) * DH + d) * 2);
        sfor<8>([&](auto J) AI { constexpr int j = J; gld2<(16 * (j >> 2) + (j & 3) - 8) * DH * 2>(e[j], vo, vc); });
      }
      if (wid < 2) touch((unsigned)((wid * 256 + within * 8 + (l >> 3)) * 8192 + xcd * 1024 + (l & 7) * 128), wq);
      if (fn < 128) waitv<5>(); else waitv<11>();
    } else {
      waitv<3>();
    }
    PIN(a0); PIN(a1);
    STAMP(1);
    float v[16];
#pragma unroll
    for (int j = 0; j < 4; ++j) { v[2 * j] = bflo(a0[j]); v[2 * j + 1] = bfhi(a0[j]); v[8 + 2 * j] = bflo(a1[j]); v[9 + 2 * j] = bfhi(a1[j]); }
    float s = 0.f;
#pragma unroll
    for (int j = 0; j < 16; ++j) s += v[j];
    const float mean = wave_sum(s) * (1.f / DM);
    float q = 0.f;
#pragma unroll
    for (int j = 0; j < 16; ++j) { const float d = v[j] - mean; q += d * d; }
    const float var = wave_sum(q) * (1.f / DM);
    const float mbf = rbf(mean), rden = __builtin_amdgcn_rcpf(rbf(__builtin_amdgcn_sqrtf(rbf(rbf(var) + 1e-5f))));
    u32x4 o0, o1;
#pragma unroll
    for (int j = 0; j < 8; ++j) {
      const unsigned dd = pk2(v[2 * j] - mbf, v[2 * j + 1] - mbf);
      const unsigned nn = pk2(bflo(dd) * rden, bfhi(dd) * rden);
      if (j < 4) o0[j] = nn; else o1[j - 4] = nn;
    }
    *(LAS u32x4*)(lds + STAGE + (l * 8 + (wid ^ (l & 7))) * 16) = o0;
    *(LAS u32x4*)(lds + STAGE + 8192 + (l * 8 + (wid ^ (l & 7))) * 16) = o1;
    *(LAS f32x2*)(lds + STAGE + 16384 + 32 + wid * 8) = (f32x2){mbf, rden};
    waitbar_l<63>();
    if (wid >= 4) {
#pragma unroll
      for (int hh = 0; hh < 4; ++hh) {
        const int p = hh * 256 + (t - 256), ch = p >> 3, r = 8 * bid + ((p & 7) ^ (ch & 7));
        const u32x4 o = *(LAS const u32x4*)(lds + STAGE + p * 16);
        gst16<0>(o, (unsigned)(((((r >> 5) * 32 + (ch >> 2)) * 2 + ((r >> 4) & 1)) * 64 + (ch & 3) * 16 + (r & 15)) * 16), ws + WS_XN);
      }
      if (t < 256 + 8) gst8(*(LAS const f32x2*)(lds + STAGE + 16384 + 32 + (t - 256) * 8), (unsigned)((8 * bid + t - 256) * 8), ws + WS_ST);
      waitv<4>();
    } else if (fn < 128) waitv<2>(); else waitv<8>();
    PIN(w0); PIN(w1); PIN(ep);
    {
      const unsigned long long tg = (((unsigned long long)(unsigned)__builtin_amdgcn_readfirstlane(ep[1]) << 32) | (unsigned)__builtin_amdgcn_readfirstlane(ep[0])) + 1ull;
      tlo = (unsigned)tg; thi = (unsigned)(tg >> 32);
    }
    float rr = 0.f;
#pragma unroll
    for (int j = 0; j < 8; ++j) {
      const unsigned nn = j < 4 ? o0[j] : o1[j - 4], ww = j < 4 ? w0[j] : w1[j - 4];
      rr += bflo(nn) * bflo(ww); rr += bfhi(nn) * bfhi(ww);
    }
    rr = wave_sum(rr);
    {
      const unsigned bits = pk2(rr, 0.f) & 0xffffu;
      const unsigned ord = (bits & 0x8000u) ? (~bits & 0xffffu) : (bits | 0x8000u);
      *(LAS unsigned*)(lds + STAGE + 16384 + wid * 4) = (ord << 11) | (unsigned)(SEQ - 1 - row);
    }
    if (wid >= 4) waitv<0>();
    waitbar_l<63>();
    if (t == 256) {
      const u32x4 ka = *(LAS const u32x4*)(lds + STAGE + 16384), kb = *(LAS const u32x4*)(lds + STAGE + 16400);
      const unsigned key = umax(umax(umax(ka[0], ka[1]), umax(ka[2], ka[3])), umax(umax(kb[0], kb[1]), umax(kb[2], kb[3])));
      gst8((u32x2){(tlo << 27) | key, (tlo >> 5) | (thi << 27)}, (unsigned)(bid * 8), ws + WS_G0);
    }
    if (wid < 4) {
      if (wid < 2) waitv<1>(); else waitv<0>();
      if (fn < 128) {
        PIN(klo); PIN(khi);
        kvo = (u32x4){klo[0], klo[1], khi[0], khi[1]};
      } else {
#pragma unroll
        for (int j = 0; j < 8; ++j) PIN(e[j]);
        kvo = (u32x4){e[0] | (e[1] << 16), e[2] | (e[3] << 16), e[4] | (e[5] << 16), e[6] | (e[7] << 16)};
      }
      gst16<0>(kvo, (unsigned)((bid * 4 + wid) * 1024 + l * 16), ws + WS_KV);
    }
    STAMP(2);
  }
  const unsigned glo = tlo << 27, ghi = (tlo >> 5) | (thi << 27);

  const char* const wq_b = (const char*)wq + head * 256;
  const unsigned wq_v = (unsigned)(((8 * wid + ((l & 31) >> 2)) * 4096 + (l >> 5) * 32 + ((l & 3) ^ (2 * (wid & 1))) * 8) * 2);
  const char* const kv_b = ws + WS_KV + (size_t)(head >> 3) * 262144;
  const unsigned kv_v = (unsigned)(wid * 4096 + l * 16);
  const char* const xn_b = ws + WS_XN + (size_t)rb * 8 * 65536 + 4096;
  const unsigned xn_v = (unsigned)(wid * 65536 + l * 16);
  const char* const wo_b = (const char*)wo + (size_t)kh * 2048 * (DM * 2) + nb * 256;
  const char* const o_b = ws + WS_O + (size_t)mb * 4 * 262144 + kh * 131072;
  const unsigned ldsw = lds0 + wid * 2048, ldsk = lds0 + wid * 4096, ldsf = lds0 + FB + wid * 1024;
  const int erow = mb * 128 + rbw * 32 + kg * 16 + c;
  bf16x8 xf[XD];
  u32x4 fm[2];
  bf16x8 fxs;
  const unsigned gu_v = (unsigned)((((4 * wid + g) * 2 + ((l >> 3) & 1)) * FF + fj * 16 + (l & 7) * 2) * 8);
  u32x2 fg;
  u32x2 xe[4];
  f32x2 st;
  unsigned key = 0;

  const unsigned fo_v = (unsigned)(((mb * 4 + rbw) * 32 + kh * 16 + c) * 8);
  const int yrow = within < 28 ? fj : 224 + xcd * 4 + within - 28;
  f32x4 o[2][2][2];
  float inv[2];
  const char* const od_b = ws + WS_O + ((size_t)(rb * 8) * 128 + head * 4) * 2048 + 4096;
  auto issue = [&](auto Sl, auto Pos) AI {
    constexpr int sl = Sl, pos = Pos, cnt = SC.count(sl), q0 = SC.first(sl) + (pos < 0 ? 0 : pos);
    constexpr int nq = pos < 0 ? cnt : pos >= cnt ? 0 : pos < 3 ? 1 : cnt - 3;
    sfor<nq>([&](auto Qi) AI {
      constexpr int k = SC.kind[q0 + Qi], i = SC.idx[q0 + Qi];
      if constexpr (k == OX && i < 64) gld16<(i & 7) * 1024 - 4096, true>(xf[i % XD], xn_v, xn_b + (size_t)(i >> 3) * 8192);
      if constexpr (k == OX && i >= 64) {
        constexpr int q = i - 64, v = q >> 2;
        gld16<0, true>(xf[q % XD2], (unsigned)(rbw * 262144 + kg * 16384 + ((q & 1) ^ kg) * 1024 + l * 16), o_b + (size_t)(((v & 3) * 4 + ((q >> 1) & 1)) * 8192 + (v >> 2) * 2048));
      }
      if constexpr (k == OG) {
        constexpr int v = i >> 2, p = i & 3;
        if constexpr (v < 8) {
          const char* sb = wq_b + (size_t)(v * 128 + (p >> 1) * 64) * 8192;
          glds<(p & 1) * 128>(ldsw + (v & 3) * 32768 + (p >> 1) * 16384 + (p & 1) * (1024 - 128), wq_v, sb);
        } else if constexpr (v < 16) {
          glds<p * 1024, true>(ldsk + (v & 3) * 32768, kv_v, kv_b + (size_t)(v - 8) * 32768);
        } else {
          const unsigned wo_v = (unsigned)((((wid >> 2) * 128 + (wid & 3) * 8 + ((l & 31) >> 2)) * DM + (l >> 5) * 32 + ((l & 3) ^ (2 * (wid & 1))) * 8) * 2);
          const char* sb = wo_b + (size_t)(((v & 3) * 4 + (p >> 1) * 2) * 128 + ((v - 16) >> 2) * 32) * (DM * 2) + (((p & 1) ^ kh ^ 1) << 7);
          glds<0>(ldsw + (v & 3) * 32768 + (p >> 1) * 16384 + (p & 1) * 1024, wo_v, sb);
        }
      }
      if constexpr (k == OF && i < 8)
        glds<0>(ldsf + (i & 3) * 8192, (unsigned)(((l >> 3) * FF + wid * 64 + ((l & 7) ^ ((i & 1) * 4 + ((l >> 3) & 2))) * 8) * 2),
                (const char*)(i < 4 ? wgate : wup) + ((size_t)(fsl * 32 + 8 * (i & 3)) * FF + fgr * 512) * 2);
      if constexpr (k == OF && i >= 8) {
        const int frho = l >> 2, fch = (l & 3) ^ (frho & 4 ? 2 : 0);
        glds<0>(ldsf + ((i - 8) % 3) * 8192, (unsigned)((frho * DM + wid * 128 + fch * 8) * 2), (const char*)wdown + (size_t)fj * 16 * DM * 2 + (i - 8) * 64);
      }
      if constexpr (k == OS && i == 0) {
        const int idx = SEQ - 1 - (int)(key & 2047u);
        gld16<0, true>(fxs, (unsigned)((((idx >> 5) * 32 + fsl) * 2 + ((idx >> 4) & 1)) * 1024 + (g * 16 + (idx & 15)) * 16), ws + WS_XN);
      }
      if constexpr (k == OS && i == 2) gld16<0, true>(fm[0], gu_v, ws + WS_GU);
      if constexpr (k == OM && i > 0) gld8<0, true>(fg, fo_v, ws + WS_FO + (i - 1) * 16384);
      if constexpr (k == OM && i == 0) gld8<0, true>(fg, (unsigned)((t & 255) * 8), wid < 4 ? ws + WS_G0 : ws + WS_FK + (head >> 3) * 2048);
      if constexpr (k == OW) {
        constexpr int os = i >> 1, qb = i & 1;
        PIN(o[os & 1][0][qb]); PIN(o[os & 1][1][qb]);
        const f32x4 a = o[os & 1][0][qb] * inv[qb], b = o[os & 1][1][qb] * inv[qb];
        gst16<i * 1024 - 4096>((u32x4){pk2(a[0], a[1]), pk2(a[2], a[3]), pk2(b[0], b[1]), pk2(b[2], b[3])}, (unsigned)(wid * 262144 + l * 16), od_b);
      }
      if constexpr (k == OE && i < 2)
        glds<0>(ldsf + i * 8192, (unsigned)(((erow - c + i * 8 + (l >> 3)) * DM + nb * 128 + kh * 64 + ((l & 7) ^ (i * 4 + (l >> 4))) * 8) * 2), x);
      if constexpr (k == OE && i == 2) gld8<0, true>(st, (unsigned)(erow * 8), ws + WS_ST);
    });
  };
  f32x4 fa0, fa1, fq[4];
  s16x4 fb0, fb1, fxa;
  bf16x8 ft[2];
  auto endslot = [&](auto Sl) AI {
    constexpr int sl = Sl, u = sl >> 3, i = sl & 7;
    if constexpr (sl == 3) {
      if (wid == 6 || wid == 7) touch((unsigned)(((wid - 4) * 256 + within * 8 + (l >> 3)) * 8192 + xcd * 1024 + (l & 7) * 128), wq);
    }
    if constexpr (sl == 2) {
      if (wid < 4 && l == 0) gst8((u32x2){tlo, thi}, (unsigned)((bid * 4 + wid) * 8), ws + WS_FK);
    }
    if constexpr (sl == 38) {
      waitv<SC.younger(OM, 0, sl)>(fg);
      const unsigned lo = wid < 4 ? glo : tlo, hi = wid < 4 ? ghi : thi, mk = wid < 4 ? 0xf8000000u : 0xffffffffu;
      for (int spin = 0; spin < SPINS; ++spin) {
        if (__builtin_expect(__all(((fg[0] & mk) == lo) & (fg[1] == hi)), 1)) break;
        __builtin_amdgcn_s_sleep(1);
        gld8<0, true>(fg, (unsigned)((t & 255) * 8), wid < 4 ? ws + WS_G0 : ws + WS_FK + (head >> 3) * 2048);
        waitv<0>(fg);
      }
      const unsigned km = wave_max_u32(fg[0] & 0x07ffffffu);
      *(LAS unsigned*)(lds + HSC + wid * 4) = km;
    }
    if constexpr (sl == 47) {
      if (bid == 0 && t == 0) gst8((u32x2){tlo, thi}, 0u, ws + WS_EP);
      const u32x4 kk = *(LAS const u32x4*)(lds + HSC);
      key = __builtin_amdgcn_readfirstlane(umax(umax(kk[0], kk[1]), umax(kk[2], kk[3])));
    }
    if constexpr (sl == 58 || sl == 61 || sl == 64 || sl == 67) {
      if (wid == 4 + (sl - 58) / 3) touch((unsigned)(((wid & 3) * 64 + l) * 128), (const char*)wdown + (size_t)fj * 16 * DM * 2);
    }
    if constexpr (sl == 81 || sl == 85 || sl == 89 || sl == 93) {
      if (wid == (sl - 81) / 4)
        touch((unsigned)((kh * 2048 + (within >> 1) * 128 + wid * 32 + ((within & 1) * 2 + (xcd & 1)) * 8 + (l >> 3)) * (DM * 2) + ((xcd >> 1) & 1) * 1024 + (l & 7) * 128), wo);
    }
    if constexpr ((sl >= 68 && sl <= 70) || (sl >= 90 && sl <= 92)) {
      constexpr int r = sl >= 90, q = sl - (r ? 90 : 68);
      if constexpr (q == 0) waitv<SC.younger(OF, 4 * r + 3, sl)>(fxs);
      if constexpr (q >= 1) {
#pragma unroll
        for (int j = 0; j < 2; ++j) { fq[2 * q - 2 + j] = __builtin_amdgcn_mfma_f32_16x16x32_bf16(fxs, ft[j], (f32x4){}, 0, 0, 0); PIN(fq[2 * q - 2 + j]); }
      }
      if constexpr (q < 2) {
        const unsigned rp = ldsf + g * 8192 + (c >> 2) * 128 + ((((g & 1) << 1) | (c >> 3)) << 5) + (c & 3) * 8;
#pragma unroll
        for (int j = 0; j < 2; ++j) { LAS const char* a = (LAS const char*)(uintptr_t)(rp ^ ((2 * q + j) << 5)); ft[j] = cat8(trd(a), trd(a + 512)); }
      }
    }
    if constexpr (sl == 71 || sl == 93) {
      const float v = sel2(g & 2, sel2(g & 1, fq[3][0], fq[2][0]), sel2(g & 1, fq[1][0], fq[0][0]));
      gst8((u32x2){__float_as_uint(v), tlo}, (unsigned)(((fsl * 2 + (sl == 93)) * FF + fgr * 512 + t) * 8), ws + WS_GU);
    }
    if constexpr (sl == 109) {
      waitv<SC.younger(OS, 2, sl)>(fm[0]);
      for (int spin = 0; spin < SPINS; ++spin) {
        if (__builtin_expect(__all((fm[0][1] == tlo) & (fm[0][3] == tlo)), 1)) break;
        __builtin_amdgcn_s_sleep(1);
        gld16<0, true>(fm[0], gu_v, ws + WS_GU);
        waitv<0>(fm[0]);
      }
      *(LAS f32x2*)(lds + RED1 + (wid * 32 + 2 * c) * 4) = (f32x2){xg_sum(__uint_as_float(fm[0][0])), xg_sum(__uint_as_float(fm[0][2]))};
    }
    if constexpr (sl == 111) {
#pragma unroll
      for (int w = 0; w < 2; ++w) { fa0[w] = *(LAS const float*)(lds + RED1 + ((2 * g + w) * 32 + c) * 4); fa0[2 + w] = *(LAS const float*)(lds + RED1 + ((2 * g + w) * 32 + 16 + c) * 4); }
    }
    if constexpr (sl == 112) { fa0[0] = xg_sum(fa0[0] + fa0[1]); fa0[2] = xg_sum(fa0[2] + fa0[3]); PIN(fa0); }
    if constexpr (sl == 113) {
      const float hh = rbf(silu_f(rbf(fa0[0]))) * rbf(fa0[2]);
      *(LAS u16*)(lds + HSC + 32 + wid * 32 + c * 2) = (u16)pk2(hh, hh);
    }
    if constexpr (sl == 114) fxa = *(LAS const s16x4*)(lds + HSC + 32 + wid * 32 + g * 8);
    constexpr int pr = fpiece(sl), pm = fpiece(sl - 1), ps = fpiece(sl - 2);
    if constexpr (ps >= 0) gst4<ps * 128>(sel2(g & 1, fa1[0], fa0[0]), (unsigned)((wid * 128 + (l & 31)) * 4), ws + WS_YP + (size_t)yrow * 4096);
    if constexpr (pm >= 0) { fa0 = MFMA16(fxa, fb0, (f32x4){}); fa1 = MFMA16(fxa, fb1, (f32x4){}); PIN(fa0); PIN(fa1); }
    if constexpr (pr >= 0) {
      waitv<SC.younger(OF, 8 + pr, sl)>();
      const unsigned fp = ldsf + (4 * g + (c >> 2)) * 64 + (g & 1) * 32 + (c & 3) * 8;
      fb0 = trd((LAS const char*)(uintptr_t)fp + (pr % 3) * 8192); fb1 = trd((LAS const char*)(uintptr_t)(fp ^ 32) + (pr % 3) * 8192);
    }
    if constexpr (sl == 111 || sl == 119 || sl == 127) {
      waitv<SC.younger(OW, (sl - 111) / 4 + 1, sl)>();
      if (l == 0) gst8((u32x2){tlo, thi}, (unsigned)(((rb * 8 + wid) * 32 + head) * 8), ws + WS_FO + ((sl - 111) / 8) * 16384);
    }
    if constexpr (sl == 123) {
      waitv<SC.younger(OM, 1, sl)>(fg);
      if (__builtin_expect(!__all((fg[0] == tlo) & (fg[1] == thi)), 0)) poll8(fo_v, ws + WS_FO, tlo, thi, 0xffffffffu);
    }
    SBAR();
    if constexpr (i == 6) {
      if constexpr (u == 5 || u == 13) waitbar_l<SC.younger(OG, 4 * u + 7, sl)>();
      else waitbar<SC.younger(OG, 4 * u + 7, sl)>();
      if constexpr (u == 0 || u == 1 || u == 5 || u == 7 || u == 8 || u == 10 || u == 12 || u == 14)
        STAMP(u == 0 ? 4 : u == 1 ? 5 : u == 5 ? 6 : u == 7 ? 7 : u == 8 ? 8 : u == 10 ? 9 : u == 12 ? 10 : 11);
    }
    if constexpr (PRIO && !(i & 1)) __builtin_amdgcn_s_setprio(i == 6 ? 3 : 2 - i / 2);
  };

  issue(IC(-2), IC(-1));
  if (wid == 5) poll8((unsigned)((rb * 32 + (l & 31)) * 8), ws + WS_G0, glo, ghi, 0xf8000000u);
  waitbar<SC.younger(OG, 3, -2)>();
  issue(IC(-1), IC(-1));
  STAMP(3);

  LBase trb[2], frb;
  trb[0].init(lds0 + g * 2048 + (c >> 2) * 64 + (g & 1) * 32 + (c & 3) * 8);
  trb[1].init(lds0 + g * 2048 + (c >> 2) * 64 + ((g & 1) ^ 1) * 32 + (c & 3) * 8);
  frb.init(lds0 + l * 16);
  f32x4 acc[8][2];
  bf16x8 wf[2][4];
  auto WRD1 = [&](auto Si, auto Base, auto Dd) AI {
    constexpr int s = Si, d = Dd, o = Base + (d >> 1) * 512;
    wf[s & 1][d] = cat8(trd(trb[d & 1].template at<o>()), trd(trb[d & 1].template at<o + 256>()));
  };
  auto WRD = [&](auto Si, auto Base) AI { sfor<4>([&](auto Dd) AI { WRD1(Si, Base, Dd); }); };
#define WA(s) IC((((s) >> 3) & 3) * 32768 + (((s) >> 1) & 3) * 8192 + ((s) & 1) * 1024)
#define WB(s) IC((((s) >> 2) & 3) * 32768 + (((s) >> 1) & 1) * 8192 + ((s) & 1) * 1024)
  bf16x8 fr[4];
  auto FRD = [&](auto Ni) AI { constexpr int n = Ni; if constexpr (n < 256) fr[n & 3] = *(LAS const bf16x8*)(frb.at<((n >> 5) & 3) * 32768 + (n & 31) * 1024>()); };

  WRD(IC(0), WA(0));
  SBAR();
  bf16x8 qf[4][2];
  auto QPACK = [&](auto Ds) AI {
    constexpr int ds = Ds;
#pragma unroll
    for (int qb = 0; qb < 2; ++qb) {
      const f32x4 a = acc[2 * ds][qb] * QSCALE, b = acc[2 * ds + 1][qb] * QSCALE;
      qf[ds][qb] = __builtin_bit_cast(bf16x8, (u32x4){pk2(a[0], a[1]), pk2(a[2], a[3]), pk2(b[0], b[1]), pk2(b[2], b[3])});
      PIN(qf[ds][qb]);
    }
  };
  sfor<64>([&](auto Si) AI {
    constexpr int s = Si, x0 = (s & ~1) % XD, x1 = (s | 1) % XD;
    if constexpr (!(s & 1)) waitv<SC.younger(OX, s + 1, s - 1)>(xf[x0], xf[x1]);
    sfor<4>([&](auto Dd) AI {
      constexpr int d = Dd, dt = (s & 1) * 4 + d;
      if constexpr (s + 1 < 64) WRD1(IC(s + 1), WA(s + 1), Dd);
      else if constexpr (d < 3) FRD(Dd);
      SBAR();
      if constexpr (s < 2) { MFZ(acc[dt][0], wf[s & 1][d], xf[x0]); MFZ(acc[dt][1], wf[s & 1][d], xf[x1]); }
      else { MF(acc[dt][0], wf[s & 1][d], xf[x0]); MF(acc[dt][1], wf[s & 1][d], xf[x1]); }
      issue(Si, Dd);
      if constexpr (s == 63 && (d & 1)) { QPACK(IC(d >> 1)); SBAR(); }
    });
    SBAR();
    endslot(Si);
  });

  f32x4 sc[2][2][2], negm[2];
  u32x4 pw[16][2];
  f32x4 lsum[2];
  bf16x8 ones = {0x3f80, 0x3f80, 0x3f80, 0x3f80, 0x3f80, 0x3f80, 0x3f80, 0x3f80};
  PIN(ones);
#define MX3(a, b, c) __builtin_fmaxf(__builtin_fmaxf((a), (b)), (c))
#define EXP2(x) __builtin_amdgcn_exp2f(x)
  auto SM = [&](auto Hi, auto Pi) AI {
    constexpr int H = Hi, p = Pi, kt = p >> 2, qb = (p >> 1) & 1, r = 2 * (p & 1);
    const float e0 = EXP2(sc[H & 1][kt][qb][r]), e1 = EXP2(sc[H & 1][kt][qb][r + 1]);
    unsigned w = pk2(e0, e1);
    PIN(w);
    pw[H][qb][kt * 2 + (p & 1)] = w;
  };
  sfor<128>([&](auto Ni) AI {
    constexpr int n = Ni, H = n >> 3, i = n & 7, kt = i >> 2, ds = i & 3, sn = H & 1;
    FRD(IC(n + 3));
    if constexpr (n < 2) QPACK(IC(2 + n));
    sfor<2>([&](auto Qb) AI {
      constexpr int qb = Qb;
      if constexpr (ds == 0 && H == 0) MFZ(sc[sn][kt][qb], fr[n & 3], qf[0][qb]);
      else if constexpr (ds == 0) MFC(sc[sn][kt][qb], fr[n & 3], qf[0][qb], negm[qb]);
      else MF(sc[sn][kt][qb], fr[n & 3], qf[ds][qb]);
    });
    if constexpr (n == 5 || n == 6) {
      constexpr int qb = n - 5;
      NOPS(3); PIN(sc[0][0][qb]);
      const float m = -xg_max(__builtin_fmaxf(MX3(sc[0][0][qb][0], sc[0][0][qb][1], sc[0][0][qb][2]), sc[0][0][qb][3]));
      negm[qb] = (f32x4){m, m, m, m};
      sc[0][0][qb] += negm[qb];
      PIN(negm[qb]); PIN(sc[0][0][qb]);
    }
    if constexpr (n == 9 || n == 10) { NOPS(3); PIN(sc[0][1][n - 9]); sc[0][1][n - 9] += negm[n - 9]; PIN(sc[0][1][n - 9]); }
    issue(IC(64 + (n >> 2)), IC(n & 3));
    if constexpr (H == 2 && (i == 1 || i == 5)) MFZ(lsum[i >> 2], ones, pw[0][i >> 2]);
    if constexpr (H > 2 && (i == 1 || i == 5)) MF(lsum[i >> 2], ones, pw[H - 2][i >> 2]);
    if constexpr (H >= 1) SM(IC(H - 1), IC(i));
    if constexpr (n == 125) { PIN(negm[0]); PIN(negm[1]); }
    SBAR();
    if constexpr ((n & 3) == 3) endslot(IC(64 + (n >> 2)));
  });
  constexpr int CHK = 16;
  sfor<128>([&](auto Mi) AI {
    constexpr int m = Mi, n = 128 + m, os = m >> 5, H = (m >> 1) & 15, par = m & 1;
    FRD(IC(n + 3));
    if constexpr (H == 0) { MFZ(o[os & 1][par][0], fr[n & 3], pw[H][0]); MFZ(o[os & 1][par][1], fr[n & 3], pw[H][1]); }
    else { MF(o[os & 1][par][0], fr[n & 3], pw[H][0]); MF(o[os & 1][par][1], fr[n & 3], pw[H][1]); }
    if constexpr (m == 1 || m == 2) MF(lsum[m - 1], ones, pw[14][m - 1]);
    if constexpr (m < 8) SM(IC(15), Mi);
    if constexpr (m == 9 || m == 10) MF(lsum[m - 9], ones, pw[15][m - 9]);
    if constexpr (m == CHK + 1 || m == CHK + 2) { inv[m - CHK - 1] = __builtin_amdgcn_rcpf(lsum[m - CHK - 1][0]); PIN(inv[m - CHK - 1]); }
    issue(IC(64 + (n >> 2)), IC(n & 3));
    if constexpr (m == 123) {
      trb[0].init(lds0 + kg * 16384 + g * 2048 + (c >> 2) * 64 + (g & 1) * 32 + (c & 3) * 8);
      trb[1].init(lds0 + kg * 16384 + g * 2048 + (c >> 2) * 64 + ((g & 1) ^ 1) * 32 + (c & 3) * 8);
    }
    if constexpr (m >= 124) WRD1(IC(0), WB(0), IC(m - 124));
    SBAR();
    if constexpr ((n & 3) == 3) endslot(IC(64 + (n >> 2)));
    if constexpr (m == CHK - 1) {
      PIN(lsum[0]); PIN(lsum[1]);
      if (__builtin_expect(__any(!(lsum[0][0] + lsum[1][0] <= LBOUND)), 0)) {
        NOPS(7); SBAR();
        auto tile = [&](unsigned off, f32x4& s0, f32x4& s1) AI {
          bf16x8 kf[4];
          SBAR(); NOPS(7);
          sfor<4>([&](auto Ds) AI { constexpr int ds = Ds; gld16<ds * 1024, true>(kf[ds], off, kv_b); });
          waitv<0>(kf[0], kf[1]);
          PIN(kf[2]); PIN(kf[3]);
#pragma unroll
          for (int ds = 0; ds < 4; ++ds) {
            s0 = __builtin_amdgcn_mfma_f32_16x16x32_bf16(kf[ds], qf[ds][0], s0, 0, 0, 0);
            s1 = __builtin_amdgcn_mfma_f32_16x16x32_bf16(kf[ds], qf[ds][1], s1, 0, 0, 0);
          }
        };
        float m0 = -__builtin_inff(), m1 = m0;
#pragma unroll 1
        for (int kt = 0; kt < 32; ++kt) {
          f32x4 s0 = {}, s1 = {};
          tile((unsigned)(kt * 4096 + l * 16), s0, s1);
          m0 = MX3(MX3(m0, s0[0], s0[1]), s0[2], s0[3]); m1 = MX3(MX3(m1, s1[0], s1[1]), s1[2], s1[3]);
        }
        m0 = -xg_max(m0); m1 = -xg_max(m1);
        lsum[0] = (f32x4){}; lsum[1] = (f32x4){};
        sfor<16>([&](auto Hi) AI {
          constexpr int H = Hi;
#pragma unroll
          for (int kt = 0; kt < 2; ++kt) {
            sc[H & 1][kt][0] = (f32x4){m0, m0, m0, m0}; sc[H & 1][kt][1] = (f32x4){m1, m1, m1, m1};
            tile((unsigned)((2 * H + kt) * 4096 + l * 16), sc[H & 1][kt][0], sc[H & 1][kt][1]);
          }
          sfor<8>([&](auto Pi) AI { SM(Hi, Pi); });
          PIN(pw[H][0]); PIN(pw[H][1]);
          lsum[0] = __builtin_amdgcn_mfma_f32_16x16x32_bf16(ones, __builtin_bit_cast(bf16x8, pw[H][0]), lsum[0], 0, 0, 0);
          lsum[1] = __builtin_amdgcn_mfma_f32_16x16x32_bf16(ones, __builtin_bit_cast(bf16x8, pw[H][1]), lsum[1], 0, 0, 0);
        });
#pragma unroll
        for (int par = 0; par < 2; ++par) { o[0][par][0] = (f32x4){}; o[0][par][1] = (f32x4){}; }
        sfor<CHK / 4>([&](auto Qi) AI {
          constexpr int q = Qi;
          bf16x8 vf[4];
          SBAR(); NOPS(7);
          sfor<4>([&](auto J) AI { constexpr int j = J; gld16<j * 1024, true>(vf[j], (unsigned)((128 + 4 * q) * 1024 + l * 16), kv_b); });
          waitv<0>(vf[0], vf[1]);
          PIN(vf[2]); PIN(vf[3]);
#pragma unroll
          for (int j = 0; j < 4; ++j)
#pragma unroll
            for (int qb = 0; qb < 2; ++qb)
              o[0][j & 1][qb] = __builtin_amdgcn_mfma_f32_16x16x32_bf16(vf[j], __builtin_bit_cast(bf16x8, pw[2 * q + (j >> 1)][qb]), o[0][j & 1][qb], 0, 0, 0);
        });
#pragma unroll
        for (int par = 0; par < 2; ++par) { PIN(o[0][par][0]); PIN(o[0][par][1]); }
        PIN(lsum[0]); PIN(lsum[1]);
        SBAR(); NOPS(7);
      }
      SBAR();
    }
  });
  STAMP(12); STAMP(13); STAMP(14); STAMP(15);

  const int idx = SEQ - 1 - (int)(key & 2047u);
  const bool owner = (idx >> 7) == mb, ownw = (idx >> 4) == mb * 8 + rbw * 2 + kg;
  f32x4 yp[7], ya[4], oth[4];
  unsigned gate[8];
  float glow;
  const unsigned ex_a = ldsf + (c >> 3) * 8192 + (c & 7) * 128 + (((g >> 1) ^ (c >> 1)) << 4) + (g & 1) * 8;
  auto item = [](int s) { return s < 48 ? s : s < 56 ? 48 + 2 * (s - 48) : 49 + 2 * (s - 56); };
  u32x2 pf;
  sfor<64>([&](auto Si) AI {
    constexpr int s = Si, u = s >> 2, it = item(s), x0 = (it & ~1) % XD2, x1 = (it | 1) % XD2;
    if constexpr (!(it & 1)) waitv<SC.younger(OX, 64 + it + 1, B0 + s - 1)>(xf[x0], xf[x1]);
    sfor<4>([&](auto Dd) AI {
      constexpr int d = Dd, dt = (it & 1) * 4 + d;
      if constexpr (s + 1 < 64) WRD1(IC(s + 1), WB(item(s + 1)), Dd);
      SBAR();
      if constexpr (s < 2) { MFZ(acc[dt][0], wf[s & 1][d], xf[x0]); MFZ(acc[dt][1], wf[s & 1][d], xf[x1]); }
      else { MF(acc[dt][0], wf[s & 1][d], xf[x0]); MF(acc[dt][1], wf[s & 1][d], xf[x1]); }
      issue(IC(B0 + s), Dd);
      if constexpr (s == 56) *(LAS f32x4*)(lds + (wid ^ 4) * 4096 + l * 16 + (d & 1) * 2048 + (d >> 1) * 32768) = acc[d][1];
      if constexpr (s == 57 && d == 0) {
#pragma unroll
        for (int e = 0; e < 4; ++e) oth[e] = *(LAS const f32x4*)(lds + wid * 4096 + l * 16 + (e & 1) * 2048 + (e >> 1) * 32768);
      }
      if constexpr (s == 57 && d >= 2) {
        gst16<(2 * d - 4) * 1024>(acc[2 * d - 4][0] + oth[2 * d - 4], (unsigned)(wid * 4096 + l * 16), ws + WS_XC + (size_t)(bid ^ 4) * 32768);
        gst16<(2 * d - 3) * 1024>(acc[2 * d - 3][0] + oth[2 * d - 3], (unsigned)(wid * 4096 + l * 16), ws + WS_XC + (size_t)(bid ^ 4) * 32768);
      }
      if constexpr (s == 34) xe[d] = *(LAS const u32x2*)(uintptr_t)(ex_a ^ (d << 5));
      if constexpr (s >= 36 && s < 44 && d == 1) {
        constexpr int j = s - 36;
        PIN(st);
        const unsigned xw = xe[j >> 1][j & 1];
        const unsigned dd = pk2(bflo(xw) - st[0], bfhi(xw) - st[0]);
        gate[j] = pk2(bflo(dd) * st[1], bfhi(dd) * st[1]);
        PIN(gate[j]);
      }
      if constexpr (s >= 47 && s < 63 && d == 1) {
        constexpr int j = (s - 47) >> 1;
        if constexpr ((s - 47) & 1) { gate[j] = pk2(glow, silu_f(bfhi(gate[j]))); PIN(gate[j]); }
        else { glow = silu_f(bflo(gate[j])); PIN(glow); }
      }
    });
    SBAR();
    if constexpr (s == 7) {
      waitv<SC.younger(OW, 7, B0 + s)>();
      if (l == 0) {
        gst8((u32x2){tlo, thi}, (unsigned)(((rb * 8 + wid) * 32 + head) * 8), ws + WS_FO + 3 * 16384);
        gst8((u32x2){tlo, thi}, (unsigned)((wid * 256 + yrow) * 8), ws + WS_FY);
      }
    }
    if constexpr (s == 4 || s == 17 || s == 33) {
      constexpr int j = s == 4 ? 1 : s == 17 ? 2 : 3;
      waitv<SC.younger(OM, j + 1, B0 + s)>(fg);
      if (__builtin_expect(!__all((fg[0] == tlo) & (fg[1] == thi)), 0)) poll8(fo_v, ws + WS_FO + j * 16384, tlo, thi, 0xffffffffu);
    }
    if constexpr (s == 60) {
      waitv<0>();
      if (l == 0) gst8((u32x2){tlo, thi}, (unsigned)(((bid ^ 4) * 8 + wid) * 64), ws + WS_FX);
    }
    if constexpr (s == 61) gld8<0, true>(pf, (unsigned)((bid * 8 + wid) * 64), ws + WS_FX);
    if constexpr (s == 13) {
      if (owner) { gld16<0, true>(fm[0], (unsigned)(nb * 2048 + (l < 56 ? l : 55) * 32), ws + WS_FY); gld16<16, true>(fm[1], (unsigned)(nb * 2048 + (l < 56 ? l : 55) * 32), ws + WS_FY); }
    }
    if constexpr (s == 28) {
      if (owner) {
        PIN(fm[0]); PIN(fm[1]);
        for (int spin = 0; spin < SPINS; ++spin) {
          const bool ok = (fm[0][0] == tlo) & (fm[0][1] == thi) & (fm[0][2] == tlo) & (fm[0][3] == thi) & (fm[1][0] == tlo) & (fm[1][1] == thi) & (fm[1][2] == tlo) & (fm[1][3] == thi);
          if (__builtin_expect(__all(ok), 1)) break;
          __builtin_amdgcn_s_sleep(1);
          gld16<0, true>(fm[0], (unsigned)(nb * 2048 + (l < 56 ? l : 55) * 32), ws + WS_FY); gld16<16, true>(fm[1], (unsigned)(nb * 2048 + (l < 56 ? l : 55) * 32), ws + WS_FY);
          waitv<0>(fm[0], fm[1]);
        }
        const unsigned yv = (unsigned)(((t >> 4) * 1024 + nb * 128 + kh * 64 + (t & 15) * 4) * 4);
        sfor<7>([&](auto I) AI { constexpr int q = I; gld16<0, true>(yp[q], yv, ws + WS_YP + (size_t)q * 32 * 4096); });
      }
    }
    if constexpr (s == 43) {
      if (owner) {
        f32x4 a = {};
#pragma unroll
        for (int q = 0; q < 7; ++q) { PIN(yp[q]); a += yp[q]; }
#pragma unroll
        for (int j = 0; j < 4; ++j) a[j] = xg_sum(a[j]);
        *(LAS f32x4*)(lds + YS + (wid * 64 + c * 4) * 4) = a;
      }
    }
    if constexpr (s == 45) {
      if (ownw) {
        float a = 0.f;
#pragma unroll
        for (int p = 0; p < 8; ++p) a += *(LAS const float*)(lds + YS + (p * 64 + l) * 4);
        const unsigned ord = key >> 11, bits = (ord & 0x8000u) ? (ord & 0x7fffu) : (~ord & 0xffffu);
        *(LAS float*)(lds + RED1 + l * 4) = rbf(rbf(a) * rbf(sigmoid_f(bflo(bits))));
#pragma unroll
        for (int q = 0; q < 4; ++q) ya[q] = *(LAS const f32x4*)(lds + RED1 + (q * 16 + 4 * g) * 4);
      }
    }
    if constexpr (s == 46) {
      if (ownw) {
#pragma unroll
        for (int j = 0; j < 8; ++j) {
          const unsigned w = pk2(bflo(gate[j]) + ya[j >> 1][2 * (j & 1)], bfhi(gate[j]) + ya[j >> 1][2 * (j & 1) + 1]);
          gate[j] = erow == idx ? w : gate[j];
          PIN(gate[j]);
        }
      }
    }
    SBAR();
    if constexpr (((s & 3) == 2 && u < 11) || s == 44) {
      if constexpr (s == 44) waitbar_l<SC.younger(OG, 64 + 4 * u + 7, B0 + s)>();
      else waitbar<SC.younger(OG, 64 + 4 * u + 7, B0 + s)>();
      if constexpr (u % 4 == 1) STAMP(16 + u / 4);
    }
    if constexpr (s == 48) waitbar<SC.younger(OG, 64 + 55, B0 + s)>();
    if constexpr (s == 50) waitbar<SC.younger(OG, 64 + 59, B0 + s)>();
    if constexpr (s == 52) { waitbar<SC.younger(OG, 64 + 63, B0 + s)>(); STAMP(19); }
    if constexpr (s == 56) waitbar_l<63>();
    if constexpr (PRIO) __builtin_amdgcn_s_setprio(3 - ((s + 2) & 3));
  });
  STAMP(20);

  f32x4 kep[4];
  {
    waitv<0>(pf);
    const bool rdy = __all((pf[0] == tlo) & (pf[1] == thi));
    if (rdy) sfor<4>([&](auto Dd) AI { constexpr int d = Dd; gld16<d * 1024, true>(oth[d], (unsigned)(wid * 4096 + l * 16), ws + WS_XC + (size_t)bid * 32768); });
    NOPS(3);
#pragma unroll
    for (int d = 0; d < 4; ++d) { PIN(acc[4 + d][1]); *(LAS f32x4*)(lds + 65536 + (wid ^ 4) * 4096 + l * 16 + (d & 1) * 2048 + (d >> 1) * 32768) = acc[4 + d][1]; }
    waitbar_l<63>();
    STAMP(21);
#pragma unroll
    for (int d = 0; d < 4; ++d) kep[d] = acc[4 + d][0] + *(LAS const f32x4*)(lds + 65536 + wid * 4096 + l * 16 + (d & 1) * 2048 + (d >> 1) * 32768);
    STAMP(22);
    if (!rdy) {
      poll8((unsigned)((bid * 8 + wid) * 64), ws + WS_FX, tlo, thi, 0xffffffffu);
      sfor<4>([&](auto Dd) AI { constexpr int d = Dd; gld16<d * 1024, true>(oth[d], (unsigned)(wid * 4096 + l * 16), ws + WS_XC + (size_t)bid * 32768); });
    }
    STAMP(23);
    waitv<0>(oth[0], oth[1]);
    PIN(oth[2]); PIN(oth[3]);
    STAMP(24);
  }
#pragma unroll
  for (int d = 0; d < 4; ++d) {
    const f32x4 a = kep[d] + oth[d];
    u32x2 res;
#pragma unroll
    for (int j = 0; j < 2; ++j) {
      const unsigned sp = pk2(a[2 * j], a[2 * j + 1]), gp = gate[2 * d + j], xw = xe[d][j];
      const unsigned pr = pk2(bflo(sp) * bflo(gp), bfhi(sp) * bfhi(gp));
      res[j] = pk2(bflo(xw) + bflo(pr), bfhi(xw) + bfhi(pr));
    }
    *(LAS u32x2*)(uintptr_t)(ex_a ^ (d << 5)) = res;
  }
#pragma unroll
  for (int i = 0; i < 2; ++i)
    *(u32x4*)(out + (size_t)(erow - c + i * 8 + (l >> 3)) * DM + nb * 128 + kh * 64 + ((l & 7) ^ (i * 4 + (l >> 4))) * 8) = *(LAS const u32x4*)(uintptr_t)(ldsf + i * 8192 + l * 16);
  STAMP(25);
  STAMP_OUT(0, 26);
}

extern "C" void kernel_launch(void* const* d_in, const int* in_sizes, int n_in, void* d_out, int out_size, void* d_ws, size_t ws_size,
                              hipStream_t stream) {
  k_fused<<<256, 512, 0, stream>>>((const u16*)d_in[0], (const u16*)d_in[1], (const u16*)d_in[2], (const u16*)d_in[3], (const u16*)d_in[4],
                                   (const u16*)d_in[6], (const u16*)d_in[7], (const u16*)d_in[8], (const u16*)d_in[9], (char*)d_ws, (u16*)d_out);
}
```

```cpp
#include <hip/hip_runtime.h>
#include <stdint.h>
#include <type_traits>

typedef unsigned short u16;
typedef short bf16x8 __attribute__((ext_vector_type(8)));
typedef short s16x4 __attribute__((ext_vector_type(4)));
typedef float f32x4 __attribute__((ext_vector_type(4)));
typedef float f32x2 __attribute__((ext_vector_type(2)));
typedef unsigned u32x4 __attribute__((ext_vector_type(4)));
typedef unsigned u32x2 __attribute__((ext_vector_type(2)));
typedef __bf16 bf2_t __attribute__((ext_vector_type(2)));
#define LAS __attribute__((address_space(3)))
#define DI __device__ __forceinline__
#define AI __attribute__((always_inline))
#define SBAR() __builtin_amdgcn_sched_barrier(0)
template <class T> __device__ __forceinline__ void PIN(T& x) { asm volatile("" : "+v"(x)); }
template <class B> DI void MF(f32x4& acc, const bf16x8& a, const B& b) { asm volatile("v_mfma_f32_16x16x32_bf16 %0, %1, %2, %0" : "+v"(acc) : "v"(a), "v"(b)); }
template <class B> DI void MFC(f32x4& d, const bf16x8& a, const B& b, const f32x4& c) { asm volatile("v_mfma_f32_16x16x32_bf16 %0, %1, %2, %3" : "=&v"(d) : "v"(a), "v"(b), "v"(c)); }
template <class B> DI void MFZ(f32x4& d, const bf16x8& a, const B& b) { asm volatile("v_mfma_f32_16x16x32_bf16 %0, %1, %2, 0" : "=&v"(d) : "v"(a), "v"(b)); }
#define NOPS(n) asm volatile("s_nop " #n)
#define MFMA16(a, b, c) __builtin_amdgcn_mfma_f32_16x16x16bf16_1k((a), (b), (c), 0, 0, 0)
#define IC(n) std::integral_constant<int, (n)>{}

constexpr int SEQ = 2048, DM = 1024, DH = 128, SK = 2048, WIN = 512, FF = 3584;
constexpr size_t WS_XN = 0;
constexpr size_t WS_KV = 4u << 20;
constexpr size_t WS_ST = 5u << 20;
constexpr size_t WS_EP = WS_ST + 16384;
constexpr size_t WS_G0 = WS_ST + 32768;
constexpr size_t WS_FK = WS_ST + 49152;
constexpr size_t WS_YP = WS_ST + 65536;
constexpr size_t WS_FO = 7u << 20;
constexpr size_t WS_FY = WS_FO + 65536;
constexpr size_t WS_FX = WS_FO + 131072;
constexpr size_t WS_O = 8u << 20;
constexpr size_t WS_GU = 32u << 20;
constexpr size_t WS_XC = 24u << 20;

template <int I, int N, class F> DI void sfor_impl(F&& f) {
  if constexpr (I < N) { f(std::integral_constant<int, I>{}); sfor_impl<I + 1, N>(f); }
}
template <int N, class F> DI void sfor(F&& f) { sfor_impl<0, N>(f); }

DI unsigned pk2(float a, float b) { f32x2 v = {a, b}; return __builtin_bit_cast(unsigned, __builtin_convertvector(v, bf2_t)); }
DI float rbf(float a) { return (float)(__bf16)a; }
DI float bflo(unsigned u) { return __uint_as_float(u << 16); }
DI float bfhi(unsigned u) { return __uint_as_float(u & 0xffff0000u); }
DI float sigmoid_f(float v) { return __builtin_amdgcn_rcpf(1.f + __builtin_amdgcn_exp2f(v * -1.4426950408889634f)); }
DI float sel2(bool c, float a, float b) { PIN(a); PIN(b); return c ? a : b; }
DI float silu_f(float v) { return v * sigmoid_f(v); }
template <int CTRL> DI float dppf(float x) { return __builtin_bit_cast(float, __builtin_amdgcn_mov_dpp(__builtin_bit_cast(int, x), CTRL, 0xf, 0xf, true)); }
template <int CTRL> DI unsigned dppu(unsigned x) { return (unsigned)__builtin_amdgcn_mov_dpp((int)x, CTRL, 0xf, 0xf, true); }
constexpr int XOR1 = 0xB1, XOR2 = 0x4E, XOR7 = 0x141, XOR8 = 0x128;
DI float xg_sum(float x) {
  auto s = __builtin_amdgcn_permlane16_swap(__float_as_uint(x), __float_as_uint(x), false, false);
  x = __uint_as_float(s[0]) + __uint_as_float(s[1]);
  auto t = __builtin_amdgcn_permlane32_swap(__float_as_uint(x), __float_as_uint(x), false, false);
  return __uint_as_float(t[0]) + __uint_as_float(t[1]);
}
DI float xg_max(float x) {
  auto s = __builtin_amdgcn_permlane16_swap(__float_as_uint(x), __float_as_uint(x), false, false);
  x = __builtin_fmaxf(__uint_as_float(s[0]), __uint_as_float(s[1]));
  auto t = __builtin_amdgcn_permlane32_swap(__float_as_uint(x), __float_as_uint(x), false, false);
  return __builtin_fmaxf(__uint_as_float(t[0]), __uint_as_float(t[1]));
}
DI float wave_sum(float v) { v += dppf<XOR1>(v); v += dppf<XOR2>(v); v += dppf<XOR7>(v); v += dppf<XOR8>(v); return xg_sum(v); }
DI unsigned umax(unsigned a, unsigned b) { return a > b ? a : b; }
DI unsigned wave_max_u32(unsigned v) {
  v = umax(v, dppu<XOR1>(v)); v = umax(v, dppu<XOR2>(v)); v = umax(v, dppu<XOR7>(v)); v = umax(v, dppu<XOR8>(v));
  auto s = __builtin_amdgcn_permlane16_swap(v, v, false, false); v = umax(s[0], s[1]);
  auto t = __builtin_amdgcn_permlane32_swap(v, v, false, false); v = umax(t[0], t[1]);
  return __builtin_amdgcn_readfirstlane(v);
}

template <int OFF, bool DEV = false> DI void glds(unsigned ldsaddr, unsigned voff, const void* sbase) {
  if constexpr (DEV) asm volatile("s_mov_b32 m0, %0\n\ts_nop 0\n\tglobal_load_lds_dwordx4 %1, %2 offset:%3 sc1" ::"s"(ldsaddr), "v"(voff), "s"(sbase), "i"(OFF) : "memory");
  else asm volatile("s_mov_b32 m0, %0\n\ts_nop 0\n\tglobal_load_lds_dwordx4 %1, %2 offset:%3" ::"s"(ldsaddr), "v"(voff), "s"(sbase), "i"(OFF) : "memory");
}
DI void touch(unsigned voff, const void* sbase) {
  asm volatile("global_load_dword v255, %0, %1" ::"v"(voff), "s"(sbase) : "memory", "v255");
}
template <int OFF, bool DEV = false, class T> DI void gld16(T& dst, unsigned voff, const void* sbase) {
  if constexpr (DEV) asm volatile("global_load_dwordx4 %0, %1, %2 offset:%3 sc1" : "=v"(dst) : "v"(voff), "s"(sbase), "i"(OFF) : "memory");
  else asm volatile("global_load_dwordx4 %0, %1, %2 offset:%3" : "=v"(dst) : "v"(voff), "s"(sbase), "i"(OFF) : "memory");
}
template <int OFF, bool DEV = false, class T> DI void gld8(T& dst, unsigned voff, const void* sbase) {
  if constexpr (DEV) asm volatile("global_load_dwordx2 %0, %1, %2 offset:%3 sc1" : "=v"(dst) : "v"(voff), "s"(sbase), "i"(OFF) : "memory");
  else asm volatile("global_load_dwordx2 %0, %1, %2 offset:%3" : "=v"(dst) : "v"(voff), "s"(sbase), "i"(OFF) : "memory");
}
template <int OFF> DI void gld2(unsigned& dst, unsigned voff, const void* sbase) {
  asm volatile("global_load_ushort %0, %1, %2 offset:%3" : "=v"(dst) : "v"(voff), "s"(sbase), "i"(OFF) : "memory");
}
template <int OFF, class T> DI void gst16(const T& v, unsigned voff, const void* sbase) {
  asm volatile("global_store_dwordx4 %0, %1, %2 offset:%3 sc1\n\ts_nop 1" ::"v"(voff), "v"(v), "s"(sbase), "i"(OFF) : "memory");
}
template <class T> DI void gst8(const T& v, unsigned voff, const void* sbase) {
  asm volatile("global_store_dwordx2 %0, %1, %2 sc1" ::"v"(voff), "v"(v), "s"(sbase) : "memory");
}
template <int OFF> DI void gst4(float v, unsigned voff, const void* sbase) {
  asm volatile("global_store_dword %0, %1, %2 offset:%3 sc1" ::"v"(voff), "v"(v), "s"(sbase), "i"(OFF) : "memory");
}
constexpr int SPINS = 1 << 16;
DI void poll8(unsigned voff, const void* sbase, unsigned lo, unsigned hi, unsigned lomask) {
  for (int spin = 0; spin < SPINS; ++spin) {
    u32x2 v;
    asm volatile("global_load_dwordx2 %0, %1, %2 sc1\n\ts_waitcnt vmcnt(0)" : "=&v"(v) : "v"(voff), "s"(sbase) : "memory");
    if (__all(((v[0] & lomask) == lo) & (v[1] == hi))) break;
    __builtin_amdgcn_s_sleep(1);
  }
}
template <int N> DI void waitv() { static_assert(N >= 0, "count"); asm volatile("s_waitcnt vmcnt(%0)" ::"n"(N > 63 ? 63 : N) : "memory"); }
template <int N, class T> DI void waitv(T& x) { static_assert(N >= 0, "count"); asm volatile("s_waitcnt vmcnt(%1)" : "+v"(x) : "n"(N > 63 ? 63 : N) : "memory"); }
template <int N, class T> DI void waitv(T& x, T& y) { static_assert(N >= 0, "count"); asm volatile("s_waitcnt vmcnt(%2)" : "+v"(x), "+v"(y) : "n"(N > 63 ? 63 : N) : "memory"); }
template <int N> DI void waitbar() { static_assert(N >= 0, "count"); asm volatile("s_waitcnt vmcnt(%0)\n\ts_barrier" ::"n"(N > 63 ? 63 : N) : "memory"); }
template <int N> DI void waitbar_l() { static_assert(N >= 0, "count"); asm volatile("s_waitcnt vmcnt(%0) lgkmcnt(0)\n\ts_barrier" ::"n"(N > 63 ? 63 : N) : "memory"); }
typedef short v4i16_t __attribute__((ext_vector_type(4)));
DI s16x4 trd(LAS const char* p) { return __builtin_amdgcn_ds_read_tr16_b64_v4i16((LAS v4i16_t*)p); }
struct LBase {
  unsigned lo, hi;
  DI void init(unsigned a) { lo = a; hi = a + 65536; PIN(lo); PIN(hi); }
  template <int OFF> DI LAS const char* at() const { return (LAS const char*)(uintptr_t)(OFF < 65536 ? lo : hi) + (OFF & 65535); }
};
DI bf16x8 cat8(s16x4 lo, s16x4 hi) { return __builtin_shufflevector(lo, hi, 0, 1, 2, 3, 4, 5, 6, 7); }

enum { OX, OG, OF, OM, OS, OP, OE, OW };
struct Sched {
  int n; short kind[400], idx[400], slot[400];
  constexpr Sched() : n(0), kind{}, idx{}, slot{} {}
  constexpr void add(int s, int k, int i) { kind[n] = (short)k; idx[n] = (short)i; slot[n] = (short)s; ++n; }
  constexpr int younger(int k, int i, int s) const {
    int p = -1;
    for (int q = 0; q < n; ++q) if (kind[q] == k && idx[q] == i) p = q;
    if (p < 0 || slot[p] > s) return -1;
    int c = 0;
    for (int q = p + 1; q < n; ++q) if (slot[q] <= s) ++c;
    return c;
  }
  constexpr int first(int s) const { int q = 0; while (q < n && slot[q] < s) ++q; return q; }
  constexpr int count(int s) const { int c = 0; for (int q = 0; q < n; ++q) if (slot[q] == s) ++c; return c; }
};

#define STAMP_DECL
#define STAMP(i)
#define STAMP_OUT(kid, n)

constexpr int RING = 4 * 32768, FB = RING, RED1 = FB + 24576, HSC = RED1 + 1024, YS = HSC + 1024, LDS_BYTES = FB + 32768;
constexpr int STAGE = 3 * 32768;
constexpr int XD = 16, XD2 = 16;
constexpr float LBOUND = 65536.f;
constexpr float QSCALE = 0.08838834764831845f * 1.4426950408889634f;
constexpr int B0 = 128;
#define PRIO 1
constexpr int FISS[4] = {96, 97, 98, 117}, FRDS[4] = {115, 118, 121, 124};
constexpr int fpiece(int sl) { for (int p = 0; p < 4; ++p) if (FRDS[p] == sl) return p; return -1; }
constexpr Sched make_sched() {
  Sched s;
#define XP 8
  for (int i = 0; i < 12; ++i) s.add(-2, OG, i);
  for (int i = 0; i < XP; ++i) s.add(-1, OX, i);
  for (int t = 0; t < 128; ++t) {
    if (t < 2) for (int i = XP + t * (16 - XP) / 2; i < XP + (t + 1) * (16 - XP) / 2; ++i) s.add(t, OX, i);
    if (t >= 2 && t < 50) s.add(t, OX, t + 14);
    for (int v = 3; v < 18; ++v) for (int p = 0; p < 4; ++p) if (8 * (v - 3) + 7 + p == t) s.add(t, OG, 4 * v + p);
    if (t == 105 || t == 113 || t == 121) { s.add(t, OW, (t - 105) / 4); s.add(t, OW, (t - 105) / 4 + 1); }
    if (t >= 124) for (int i = 0; i < 3; ++i) s.add(t, OX, 64 + 3 * (t - 124) + i);
    if (t == 127) { s.add(t, OG, 72); s.add(t, OG, 73); }
    if (t == 20) s.add(t, OM, 0);
    if (t == 120) s.add(t, OM, 1);
    if (t >= 49 && t < 57 && (t & 1)) s.add(t, OF, (t - 49) / 2);
    if (t >= 71 && t < 79 && (t & 1)) s.add(t, OF, 4 + (t - 71) / 2);
    if (t == 48) s.add(t, OS, 0);
    if (t == 104) s.add(t, OS, 2);
    for (int p = 0; p < 4; ++p) if (FISS[p] == t) s.add(t, OF, 8 + p);
  }
  for (int t = 0; t < 64; ++t) {
    if (t == 0) { s.add(B0, OM, 2); s.add(B0, OG, 74); s.add(B0, OG, 75); s.add(B0, OW, 6); s.add(B0, OW, 7); }
    if (t == 1 || t == 2) { s.add(B0 + t, OX, 74 + 2 * t); s.add(B0 + t, OX, 75 + 2 * t); }
    if (t == 5 || t == 6 || t == 9) s.add(B0 + t, OX, 64 + (t == 5 ? 16 : t == 6 ? 18 : 22));
    if (t >= 5 && t < 50) s.add(B0 + t, OX, 64 + (t == 5 ? 17 : t < 9 ? t + 13 : t + 14));
    for (int v = 3; v < 16; ++v) for (int p = 0; p < 4; ++p) if ((v < 14 ? 4 * (v - 3) + 3 + (p >> 1) : v == 14 ? 45 + (p >> 1) : 49) == t) s.add(B0 + t, OG, 64 + 4 * v + p);
    if (t == 10 || t == 26) s.add(B0 + t, OM, t == 10 ? 3 : 4);
    if (t == 13 || t == 14 || t == 18) s.add(B0 + t, OE, t == 18 ? 2 : t - 13);
  }
  return s;
}
constexpr Sched SC = make_sched();

__global__ __launch_bounds__(512, 2) void k_fused(const u16* __restrict__ x, const u16* __restrict__ kc, const u16* __restrict__ vc,
                                                  const u16* __restrict__ wq, const u16* __restrict__ wo, const u16* __restrict__ wr,
                                                  const u16* __restrict__ wgate, const u16* __restrict__ wup, const u16* __restrict__ wdown,
                                                  char* __restrict__ ws, u16* __restrict__ out) {
  __shared__ __attribute__((aligned(16))) char lds_raw[LDS_BYTES];
  LAS char* const lds = (LAS char*)lds_raw;
  const int t = threadIdx.x, l = t & 63, bid = blockIdx.x, c = l & 15, g = l >> 4;
  const int wid = __builtin_amdgcn_readfirstlane(t >> 6);
  const int xcd = bid & 7, within = bid >> 3, head = xcd * 4 + (within & 3), rb = within >> 2;
  const int fj = xcd * 28 + (within < 28 ? within : within - 28);
  const int fsl = fj / 7, fgr = fj - fsl * 7;
  const int kh = xcd >> 2, mb = 2 * rb + (xcd & 1), nb = ((xcd >> 1) & 1) * 4 + (within & 3);
  const int rbw = wid & 3, kg = wid >> 2;
  const unsigned lds0 = (unsigned)(uintptr_t)lds;
  STAMP_DECL
  STAMP(0);

  unsigned tlo, thi;
  {
    const int row = 8 * bid + wid;
    u32x4 a0, a1, w0, w1, kvo;
    u32x2 ep, klo, khi;
    unsigned e[8];
    gld16<0>(a0, (unsigned)(row * DM * 2 + l * 16), x); gld16<1024>(a1, (unsigned)(row * DM * 2 + l * 16), x);
    gld16<0>(w0, (unsigned)(l * 16), wr); gld16<1024>(w1, (unsigned)(l * 16), wr);
    gld8<0, true>(ep, 0u, ws + WS_EP);
    if (wid == 3) {
      const char* pc;
      asm volatile("s_getpc_b64 %0" : "=s"(pc));
      touch((unsigned)((within * 12 + (l < 12 ? l : 11)) * 128), pc);
    }
    __builtin_amdgcn_s_barrier();
    if (wid == 2) {
      asm volatile("global_load_dword v255, %0, %1 sc1" ::"v"((unsigned)((l < 40 ? l : 39) << 20) + 64 * 1024 - 128), "s"(ws) : "memory", "v255");
      const char* pb = l < 8 ? (const char*)wo : l < 16 ? (const char*)wgate : l < 24 ? (const char*)wup : l < 32 ? (const char*)wdown : (const char*)out;
      pb += (size_t)(l & 7) * (l < 8 ? 1048576 : l < 32 ? 917504 : 524288);
      asm volatile("global_load_dword v255, %0, off" ::"v"(pb) : "memory", "v255");
    }
    const int fn = (bid * 4 + wid) & 255, kvh = bid >> 6;
    if (wid < 4) {
      if (fn < 128) {
        const unsigned ko = (unsigned)(((kvh * SK + (SK - WIN) + (fn >> 2) * 16 + c) * DH + (fn & 3) * 32 + 4 * g) * 2);
        gld8<0>(klo, ko, kc); gld8<32>(khi, ko, kc);
      } else {
        const int d = 32 * ((fn - 128) >> 5) + 8 * (c >> 2) + 4 * (fn & 1) + (c & 3);
        const unsigned vo = (unsigned)(((kvh * SK + (SK - WIN) + ((fn >> 1) & 15) * 32 + 4 * g + 8) * DH + d) * 2);
        sfor<8>([&](auto J) AI { constexpr int j = J; gld2<(16 * (j >> 2) + (j & 3) - 8) * DH * 2>(e[j], vo, vc); });
      }
      if (wid < 2) touch((unsigned)((wid * 256 + within * 8 + (l >> 3)) * 8192 + xcd * 1024 + (l & 7) * 128), wq);
      if (fn < 128) waitv<5>(); else waitv<11>();
    } else {
      waitv<3>();
    }
    PIN(a0); PIN(a1);
    STAMP(1);
    float v[16];
#pragma unroll
    for (int j = 0; j < 4; ++j) { v[2 * j] = bflo(a0[j]); v[2 * j + 1] = bfhi(a0[j]); v[8 + 2 * j] = bflo(a1[j]); v[9 + 2 * j] = bfhi(a1[j]); }
    float s = 0.f;
#pragma unroll
    for (int j = 0; j < 16; ++j) s += v[j];
    const float mean = wave_sum(s) * (1.f / DM);
    float q = 0.f;
#pragma unroll
    for (int j = 0; j < 16; ++j) { const float d = v[j] - mean; q += d * d; }
    const float var = wave_sum(q) * (1.f / DM);
    const float mbf = rbf(mean), rden = __builtin_amdgcn_rcpf(rbf(__builtin_amdgcn_sqrtf(rbf(rbf(var) + 1e-5f))));
    u32x4 o0, o1;
#pragma unroll
    for (int j = 0; j < 8; ++j) {
      const unsigned dd = pk2(v[2 * j] - mbf, v[2 * j + 1] - mbf);
      const unsigned nn = pk2(bflo(dd) * rden, bfhi(dd) * rden);
      if (j < 4) o0[j] = nn; else o1[j - 4] = nn;
    }
    *(LAS u32x4*)(lds + STAGE + (l * 8 + (wid ^ (l & 7))) * 16) = o0;
    *(LAS u32x4*)(lds + STAGE + 8192 + (l * 8 + (wid ^ (l & 7))) * 16) = o1;
    *(LAS f32x2*)(lds + STAGE + 16384 + 32 + wid * 8) = (f32x2){mbf, rden};
    waitbar_l<63>();
    if (wid >= 4) {
#pragma unroll
      for (int hh = 0; hh < 4; ++hh) {
        const int p = hh * 256 + (t - 256), ch = p >> 3, r = 8 * bid + ((p & 7) ^ (ch & 7));
        const u32x4 o = *(LAS const u32x4*)(lds + STAGE + p * 16);
        gst16<0>(o, (unsigned)(((((r >> 5) * 32 + (ch >> 2)) * 2 + ((r >> 4) & 1)) * 64 + (ch & 3) * 16 + (r & 15)) * 16), ws + WS_XN);
      }
      if (t < 256 + 8) gst8(*(LAS const f32x2*)(lds + STAGE + 16384 + 32 + (t - 256) * 8), (unsigned)((8 * bid + t - 256) * 8), ws + WS_ST);
      waitv<4>();
    } else if (fn < 128) waitv<2>(); else waitv<8>();
    PIN(w0); PIN(w1); PIN(ep);
    {
      const unsigned long long tg = (((unsigned long long)(unsigned)__builtin_amdgcn_readfirstlane(ep[1]) << 32) | (unsigned)__builtin_amdgcn_readfirstlane(ep[0])) + 1ull;
      tlo = (unsigned)tg; thi = (unsigned)(tg >> 32);
    }
    float rr = 0.f;
#pragma unroll
    for (int j = 0; j < 8; ++j) {
      const unsigned nn = j < 4 ? o0[j] : o1[j - 4], ww = j < 4 ? w0[j] : w1[j - 4];
      rr += bflo(nn) * bflo(ww); rr += bfhi(nn) * bfhi(ww);
    }
    rr = wave_sum(rr);
    {
      const unsigned bits = pk2(rr, 0.f) & 0xffffu;
      const unsigned ord = (bits & 0x8000u) ? (~bits & 0xffffu) : (bits | 0x8000u);
      *(LAS unsigned*)(lds + STAGE + 16384 + wid * 4) = (ord << 11) | (unsigned)(SEQ - 1 - row);
    }
    if (wid >= 4) waitv<0>();
    waitbar_l<63>();
    if (t == 256) {
      const u32x4 ka = *(LAS const u32x4*)(lds + STAGE + 16384), kb = *(LAS const u32x4*)(lds + STAGE + 16400);
      const unsigned key = umax(umax(umax(ka[0], ka[1]), umax(ka[2], ka[3])), umax(umax(kb[0], kb[1]), umax(kb[2], kb[3])));
      gst8((u32x2){(tlo << 27) | key, (tlo >> 5) | (thi << 27)}, (unsigned)(bid * 8), ws + WS_G0);
    }
    if (wid < 4) {
      if (wid < 2) waitv<1>(); else waitv<0>();
      if (fn < 128) {
        PIN(klo); PIN(khi);
        kvo = (u32x4){klo[0], klo[1], khi[0], khi[1]};
      } else {
#pragma unroll
        for (int j = 0; j < 8; ++j) PIN(e[j]);
        kvo = (u32x4){e[0] | (e[1] << 16), e[2] | (e[3] << 16), e[4] | (e[5] << 16), e[6] | (e[7] << 16)};
      }
      gst16<0>(kvo, (unsigned)((bid * 4 + wid) * 1024 + l * 16), ws + WS_KV);
    }
    STAMP(2);
  }
  const unsigned glo = tlo << 27, ghi = (tlo >> 5) | (thi << 27);

  const char* const wq_b = (const char*)wq + head * 256;
  const unsigned wq_v = (unsigned)(((8 * wid + ((l & 31) >> 2)) * 4096 + (l >> 5) * 32 + ((l & 3) ^ (2 * (wid & 1))) * 8) * 2);
  const char* const kv_b = ws + WS_KV + (size_t)(head >> 3) * 262144;
  const unsigned kv_v = (unsigned)(wid * 4096 + l * 16);
  const char* const xn_b = ws + WS_XN + (size_t)rb * 8 * 65536 + 4096;
  const unsigned xn_v = (unsigned)(wid * 65536 + l * 16);
  const char* const wo_b = (const char*)wo + (size_t)kh * 2048 * (DM * 2) + nb * 256;
  const char* const o_b = ws + WS_O + (size_t)mb * 4 * 262144 + kh * 131072;
  const unsigned ldsw = lds0 + wid * 2048, ldsk = lds0 + wid * 4096, ldsf = lds0 + FB + wid * 1024;
  const int erow = mb * 128 + rbw * 32 + kg * 16 + c;
  bf16x8 xf[XD];
  u32x4 fm[2];
  bf16x8 fxs;
  const unsigned gu_v = (unsigned)((((4 * wid + g) * 2 + ((l >> 3) & 1)) * FF + fj * 16 + (l & 7) * 2) * 8);
  u32x2 fg;
  u32x2 xe[4];
  f32x2 st;
  unsigned key = 0;

  const unsigned fo_v = (unsigned)(((mb * 4 + rbw) * 32 + kh * 16 + c) * 8);
  const int yrow = within < 28 ? fj : 224 + xcd * 4 + within - 28;
  f32x4 o[2][2][2];
  float inv[2];
  const char* const od_b = ws + WS_O + ((size_t)(rb * 8) * 128 + head * 4) * 2048 + 4096;
  auto issue = [&](auto Sl, auto Pos) AI {
    constexpr int sl = Sl, pos = Pos, cnt = SC.count(sl), q0 = SC.first(sl) + (pos < 0 ? 0 : pos);
    constexpr int nq = pos < 0 ? cnt : pos >= cnt ? 0 : pos < 3 ? 1 : cnt - 3;
    sfor<nq>([&](auto Qi) AI {
      constexpr int k = SC.kind[q0 + Qi], i = SC.idx[q0 + Qi];
      if constexpr (k == OX && i < 64) gld16<(i & 7) * 1024 - 4096, true>(xf[i % XD], xn_v, xn_b + (size_t)(i >> 3) * 8192);
      if constexpr (k == OX && i >= 64) {
        constexpr int q = i - 64, v = q >> 2;
        gld16<0, true>(xf[q % XD2], (unsigned)(rbw * 262144 + kg * 16384 + ((q & 1) ^ kg) * 1024 + l * 16), o_b + (size_t)(((v & 3) * 4 + ((q >> 1) & 1)) * 8192 + (v >> 2) * 2048));
      }
      if constexpr (k == OG) {
        constexpr int v = i >> 2, p = i & 3;
        if constexpr (v < 8) {
          const char* sb = wq_b + (size_t)(v * 128 + (p >> 1) * 64) * 8192;
          glds<(p & 1) * 128>(ldsw + (v & 3) * 32768 + (p >> 1) * 16384 + (p & 1) * (1024 - 128), wq_v, sb);
        } else if constexpr (v < 16) {
          glds<p * 1024, true>(ldsk + (v & 3) * 32768, kv_v, kv_b + (size_t)(v - 8) * 32768);
        } else {
          const unsigned wo_v = (unsigned)((((wid >> 2) * 128 + (wid & 3) * 8 + ((l & 31) >> 2)) * DM + (l >> 5) * 32 + ((l & 3) ^ (2 * (wid & 1))) * 8) * 2);
          const char* sb = wo_b + (size_t)(((v & 3) * 4 + (p >> 1) * 2) * 128 + ((v - 16) >> 2) * 32) * (DM * 2) + (((p & 1) ^ kh ^ 1) << 7);
          glds<0>(ldsw + (v & 3) * 32768 + (p >> 1) * 16384 + (p & 1) * 1024, wo_v, sb);
        }
      }
      if constexpr (k == OF && i < 8)
        glds<0>(ldsf + (i & 3) * 8192, (unsigned)(((l >> 3) * FF + wid * 64 + ((l & 7) ^ ((i & 1) * 4 + ((l >> 3) & 2))) * 8) * 2),
                (const char*)(i < 4 ? wgate : wup) + ((size_t)(fsl * 32 + 8 * (i & 3)) * FF + fgr * 512) * 2);
      if constexpr (k == OF && i >= 8) {
        const int frho = l >> 2, fch = (l & 3) ^ (frho & 4 ? 2 : 0);
        glds<0>(ldsf + ((i - 8) % 3) * 8192, (unsigned)((frho * DM + wid * 128 + fch * 8) * 2), (const char*)wdown + (size_t)fj * 16 * DM * 2 + (i - 8) * 64);
      }
      if constexpr (k == OS && i == 0) {
        const int idx = SEQ - 1 - (int)(key & 2047u);
        gld16<0, true>(fxs, (unsigned)((((idx >> 5) * 32 + fsl) * 2 + ((idx >> 4) & 1)) * 1024 + (g * 16 + (idx & 15)) * 16), ws + WS_XN);
      }
      if constexpr (k == OS && i == 2) gld16<0, true>(fm[0], gu_v, ws + WS_GU);
      if constexpr (k == OM && i > 0) gld8<0, true>(fg, fo_v, ws + WS_FO + (i - 1) * 16384);
      if constexpr (k == OM && i == 0) gld8<0, true>(fg, (unsigned)((t & 255) * 8), wid < 4 ? ws + WS_G0 : ws + WS_FK + (head >> 3) * 2048);
      if constexpr (k == OW) {
        constexpr int os = i >> 1, qb = i & 1;
        PIN(o[os & 1][0][qb]); PIN(o[os & 1][1][qb]);
        const f32x4 a = o[os & 1][0][qb] * inv[qb], b = o[os & 1][1][qb] * inv[qb];
        gst16<i * 1024 - 4096>((u32x4){pk2(a[0], a[1]), pk2(a[2], a[3]), pk2(b[0], b[1]), pk2(b[2], b[3])}, (unsigned)(wid * 262144 + l * 16), od_b);
      }
      if constexpr (k == OE && i < 2)
        glds<0>(ldsf + i * 8192, (unsigned)(((erow - c + i * 8 + (l >> 3)) * DM + nb * 128 + kh * 64 + ((l & 7) ^ (i * 4 + (l >> 4))) * 8) * 2), x);
      if constexpr (k == OE && i == 2) gld8<0, true>(st, (unsigned)(erow * 8), ws + WS_ST);
    });
  };
  f32x4 fa0, fa1, fq[4];
  s16x4 fb0, fb1, fxa;
  bf16x8 ft[2];
  auto endslot = [&](auto Sl) AI {
    constexpr int sl = Sl, u = sl >> 3, i = sl & 7;
    if constexpr (sl == 3) {
      if (wid == 6 || wid == 7) touch((unsigned)(((wid - 4) * 256 + within * 8 + (l >> 3)) * 8192 + xcd * 1024 + (l & 7) * 128), wq);
    }
    if constexpr (sl == 2) {
      if (wid < 4 && l == 0) gst8((u32x2){tlo, thi}, (unsigned)((bid * 4 + wid) * 8), ws + WS_FK);
    }
    if constexpr (sl == 38) {
      waitv<SC.younger(OM, 0, sl)>(fg);
      const unsigned lo = wid < 4 ? glo : tlo, hi = wid < 4 ? ghi : thi, mk = wid < 4 ? 0xf8000000u : 0xffffffffu;
      for (int spin = 0; spin < SPINS; ++spin) {
        if (__builtin_expect(__all(((fg[0] & mk) == lo) & (fg[1] == hi)), 1)) break;
        __builtin_amdgcn_s_sleep(1);
        gld8<0, true>(fg, (unsigned)((t & 255) * 8), wid < 4 ? ws + WS_G0 : ws + WS_FK + (head >> 3) * 2048);
        waitv<0>(fg);
      }
      const unsigned km = wave_max_u32(fg[0] & 0x07ffffffu);
      *(LAS unsigned*)(lds + HSC + wid * 4) = km;
    }
    if constexpr (sl == 57) {
      if (wid == 3) {
        const char* pc;
        asm volatile("s_getpc_b64 %0" : "=s"(pc));
        touch((unsigned)((within * 8 + (l & 7)) * 128), pc);
      }
    }
    if constexpr (sl == 47) {
      if (bid == 0 && t == 0) gst8((u32x2){tlo, thi}, 0u, ws + WS_EP);
      const u32x4 kk = *(LAS const u32x4*)(lds + HSC);
      key = __builtin_amdgcn_readfirstlane(umax(umax(kk[0], kk[1]), umax(kk[2], kk[3])));
    }
    if constexpr (sl == 58 || sl == 61 || sl == 64 || sl == 67) {
      if (wid == 4 + (sl - 58) / 3) touch((unsigned)(((wid & 3) * 64 + l) * 128), (const char*)wdown + (size_t)fj * 16 * DM * 2);
    }
    if constexpr (sl == 81 || sl == 85 || sl == 89 || sl == 93) {
      if (wid == (sl - 81) / 4)
        touch((unsigned)((kh * 2048 + (within >> 1) * 128 + wid * 32 + ((within & 1) * 2 + (xcd & 1)) * 8 + (l >> 3)) * (DM * 2) + ((xcd >> 1) & 1) * 1024 + (l & 7) * 128), wo);
    }
    if constexpr ((sl >= 68 && sl <= 70) || (sl >= 90 && sl <= 92)) {
      constexpr int r = sl >= 90, q = sl - (r ? 90 : 68);
      if constexpr (q == 0) waitv<SC.younger(OF, 4 * r + 3, sl)>(fxs);
      if constexpr (q >= 1) {
#pragma unroll
        for (int j = 0; j < 2; ++j) { fq[2 * q - 2 + j] = __builtin_amdgcn_mfma_f32_16x16x32_bf16(fxs, ft[j], (f32x4){}, 0, 0, 0); PIN(fq[2 * q - 2 + j]); }
      }
      if constexpr (q < 2) {
        const unsigned rp = ldsf + g * 8192 + (c >> 2) * 128 + ((((g & 1) << 1) | (c >> 3)) << 5) + (c & 3) * 8;
#pragma unroll
        for (int j = 0; j < 2; ++j) { LAS const char* a = (LAS const char*)(uintptr_t)(rp ^ ((2 * q + j) << 5)); ft[j] = cat8(trd(a), trd(a + 512)); }
      }
    }
    if constexpr (sl == 71 || sl == 93) {
      const float v = sel2(g & 2, sel2(g & 1, fq[3][0], fq[2][0]), sel2(g & 1, fq[1][0], fq[0][0]));
      gst8((u32x2){__float_as_uint(v), tlo}, (unsigned)(((fsl * 2 + (sl == 93)) * FF + fgr * 512 + t) * 8), ws + WS_GU);
    }
    if constexpr (sl == 109) {
      waitv<SC.younger(OS, 2, sl)>(fm[0]);
      for (int spin = 0; spin < SPINS; ++spin) {
        if (__builtin_expect(__all((fm[0][1] == tlo) & (fm[0][3] == tlo)), 1)) break;
        __builtin_amdgcn_s_sleep(1);
        gld16<0, true>(fm[0], gu_v, ws + WS_GU);
        waitv<0>(fm[0]);
      }
      *(LAS f32x2*)(lds + RED1 + (wid * 32 + 2 * c) * 4) = (f32x2){xg_sum(__uint_as_float(fm[0][0])), xg_sum(__uint_as_float(fm[0][2]))};
    }
    if constexpr (sl == 111) {
#pragma unroll
      for (int w = 0; w < 2; ++w) { fa0[w] = *(LAS const float*)(lds + RED1 + ((2 * g + w) * 32 + c) * 4); fa0[2 + w] = *(LAS const float*)(lds + RED1 + ((2 * g + w) * 32 + 16 + c) * 4); }
    }
    if constexpr (sl == 112) { fa0[0] = xg_sum(fa0[0] + fa0[1]); fa0[2] = xg_sum(fa0[2] + fa0[3]); PIN(fa0); }
    if constexpr (sl == 113) {
      const float hh = rbf(silu_f(rbf(fa0[0]))) * rbf(fa0[2]);
      *(LAS u16*)(lds + HSC + 32 + wid * 32 + c * 2) = (u16)pk2(hh, hh);
    }
    if constexpr (sl == 114) fxa = *(LAS const s16x4*)(lds + HSC + 32 + wid * 32 + g * 8);
    constexpr int pr = fpiece(sl), pm = fpiece(sl - 1), ps = fpiece(sl - 2);
    if constexpr (ps >= 0) gst4<ps * 128>(sel2(g & 1, fa1[0], fa0[0]), (unsigned)((wid * 128 + (l & 31)) * 4), ws + WS_YP + (size_t)yrow * 4096);
    if constexpr (pm >= 0) { fa0 = MFMA16(fxa, fb0, (f32x4){}); fa1 = MFMA16(fxa, fb1, (f32x4){}); PIN(fa0); PIN(fa1); }
    if constexpr (pr >= 0) {
      waitv<SC.younger(OF, 8 + pr, sl)>();
      const unsigned fp = ldsf + (4 * g + (c >> 2)) * 64 + (g & 1) * 32 + (c & 3) * 8;
      fb0 = trd((LAS const char*)(uintptr_t)fp + (pr % 3) * 8192); fb1 = trd((LAS const char*)(uintptr_t)(fp ^ 32) + (pr % 3) * 8192);
    }
    if constexpr (sl == 111 || sl == 119 || sl == 127) {
      waitv<SC.younger(OW, (sl - 111) / 4 + 1, sl)>();
      if (l == 0) gst8((u32x2){tlo, thi}, (unsigned)(((rb * 8 + wid) * 32 + head) * 8), ws + WS_FO + ((sl - 111) / 8) * 16384);
    }
    if constexpr (sl == 123) {
      waitv<SC.younger(OM, 1, sl)>(fg);
      if (__builtin_expect(!__all((fg[0] == tlo) & (fg[1] == thi)), 0)) poll8(fo_v, ws + WS_FO, tlo, thi, 0xffffffffu);
    }
    SBAR();
    if constexpr (i == 6) {
      if constexpr (u == 5 || u == 13) waitbar_l<SC.younger(OG, 4 * u + 7, sl)>();
      else waitbar<SC.younger(OG, 4 * u + 7, sl)>();
      if constexpr (u == 0 || u == 1 || u == 5 || u == 7 || u == 8 || u == 10 || u == 12 || u == 14)
        STAMP(u == 0 ? 4 : u == 1 ? 5 : u == 5 ? 6 : u == 7 ? 7 : u == 8 ? 8 : u == 10 ? 9 : u == 12 ? 10 : 11);
    }
    if constexpr (PRIO && !(i & 1)) __builtin_amdgcn_s_setprio(i == 6 ? 3 : 2 - i / 2);
  };

  issue(IC(-2), IC(-1));
  if (wid == 5) poll8((unsigned)((rb * 32 + (l & 31)) * 8), ws + WS_G0, glo, ghi, 0xf8000000u);
  waitbar<SC.younger(OG, 3, -2)>();
  issue(IC(-1), IC(-1));
  STAMP(3);

  LBase trb[2], frb;
  trb[0].init(lds0 + g * 2048 + (c >> 2) * 64 + (g & 1) * 32 + (c & 3) * 8);
  trb[1].init(lds0 + g * 2048 + (c >> 2) * 64 + ((g & 1) ^ 1) * 32 + (c & 3) * 8);
  frb.init(lds0 + l * 16);
  f32x4 acc[8][2];
  bf16x8 wf[2][4];
  auto WRD1 = [&](auto Si, auto Base, auto Dd) AI {
    constexpr int s = Si, d = Dd, o = Base + (d >> 1) * 512;
    wf[s & 1][d] = cat8(trd(trb[d & 1].template at<o>()), trd(trb[d & 1].template at<o + 256>()));
  };
  auto WRD = [&](auto Si, auto Base) AI { sfor<4>([&](auto Dd) AI { WRD1(Si, Base, Dd); }); };
#define WA(s) IC((((s) >> 3) & 3) * 32768 + (((s) >> 1) & 3) * 8192 + ((s) & 1) * 1024)
#define WB(s) IC((((s) >> 2) & 3) * 32768 + (((s) >> 1) & 1) * 8192 + ((s) & 1) * 1024)
  bf16x8 fr[4];
  auto FRD = [&](auto Ni) AI { constexpr int n = Ni; if constexpr (n < 256) fr[n & 3] = *(LAS const bf16x8*)(frb.at<((n >> 5) & 3) * 32768 + (n & 31) * 1024>()); };

  WRD(IC(0), WA(0));
  SBAR();
  bf16x8 qf[4][2];
  auto QPACK = [&](auto Ds) AI {
    constexpr int ds = Ds;
#pragma unroll
    for (int qb = 0; qb < 2; ++qb) {
      const f32x4 a = acc[2 * ds][qb] * QSCALE, b = acc[2 * ds + 1][qb] * QSCALE;
      qf[ds][qb] = __builtin_bit_cast(bf16x8, (u32x4){pk2(a[0], a[1]), pk2(a[2], a[3]), pk2(b[0], b[1]), pk2(b[2], b[3])});
      PIN(qf[ds][qb]);
    }
  };
  sfor<64>([&](auto Si) AI {
    constexpr int s = Si, x0 = (s & ~1) % XD, x1 = (s | 1) % XD;
    if constexpr (!(s & 1)) waitv<SC.younger(OX, s + 1, s - 1)>(xf[x0], xf[x1]);
    sfor<4>([&](auto Dd) AI {
      constexpr int d = Dd, dt = (s & 1) * 4 + d;
      if constexpr (s + 1 < 64) WRD1(IC(s + 1), WA(s + 1), Dd);
      else if constexpr (d < 3) FRD(Dd);
      SBAR();
      if constexpr (s < 2) { MFZ(acc[dt][0], wf[s & 1][d], xf[x0]); MFZ(acc[dt][1], wf[s & 1][d], xf[x1]); }
      else { MF(acc[dt][0], wf[s & 1][d], xf[x0]); MF(acc[dt][1], wf[s & 1][d], xf[x1]); }
      issue(Si, Dd);
      if constexpr (s == 63 && (d & 1)) { QPACK(IC(d >> 1)); SBAR(); }
    });
    SBAR();
    endslot(Si);
  });

  f32x4 sc[2][2][2], negm[2];
  u32x4 pw[16][2];
  f32x4 lsum[2];
  bf16x8 ones = {0x3f80, 0x3f80, 0x3f80, 0x3f80, 0x3f80, 0x3f80, 0x3f80, 0x3f80};
  PIN(ones);
#define MX3(a, b, c) __builtin_fmaxf(__builtin_fmaxf((a), (b)), (c))
#define EXP2(x) __builtin_amdgcn_exp2f(x)
  auto SM = [&](auto Hi, auto Pi) AI {
    constexpr int H = Hi, p = Pi, kt = p >> 2, qb = (p >> 1) & 1, r = 2 * (p & 1);
    const float e0 = EXP2(sc[H & 1][kt][qb][r]), e1 = EXP2(sc[H & 1][kt][qb][r + 1]);
    unsigned w = pk2(e0, e1);
    PIN(w);
    pw[H][qb][kt * 2 + (p & 1)] = w;
  };
  sfor<128>([&](auto Ni) AI {
    constexpr int n = Ni, H = n >> 3, i = n & 7, kt = i >> 2, ds = i & 3, sn = H & 1;
    FRD(IC(n + 3));
    if constexpr (n < 2) QPACK(IC(2 + n));
    sfor<2>([&](auto Qb) AI {
      constexpr int qb = Qb;
      if constexpr (ds == 0 && H == 0) MFZ(sc[sn][kt][qb], fr[n & 3], qf[0][qb]);
      else if constexpr (ds == 0) MFC(sc[sn][kt][qb], fr[n & 3], qf[0][qb], negm[qb]);
      else MF(sc[sn][kt][qb], fr[n & 3], qf[ds][qb]);
    });
    if constexpr (n == 5 || n == 6) {
      constexpr int qb = n - 5;
      NOPS(3); PIN(sc[0][0][qb]);
      const float m = -xg_max(__builtin_fmaxf(MX3(sc[0][0][qb][0], sc[0][0][qb][1], sc[0][0][qb][2]), sc[0][0][qb][3]));
      negm[qb] = (f32x4){m, m, m, m};
      sc[0][0][qb] += negm[qb];
      PIN(negm[qb]); PIN(sc[0][0][qb]);
    }
    if constexpr (n == 9 || n == 10) { NOPS(3); PIN(sc[0][1][n - 9]); sc[0][1][n - 9] += negm[n - 9]; PIN(sc[0][1][n - 9]); }
    issue(IC(64 + (n >> 2)), IC(n & 3));
    if constexpr (H == 2 && (i == 1 || i == 5)) MFZ(lsum[i >> 2], ones, pw[0][i >> 2]);
    if constexpr (H > 2 && (i == 1 || i == 5)) MF(lsum[i >> 2], ones, pw[H - 2][i >> 2]);
    if constexpr (H >= 1) SM(IC(H - 1), IC(i));
    if constexpr (n == 125) { PIN(negm[0]); PIN(negm[1]); }
    SBAR();
    if constexpr ((n & 3) == 3) endslot(IC(64 + (n >> 2)));
  });
  constexpr int CHK = 16;
  sfor<128>([&](auto Mi) AI {
    constexpr int m = Mi, n = 128 + m, os = m >> 5, H = (m >> 1) & 15, par = m & 1;
    FRD(IC(n + 3));
    if constexpr (H == 0) { MFZ(o[os & 1][par][0], fr[n & 3], pw[H][0]); MFZ(o[os & 1][par][1], fr[n & 3], pw[H][1]); }
    else { MF(o[os & 1][par][0], fr[n & 3], pw[H][0]); MF(o[os & 1][par][1], fr[n & 3], pw[H][1]); }
    if constexpr (m == 1 || m == 2) MF(lsum[m - 1], ones, pw[14][m - 1]);
    if constexpr (m < 8) SM(IC(15), Mi);
    if constexpr (m == 9 || m == 10) MF(lsum[m - 9], ones, pw[15][m - 9]);
    if constexpr (m == CHK + 1 || m == CHK + 2) { inv[m - CHK - 1] = __builtin_amdgcn_rcpf(lsum[m - CHK - 1][0]); PIN(inv[m - CHK - 1]); }
    issue(IC(64 + (n >> 2)), IC(n & 3));
    if constexpr (m == 123) {
      trb[0].init(lds0 + kg * 16384 + g * 2048 + (c >> 2) * 64 + (g & 1) * 32 + (c & 3) * 8);
      trb[1].init(lds0 + kg * 16384 + g * 2048 + (c >> 2) * 64 + ((g & 1) ^ 1) * 32 + (c & 3) * 8);
    }
    if constexpr (m >= 124) WRD1(IC(0), WB(0), IC(m - 124));
    SBAR();
    if constexpr ((n & 3) == 3) endslot(IC(64 + (n >> 2)));
    if constexpr (m == CHK - 1) {
      PIN(lsum[0]); PIN(lsum[1]);
      if (__builtin_expect(__any(!(lsum[0][0] + lsum[1][0] <= LBOUND)), 0)) {
        NOPS(7); SBAR();
        auto tile = [&](unsigned off, f32x4& s0, f32x4& s1) AI {
          bf16x8 kf[4];
          SBAR(); NOPS(7);
          sfor<4>([&](auto Ds) AI { constexpr int ds = Ds; gld16<ds * 1024, true>(kf[ds], off, kv_b); });
          waitv<0>(kf[0], kf[1]);
          PIN(kf[2]); PIN(kf[3]);
#pragma unroll
          for (int ds = 0; ds < 4; ++ds) {
            s0 = __builtin_amdgcn_mfma_f32_16x16x32_bf16(kf[ds], qf[ds][0], s0, 0, 0, 0);
            s1 = __builtin_amdgcn_mfma_f32_16x16x32_bf16(kf[ds], qf[ds][1], s1, 0, 0, 0);
          }
        };
        float m0 = -__builtin_inff(), m1 = m0;
#pragma unroll 1
        for (int kt = 0; kt < 32; ++kt) {
          f32x4 s0 = {}, s1 = {};
          tile((unsigned)(kt * 4096 + l * 16), s0, s1);
          m0 = MX3(MX3(m0, s0[0], s0[1]), s0[2], s0[3]); m1 = MX3(MX3(m1, s1[0], s1[1]), s1[2], s1[3]);
        }
        m0 = -xg_max(m0); m1 = -xg_max(m1);
        lsum[0] = (f32x4){}; lsum[1] = (f32x4){};
        sfor<16>([&](auto Hi) AI {
          constexpr int H = Hi;
#pragma unroll
          for (int kt = 0; kt < 2; ++kt) {
            sc[H & 1][kt][0] = (f32x4){m0, m0, m0, m0}; sc[H & 1][kt][1] = (f32x4){m1, m1, m1, m1};
            tile((unsigned)((2 * H + kt) * 4096 + l * 16), sc[H & 1][kt][0], sc[H & 1][kt][1]);
          }
          sfor<8>([&](auto Pi) AI { SM(Hi, Pi); });
          PIN(pw[H][0]); PIN(pw[H][1]);
          lsum[0] = __builtin_amdgcn_mfma_f32_16x16x32_bf16(ones, __builtin_bit_cast(bf16x8, pw[H][0]), lsum[0], 0, 0, 0);
          lsum[1] = __builtin_amdgcn_mfma_f32_16x16x32_bf16(ones, __builtin_bit_cast(bf16x8, pw[H][1]), lsum[1], 0, 0, 0);
        });
#pragma unroll
        for (int par = 0; par < 2; ++par) { o[0][par][0] = (f32x4){}; o[0][par][1] = (f32x4){}; }
        sfor<CHK / 4>([&](auto Qi) AI {
          constexpr int q = Qi;
          bf16x8 vf[4];
          SBAR(); NOPS(7);
          sfor<4>([&](auto J) AI { constexpr int j = J; gld16<j * 1024, true>(vf[j], (unsigned)((128 + 4 * q) * 1024 + l * 16), kv_b); });
          waitv<0>(vf[0], vf[1]);
          PIN(vf[2]); PIN(vf[3]);
#pragma unroll
          for (int j = 0; j < 4; ++j)
#pragma unroll
            for (int qb = 0; qb < 2; ++qb)
              o[0][j & 1][qb] = __builtin_amdgcn_mfma_f32_16x16x32_bf16(vf[j], __builtin_bit_cast(bf16x8, pw[2 * q + (j >> 1)][qb]), o[0][j & 1][qb], 0, 0, 0);
        });
#pragma unroll
        for (int par = 0; par < 2; ++par) { PIN(o[0][par][0]); PIN(o[0][par][1]); }
        PIN(lsum[0]); PIN(lsum[1]);
        SBAR(); NOPS(7);
      }
      SBAR();
    }
  });
  STAMP(12); STAMP(13); STAMP(14); STAMP(15);

  const int idx = SEQ - 1 - (int)(key & 2047u);
  const bool owner = (idx >> 7) == mb, ownw = (idx >> 4) == mb * 8 + rbw * 2 + kg;
  f32x4 yp[7], ya[4], oth[4];
  unsigned gate[8];
  float glow;
  const unsigned ex_a = ldsf + (c >> 3) * 8192 + (c & 7) * 128 + (((g >> 1) ^ (c >> 1)) << 4) + (g & 1) * 8;
  auto item = [](int s) { return s < 48 ? s : s < 56 ? 48 + 2 * (s - 48) : 49 + 2 * (s - 56); };
  u32x2 pf;
  sfor<64>([&](auto Si) AI {
    constexpr int s = Si, u = s >> 2, it = item(s), x0 = (it & ~1) % XD2, x1 = (it | 1) % XD2;
    if constexpr (!(it & 1)) waitv<SC.younger(OX, 64 + it + 1, B0 + s - 1)>(xf[x0], xf[x1]);
    sfor<4>([&](auto Dd) AI {
      constexpr int d = Dd, dt = (it & 1) * 4 + d;
      if constexpr (s + 1 < 64) WRD1(IC(s + 1), WB(item(s + 1)), Dd);
      SBAR();
      if constexpr (s < 2) { MFZ(acc[dt][0], wf[s & 1][d], xf[x0]); MFZ(acc[dt][1], wf[s & 1][d], xf[x1]); }
      else { MF(acc[dt][0], wf[s & 1][d], xf[x0]); MF(acc[dt][1], wf[s & 1][d], xf[x1]); }
      issue(IC(B0 + s), Dd);
      if constexpr (s == 56) *(LAS f32x4*)(lds + (wid ^ 4) * 4096 + l * 16 + (d & 1) * 2048 + (d >> 1) * 32768) = acc[d][1];
      if constexpr (s == 57 && d == 0) {
#pragma unroll
        for (int e = 0; e < 4; ++e) oth[e] = *(LAS const f32x4*)(lds + wid * 4096 + l * 16 + (e & 1) * 2048 + (e >> 1) * 32768);
      }
      if constexpr (s == 57 && d >= 2) {
        gst16<(2 * d - 4) * 1024>(acc[2 * d - 4][0] + oth[2 * d - 4], (unsigned)(wid * 4096 + l * 16), ws + WS_XC + (size_t)(bid ^ 4) * 32768);
        gst16<(2 * d - 3) * 1024>(acc[2 * d - 3][0] + oth[2 * d - 3], (unsigned)(wid * 4096 + l * 16), ws + WS_XC + (size_t)(bid ^ 4) * 32768);
      }
      if constexpr (s == 34) xe[d] = *(LAS const u32x2*)(uintptr_t)(ex_a ^ (d << 5));
      if constexpr (s >= 36 && s < 44 && d == 1) {
        constexpr int j = s - 36;
        PIN(st);
        const unsigned xw = xe[j >> 1][j & 1];
        const unsigned dd = pk2(bflo(xw) - st[0], bfhi(xw) - st[0]);
        gate[j] = pk2(bflo(dd) * st[1], bfhi(dd) * st[1]);
        PIN(gate[j]);
      }
      if constexpr (s >= 47 && s < 63 && d == 1) {
        constexpr int j = (s - 47) >> 1;
        if constexpr ((s - 47) & 1) { gate[j] = pk2(glow, silu_f(bfhi(gate[j]))); PIN(gate[j]); }
        else { glow = silu_f(bflo(gate[j])); PIN(glow); }
      }
    });
    SBAR();
    if constexpr (s == 7) {
      waitv<SC.younger(OW, 7, B0 + s)>();
      if (l == 0) {
        gst8((u32x2){tlo, thi}, (unsigned)(((rb * 8 + wid) * 32 + head) * 8), ws + WS_FO + 3 * 16384);
        gst8((u32x2){tlo, thi}, (unsigned)((wid * 256 + yrow) * 8), ws + WS_FY);
      }
    }
    if constexpr (s == 4 || s == 17 || s == 33) {
      constexpr int j = s == 4 ? 1 : s == 17 ? 2 : 3;
      waitv<SC.younger(OM, j + 1, B0 + s)>(fg);
      if (__builtin_expect(!__all((fg[0] == tlo) & (fg[1] == thi)), 0)) poll8(fo_v, ws + WS_FO + j * 16384, tlo, thi, 0xffffffffu);
    }
    if constexpr (s == 60) {
      waitv<0>();
      if (l == 0) gst8((u32x2){tlo, thi}, (unsigned)(((bid ^ 4) * 8 + wid) * 64), ws + WS_FX);
    }
    if constexpr (s == 61) gld8<0, true>(pf, (unsigned)((bid * 8 + wid) * 64), ws + WS_FX);
    if constexpr (s == 13) {
      if (owner) { gld16<0, true>(fm[0], (unsigned)(nb * 2048 + (l < 56 ? l : 55) * 32), ws + WS_FY); gld16<16, true>(fm[1], (unsigned)(nb * 2048 + (l < 56 ? l : 55) * 32), ws + WS_FY); }
    }
    if constexpr (s == 28) {
      if (owner) {
        PIN(fm[0]); PIN(fm[1]);
        for (int spin = 0; spin < SPINS; ++spin) {
          const bool ok = (fm[0][0] == tlo) & (fm[0][1] == thi) & (fm[0][2] == tlo) & (fm[0][3] == thi) & (fm[1][0] == tlo) & (fm[1][1] == thi) & (fm[1][2] == tlo) & (fm[1][3] == thi);
          if (__builtin_expect(__all(ok), 1)) break;
          __builtin_amdgcn_s_sleep(1);
          gld16<0, true>(fm[0], (unsigned)(nb * 2048 + (l < 56 ? l : 55) * 32), ws + WS_FY); gld16<16, true>(fm[1], (unsigned)(nb * 2048 + (l < 56 ? l : 55) * 32), ws + WS_FY);
          waitv<0>(fm[0], fm[1]);
        }
        const unsigned yv = (unsigned)(((t >> 4) * 1024 + nb * 128 + kh * 64 + (t & 15) * 4) * 4);
        sfor<7>([&](auto I) AI { constexpr int q = I; gld16<0, true>(yp[q], yv, ws + WS_YP + (size_t)q * 32 * 4096); });
      }
    }
    if constexpr (s == 43) {
      if (owner) {
        f32x4 a = {};
#pragma unroll
        for (int q = 0; q < 7; ++q) { PIN(yp[q]); a += yp[q]; }
#pragma unroll
        for (int j = 0; j < 4; ++j) a[j] = xg_sum(a[j]);
        *(LAS f32x4*)(lds + YS + (wid * 64 + c * 4) * 4) = a;
      }
    }
    if constexpr (s == 45) {
      if (ownw) {
        float a = 0.f;
#pragma unroll
        for (int p = 0; p < 8; ++p) a += *(LAS const float*)(lds + YS + (p * 64 + l) * 4);
        const unsigned ord = key >> 11, bits = (ord & 0x8000u) ? (ord & 0x7fffu) : (~ord & 0xffffu);
        *(LAS float*)(lds + RED1 + l * 4) = rbf(rbf(a) * rbf(sigmoid_f(bflo(bits))));
#pragma unroll
        for (int q = 0; q < 4; ++q) ya[q] = *(LAS const f32x4*)(lds + RED1 + (q * 16 + 4 * g) * 4);
      }
    }
    if constexpr (s == 46) {
      if (ownw) {
#pragma unroll
        for (int j = 0; j < 8; ++j) {
          const unsigned w = pk2(bflo(gate[j]) + ya[j >> 1][2 * (j & 1)], bfhi(gate[j]) + ya[j >> 1][2 * (j & 1) + 1]);
          gate[j] = erow == idx ? w : gate[j];
          PIN(gate[j]);
        }
      }
    }
    SBAR();
    if constexpr (((s & 3) == 2 && u < 11) || s == 44) {
      if constexpr (s == 44) waitbar_l<SC.younger(OG, 64 + 4 * u + 7, B0 + s)>();
      else waitbar<SC.younger(OG, 64 + 4 * u + 7, B0 + s)>();
      if constexpr (u % 4 == 1) STAMP(16 + u / 4);
    }
    if constexpr (s == 48) waitbar<SC.younger(OG, 64 + 55, B0 + s)>();
    if constexpr (s == 50) waitbar<SC.younger(OG, 64 + 59, B0 + s)>();
    if constexpr (s == 52) { waitbar<SC.younger(OG, 64 + 63, B0 + s)>(); STAMP(19); }
    if constexpr (s == 56) waitbar_l<63>();
    if constexpr (PRIO) __builtin_amdgcn_s_setprio(3 - ((s + 2) & 3));
  });
  STAMP(20);

  f32x4 kep[4];
  {
    waitv<0>(pf);
    const bool rdy = __all((pf[0] == tlo) & (pf[1] == thi));
    if (rdy) sfor<4>([&](auto Dd) AI { constexpr int d = Dd; gld16<d * 1024, true>(oth[d], (unsigned)(wid * 4096 + l * 16), ws + WS_XC + (size_t)bid * 32768); });
    NOPS(3);
#pragma unroll
    for (int d = 0; d < 4; ++d) { PIN(acc[4 + d][1]); *(LAS f32x4*)(lds + 65536 + (wid ^ 4) * 4096 + l * 16 + (d & 1) * 2048 + (d >> 1) * 32768) = acc[4 + d][1]; }
    waitbar_l<63>();
    STAMP(21);
#pragma unroll
    for (int d = 0; d < 4; ++d) kep[d] = acc[4 + d][0] + *(LAS const f32x4*)(lds + 65536 + wid * 4096 + l * 16 + (d & 1) * 2048 + (d >> 1) * 32768);
    STAMP(22);
    if (!rdy) {
      poll8((unsigned)((bid * 8 + wid) * 64), ws + WS_FX, tlo, thi, 0xffffffffu);
      sfor<4>([&](auto Dd) AI { constexpr int d = Dd; gld16<d * 1024, true>(oth[d], (unsigned)(wid * 4096 + l * 16), ws + WS_XC + (size_t)bid * 32768); });
    }
    STAMP(23);
    waitv<0>(oth[0], oth[1]);
    PIN(oth[2]); PIN(oth[3]);
    STAMP(24);
  }
#pragma unroll
  for (int d = 0; d < 4; ++d) {
    const f32x4 a = kep[d] + oth[d];
    u32x2 res;
#pragma unroll
    for (int j = 0; j < 2; ++j) {
      const unsigned sp = pk2(a[2 * j], a[2 * j + 1]), gp = gate[2 * d + j], xw = xe[d][j];
      const unsigned pr = pk2(bflo(sp) * bflo(gp), bfhi(sp) * bfhi(gp));
      res[j] = pk2(bflo(xw) + bflo(pr), bfhi(xw) + bfhi(pr));
    }
    *(LAS u32x2*)(uintptr_t)(ex_a ^ (d << 5)) = res;
  }
#pragma unroll
  for (int i = 0; i < 2; ++i)
    *(u32x4*)(out + (size_t)(erow - c + i * 8 + (l >> 3)) * DM + nb * 128 + kh * 64 + ((l & 7) ^ (i * 4 + (l >> 4))) * 8) = *(LAS const u32x4*)(uintptr_t)(ldsf + i * 8192 + l * 16);
  STAMP(25);
  STAMP_OUT(0, 26);
}

extern "C" void kernel_launch(void* const* d_in, const int* in_sizes, int n_in, void* d_out, int out_size, void* d_ws, size_t ws_size,
                              hipStream_t stream) {
  k_fused<<<256, 512, 0, stream>>>((const u16*)d_in[0], (const u16*)d_in[1], (const u16*)d_in[2], (const u16*)d_in[3], (const u16*)d_in[4],
                                   (const u16*)d_in[6], (const u16*)d_in[7], (const u16*)d_in[8], (const u16*)d_in[9], (char*)d_ws, (u16*)d_out);
}
```

```cpp
#include <hip/hip_runtime.h>
#include <stdint.h>
#include <type_traits>

typedef unsigned short u16;
typedef short bf16x8 __attribute__((ext_vector_type(8)));
typedef short s16x4 __attribute__((ext_vector_type(4)));
typedef float f32x4 __attribute__((ext_vector_type(4)));
typedef float f32x2 __attribute__((ext_vector_type(2)));
typedef unsigned u32x4 __attribute__((ext_vector_type(4)));
typedef unsigned u32x2 __attribute__((ext_vector_type(2)));
typedef __bf16 bf2_t __attribute__((ext_vector_type(2)));
#define LAS __attribute__((address_space(3)))
#define DI __device__ __forceinline__
#define AI __attribute__((always_inline))
#define SBAR() __builtin_amdgcn_sched_barrier(0)
template <class T> __device__ __forceinline__ void PIN(T& x) { asm volatile("" : "+v"(x)); }
template <class B> DI void MF(f32x4& acc, const bf16x8& a, const B& b) { asm volatile("v_mfma_f32_16x16x32_bf16 %0, %1, %2, %0" : "+v"(acc) : "v"(a), "v"(b)); }
template <class B> DI void MFC(f32x4& d, const bf16x8& a, const B& b, const f32x4& c) { asm volatile("v_mfma_f32_16x16x32_bf16 %0, %1, %2, %3" : "=&v"(d) : "v"(a), "v"(b), "v"(c)); }
template <class B> DI void MFZ(f32x4& d, const bf16x8& a, const B& b) { asm volatile("v_mfma_f32_16x16x32_bf16 %0, %1, %2, 0" : "=&v"(d) : "v"(a), "v"(b)); }
#define NOPS(n) asm volatile("s_nop " #n)
#define MFMA16(a, b, c) __builtin_amdgcn_mfma_f32_16x16x16bf16_1k((a), (b), (c), 0, 0, 0)
#define IC(n) std::integral_constant<int, (n)>{}

constexpr int SEQ = 2048, DM = 1024, DH = 128, SK = 2048, WIN = 512, FF = 3584;
constexpr size_t WS_XN = 0;
constexpr size_t WS_KV = 4u << 20;
constexpr size_t WS_ST = 5u << 20;
constexpr size_t WS_EP = WS_ST + 16384;
constexpr size_t WS_G0 = WS_ST + 32768;
constexpr size_t WS_FK = WS_ST + 49152;
constexpr size_t WS_YP = WS_ST + 65536;
constexpr size_t WS_FO = 7u << 20;
constexpr size_t WS_FY = WS_FO + 65536;
constexpr size_t WS_FX = WS_FO + 131072;
constexpr size_t WS_O = 8u << 20;
constexpr size_t WS_GU = 32u << 20;
constexpr size_t WS_XC = 24u << 20;

template <int I, int N, class F> DI void sfor_impl(F&& f) {
  if constexpr (I < N) { f(std::integral_constant<int, I>{}); sfor_impl<I + 1, N>(f); }
}
template <int N, class F> DI void sfor(F&& f) { sfor_impl<0, N>(f); }

DI unsigned pk2(float a, float b) { f32x2 v = {a, b}; return __builtin_bit_cast(unsigned, __builtin_convertvector(v, bf2_t)); }
DI float rbf(float a) { return (float)(__bf16)a; }
DI float bflo(unsigned u) { return __uint_as_float(u << 16); }
DI float bfhi(unsigned u) { return __uint_as_float(u & 0xffff0000u); }
DI float sigmoid_f(float v) { return __builtin_amdgcn_rcpf(1.f + __builtin_amdgcn_exp2f(v * -1.4426950408889634f)); }
DI float sel2(bool c, float a, float b) { PIN(a); PIN(b); return c ? a : b; }
DI float silu_f(float v) { return v * sigmoid_f(v); }
template <int CTRL> DI float dppf(float x) { return __builtin_bit_cast(float, __builtin_amdgcn_mov_dpp(__builtin_bit_cast(int, x), CTRL, 0xf, 0xf, true)); }
template <int CTRL> DI unsigned dppu(unsigned x) { return (unsigned)__builtin_amdgcn_mov_dpp((int)x, CTRL, 0xf, 0xf, true); }
constexpr int XOR1 = 0xB1, XOR2 = 0x4E, XOR7 = 0x141, XOR8 = 0x128;
DI float xg_sum(float x) {
  auto s = __builtin_amdgcn_permlane16_swap(__float_as_uint(x), __float_as_uint(x), false, false);
  x = __uint_as_float(s[0]) + __uint_as_float(s[1]);
  auto t = __builtin_amdgcn_permlane32_swap(__float_as_uint(x), __float_as_uint(x), false, false);
  return __uint_as_float(t[0]) + __uint_as_float(t[1]);
}
DI float xg_max(float x) {
  auto s = __builtin_amdgcn_permlane16_swap(__float_as_uint(x), __float_as_uint(x), false, false);
  x = __builtin_fmaxf(__uint_as_float(s[0]), __uint_as_float(s[1]));
  auto t = __builtin_amdgcn_permlane32_swap(__float_as_uint(x), __float_as_uint(x), false, false);
  return __builtin_fmaxf(__uint_as_float(t[0]), __uint_as_float(t[1]));
}
DI float wave_sum(float v) { v += dppf<XOR1>(v); v += dppf<XOR2>(v); v += dppf<XOR7>(v); v += dppf<XOR8>(v); return xg_sum(v); }
DI unsigned umax(unsigned a, unsigned b) { return a > b ? a : b; }
DI unsigned wave_max_u32(unsigned v) {
  v = umax(v, dppu<XOR1>(v)); v = umax(v, dppu<XOR2>(v)); v = umax(v, dppu<XOR7>(v)); v = umax(v, dppu<XOR8>(v));
  auto s = __builtin_amdgcn_permlane16_swap(v, v, false, false); v = umax(s[0], s[1]);
  auto t = __builtin_amdgcn_permlane32_swap(v, v, false, false); v = umax(t[0], t[1]);
  return __builtin_amdgcn_readfirstlane(v);
}

template <int OFF, bool DEV = false> DI void glds(unsigned ldsaddr, unsigned voff, const void* sbase) {
  if constexpr (DEV) asm volatile("s_mov_b32 m0, %0\n\ts_nop 0\n\tglobal_load_lds_dwordx4 %1, %2 offset:%3 sc1" ::"s"(ldsaddr), "v"(voff), "s"(sbase), "i"(OFF) : "memory");
  else asm volatile("s_mov_b32 m0, %0\n\ts_nop 0\n\tglobal_load_lds_dwordx4 %1, %2 offset:%3" ::"s"(ldsaddr), "v"(voff), "s"(sbase), "i"(OFF) : "memory");
}
DI void touch(unsigned voff, const void* sbase) {
  asm volatile("global_load_dword v255, %0, %1" ::"v"(voff), "s"(sbase) : "memory", "v255");
}
template <int OFF, bool DEV = false, class T> DI void gld16(T& dst, unsigned voff, const void* sbase) {
  if constexpr (DEV) asm volatile("global_load_dwordx4 %0, %1, %2 offset:%3 sc1" : "=v"(dst) : "v"(voff), "s"(sbase), "i"(OFF) : "memory");
  else asm volatile("global_load_dwordx4 %0, %1, %2 offset:%3" : "=v"(dst) : "v"(voff), "s"(sbase), "i"(OFF) : "memory");
}
template <int OFF, bool DEV = false, class T> DI void gld8(T& dst, unsigned voff, const void* sbase) {
  if constexpr (DEV) asm volatile("global_load_dwordx2 %0, %1, %2 offset:%3 sc1" : "=v"(dst) : "v"(voff), "s"(sbase), "i"(OFF) : "memory");
  else asm volatile("global_load_dwordx2 %0, %1, %2 offset:%3" : "=v"(dst) : "v"(voff), "s"(sbase), "i"(OFF) : "memory");
}
template <int OFF> DI void gld2(unsigned& dst, unsigned voff, const void* sbase) {
  asm volatile("global_load_ushort %0, %1, %2 offset:%3" : "=v"(dst) : "v"(voff), "s"(sbase), "i"(OFF) : "memory");
}
template <int OFF, class T> DI void gst16(const T& v, unsigned voff, const void* sbase) {
  asm volatile("global_store_dwordx4 %0, %1, %2 offset:%3 sc1\n\ts_nop 1" ::"v"(voff), "v"(v), "s"(sbase), "i"(OFF) : "memory");
}
template <class T> DI void gst8(const T& v, unsigned voff, const void* sbase) {
  asm volatile("global_store_dwordx2 %0, %1, %2 sc1" ::"v"(voff), "v"(v), "s"(sbase) : "memory");
}
template <int OFF> DI void gst4(float v, unsigned voff, const void* sbase) {
  asm volatile("global_store_dword %0, %1, %2 offset:%3 sc1" ::"v"(voff), "v"(v), "s"(sbase), "i"(OFF) : "memory");
}
constexpr int SPINS = 1 << 16;
DI void poll8(unsigned voff, const void* sbase, unsigned lo, unsigned hi, unsigned lomask) {
  for (int spin = 0; spin < SPINS; ++spin) {
    u32x2 v;
    asm volatile("global_load_dwordx2 %0, %1, %2 sc1\n\ts_waitcnt vmcnt(0)" : "=&v"(v) : "v"(voff), "s"(sbase) : "memory");
    if (__all(((v[0] & lomask) == lo) & (v[1] == hi))) break;
    __builtin_amdgcn_s_sleep(1);
  }
}
template <int N> DI void waitv() { static_assert(N >= 0, "count"); asm volatile("s_waitcnt vmcnt(%0)" ::"n"(N > 63 ? 63 : N) : "memory"); }
template <int N, class T> DI void waitv(T& x) { static_assert(N >= 0, "count"); asm volatile("s_waitcnt vmcnt(%1)" : "+v"(x) : "n"(N > 63 ? 63 : N) : "memory"); }
template <int N, class T> DI void waitv(T& x, T& y) { static_assert(N >= 0, "count"); asm volatile("s_waitcnt vmcnt(%2)" : "+v"(x), "+v"(y) : "n"(N > 63 ? 63 : N) : "memory"); }
template <int N> DI void waitbar() { static_assert(N >= 0, "count"); asm volatile("s_waitcnt vmcnt(%0)\n\ts_barrier" ::"n"(N > 63 ? 63 : N) : "memory"); }
template <int N> DI void waitbar_l() { static_assert(N >= 0, "count"); asm volatile("s_waitcnt vmcnt(%0) lgkmcnt(0)\n\ts_barrier" ::"n"(N > 63 ? 63 : N) : "memory"); }
typedef short v4i16_t __attribute__((ext_vector_type(4)));
DI s16x4 trd(LAS const char* p) { return __builtin_amdgcn_ds_read_tr16_b64_v4i16((LAS v4i16_t*)p); }
struct LBase {
  unsigned lo, hi;
  DI void init(unsigned a) { lo = a; hi = a + 65536; PIN(lo); PIN(hi); }
  template <int OFF> DI LAS const char* at() const { return (LAS const char*)(uintptr_t)(OFF < 65536 ? lo : hi) + (OFF & 65535); }
};
DI bf16x8 cat8(s16x4 lo, s16x4 hi) { return __builtin_shufflevector(lo, hi, 0, 1, 2, 3, 4, 5, 6, 7); }

enum { OX, OG, OF, OM, OS, OP, OE, OW };
struct Sched {
  int n; short kind[400], idx[400], slot[400];
  constexpr Sched() : n(0), kind{}, idx{}, slot{} {}
  constexpr void add(int s, int k, int i) { kind[n] = (short)k; idx[n] = (short)i; slot[n] = (short)s; ++n; }
  constexpr int younger(int k, int i, int s) const {
    int p = -1;
    for (int q = 0; q < n; ++q) if (kind[q] == k && idx[q] == i) p = q;
    if (p < 0 || slot[p] > s) return -1;
    int c = 0;
    for (int q = p + 1; q < n; ++q) if (slot[q] <= s) ++c;
    return c;
  }
  constexpr int first(int s) const { int q = 0; while (q < n && slot[q] < s) ++q; return q; }
  constexpr int count(int s) const { int c = 0; for (int q = 0; q < n; ++q) if (slot[q] == s) ++c; return c; }
};

#define STAMP_DECL
#define STAMP(i)
#define STAMP_OUT(kid, n)

constexpr int RING = 4 * 32768, FB = RING, RED1 = FB + 24576, HSC = RED1 + 1024, YS = HSC + 1024, LDS_BYTES = FB + 32768;
constexpr int STAGE = 3 * 32768;
constexpr int XD = 16, XD2 = 16;
constexpr float LBOUND = 65536.f;
constexpr float QSCALE = 0.08838834764831845f * 1.4426950408889634f;
constexpr int B0 = 128;
#define PRIO 1
constexpr int FISS[4] = {96, 97, 98, 117}, FRDS[4] = {115, 118, 121, 124};
constexpr int fpiece(int sl) { for (int p = 0; p < 4; ++p) if (FRDS[p] == sl) return p; return -1; }
constexpr Sched make_sched() {
  Sched s;
#define XP 8
  for (int i = 0; i < 12; ++i) s.add(-2, OG, i);
  for (int i = 0; i < XP; ++i) s.add(-1, OX, i);
  for (int t = 0; t < 128; ++t) {
    if (t < 2) for (int i = XP + t * (16 - XP) / 2; i < XP + (t + 1) * (16 - XP) / 2; ++i) s.add(t, OX, i);
    if (t >= 2 && t < 50) s.add(t, OX, t + 14);
    for (int v = 3; v < 18; ++v) for (int p = 0; p < 4; ++p) if (8 * (v - 3) + 7 + p == t) s.add(t, OG, 4 * v + p);
    if (t == 105 || t == 113 || t == 121) { s.add(t, OW, (t - 105) / 4); s.add(t, OW, (t - 105) / 4 + 1); }
    if (t >= 124) for (int i = 0; i < 3; ++i) s.add(t, OX, 64 + 3 * (t - 124) + i);
    if (t == 127) { s.add(t, OG, 72); s.add(t, OG, 73); }
    if (t == 20) s.add(t, OM, 0);
    if (t == 120) s.add(t, OM, 1);
    if (t >= 49 && t < 57 && (t & 1)) s.add(t, OF, (t - 49) / 2);
    if (t >= 71 && t < 79 && (t & 1)) s.add(t, OF, 4 + (t - 71) / 2);
    if (t == 48) s.add(t, OS, 0);
    if (t == 104) s.add(t, OS, 2);
    for (int p = 0; p < 4; ++p) if (FISS[p] == t) s.add(t, OF, 8 + p);
  }
  for (int t = 0; t < 64; ++t) {
    if (t == 0) { s.add(B0, OM, 2); s.add(B0, OG, 74); s.add(B0, OG, 75); s.add(B0, OW, 6); s.add(B0, OW, 7); }
    if (t == 1 || t == 2) { s.add(B0 + t, OX, 74 + 2 * t); s.add(B0 + t, OX, 75 + 2 * t); }
    if (t == 5 || t == 6 || t == 9) s.add(B0 + t, OX, 64 + (t == 5 ? 16 : t == 6 ? 18 : 22));
    if (t >= 5 && t < 50) s.add(B0 + t, OX, 64 + (t == 5 ? 17 : t < 9 ? t + 13 : t + 14));
    for (int v = 3; v < 16; ++v) for (int p = 0; p < 4; ++p) if ((v < 14 ? 4 * (v - 3) + 3 + (p >> 1) : v == 14 ? 45 + (p >> 1) : 49) == t) s.add(B0 + t, OG, 64 + 4 * v + p);
    if (t == 10 || t == 26) s.add(B0 + t, OM, t == 10 ? 3 : 4);
    if (t == 13 || t == 14 || t == 18) s.add(B0 + t, OE, t == 18 ? 2 : t - 13);
  }
  return s;
}
constexpr Sched SC = make_sched();

__global__ __launch_bounds__(512, 2) void k_fused(const u16* __restrict__ x, const u16* __restrict__ kc, const u16* __restrict__ vc,
                                                  const u16* __restrict__ wq, const u16* __restrict__ wo, const u16* __restrict__ wr,
                                                  const u16* __restrict__ wgate, const u16* __restrict__ wup, const u16* __restrict__ wdown,
                                                  char* __restrict__ ws, u16* __restrict__ out) {
  __shared__ __attribute__((aligned(16))) char lds_raw[LDS_BYTES];
  LAS char* const lds = (LAS char*)lds_raw;
  const int t = threadIdx.x, l = t & 63, bid = blockIdx.x, c = l & 15, g = l >> 4;
  const int wid = __builtin_amdgcn_readfirstlane(t >> 6);
  const int xcd = bid & 7, within = bid >> 3, head = xcd * 4 + (within & 3), rb = within >> 2;
  const int fj = xcd * 28 + (within < 28 ? within : within - 28);
  const int fsl = fj / 7, fgr = fj - fsl * 7;
  const int kh = xcd >> 2, mb = 2 * rb + (xcd & 1), nb = ((xcd >> 1) & 1) * 4 + (within & 3);
  const int rbw = wid & 3, kg = wid >> 2;
  const unsigned lds0 = (unsigned)(uintptr_t)lds;
  STAMP_DECL
  STAMP(0);

  unsigned tlo, thi;
  {
    const int row = 8 * bid + wid;
    u32x4 a0, a1, w0, w1, kvo;
    u32x2 ep, klo, khi;
    unsigned e[8];
    gld16<0>(a0, (unsigned)(row * DM * 2 + l * 16), x); gld16<1024>(a1, (unsigned)(row * DM * 2 + l * 16), x);
    gld16<0>(w0, (unsigned)(l * 16), wr); gld16<1024>(w1, (unsigned)(l * 16), wr);
    gld8<0, true>(ep, 0u, ws + WS_EP);
    if (wid == 1)
      asm volatile("global_load_dword v255, %0, %1 sc1" ::"v"(l == 0 ? (unsigned)(WS_G0 - WS_ST) + bid * 8 : l == 1 ? (unsigned)(WS_FK - WS_ST) + bid * 32 : (unsigned)(bid * 64)), "s"(ws + WS_ST) : "memory", "v255");
    if (wid == 3) {
      const char* pc;
      asm volatile("s_getpc_b64 %0" : "=s"(pc));
      touch((unsigned)((within * 12 + (l < 12 ? l : 11)) * 128), pc);
    }
    __builtin_amdgcn_s_barrier();
    if (wid == 2) {
      asm volatile("global_load_dword v255, %0, %1 sc1" ::"v"((unsigned)((l < 40 ? l : 39) << 20) + 64 * 1024 - 128), "s"(ws) : "memory", "v255");
      const char* pb = l < 8 ? (const char*)wo : l < 16 ? (const char*)wgate : l < 24 ? (const char*)wup : l < 32 ? (const char*)wdown : (const char*)out;
      pb += (size_t)(l & 7) * (l < 8 ? 1048576 : l < 32 ? 917504 : 524288);
      asm volatile("global_load_dword v255, %0, off" ::"v"(pb) : "memory", "v255");
    }
    const int fn = (bid * 4 + wid) & 255, kvh = bid >> 6;
    if (wid < 4) {
      if (fn < 128) {
        const unsigned ko = (unsigned)(((kvh * SK + (SK - WIN) + (fn >> 2) * 16 + c) * DH + (fn & 3) * 32 + 4 * g) * 2);
        gld8<0>(klo, ko, kc); gld8<32>(khi, ko, kc);
      } else {
        const int d = 32 * ((fn - 128) >> 5) + 8 * (c >> 2) + 4 * (fn & 1) + (c & 3);
        const unsigned vo = (unsigned)(((kvh * SK + (SK - WIN) + ((fn >> 1) & 15) * 32 + 4 * g + 8) * DH + d) * 2);
        sfor<8>([&](auto J) AI { constexpr int j = J; gld2<(16 * (j >> 2) + (j & 3) - 8) * DH * 2>(e[j], vo, vc); });
      }
      if (wid < 2) touch((unsigned)((wid * 256 + within * 8 + (l >> 3)) * 8192 + xcd * 1024 + (l & 7) * 128), wq);
      if (fn < 128) waitv<5>(); else waitv<11>();
    } else {
      waitv<3>();
    }
    PIN(a0); PIN(a1);
    STAMP(1);
    float v[16];
#pragma unroll
    for (int j = 0; j < 4; ++j) { v[2 * j] = bflo(a0[j]); v[2 * j + 1] = bfhi(a0[j]); v[8 + 2 * j] = bflo(a1[j]); v[9 + 2 * j] = bfhi(a1[j]); }
    float s = 0.f;
#pragma unroll
    for (int j = 0; j < 16; ++j) s += v[j];
    const float mean = wave_sum(s) * (1.f / DM);
    float q = 0.f;
#pragma unroll
    for (int j = 0; j < 16; ++j) { const float d = v[j] - mean; q += d * d; }
    const float var = wave_sum(q) * (1.f / DM);
    const float mbf = rbf(mean), rden = __builtin_amdgcn_rcpf(rbf(__builtin_amdgcn_sqrtf(rbf(rbf(var) + 1e-5f))));
    u32x4 o0, o1;
#pragma unroll
    for (int j = 0; j < 8; ++j) {
      const unsigned dd = pk2(v[2 * j] - mbf, v[2 * j + 1] - mbf);
      const unsigned nn = pk2(bflo(dd) * rden, bfhi(dd) * rden);
      if (j < 4) o0[j] = nn; else o1[j - 4] = nn;
    }
    *(LAS u32x4*)(lds + STAGE + (l * 8 + (wid ^ (l & 7))) * 16) = o0;
    *(LAS u32x4*)(lds + STAGE + 8192 + (l * 8 + (wid ^ (l & 7))) * 16) = o1;
    *(LAS f32x2*)(lds + STAGE + 16384 + 32 + wid * 8) = (f32x2){mbf, rden};
    waitbar_l<63>();
    if (wid >= 4) {
#pragma unroll
      for (int hh = 0; hh < 4; ++hh) {
        const int p = hh * 256 + (t - 256), ch = p >> 3, r = 8 * bid + ((p & 7) ^ (ch & 7));
        const u32x4 o = *(LAS const u32x4*)(lds + STAGE + p * 16);
        gst16<0>(o, (unsigned)(((((r >> 5) * 32 + (ch >> 2)) * 2 + ((r >> 4) & 1)) * 64 + (ch & 3) * 16 + (r & 15)) * 16), ws + WS_XN);
      }
      if (t < 256 + 8) gst8(*(LAS const f32x2*)(lds + STAGE + 16384 + 32 + (t - 256) * 8), (unsigned)((8 * bid + t - 256) * 8), ws + WS_ST);
      waitv<4>();
    } else if (fn < 128) waitv<2>(); else waitv<8>();
    PIN(w0); PIN(w1); PIN(ep);
    {
      const unsigned long long tg = (((unsigned long long)(unsigned)__builtin_amdgcn_readfirstlane(ep[1]) << 32) | (unsigned)__builtin_amdgcn_readfirstlane(ep[0])) + 1ull;
      tlo = (unsigned)tg; thi = (unsigned)(tg >> 32);
    }
    float rr = 0.f;
#pragma unroll
    for (int j = 0; j < 8; ++j) {
      const unsigned nn = j < 4 ? o0[j] : o1[j - 4], ww = j < 4 ? w0[j] : w1[j - 4];
      rr += bflo(nn) * bflo(ww); rr += bfhi(nn) * bfhi(ww);
    }
    rr = wave_sum(rr);
    {
      const unsigned bits = pk2(rr, 0.f) & 0xffffu;
      const unsigned ord = (bits & 0x8000u) ? (~bits & 0xffffu) : (bits | 0x8000u);
      *(LAS unsigned*)(lds + STAGE + 16384 + wid * 4) = (ord << 11) | (unsigned)(SEQ - 1 - row);
    }
    if (wid >= 4) waitv<0>();
    waitbar_l<63>();
    if (t == 256) {
      const u32x4 ka = *(LAS const u32x4*)(lds + STAGE + 16384), kb = *(LAS const u32x4*)(lds + STAGE + 16400);
      const unsigned key = umax(umax(umax(ka[0], ka[1]), umax(ka[2], ka[3])), umax(umax(kb[0], kb[1]), umax(kb[2], kb[3])));
      gst8((u32x2){(tlo << 27) | key, (tlo >> 5) | (thi << 27)}, (unsigned)(bid * 8), ws + WS_G0);
    }
    if (wid < 4) {
      if (wid < 2) waitv<1>(); else waitv<0>();
      if (fn < 128) {
        PIN(klo); PIN(khi);
        kvo = (u32x4){klo[0], klo[1], khi[0], khi[1]};
      } else {
#pragma unroll
        for (int j = 0; j < 8; ++j) PIN(e[j]);
        kvo = (u32x4){e[0] | (e[1] << 16), e[2] | (e[3] << 16), e[4] | (e[5] << 16), e[6] | (e[7] << 16)};
      }
      gst16<0>(kvo, (unsigned)((bid * 4 + wid) * 1024 + l * 16), ws + WS_KV);
    }
    STAMP(2);
  }
  const unsigned glo = tlo << 27, ghi = (tlo >> 5) | (thi << 27);

  const char* const wq_b = (const char*)wq + head * 256;
  const unsigned wq_v = (unsigned)(((8 * wid + ((l & 31) >> 2)) * 4096 + (l >> 5) * 32 + ((l & 3) ^ (2 * (wid & 1))) * 8) * 2);
  const char* const kv_b = ws + WS_KV + (size_t)(head >> 3) * 262144;
  const unsigned kv_v = (unsigned)(wid * 4096 + l * 16);
  const char* const xn_b = ws + WS_XN + (size_t)rb * 8 * 65536 + 4096;
  const unsigned xn_v = (unsigned)(wid * 65536 + l * 16);
  const char* const wo_b = (const char*)wo + (size_t)kh * 2048 * (DM * 2) + nb * 256;
  const char* const o_b = ws + WS_O + (size_t)mb * 4 * 262144 + kh * 131072;
  const unsigned ldsw = lds0 + wid * 2048, ldsk = lds0 + wid * 4096, ldsf = lds0 + FB + wid * 1024;
  const int erow = mb * 128 + rbw * 32 + kg * 16 + c;
  bf16x8 xf[XD];
  u32x4 fm[2];
  bf16x8 fxs;
  const unsigned gu_v = (unsigned)((((4 * wid + g) * 2 + ((l >> 3) & 1)) * FF + fj * 16 + (l & 7) * 2) * 8);
  u32x2 fg;
  u32x2 xe[4];
  f32x2 st;
  unsigned key = 0;

  const unsigned fo_v = (unsigned)(((mb * 4 + rbw) * 32 + kh * 16 + c) * 8);
  const int yrow = within < 28 ? fj : 224 + xcd * 4 + within - 28;
  f32x4 o[2][2][2];
  float inv[2];
  const char* const od_b = ws + WS_O + ((size_t)(rb * 8) * 128 + head * 4) * 2048 + 4096;
  auto issue = [&](auto Sl, auto Pos) AI {
    constexpr int sl = Sl, pos = Pos, cnt = SC.count(sl), q0 = SC.first(sl) + (pos < 0 ? 0 : pos);
    constexpr int nq = pos < 0 ? cnt : pos >= cnt ? 0 : pos < 3 ? 1 : cnt - 3;
    sfor<nq>([&](auto Qi) AI {
      constexpr int k = SC.kind[q0 + Qi], i = SC.idx[q0 + Qi];
      if constexpr (k == OX && i < 64) gld16<(i & 7) * 1024 - 4096, true>(xf[i % XD], xn_v, xn_b + (size_t)(i >> 3) * 8192);
      if constexpr (k == OX && i >= 64) {
        constexpr int q = i - 64, v = q >> 2;
        gld16<0, true>(xf[q % XD2], (unsigned)(rbw * 262144 + kg * 16384 + ((q & 1) ^ kg) * 1024 + l * 16), o_b + (size_t)(((v & 3) * 4 + ((q >> 1) & 1)) * 8192 + (v >> 2) * 2048));
      }
      if constexpr (k == OG) {
        constexpr int v = i >> 2, p = i & 3;
        if constexpr (v < 8) {
          const char* sb = wq_b + (size_t)(v * 128 + (p >> 1) * 64) * 8192;
          glds<(p & 1) * 128>(ldsw + (v & 3) * 32768 + (p >> 1) * 16384 + (p & 1) * (1024 - 128), wq_v, sb);
        } else if constexpr (v < 16) {
          glds<p * 1024, true>(ldsk + (v & 3) * 32768, kv_v, kv_b + (size_t)(v - 8) * 32768);
        } else {
          const unsigned wo_v = (unsigned)((((wid >> 2) * 128 + (wid & 3) * 8 + ((l & 31) >> 2)) * DM + (l >> 5) * 32 + ((l & 3) ^ (2 * (wid & 1))) * 8) * 2);
          const char* sb = wo_b + (size_t)(((v & 3) * 4 + (p >> 1) * 2) * 128 + ((v - 16) >> 2) * 32) * (DM * 2) + (((p & 1) ^ kh ^ 1) << 7);
          glds<0>(ldsw + (v & 3) * 32768 + (p >> 1) * 16384 + (p & 1) * 1024, wo_v, sb);
        }
      }
      if constexpr (k == OF && i < 8)
        glds<0>(ldsf + (i & 3) * 8192, (unsigned)(((l >> 3) * FF + wid * 64 + ((l & 7) ^ ((i & 1) * 4 + ((l >> 3) & 2))) * 8) * 2),
                (const char*)(i < 4 ? wgate : wup) + ((size_t)(fsl * 32 + 8 * (i & 3)) * FF + fgr * 512) * 2);
      if constexpr (k == OF && i >= 8) {
        const int frho = l >> 2, fch = (l & 3) ^ (frho & 4 ? 2 : 0);
        glds<0>(ldsf + ((i - 8) % 3) * 8192, (unsigned)((frho * DM + wid * 128 + fch * 8) * 2), (const char*)wdown + (size_t)fj * 16 * DM * 2 + (i - 8) * 64);
      }
      if constexpr (k == OS && i == 0) {
        const int idx = SEQ - 1 - (int)(key & 2047u);
        gld16<0, true>(fxs, (unsigned)((((idx >> 5) * 32 + fsl) * 2 + ((idx >> 4) & 1)) * 1024 + (g * 16 + (idx & 15)) * 16), ws + WS_XN);
      }
      if constexpr (k == OS && i == 2) gld16<0, true>(fm[0], gu_v, ws + WS_GU);
      if constexpr (k == OM && i > 0) gld8<0, true>(fg, fo_v, ws + WS_FO + (i - 1) * 16384);
      if constexpr (k == OM && i == 0) gld8<0, true>(fg, (unsigned)((t & 255) * 8), wid < 4 ? ws + WS_G0 : ws + WS_FK + (head >> 3) * 2048);
      if constexpr (k == OW) {
        constexpr int os = i >> 1, qb = i & 1;
        PIN(o[os & 1][0][qb]); PIN(o[os & 1][1][qb]);
        const f32x4 a = o[os & 1][0][qb] * inv[qb], b = o[os & 1][1][qb] * inv[qb];
        gst16<i * 1024 - 4096>((u32x4){pk2(a[0], a[1]), pk2(a[2], a[3]), pk2(b[0], b[1]), pk2(b[2], b[3])}, (unsigned)(wid * 262144 + l * 16), od_b);
      }
      if constexpr (k == OE && i < 2)
        glds<0>(ldsf + i * 8192, (unsigned)(((erow - c + i * 8 + (l >> 3)) * DM + nb * 128 + kh * 64 + ((l & 7) ^ (i * 4 + (l >> 4))) * 8) * 2), x);
      if constexpr (k == OE && i == 2) gld8<0, true>(st, (unsigned)(erow * 8), ws + WS_ST);
    });
  };
  f32x4 fa0, fa1, fq[4];
  s16x4 fb0, fb1, fxa;
  bf16x8 ft[2];
  auto endslot = [&](auto Sl) AI {
    constexpr int sl = Sl, u = sl >> 3, i = sl & 7;
    if constexpr (sl == 3) {
      if (wid == 6 || wid == 7) touch((unsigned)(((wid - 4) * 256 + within * 8 + (l >> 3)) * 8192 + xcd * 1024 + (l & 7) * 128), wq);
    }
    if constexpr (sl == 2) {
      if (wid < 4 && l == 0) gst8((u32x2){tlo, thi}, (unsigned)((bid * 4 + wid) * 8), ws + WS_FK);
    }
    if constexpr (sl == 38) {
      waitv<SC.younger(OM, 0, sl)>(fg);
      const unsigned lo = wid < 4 ? glo : tlo, hi = wid < 4 ? ghi : thi, mk = wid < 4 ? 0xf8000000u : 0xffffffffu;
      for (int spin = 0; spin < SPINS; ++spin) {
        if (__builtin_expect(__all(((fg[0] & mk) == lo) & (fg[1] == hi)), 1)) break;
        __builtin_amdgcn_s_sleep(1);
        gld8<0, true>(fg, (unsigned)((t & 255) * 8), wid < 4 ? ws + WS_G0 : ws + WS_FK + (head >> 3) * 2048);
        waitv<0>(fg);
      }
      const unsigned km = wave_max_u32(fg[0] & 0x07ffffffu);
      *(LAS unsigned*)(lds + HSC + wid * 4) = km;
    }
    if constexpr (sl == 57) {
      if (wid == 3) {
        const char* pc;
        asm volatile("s_getpc_b64 %0" : "=s"(pc));
        touch((unsigned)((within * 8 + (l & 7)) * 128), pc);
      }
    }
    if constexpr (sl == 47) {
      if (bid == 0 && t == 0) gst8((u32x2){tlo, thi}, 0u, ws + WS_EP);
      const u32x4 kk = *(LAS const u32x4*)(lds + HSC);
      key = __builtin_amdgcn_readfirstlane(umax(umax(kk[0], kk[1]), umax(kk[2], kk[3])));
    }
    if constexpr (sl == 58 || sl == 61 || sl == 64 || sl == 67) {
      if (wid == 4 + (sl - 58) / 3) touch((unsigned)(((wid & 3) * 64 + l) * 128), (const char*)wdown + (size_t)fj * 16 * DM * 2);
    }
    if constexpr (sl == 81 || sl == 85 || sl == 89 || sl == 93) {
      if (wid == (sl - 81) / 4)
        touch((unsigned)((kh * 2048 + (within >> 1) * 128 + wid * 32 + ((within & 1) * 2 + (xcd & 1)) * 8 + (l >> 3)) * (DM * 2) + ((xcd >> 1) & 1) * 1024 + (l & 7) * 128), wo);
    }
    if constexpr (sl == 66) {
      const unsigned fo = (unsigned)(WS_FO - WS_ST) + (l & 3) * 16384 + ((mb * 4 + rbw) * 32 + kh * 16) * 8, fx = (unsigned)(WS_FX - WS_ST) + (bid * 8 + wid) * 64;
      const unsigned fy = (unsigned)(WS_FY - WS_ST) + (wid * 256 + yrow) * 8;
      asm volatile("global_load_dword v255, %0, %1 sc1" ::"v"(l < 4 ? fo : l < 8 ? fy : fx), "s"(ws + WS_ST) : "memory", "v255");
    }
    if constexpr (sl == 97 || sl == 99) {
      if (wid == 4 + (sl - 97) / 2) touch((unsigned)(((mb * 128 + (wid - 4) * 64 + l) * DM + nb * 128 + kh * 64) * 2), x);
    }
    if constexpr ((sl >= 68 && sl <= 70) || (sl >= 90 && sl <= 92)) {
      constexpr int r = sl >= 90, q = sl - (r ? 90 : 68);
      if constexpr (q == 0) waitv<SC.younger(OF, 4 * r + 3, sl)>(fxs);
      if constexpr (q >= 1) {
#pragma unroll
        for (int j = 0; j < 2; ++j) { fq[2 * q - 2 + j] = __builtin_amdgcn_mfma_f32_16x16x32_bf16(fxs, ft[j], (f32x4){}, 0, 0, 0); PIN(fq[2 * q - 2 + j]); }
      }
      if constexpr (q < 2) {
        const unsigned rp = ldsf + g * 8192 + (c >> 2) * 128 + ((((g & 1) << 1) | (c >> 3)) << 5) + (c & 3) * 8;
#pragma unroll
        for (int j = 0; j < 2; ++j) { LAS const char* a = (LAS const char*)(uintptr_t)(rp ^ ((2 * q + j) << 5)); ft[j] = cat8(trd(a), trd(a + 512)); }
      }
    }
    if constexpr (sl == 71 || sl == 93) {
      const float v = sel2(g & 2, sel2(g & 1, fq[3][0], fq[2][0]), sel2(g & 1, fq[1][0], fq[0][0]));
      gst8((u32x2){__float_as_uint(v), tlo}, (unsigned)(((fsl * 2 + (sl == 93)) * FF + fgr * 512 + t) * 8), ws + WS_GU);
    }
    if constexpr (sl == 109) {
      waitv<SC.younger(OS, 2, sl)>(fm[0]);
      for (int spin = 0; spin < SPINS; ++spin) {
        if (__builtin_expect(__all((fm[0][1] == tlo) & (fm[0][3] == tlo)), 1)) break;
        __builtin_amdgcn_s_sleep(1);
        gld16<0, true>(fm[0], gu_v, ws + WS_GU);
        waitv<0>(fm[0]);
      }
      *(LAS f32x2*)(lds + RED1 + (wid * 32 + 2 * c) * 4) = (f32x2){xg_sum(__uint_as_float(fm[0][0])), xg_sum(__uint_as_float(fm[0][2]))};
    }
    if constexpr (sl == 111) {
#pragma unroll
      for (int w = 0; w < 2; ++w) { fa0[w] = *(LAS const float*)(lds + RED1 + ((2 * g + w) * 32 + c) * 4); fa0[2 + w] = *(LAS const float*)(lds + RED1 + ((2 * g + w) * 32 + 16 + c) * 4); }
    }
    if constexpr (sl == 112) { fa0[0] = xg_sum(fa0[0] + fa0[1]); fa0[2] = xg_sum(fa0[2] + fa0[3]); PIN(fa0); }
    if constexpr (sl == 113) {
      const float hh = rbf(silu_f(rbf(fa0[0]))) * rbf(fa0[2]);
      *(LAS u16*)(lds + HSC + 32 + wid * 32 + c * 2) = (u16)pk2(hh, hh);
    }
    if constexpr (sl == 114) fxa = *(LAS const s16x4*)(lds + HSC + 32 + wid * 32 + g * 8);
    constexpr int pr = fpiece(sl), pm = fpiece(sl - 1), ps = fpiece(sl - 2);
    if constexpr (ps >= 0) gst4<ps * 128>(sel2(g & 1, fa1[0], fa0[0]), (unsigned)((wid * 128 + (l & 31)) * 4), ws + WS_YP + (size_t)yrow * 4096);
    if constexpr (pm >= 0) { fa0 = MFMA16(fxa, fb0, (f32x4){}); fa1 = MFMA16(fxa, fb1, (f32x4){}); PIN(fa0); PIN(fa1); }
    if constexpr (pr >= 0) {
      waitv<SC.younger(OF, 8 + pr, sl)>();
      const unsigned fp = ldsf + (4 * g + (c >> 2)) * 64 + (g & 1) * 32 + (c & 3) * 8;
      fb0 = trd((LAS const char*)(uintptr_t)fp + (pr % 3) * 8192); fb1 = trd((LAS const char*)(uintptr_t)(fp ^ 32) + (pr % 3) * 8192);
    }
    if constexpr (sl == 111 || sl == 119 || sl == 127) {
      waitv<SC.younger(OW, (sl - 111) / 4 + 1, sl)>();
      if (l == 0) gst8((u32x2){tlo, thi}, (unsigned)(((rb * 8 + wid) * 32 + head) * 8), ws + WS_FO + ((sl - 111) / 8) * 16384);
    }
    if constexpr (sl == 123) {
      waitv<SC.younger(OM, 1, sl)>(fg);
      if (__builtin_expect(!__all((fg[0] == tlo) & (fg[1] == thi)), 0)) poll8(fo_v, ws + WS_FO, tlo, thi, 0xffffffffu);
    }
    SBAR();
    if constexpr (i == 6) {
      if constexpr (u == 5 || u == 13) waitbar_l<SC.younger(OG, 4 * u + 7, sl)>();
      else waitbar<SC.younger(OG, 4 * u + 7, sl)>();
      if constexpr (u == 0 || u == 1 || u == 5 || u == 7 || u == 8 || u == 10 || u == 12 || u == 14)
        STAMP(u == 0 ? 4 : u == 1 ? 5 : u == 5 ? 6 : u == 7 ? 7 : u == 8 ? 8 : u == 10 ? 9 : u == 12 ? 10 : 11);
    }
    if constexpr (PRIO && !(i & 1)) __builtin_amdgcn_s_setprio(i == 6 ? 3 : 2 - i / 2);
  };

  issue(IC(-2), IC(-1));
  if (wid == 5) poll8((unsigned)((rb * 32 + (l & 31)) * 8), ws + WS_G0, glo, ghi, 0xf8000000u);
  waitbar<SC.younger(OG, 3, -2)>();
  issue(IC(-1), IC(-1));
  STAMP(3);

  LBase trb[2], frb;
  trb[0].init(lds0 + g * 2048 + (c >> 2) * 64 + (g & 1) * 32 + (c & 3) * 8);
  trb[1].init(lds0 + g * 2048 + (c >> 2) * 64 + ((g & 1) ^ 1) * 32 + (c & 3) * 8);
  frb.init(lds0 + l * 16);
  f32x4 acc[8][2];
  bf16x8 wf[2][4];
  auto WRD1 = [&](auto Si, auto Base, auto Dd) AI {
    constexpr int s = Si, d = Dd, o = Base + (d >> 1) * 512;
    wf[s & 1][d] = cat8(trd(trb[d & 1].template at<o>()), trd(trb[d & 1].template at<o + 256>()));
  };
  auto WRD = [&](auto Si, auto Base) AI { sfor<4>([&](auto Dd) AI { WRD1(Si, Base, Dd); }); };
#define WA(s) IC((((s) >> 3) & 3) * 32768 + (((s) >> 1) & 3) * 8192 + ((s) & 1) * 1024)
#define WB(s) IC((((s) >> 2) & 3) * 32768 + (((s) >> 1) & 1) * 8192 + ((s) & 1) * 1024)
  bf16x8 fr[4];
  auto FRD = [&](auto Ni) AI { constexpr int n = Ni; if constexpr (n < 256) fr[n & 3] = *(LAS const bf16x8*)(frb.at<((n >> 5) & 3) * 32768 + (n & 31) * 1024>()); };

  WRD(IC(0), WA(0));
  SBAR();
  bf16x8 qf[4][2];
  auto QPACK = [&](auto Ds) AI {
    constexpr int ds = Ds;
#pragma unroll
    for (int qb = 0; qb < 2; ++qb) {
      const f32x4 a = acc[2 * ds][qb] * QSCALE, b = acc[2 * ds + 1][qb] * QSCALE;
      qf[ds][qb] = __builtin_bit_cast(bf16x8, (u32x4){pk2(a[0], a[1]), pk2(a[2], a[3]), pk2(b[0], b[1]), pk2(b[2], b[3])});
      PIN(qf[ds][qb]);
    }
  };
  sfor<64>([&](auto Si) AI {
    constexpr int s = Si, x0 = (s & ~1) % XD, x1 = (s | 1) % XD;
    if constexpr (!(s & 1)) waitv<SC.younger(OX, s + 1, s - 1)>(xf[x0], xf[x1]);
    sfor<4>([&](auto Dd) AI {
      constexpr int d = Dd, dt = (s & 1) * 4 + d;
      if constexpr (s + 1 < 64) WRD1(IC(s + 1), WA(s + 1), Dd);
      else if constexpr (d < 3) FRD(Dd);
      SBAR();
      if constexpr (s < 2) { MFZ(acc[dt][0], wf[s & 1][d], xf[x0]); MFZ(acc[dt][1], wf[s & 1][d], xf[x1]); }
      else { MF(acc[dt][0], wf[s & 1][d], xf[x0]); MF(acc[dt][1], wf[s & 1][d], xf[x1]); }
      issue(Si, Dd);
      if constexpr (s == 63 && (d & 1)) { QPACK(IC(d >> 1)); SBAR(); }
    });
    SBAR();
    endslot(Si);
  });

  f32x4 sc[2][2][2], negm[2];
  u32x4 pw[16][2];
  f32x4 lsum[2];
  bf16x8 ones = {0x3f80, 0x3f80, 0x3f80, 0x3f80, 0x3f80, 0x3f80, 0x3f80, 0x3f80};
  PIN(ones);
#define MX3(a, b, c) __builtin_fmaxf(__builtin_fmaxf((a), (b)), (c))
#define EXP2(x) __builtin_amdgcn_exp2f(x)
  auto SM = [&](auto Hi, auto Pi) AI {
    constexpr int H = Hi, p = Pi, kt = p >> 2, qb = (p >> 1) & 1, r = 2 * (p & 1);
    const float e0 = EXP2(sc[H & 1][kt][qb][r]), e1 = EXP2(sc[H & 1][kt][qb][r + 1]);
    unsigned w = pk2(e0, e1);
    PIN(w);
    pw[H][qb][kt * 2 + (p & 1)] = w;
  };
  sfor<128>([&](auto Ni) AI {
    constexpr int n = Ni, H = n >> 3, i = n & 7, kt = i >> 2, ds = i & 3, sn = H & 1;
    FRD(IC(n + 3));
    if constexpr (n < 2) QPACK(IC(2 + n));
    sfor<2>([&](auto Qb) AI {
      constexpr int qb = Qb;
      if constexpr (ds == 0 && H == 0) MFZ(sc[sn][kt][qb], fr[n & 3], qf[0][qb]);
      else if constexpr (ds == 0) MFC(sc[sn][kt][qb], fr[n & 3], qf[0][qb], negm[qb]);
      else MF(sc[sn][kt][qb], fr[n & 3], qf[ds][qb]);
    });
    if constexpr (n == 5 || n == 6) {
      constexpr int qb = n - 5;
      NOPS(3); PIN(sc[0][0][qb]);
      const float m = -xg_max(__builtin_fmaxf(MX3(sc[0][0][qb][0], sc[0][0][qb][1], sc[0][0][qb][2]), sc[0][0][qb][3]));
      negm[qb] = (f32x4){m, m, m, m};
      sc[0][0][qb] += negm[qb];
      PIN(negm[qb]); PIN(sc[0][0][qb]);
    }
    if constexpr (n == 9 || n == 10) { NOPS(3); PIN(sc[0][1][n - 9]); sc[0][1][n - 9] += negm[n - 9]; PIN(sc[0][1][n - 9]); }
    issue(IC(64 + (n >> 2)), IC(n & 3));
    if constexpr (H == 2 && (i == 1 || i == 5)) MFZ(lsum[i >> 2], ones, pw[0][i >> 2]);
    if constexpr (H > 2 && (i == 1 || i == 5)) MF(lsum[i >> 2], ones, pw[H - 2][i >> 2]);
    if constexpr (H >= 1) SM(IC(H - 1), IC(i));
    if constexpr (n == 125) { PIN(negm[0]); PIN(negm[1]); }
    SBAR();
    if constexpr ((n & 3) == 3) endslot(IC(64 + (n >> 2)));
  });
  constexpr int CHK = 16;
  sfor<128>([&](auto Mi) AI {
    constexpr int m = Mi, n = 128 + m, os = m >> 5, H = (m >> 1) & 15, par = m & 1;
    FRD(IC(n + 3));
    if constexpr (H == 0) { MFZ(o[os & 1][par][0], fr[n & 3], pw[H][0]); MFZ(o[os & 1][par][1], fr[n & 3], pw[H][1]); }
    else { MF(o[os & 1][par][0], fr[n & 3], pw[H][0]); MF(o[os & 1][par][1], fr[n & 3], pw[H][1]); }
    if constexpr (m == 1 || m == 2) MF(lsum[m - 1], ones, pw[14][m - 1]);
    if constexpr (m < 8) SM(IC(15), Mi);
    if constexpr (m == 9 || m == 10) MF(lsum[m - 9], ones, pw[15][m - 9]);
    if constexpr (m == CHK + 1 || m == CHK + 2) { inv[m - CHK - 1] = __builtin_amdgcn_rcpf(lsum[m - CHK - 1][0]); PIN(inv[m - CHK - 1]); }
    issue(IC(64 + (n >> 2)), IC(n & 3));
    if constexpr (m == 123) {
      trb[0].init(lds0 + kg * 16384 + g * 2048 + (c >> 2) * 64 + (g & 1) * 32 + (c & 3) * 8);
      trb[1].init(lds0 + kg * 16384 + g * 2048 + (c >> 2) * 64 + ((g & 1) ^ 1) * 32 + (c & 3) * 8);
    }
    if constexpr (m >= 124) WRD1(IC(0), WB(0), IC(m - 124));
    SBAR();
    if constexpr ((n & 3) == 3) endslot(IC(64 + (n >> 2)));
    if constexpr (m == CHK - 1) {
      PIN(lsum[0]); PIN(lsum[1]);
      if (__builtin_expect(__any(!(lsum[0][0] + lsum[1][0] <= LBOUND)), 0)) {
        NOPS(7); SBAR();
        auto tile = [&](unsigned off, f32x4& s0, f32x4& s1) AI {
          bf16x8 kf[4];
          SBAR(); NOPS(7);
          sfor<4>([&](auto Ds) AI { constexpr int ds = Ds; gld16<ds * 1024, true>(kf[ds], off, kv_b); });
          waitv<0>(kf[0], kf[1]);
          PIN(kf[2]); PIN(kf[3]);
#pragma unroll
          for (int ds = 0; ds < 4; ++ds) {
            s0 = __builtin_amdgcn_mfma_f32_16x16x32_bf16(kf[ds], qf[ds][0], s0, 0, 0, 0);
            s1 = __builtin_amdgcn_mfma_f32_16x16x32_bf16(kf[ds], qf[ds][1], s1, 0, 0, 0);
          }
        };
        float m0 = -__builtin_inff(), m1 = m0;
#pragma unroll 1
        for (int kt = 0; kt < 32; ++kt) {
          f32x4 s0 = {}, s1 = {};
          tile((unsigned)(kt * 4096 + l * 16), s0, s1);
          m0 = MX3(MX3(m0, s0[0], s0[1]), s0[2], s0[3]); m1 = MX3(MX3(m1, s1[0], s1[1]), s1[2], s1[3]);
        }
        m0 = -xg_max(m0); m1 = -xg_max(m1);
        lsum[0] = (f32x4){}; lsum[1] = (f32x4){};
        sfor<16>([&](auto Hi) AI {
          constexpr int H = Hi;
#pragma unroll
          for (int kt = 0; kt < 2; ++kt) {
            sc[H & 1][kt][0] = (f32x4){m0, m0, m0, m0}; sc[H & 1][kt][1] = (f32x4){m1, m1, m1, m1};
            tile((unsigned)((2 * H + kt) * 4096 + l * 16), sc[H & 1][kt][0], sc[H & 1][kt][1]);
          }
          sfor<8>([&](auto Pi) AI { SM(Hi, Pi); });
          PIN(pw[H][0]); PIN(pw[H][1]);
          lsum[0] = __builtin_amdgcn_mfma_f32_16x16x32_bf16(ones, __builtin_bit_cast(bf16x8, pw[H][0]), lsum[0], 0, 0, 0);
          lsum[1] = __builtin_amdgcn_mfma_f32_16x16x32_bf16(ones, __builtin_bit_cast(bf16x8, pw[H][1]), lsum[1], 0, 0, 0);
        });
#pragma unroll
        for (int par = 0; par < 2; ++par) { o[0][par][0] = (f32x4){}; o[0][par][1] = (f32x4){}; }
        sfor<CHK / 4>([&](auto Qi) AI {
          constexpr int q = Qi;
          bf16x8 vf[4];
          SBAR(); NOPS(7);
          sfor<4>([&](auto J) AI { constexpr int j = J; gld16<j * 1024, true>(vf[j], (unsigned)((128 + 4 * q) * 1024 + l * 16), kv_b); });
          waitv<0>(vf[0], vf[1]);
          PIN(vf[2]); PIN(vf[3]);
#pragma unroll
          for (int j = 0; j < 4; ++j)
#pragma unroll
            for (int qb = 0; qb < 2; ++qb)
              o[0][j & 1][qb] = __builtin_amdgcn_mfma_f32_16x16x32_bf16(vf[j], __builtin_bit_cast(bf16x8, pw[2 * q + (j >> 1)][qb]), o[0][j & 1][qb], 0, 0, 0);
        });
#pragma unroll
        for (int par = 0; par < 2; ++par) { PIN(o[0][par][0]); PIN(o[0][par][1]); }
        PIN(lsum[0]); PIN(lsum[1]);
        SBAR(); NOPS(7);
      }
      SBAR();
    }
  });
  STAMP(12); STAMP(13); STAMP(14); STAMP(15);

  const int idx = SEQ - 1 - (int)(key & 2047u);
  const bool owner = (idx >> 7) == mb, ownw = (idx >> 4) == mb * 8 + rbw * 2 + kg;
  f32x4 yp[7], ya[4], oth[4];
  unsigned gate[8];
  float glow;
  const unsigned ex_a = ldsf + (c >> 3) * 8192 + (c & 7) * 128 + (((g >> 1) ^ (c >> 1)) << 4) + (g & 1) * 8;
  auto item = [](int s) { return s < 48 ? s : s < 56 ? 48 + 2 * (s - 48) : 49 + 2 * (s - 56); };
  u32x2 pf;
  bool rdy = false;
  sfor<64>([&](auto Si) AI {
    constexpr int s = Si, u = s >> 2, it = item(s), x0 = (it & ~1) % XD2, x1 = (it | 1) % XD2;
    if constexpr (!(it & 1)) waitv<SC.younger(OX, 64 + it + 1, B0 + s - 1)>(xf[x0], xf[x1]);
    sfor<4>([&](auto Dd) AI {
      constexpr int d = Dd, dt = (it & 1) * 4 + d;
      if constexpr (s + 1 < 64) WRD1(IC(s + 1), WB(item(s + 1)), Dd);
      SBAR();
      if constexpr (s < 2) { MFZ(acc[dt][0], wf[s & 1][d], xf[x0]); MFZ(acc[dt][1], wf[s & 1][d], xf[x1]); }
      else { MF(acc[dt][0], wf[s & 1][d], xf[x0]); MF(acc[dt][1], wf[s & 1][d], xf[x1]); }
      issue(IC(B0 + s), Dd);
      if constexpr (s == 56) *(LAS f32x4*)(lds + (wid ^ 4) * 4096 + l * 16 + (d & 1) * 2048 + (d >> 1) * 32768) = acc[d][1];
      if constexpr (s == 57 && d == 0) {
#pragma unroll
        for (int e = 0; e < 4; ++e) oth[e] = *(LAS const f32x4*)(lds + wid * 4096 + l * 16 + (e & 1) * 2048 + (e >> 1) * 32768);
      }
      if constexpr (s == 57 && d >= 2) {
        gst16<(2 * d - 4) * 1024>(acc[2 * d - 4][0] + oth[2 * d - 4], (unsigned)(wid * 4096 + l * 16), ws + WS_XC + (size_t)(bid ^ 4) * 32768);
        gst16<(2 * d - 3) * 1024>(acc[2 * d - 3][0] + oth[2 * d - 3], (unsigned)(wid * 4096 + l * 16), ws + WS_XC + (size_t)(bid ^ 4) * 32768);
      }
      if constexpr (s == 34) xe[d] = *(LAS const u32x2*)(uintptr_t)(ex_a ^ (d << 5));
      if constexpr (s >= 36 && s < 44 && d == 1) {
        constexpr int j = s - 36;
        PIN(st);
        const unsigned xw = xe[j >> 1][j & 1];
        const unsigned dd = pk2(bflo(xw) - st[0], bfhi(xw) - st[0]);
        gate[j] = pk2(bflo(dd) * st[1], bfhi(dd) * st[1]);
        PIN(gate[j]);
      }
      if constexpr (s >= 47 && s < 63 && d == 1) {
        constexpr int j = (s - 47) >> 1;
        if constexpr ((s - 47) & 1) { gate[j] = pk2(glow, silu_f(bfhi(gate[j]))); PIN(gate[j]); }
        else { glow = silu_f(bflo(gate[j])); PIN(glow); }
      }
    });
    SBAR();
    if constexpr (s == 7) {
      waitv<SC.younger(OW, 7, B0 + s)>();
      if (l == 0) {
        gst8((u32x2){tlo, thi}, (unsigned)(((rb * 8 + wid) * 32 + head) * 8), ws + WS_FO + 3 * 16384);
        gst8((u32x2){tlo, thi}, (unsigned)((wid * 256 + yrow) * 8), ws + WS_FY);
      }
    }
    if constexpr (s == 4 || s == 17 || s == 33) {
      constexpr int j = s == 4 ? 1 : s == 17 ? 2 : 3;
      waitv<SC.younger(OM, j + 1, B0 + s)>(fg);
      if (__builtin_expect(!__all((fg[0] == tlo) & (fg[1] == thi)), 0)) poll8(fo_v, ws + WS_FO + j * 16384, tlo, thi, 0xffffffffu);
    }
    if constexpr (s == 59) gld8<0, true>(pf, (unsigned)((bid * 8 + wid) * 64), ws + WS_FX);
    if constexpr (s == 60) {
      waitv<1>();
      if (l == 0) gst8((u32x2){tlo, thi}, (unsigned)(((bid ^ 4) * 8 + wid) * 64), ws + WS_FX);
    }
    if constexpr (s == 62) {
      waitv<1>(pf);
      rdy = __all((pf[0] == tlo) & (pf[1] == thi));
      if (rdy) sfor<4>([&](auto Dd) AI { constexpr int d = Dd; gld16<d * 1024, true>(oth[d], (unsigned)(wid * 4096 + l * 16), ws + WS_XC + (size_t)bid * 32768); });
    }
    if constexpr (s == 13) {
      if (owner) { gld16<0, true>(fm[0], (unsigned)(nb * 2048 + (l < 56 ? l : 55) * 32), ws + WS_FY); gld16<16, true>(fm[1], (unsigned)(nb * 2048 + (l < 56 ? l : 55) * 32), ws + WS_FY); }
    }
    if constexpr (s == 28) {
      if (owner) {
        PIN(fm[0]); PIN(fm[1]);
        for (int spin = 0; spin < SPINS; ++spin) {
          const bool ok = (fm[0][0] == tlo) & (fm[0][1] == thi) & (fm[0][2] == tlo) & (fm[0][3] == thi) & (fm[1][0] == tlo) & (fm[1][1] == thi) & (fm[1][2] == tlo) & (fm[1][3] == thi);
          if (__builtin_expect(__all(ok), 1)) break;
          __builtin_amdgcn_s_sleep(1);
          gld16<0, true>(fm[0], (unsigned)(nb * 2048 + (l < 56 ? l : 55) * 32), ws + WS_FY); gld16<16, true>(fm[1], (unsigned)(nb * 2048 + (l < 56 ? l : 55) * 32), ws + WS_FY);
          waitv<0>(fm[0], fm[1]);
        }
        const unsigned yv = (unsigned)(((t >> 4) * 1024 + nb * 128 + kh * 64 + (t & 15) * 4) * 4);
        sfor<7>([&](auto I) AI { constexpr int q = I; gld16<0, true>(yp[q], yv, ws + WS_YP + (size_t)q * 32 * 4096); });
      }
    }
    if constexpr (s == 43) {
      if (owner) {
        f32x4 a = {};
#pragma unroll
        for (int q = 0; q < 7; ++q) { PIN(yp[q]); a += yp[q]; }
#pragma unroll
        for (int j = 0; j < 4; ++j) a[j] = xg_sum(a[j]);
        *(LAS f32x4*)(lds + YS + (wid * 64 + c * 4) * 4) = a;
      }
    }
    if constexpr (s == 45) {
      if (ownw) {
        float a = 0.f;
#pragma unroll
        for (int p = 0; p < 8; ++p) a += *(LAS const float*)(lds + YS + (p * 64 + l) * 4);
        const unsigned ord = key >> 11, bits = (ord & 0x8000u) ? (ord & 0x7fffu) : (~ord & 0xffffu);
        *(LAS float*)(lds + RED1 + l * 4) = rbf(rbf(a) * rbf(sigmoid_f(bflo(bits))));
#pragma unroll
        for (int q = 0; q < 4; ++q) ya[q] = *(LAS const f32x4*)(lds + RED1 + (q * 16 + 4 * g) * 4);
      }
    }
    if constexpr (s == 46) {
      if (ownw) {
#pragma unroll
        for (int j = 0; j < 8; ++j) {
          const unsigned w = pk2(bflo(gate[j]) + ya[j >> 1][2 * (j & 1)], bfhi(gate[j]) + ya[j >> 1][2 * (j & 1) + 1]);
          gate[j] = erow == idx ? w : gate[j];
          PIN(gate[j]);
        }
      }
    }
    SBAR();
    if constexpr (((s & 3) == 2 && u < 11) || s == 44) {
      if constexpr (s == 44) waitbar_l<SC.younger(OG, 64 + 4 * u + 7, B0 + s)>();
      else waitbar<SC.younger(OG, 64 + 4 * u + 7, B0 + s)>();
      if constexpr (u % 4 == 1) STAMP(16 + u / 4);
    }
    if constexpr (s == 48) waitbar<SC.younger(OG, 64 + 55, B0 + s)>();
    if constexpr (s == 50) waitbar<SC.younger(OG, 64 + 59, B0 + s)>();
    if constexpr (s == 52) { waitbar<SC.younger(OG, 64 + 63, B0 + s)>(); STAMP(19); }
    if constexpr (s == 56) waitbar_l<63>();
    if constexpr (PRIO) __builtin_amdgcn_s_setprio(3 - ((s + 2) & 3));
  });
  STAMP(20);

  f32x4 kep[4];
  {
    NOPS(7);
#pragma unroll
    for (int d = 0; d < 4; ++d) { PIN(acc[4 + d][1]); *(LAS f32x4*)(lds + 65536 + (wid ^ 4) * 4096 + l * 16 + (d & 1) * 2048 + (d >> 1) * 32768) = acc[4 + d][1]; }
    if (!rdy) {
      poll8((unsigned)((bid * 8 + wid) * 64), ws + WS_FX, tlo, thi, 0xffffffffu);
      sfor<4>([&](auto Dd) AI { constexpr int d = Dd; gld16<d * 1024, true>(oth[d], (unsigned)(wid * 4096 + l * 16), ws + WS_XC + (size_t)bid * 32768); });
    }
    STAMP(21);
    waitbar_l<63>();
#pragma unroll
    for (int d = 0; d < 4; ++d) kep[d] = acc[4 + d][0] + *(LAS const f32x4*)(lds + 65536 + wid * 4096 + l * 16 + (d & 1) * 2048 + (d >> 1) * 32768);
    STAMP(22);
    STAMP(23);
    waitv<0>(oth[0], oth[1]);
    PIN(oth[2]); PIN(oth[3]);
    STAMP(24);
  }
#pragma unroll
  for (int d = 0; d < 4; ++d) {
    const f32x4 a = kep[d] + oth[d];
    u32x2 res;
#pragma unroll
    for (int j = 0; j < 2; ++j) {
      const unsigned sp = pk2(a[2 * j], a[2 * j + 1]), gp = gate[2 * d + j], xw = xe[d][j];
      const unsigned pr = pk2(bflo(sp) * bflo(gp), bfhi(sp) * bfhi(gp));
      res[j] = pk2(bflo(xw) + bflo(pr), bfhi(xw) + bfhi(pr));
    }
    *(LAS u32x2*)(uintptr_t)(ex_a ^ (d << 5)) = res;
  }
#pragma unroll
  for (int i = 0; i < 2; ++i)
    gst16<0>(*(LAS const u32x4*)(uintptr_t)(ldsf + i * 8192 + l * 16), (unsigned)(((erow - c + i * 8 + (l >> 3)) * DM + nb * 128 + kh * 64 + ((l & 7) ^ (i * 4 + (l >> 4))) * 8) * 2), out);
  STAMP(25);
  STAMP_OUT(0, 26);
}

extern "C" void kernel_launch(void* const* d_in, const int* in_sizes, int n_in, void* d_out, int out_size, void* d_ws, size_t ws_size,
                              hipStream_t stream) {
  k_fused<<<256, 512, 0, stream>>>((const u16*)d_in[0], (const u16*)d_in[1], (const u16*)d_in[2], (const u16*)d_in[3], (const u16*)d_in[4],
                                   (const u16*)d_in[6], (const u16*)d_in[7], (const u16*)d_in[8], (const u16*)d_in[9], (char*)d_ws, (u16*)d_out);
}
```

```cpp
#include <hip/hip_runtime.h>
#include <stdint.h>
#include <type_traits>

typedef unsigned short u16;
typedef short bf16x8 __attribute__((ext_vector_type(8)));
typedef short s16x4 __attribute__((ext_vector_type(4)));
typedef float f32x4 __attribute__((ext_vector_type(4)));
typedef float f32x2 __attribute__((ext_vector_type(2)));
typedef unsigned u32x4 __attribute__((ext_vector_type(4)));
typedef unsigned u32x2 __attribute__((ext_vector_type(2)));
typedef __bf16 bf2_t __attribute__((ext_vector_type(2)));
#define LAS __attribute__((address_space(3)))
#define DI __device__ __forceinline__
#define AI __attribute__((always_inline))
#define SBAR() __builtin_amdgcn_sched_barrier(0)
template <class T> __device__ __forceinline__ void PIN(T& x) { asm volatile("" : "+v"(x)); }
template <class B> DI void MF(f32x4& acc, const bf16x8& a, const B& b) { asm volatile("v_mfma_f32_16x16x32_bf16 %0, %1, %2, %0" : "+v"(acc) : "v"(a), "v"(b)); }
template <class B> DI void MFC(f32x4& d, const bf16x8& a, const B& b, const f32x4& c) { asm volatile("v_mfma_f32_16x16x32_bf16 %0, %1, %2, %3" : "=&v"(d) : "v"(a), "v"(b), "v"(c)); }
template <class B> DI void MFZ(f32x4& d, const bf16x8& a, const B& b) { asm volatile("v_mfma_f32_16x16x32_bf16 %0, %1, %2, 0" : "=&v"(d) : "v"(a), "v"(b)); }
#define NOPS(n) asm volatile("s_nop " #n)
#define MFMA16(a, b, c) __builtin_amdgcn_mfma_f32_16x16x16bf16_1k((a), (b), (c), 0, 0, 0)
#define IC(n) std::integral_constant<int, (n)>{}

constexpr int SEQ = 2048, DM = 1024, DH = 128, SK = 2048, WIN = 512, FF = 3584;
constexpr size_t WS_XN = 0;
constexpr size_t WS_KV = 4u << 20;
constexpr size_t WS_ST = 5u << 20;
constexpr size_t WS_EP = WS_ST + 16384;
constexpr size_t WS_G0 = WS_ST + 32768;
constexpr size_t WS_FK = WS_ST + 49152;
constexpr size_t WS_YP = WS_ST + 65536;
constexpr size_t WS_FO = 7u << 20;
constexpr size_t WS_FY = WS_FO + 65536;
constexpr size_t WS_FX = WS_FO + 131072;
constexpr size_t WS_O = 8u << 20;
constexpr size_t WS_GU = 32u << 20;
constexpr size_t WS_XC = 24u << 20;

template <int I, int N, class F> DI void sfor_impl(F&& f) {
  if constexpr (I < N) { f(std::integral_constant<int, I>{}); sfor_impl<I + 1, N>(f); }
}
template <int N, class F> DI void sfor(F&& f) { sfor_impl<0, N>(f); }

DI unsigned pk2(float a, float b) { f32x2 v = {a, b}; return __builtin_bit_cast(unsigned, __builtin_convertvector(v, bf2_t)); }
DI float rbf(float a) { return (float)(__bf16)a; }
DI float bflo(unsigned u) { return __uint_as_float(u << 16); }
DI float bfhi(unsigned u) { return __uint_as_float(u & 0xffff0000u); }
DI float sigmoid_f(float v) { return __builtin_amdgcn_rcpf(1.f + __builtin_amdgcn_exp2f(v * -1.4426950408889634f)); }
DI float sel2(bool c, float a, float b) { PIN(a); PIN(b); return c ? a : b; }
DI float silu_f(float v) { return v * sigmoid_f(v); }
template <int CTRL> DI float dppf(float x) { return __builtin_bit_cast(float, __builtin_amdgcn_mov_dpp(__builtin_bit_cast(int, x), CTRL, 0xf, 0xf, true)); }
template <int CTRL> DI unsigned dppu(unsigned x) { return (unsigned)__builtin_amdgcn_mov_dpp((int)x, CTRL, 0xf, 0xf, true); }
constexpr int XOR1 = 0xB1, XOR2 = 0x4E, XOR7 = 0x141, XOR8 = 0x128;
DI float xg_sum(float x) {
  auto s = __builtin_amdgcn_permlane16_swap(__float_as_uint(x), __float_as_uint(x), false, false);
  x = __uint_as_float(s[0]) + __uint_as_float(s[1]);
  auto t = __builtin_amdgcn_permlane32_swap(__float_as_uint(x), __float_as_uint(x), false, false);
  return __uint_as_float(t[0]) + __uint_as_float(t[1]);
}
DI float xg_max(float x) {
  auto s = __builtin_amdgcn_permlane16_swap(__float_as_uint(x), __float_as_uint(x), false, false);
  x = __builtin_fmaxf(__uint_as_float(s[0]), __uint_as_float(s[1]));
  auto t = __builtin_amdgcn_permlane32_swap(__float_as_uint(x), __float_as_uint(x), false, false);
  return __builtin_fmaxf(__uint_as_float(t[0]), __uint_as_float(t[1]));
}
DI float wave_sum(float v) { v += dppf<XOR1>(v); v += dppf<XOR2>(v); v += dppf<XOR7>(v); v += dppf<XOR8>(v); return xg_sum(v); }
DI unsigned umax(unsigned a, unsigned b) { return a > b ? a : b; }
DI unsigned wave_max_u32(unsigned v) {
  v = umax(v, dppu<XOR1>(v)); v = umax(v, dppu<XOR2>(v)); v = umax(v, dppu<XOR7>(v)); v = umax(v, dppu<XOR8>(v));
  auto s = __builtin_amdgcn_permlane16_swap(v, v, false, false); v = umax(s[0], s[1]);
  auto t = __builtin_amdgcn_permlane32_swap(v, v, false, false); v = umax(t[0], t[1]);
  return __builtin_amdgcn_readfirstlane(v);
}

template <int OFF, bool DEV = false> DI void glds(unsigned ldsaddr, unsigned voff, const void* sbase) {
  if constexpr (DEV) asm volatile("s_mov_b32 m0, %0\n\ts_nop 0\n\tglobal_load_lds_dwordx4 %1, %2 offset:%3 sc1" ::"s"(ldsaddr), "v"(voff), "s"(sbase), "i"(OFF) : "memory");
  else asm volatile("s_mov_b32 m0, %0\n\ts_nop 0\n\tglobal_load_lds_dwordx4 %1, %2 offset:%3" ::"s"(ldsaddr), "v"(voff), "s"(sbase), "i"(OFF) : "memory");
}
DI void touch(unsigned voff, const void* sbase) {
  asm volatile("global_load_dword v255, %0, %1" ::"v"(voff), "s"(sbase) : "memory", "v255");
}
template <int OFF, bool DEV = false, class T> DI void gld16(T& dst, unsigned voff, const void* sbase) {
  if constexpr (DEV) asm volatile("global_load_dwordx4 %0, %1, %2 offset:%3 sc1" : "=v"(dst) : "v"(voff), "s"(sbase), "i"(OFF) : "memory");
  else asm volatile("global_load_dwordx4 %0, %1, %2 offset:%3" : "=v"(dst) : "v"(voff), "s"(sbase), "i"(OFF) : "memory");
}
template <int OFF, bool DEV = false, class T> DI void gld8(T& dst, unsigned voff, const void* sbase) {
  if constexpr (DEV) asm volatile("global_load_dwordx2 %0, %1, %2 offset:%3 sc1" : "=v"(dst) : "v"(voff), "s"(sbase), "i"(OFF) : "memory");
  else asm volatile("global_load_dwordx2 %0, %1, %2 offset:%3" : "=v"(dst) : "v"(voff), "s"(sbase), "i"(OFF) : "memory");
}
template <int OFF> DI void gld2(unsigned& dst, unsigned voff, const void* sbase) {
  asm volatile("global_load_ushort %0, %1, %2 offset:%3" : "=v"(dst) : "v"(voff), "s"(sbase), "i"(OFF) : "memory");
}
template <int OFF, class T> DI void gst16(const T& v, unsigned voff, const void* sbase) {
  asm volatile("global_store_dwordx4 %0, %1, %2 offset:%3 sc1\n\ts_nop 1" ::"v"(voff), "v"(v), "s"(sbase), "i"(OFF) : "memory");
}
template <class T> DI void gst8(const T& v, unsigned voff, const void* sbase) {
  asm volatile("global_store_dwordx2 %0, %1, %2 sc1" ::"v"(voff), "v"(v), "s"(sbase) : "memory");
}
template <int OFF> DI void gst4(float v, unsigned voff, const void* sbase) {
  asm volatile("global_store_dword %0, %1, %2 offset:%3 sc1" ::"v"(voff), "v"(v), "s"(sbase), "i"(OFF) : "memory");
}
constexpr int SPINS = 1 << 16;
DI void poll8(unsigned voff, const void* sbase, unsigned lo, unsigned hi, unsigned lomask) {
  for (int spin = 0; spin < SPINS; ++spin) {
    u32x2 v;
    asm volatile("global_load_dwordx2 %0, %1, %2 sc1\n\ts_waitcnt vmcnt(0)" : "=&v"(v) : "v"(voff), "s"(sbase) : "memory");
    if (__all(((v[0] & lomask) == lo) & (v[1] == hi))) break;
    __builtin_amdgcn_s_sleep(1);
  }
}
template <int N> DI void waitv() { static_assert(N >= 0, "count"); asm volatile("s_waitcnt vmcnt(%0)" ::"n"(N > 63 ? 63 : N) : "memory"); }
template <int N, class T> DI void waitv(T& x) { static_assert(N >= 0, "count"); asm volatile("s_waitcnt vmcnt(%1)" : "+v"(x) : "n"(N > 63 ? 63 : N) : "memory"); }
template <int N, class T> DI void waitv(T& x, T& y) { static_assert(N >= 0, "count"); asm volatile("s_waitcnt vmcnt(%2)" : "+v"(x), "+v"(y) : "n"(N > 63 ? 63 : N) : "memory"); }
template <int N> DI void waitbar() { static_assert(N >= 0, "count"); asm volatile("s_waitcnt vmcnt(%0)\n\ts_barrier" ::"n"(N > 63 ? 63 : N) : "memory"); }
template <int N> DI void waitbar_l() { static_assert(N >= 0, "count"); asm volatile("s_waitcnt vmcnt(%0) lgkmcnt(0)\n\ts_barrier" ::"n"(N > 63 ? 63 : N) : "memory"); }
typedef short v4i16_t __attribute__((ext_vector_type(4)));
DI s16x4 trd(LAS const char* p) { return __builtin_amdgcn_ds_read_tr16_b64_v4i16((LAS v4i16_t*)p); }
struct LBase {
  unsigned lo, hi;
  DI void init(unsigned a) { lo = a; hi = a + 65536; PIN(lo); PIN(hi); }
  template <int OFF> DI LAS const char* at() const { return (LAS const char*)(uintptr_t)(OFF < 65536 ? lo : hi) + (OFF & 65535); }
};
DI bf16x8 cat8(s16x4 lo, s16x4 hi) { return __builtin_shufflevector(lo, hi, 0, 1, 2, 3, 4, 5, 6, 7); }

enum { OX, OG, OF, OM, OS, OP, OE, OW };
struct Sched {
  int n; short kind[400], idx[400], slot[400];
  constexpr Sched() : n(0), kind{}, idx{}, slot{} {}
  constexpr void add(int s, int k, int i) { kind[n] = (short)k; idx[n] = (short)i; slot[n] = (short)s; ++n; }
  constexpr int younger(int k, int i, int s) const {
    int p = -1;
    for (int q = 0; q < n; ++q) if (kind[q] == k && idx[q] == i) p = q;
    if (p < 0 || slot[p] > s) return -1;
    int c = 0;
    for (int q = p + 1; q < n; ++q) if (slot[q] <= s) ++c;
    return c;
  }
  constexpr int first(int s) const { int q = 0; while (q < n && slot[q] < s) ++q; return q; }
  constexpr int count(int s) const { int c = 0; for (int q = 0; q < n; ++q) if (slot[q] == s) ++c; return c; }
};

#define STAMP_DECL
#define STAMP(i)
#define STAMP_OUT(kid, n)

constexpr int RING = 4 * 32768, FB = RING, RED1 = FB + 24576, HSC = RED1 + 1024, YS = HSC + 1024, LDS_BYTES = FB + 32768;
constexpr int STAGE = 3 * 32768;
constexpr int XD = 16, XD2 = 16;
constexpr float LBOUND = 65536.f;
constexpr float QSCALE = 0.08838834764831845f * 1.4426950408889634f;
constexpr int B0 = 128;
#define PRIO 1
constexpr int FISS[4] = {96, 97, 98, 117}, FRDS[4] = {115, 118, 121, 124};
constexpr int fpiece(int sl) { for (int p = 0; p < 4; ++p) if (FRDS[p] == sl) return p; return -1; }
constexpr Sched make_sched() {
  Sched s;
#define XP 8
  for (int i = 0; i < 12; ++i) s.add(-2, OG, i);
  for (int i = 0; i < XP; ++i) s.add(-1, OX, i);
  for (int t = 0; t < 128; ++t) {
    if (t < 2) for (int i = XP + t * (16 - XP) / 2; i < XP + (t + 1) * (16 - XP) / 2; ++i) s.add(t, OX, i);
    if (t >= 2 && t < 50) s.add(t, OX, t + 14);
    for (int v = 3; v < 18; ++v) for (int p = 0; p < 4; ++p) if (8 * (v - 3) + 7 + p == t) s.add(t, OG, 4 * v + p);
    if (t == 105 || t == 113 || t == 121) { s.add(t, OW, (t - 105) / 4); s.add(t, OW, (t - 105) / 4 + 1); }
    if (t >= 124) for (int i = 0; i < 3; ++i) s.add(t, OX, 64 + 3 * (t - 124) + i);
    if (t == 127) { s.add(t, OG, 72); s.add(t, OG, 73); }
    if (t == 20) s.add(t, OM, 0);
    if (t == 120) s.add(t, OM, 1);
    if (t >= 49 && t < 57 && (t & 1)) s.add(t, OF, (t - 49) / 2);
    if (t >= 71 && t < 79 && (t & 1)) s.add(t, OF, 4 + (t - 71) / 2);
    if (t == 48) s.add(t, OS, 0);
    if (t == 104) s.add(t, OS, 2);
    for (int p = 0; p < 4; ++p) if (FISS[p] == t) s.add(t, OF, 8 + p);
  }
  for (int t = 0; t < 64; ++t) {
    if (t == 0) { s.add(B0, OM, 2); s.add(B0, OG, 74); s.add(B0, OG, 75); s.add(B0, OW, 6); s.add(B0, OW, 7); }
    if (t == 1 || t == 2) { s.add(B0 + t, OX, 74 + 2 * t); s.add(B0 + t, OX, 75 + 2 * t); }
    if (t == 5 || t == 6 || t == 9) s.add(B0 + t, OX, 64 + (t == 5 ? 16 : t == 6 ? 18 : 22));
    if (t >= 5 && t < 50) s.add(B0 + t, OX, 64 + (t == 5 ? 17 : t < 9 ? t + 13 : t + 14));
    for (int v = 3; v < 16; ++v) for (int p = 0; p < 4; ++p) if ((v < 14 ? 4 * (v - 3) + 3 + (p >> 1) : v == 14 ? 45 + (p >> 1) : 49) == t) s.add(B0 + t, OG, 64 + 4 * v + p);
    if (t == 10 || t == 26) s.add(B0 + t, OM, t == 10 ? 3 : 4);
    if (t == 13 || t == 14 || t == 18) s.add(B0 + t, OE, t == 18 ? 2 : t - 13);
  }
  return s;
}
constexpr Sched SC = make_sched();

__global__ __launch_bounds__(512, 2) void k_fused(const u16* __restrict__ x, const u16* __restrict__ kc, const u16* __restrict__ vc,
                                                  const u16* __restrict__ wq, const u16* __restrict__ wo, const u16* __restrict__ wr,
                                                  const u16* __restrict__ wgate, const u16* __restrict__ wup, const u16* __restrict__ wdown,
                                                  char* __restrict__ ws, u16* __restrict__ out) {
  __shared__ __attribute__((aligned(16))) char lds_raw[LDS_BYTES];
  LAS char* const lds = (LAS char*)lds_raw;
  const int t = threadIdx.x, l = t & 63, bid = blockIdx.x, c = l & 15, g = l >> 4;
  const int wid = __builtin_amdgcn_readfirstlane(t >> 6);
  const int xcd = bid & 7, within = bid >> 3, head = xcd * 4 + (within & 3), rb = within >> 2;
  const int fj = xcd * 28 + (within < 28 ? within : within - 28);
  const int fsl = fj / 7, fgr = fj - fsl * 7;
  const int kh = xcd >> 2, mb = 2 * rb + (xcd & 1), nb = ((xcd >> 1) & 1) * 4 + (within & 3);
  const int rbw = wid & 3, kg = wid >> 2;
  const unsigned lds0 = (unsigned)(uintptr_t)lds;
  STAMP_DECL
  STAMP(0);

  unsigned tlo, thi;
  {
    const int row = 8 * bid + wid;
    u32x4 a0, a1, w0, w1, kvo;
    u32x2 ep, klo, khi;
    unsigned e[8];
    gld16<0>(a0, (unsigned)(row * DM * 2 + l * 16), x); gld16<1024>(a1, (unsigned)(row * DM * 2 + l * 16), x);
    gld16<0>(w0, (unsigned)(l * 16), wr); gld16<1024>(w1, (unsigned)(l * 16), wr);
    gld8<0, true>(ep, 0u, ws + WS_EP);
    if (wid == 1)
      asm volatile("global_load_dword v255, %0, %1 sc1" ::"v"(l == 0 ? (unsigned)(WS_G0 - WS_ST) + bid * 8 : l == 1 ? (unsigned)(WS_FK - WS_ST) + bid * 32 : (unsigned)(bid * 64)), "s"(ws + WS_ST) : "memory", "v255");
    if (wid == 3) {
      const char* pc;
      asm volatile("s_getpc_b64 %0" : "=s"(pc));
      touch((unsigned)((within * 12 + (l < 12 ? l : 11)) * 128), pc);
    }
    __builtin_amdgcn_s_barrier();
    if (wid == 2) {
      asm volatile("global_load_dword v255, %0, %1 sc1" ::"v"((unsigned)((l < 40 ? l : 39) << 20) + 64 * 1024 - 128), "s"(ws) : "memory", "v255");
      const char* pb = l < 8 ? (const char*)wo : l < 16 ? (const char*)wgate : l < 24 ? (const char*)wup : l < 32 ? (const char*)wdown : (const char*)out;
      pb += (size_t)(l & 7) * (l < 8 ? 1048576 : l < 32 ? 917504 : 524288);
      asm volatile("global_load_dword v255, %0, off" ::"v"(pb) : "memory", "v255");
    }
    const int fn = (bid * 4 + wid) & 255, kvh = bid >> 6;
    if (wid < 4) {
      if (fn < 128) {
        const unsigned ko = (unsigned)(((kvh * SK + (SK - WIN) + (fn >> 2) * 16 + c) * DH + (fn & 3) * 32 + 4 * g) * 2);
        gld8<0>(klo, ko, kc); gld8<32>(khi, ko, kc);
      } else {
        const int d = 32 * ((fn - 128) >> 5) + 8 * (c >> 2) + 4 * (fn & 1) + (c & 3);
        const unsigned vo = (unsigned)(((kvh * SK + (SK - WIN) + ((fn >> 1) & 15) * 32 + 4 * g + 8) * DH + d) * 2);
        sfor<8>([&](auto J) AI { constexpr int j = J; gld2<(16 * (j >> 2) + (j & 3) - 8) * DH * 2>(e[j], vo, vc); });
      }
      if (wid < 2) touch((unsigned)((wid * 256 + within * 8 + (l >> 3)) * 8192 + xcd * 1024 + (l & 7) * 128), wq);
      if (fn < 128) waitv<5>(); else waitv<11>();
    } else {
      waitv<3>();
    }
    PIN(a0); PIN(a1);
    STAMP(1);
    float v[16];
#pragma unroll
    for (int j = 0; j < 4; ++j) { v[2 * j] = bflo(a0[j]); v[2 * j + 1] = bfhi(a0[j]); v[8 + 2 * j] = bflo(a1[j]); v[9 + 2 * j] = bfhi(a1[j]); }
    float s = 0.f;
#pragma unroll
    for (int j = 0; j < 16; ++j) s += v[j];
    const float mean = wave_sum(s) * (1.f / DM);
    float q = 0.f;
#pragma unroll
    for (int j = 0; j < 16; ++j) { const float d = v[j] - mean; q += d * d; }
    const float var = wave_sum(q) * (1.f / DM);
    const float mbf = rbf(mean), rden = __builtin_amdgcn_rcpf(rbf(__builtin_amdgcn_sqrtf(rbf(rbf(var) + 1e-5f))));
    u32x4 o0, o1;
#pragma unroll
    for (int j = 0; j < 8; ++j) {
      const unsigned dd = pk2(v[2 * j] - mbf, v[2 * j + 1] - mbf);
      const unsigned nn = pk2(bflo(dd) * rden, bfhi(dd) * rden);
      if (j < 4) o0[j] = nn; else o1[j - 4] = nn;
    }
    *(LAS u32x4*)(lds + STAGE + (l * 8 + (wid ^ (l & 7))) * 16) = o0;
    *(LAS u32x4*)(lds + STAGE + 8192 + (l * 8 + (wid ^ (l & 7))) * 16) = o1;
    *(LAS f32x2*)(lds + STAGE + 16384 + 32 + wid * 8) = (f32x2){mbf, rden};
    waitbar_l<63>();
    if (wid >= 4) {
#pragma unroll
      for (int hh = 0; hh < 4; ++hh) {
        const int p = hh * 256 + (t - 256), ch = p >> 3, r = 8 * bid + ((p & 7) ^ (ch & 7));
        const u32x4 o = *(LAS const u32x4*)(lds + STAGE + p * 16);
        gst16<0>(o, (unsigned)(((((r >> 5) * 32 + (ch >> 2)) * 2 + ((r >> 4) & 1)) * 64 + (ch & 3) * 16 + (r & 15)) * 16), ws + WS_XN);
      }
      if (t < 256 + 8) gst8(*(LAS const f32x2*)(lds + STAGE + 16384 + 32 + (t - 256) * 8), (unsigned)((8 * bid + t - 256) * 8), ws + WS_ST);
      waitv<4>();
    } else if (fn < 128) waitv<2>(); else waitv<8>();
    PIN(w0); PIN(w1); PIN(ep);
    {
      const unsigned long long tg = (((unsigned long long)(unsigned)__builtin_amdgcn_readfirstlane(ep[1]) << 32) | (unsigned)__builtin_amdgcn_readfirstlane(ep[0])) + 1ull;
      tlo = (unsigned)tg; thi = (unsigned)(tg >> 32);
    }
    float rr = 0.f;
#pragma unroll
    for (int j = 0; j < 8; ++j) {
      const unsigned nn = j < 4 ? o0[j] : o1[j - 4], ww = j < 4 ? w0[j] : w1[j - 4];
      rr += bflo(nn) * bflo(ww); rr += bfhi(nn) * bfhi(ww);
    }
    rr = wave_sum(rr);
    {
      const unsigned bits = pk2(rr, 0.f) & 0xffffu;
      const unsigned ord = (bits & 0x8000u) ? (~bits & 0xffffu) : (bits | 0x8000u);
      *(LAS unsigned*)(lds + STAGE + 16384 + wid * 4) = (ord << 11) | (unsigned)(SEQ - 1 - row);
    }
    if (wid >= 4) waitv<0>();
    waitbar_l<63>();
    if (t == 256) {
      const u32x4 ka = *(LAS const u32x4*)(lds + STAGE + 16384), kb = *(LAS const u32x4*)(lds + STAGE + 16400);
      const unsigned key = umax(umax(umax(ka[0], ka[1]), umax(ka[2], ka[3])), umax(umax(kb[0], kb[1]), umax(kb[2], kb[3])));
      gst8((u32x2){(tlo << 27) | key, (tlo >> 5) | (thi << 27)}, (unsigned)(bid * 8), ws + WS_G0);
    }
    if (wid < 4) {
      if (wid < 2) waitv<1>(); else waitv<0>();
      if (fn < 128) {
        PIN(klo); PIN(khi);
        kvo = (u32x4){klo[0], klo[1], khi[0], khi[1]};
      } else {
#pragma unroll
        for (int j = 0; j < 8; ++j) PIN(e[j]);
        kvo = (u32x4){e[0] | (e[1] << 16), e[2] | (e[3] << 16), e[4] | (e[5] << 16), e[6] | (e[7] << 16)};
      }
      gst16<0>(kvo, (unsigned)((bid * 4 + wid) * 1024 + l * 16), ws + WS_KV);
    }
    STAMP(2);
  }
  const unsigned glo = tlo << 27, ghi = (tlo >> 5) | (thi << 27);

  const char* const wq_b = (const char*)wq + head * 256;
  const unsigned wq_v = (unsigned)(((8 * wid + ((l & 31) >> 2)) * 4096 + (l >> 5) * 32 + ((l & 3) ^ (2 * (wid & 1))) * 8) * 2);
  const char* const kv_b = ws + WS_KV + (size_t)(head >> 3) * 262144;
  const unsigned kv_v = (unsigned)(wid * 4096 + l * 16);
  const char* const xn_b = ws + WS_XN + (size_t)rb * 8 * 65536 + 4096;
  const unsigned xn_v = (unsigned)(wid * 65536 + l * 16);
  const char* const wo_b = (const char*)wo + (size_t)kh * 2048 * (DM * 2) + nb * 256;
  const char* const o_b = ws + WS_O + (size_t)mb * 4 * 262144 + kh * 131072;
  const unsigned ldsw = lds0 + wid * 2048, ldsk = lds0 + wid * 4096, ldsf = lds0 + FB + wid * 1024;
  const int erow = mb * 128 + rbw * 32 + kg * 16 + c;
  bf16x8 xf[XD];
  u32x4 fm[2];
  bf16x8 fxs;
  const unsigned gu_v = (unsigned)((((4 * wid + g) * 2 + ((l >> 3) & 1)) * FF + fj * 16 + (l & 7) * 2) * 8);
  u32x2 fg;
  u32x2 xe[4];
  f32x2 st;
  unsigned key = 0;

  const unsigned fo_v = (unsigned)(((mb * 4 + rbw) * 32 + kh * 16 + c) * 8);
  const int yrow = within < 28 ? fj : 224 + xcd * 4 + within - 28;
  f32x4 o[2][2][2];
  float inv[2];
  const char* const od_b = ws + WS_O + ((size_t)(rb * 8) * 128 + head * 4) * 2048 + 4096;
  auto issue = [&](auto Sl, auto Pos) AI {
    constexpr int sl = Sl, pos = Pos, cnt = SC.count(sl), q0 = SC.first(sl) + (pos < 0 ? 0 : pos);
    constexpr int nq = pos < 0 ? cnt : pos >= cnt ? 0 : pos < 3 ? 1 : cnt - 3;
    sfor<nq>([&](auto Qi) AI {
      constexpr int k = SC.kind[q0 + Qi], i = SC.idx[q0 + Qi];
      if constexpr (k == OX && i < 64) gld16<(i & 7) * 1024 - 4096, true>(xf[i % XD], xn_v, xn_b + (size_t)(i >> 3) * 8192);
      if constexpr (k == OX && i >= 64) {
        constexpr int q = i - 64, v = q >> 2;
        gld16<0, true>(xf[q % XD2], (unsigned)(rbw * 262144 + kg * 16384 + ((q & 1) ^ kg) * 1024 + l * 16), o_b + (size_t)(((v & 3) * 4 + ((q >> 1) & 1)) * 8192 + (v >> 2) * 2048));
      }
      if constexpr (k == OG) {
        constexpr int v = i >> 2, p = i & 3;
        if constexpr (v < 8) {
          const char* sb = wq_b + (size_t)(v * 128 + (p >> 1) * 64) * 8192;
          glds<(p & 1) * 128>(ldsw + (v & 3) * 32768 + (p >> 1) * 16384 + (p & 1) * (1024 - 128), wq_v, sb);
        } else if constexpr (v < 16) {
          glds<p * 1024, true>(ldsk + (v & 3) * 32768, kv_v, kv_b + (size_t)(v - 8) * 32768);
        } else {
          const unsigned wo_v = (unsigned)((((wid >> 2) * 128 + (wid & 3) * 8 + ((l & 31) >> 2)) * DM + (l >> 5) * 32 + ((l & 3) ^ (2 * (wid & 1))) * 8) * 2);
          const char* sb = wo_b + (size_t)(((v & 3) * 4 + (p >> 1) * 2) * 128 + ((v - 16) >> 2) * 32) * (DM * 2) + (((p & 1) ^ kh ^ 1) << 7);
          glds<0>(ldsw + (v & 3) * 32768 + (p >> 1) * 16384 + (p & 1) * 1024, wo_v, sb);
        }
      }
      if constexpr (k == OF && i < 8)
        glds<0>(ldsf + (i & 3) * 8192, (unsigned)(((l >> 3) * FF + wid * 64 + ((l & 7) ^ ((i & 1) * 4 + ((l >> 3) & 2))) * 8) * 2),
                (const char*)(i < 4 ? wgate : wup) + ((size_t)(fsl * 32 + 8 * (i & 3)) * FF + fgr * 512) * 2);
      if constexpr (k == OF && i >= 8) {
        const int frho = l >> 2, fch = (l & 3) ^ (frho & 4 ? 2 : 0);
        glds<0>(ldsf + ((i - 8) % 3) * 8192, (unsigned)((frho * DM + wid * 128 + fch * 8) * 2), (const char*)wdown + (size_t)fj * 16 * DM * 2 + (i - 8) * 64);
      }
      if constexpr (k == OS && i == 0) {
        const int idx = SEQ - 1 - (int)(key & 2047u);
        gld16<0, true>(fxs, (unsigned)((((idx >> 5) * 32 + fsl) * 2 + ((idx >> 4) & 1)) * 1024 + (g * 16 + (idx & 15)) * 16), ws + WS_XN);
      }
      if constexpr (k == OS && i == 2) gld16<0, true>(fm[0], gu_v, ws + WS_GU);
      if constexpr (k == OM && i > 0) gld8<0, true>(fg, fo_v, ws + WS_FO + (i - 1) * 16384);
      if constexpr (k == OM && i == 0) gld8<0, true>(fg, (unsigned)((t & 255) * 8), wid < 4 ? ws + WS_G0 : ws + WS_FK + (head >> 3) * 2048);
      if constexpr (k == OW) {
        constexpr int os = i >> 1, qb = i & 1;
        PIN(o[os & 1][0][qb]); PIN(o[os & 1][1][qb]);
        const f32x4 a = o[os & 1][0][qb] * inv[qb], b = o[os & 1][1][qb] * inv[qb];
        gst16<i * 1024 - 4096>((u32x4){pk2(a[0], a[1]), pk2(a[2], a[3]), pk2(b[0], b[1]), pk2(b[2], b[3])}, (unsigned)(wid * 262144 + l * 16), od_b);
      }
      if constexpr (k == OE && i < 2)
        glds<0>(ldsf + i * 8192, (unsigned)(((erow - c + i * 8 + (l >> 3)) * DM + nb * 128 + kh * 64 + ((l & 7) ^ (i * 4 + (l >> 4))) * 8) * 2), x);
      if constexpr (k == OE && i == 2) gld8<0, true>(st, (unsigned)(erow * 8), ws + WS_ST);
    });
  };
  f32x4 fa0, fa1, fq[4];
  s16x4 fb0, fb1, fxa;
  bf16x8 ft[2];
  auto endslot = [&](auto Sl) AI {
    constexpr int sl = Sl, u = sl >> 3, i = sl & 7;
    if constexpr (sl == 3) {
      if (wid == 6 || wid == 7) touch((unsigned)(((wid - 4) * 256 + within * 8 + (l >> 3)) * 8192 + xcd * 1024 + (l & 7) * 128), wq);
    }
    if constexpr (sl == 2) {
      if (wid < 4 && l == 0) gst8((u32x2){tlo, thi}, (unsigned)((bid * 4 + wid) * 8), ws + WS_FK);
    }
    if constexpr (sl == 38) {
      waitv<SC.younger(OM, 0, sl)>(fg);
      const unsigned lo = wid < 4 ? glo : tlo, hi = wid < 4 ? ghi : thi, mk = wid < 4 ? 0xf8000000u : 0xffffffffu;
      for (int spin = 0; spin < SPINS; ++spin) {
        if (__builtin_expect(__all(((fg[0] & mk) == lo) & (fg[1] == hi)), 1)) break;
        __builtin_amdgcn_s_sleep(1);
        gld8<0, true>(fg, (unsigned)((t & 255) * 8), wid < 4 ? ws + WS_G0 : ws + WS_FK + (head >> 3) * 2048);
        waitv<0>(fg);
      }
      const unsigned km = wave_max_u32(fg[0] & 0x07ffffffu);
      *(LAS unsigned*)(lds + HSC + wid * 4) = km;
    }
    if constexpr (sl == 57) {
      if (wid == 3) {
        const char* pc;
        asm volatile("s_getpc_b64 %0" : "=s"(pc));
        touch((unsigned)((within * 8 + (l & 7)) * 128), pc);
      }
    }
    if constexpr (sl == 47) {
      if (bid == 0 && t == 0) gst8((u32x2){tlo, thi}, 0u, ws + WS_EP);
      const u32x4 kk = *(LAS const u32x4*)(lds + HSC);
      key = __builtin_amdgcn_readfirstlane(umax(umax(kk[0], kk[1]), umax(kk[2], kk[3])));
    }
    if constexpr (sl == 58 || sl == 61 || sl == 64 || sl == 67) {
      if (wid == 4 + (sl - 58) / 3) touch((unsigned)(((wid & 3) * 64 + l) * 128), (const char*)wdown + (size_t)fj * 16 * DM * 2);
    }
    if constexpr (sl == 81 || sl == 85 || sl == 89 || sl == 93) {
      if (wid == (sl - 81) / 4)
        touch((unsigned)((kh * 2048 + (within >> 1) * 128 + wid * 32 + ((within & 1) * 2 + (xcd & 1)) * 8 + (l >> 3)) * (DM * 2) + ((xcd >> 1) & 1) * 1024 + (l & 7) * 128), wo);
    }
    if constexpr (sl == 66) {
      const unsigned fo = (unsigned)(WS_FO - WS_ST) + (l & 3) * 16384 + ((mb * 4 + rbw) * 32 + kh * 16) * 8, fx = (unsigned)(WS_FX - WS_ST) + (bid * 8 + wid) * 64;
      const unsigned fy = (unsigned)(WS_FY - WS_ST) + (wid * 256 + yrow) * 8;
      asm volatile("global_load_dword v255, %0, %1 sc1" ::"v"(l < 4 ? fo : l < 8 ? fy : fx), "s"(ws + WS_ST) : "memory", "v255");
    }
    if constexpr (sl == 97 || sl == 99) {
      if (wid == 4 + (sl - 97) / 2) touch((unsigned)(((mb * 128 + (wid - 4) * 64 + l) * DM + nb * 128 + kh * 64) * 2), x);
    }
    if constexpr ((sl >= 68 && sl <= 70) || (sl >= 90 && sl <= 92)) {
      constexpr int r = sl >= 90, q = sl - (r ? 90 : 68);
      if constexpr (q == 0) waitv<SC.younger(OF, 4 * r + 3, sl)>(fxs);
      if constexpr (q >= 1) {
#pragma unroll
        for (int j = 0; j < 2; ++j) { fq[2 * q - 2 + j] = __builtin_amdgcn_mfma_f32_16x16x32_bf16(fxs, ft[j], (f32x4){}, 0, 0, 0); PIN(fq[2 * q - 2 + j]); }
      }
      if constexpr (q < 2) {
        const unsigned rp = ldsf + g * 8192 + (c >> 2) * 128 + ((((g & 1) << 1) | (c >> 3)) << 5) + (c & 3) * 8;
#pragma unroll
        for (int j = 0; j < 2; ++j) { LAS const char* a = (LAS const char*)(uintptr_t)(rp ^ ((2 * q + j) << 5)); ft[j] = cat8(trd(a), trd(a + 512)); }
      }
    }
    if constexpr (sl == 71 || sl == 93) {
      const float v = sel2(g & 2, sel2(g & 1, fq[3][0], fq[2][0]), sel2(g & 1, fq[1][0], fq[0][0]));
      gst8((u32x2){__float_as_uint(v), tlo}, (unsigned)(((fsl * 2 + (sl == 93)) * FF + fgr * 512 + t) * 8), ws + WS_GU);
    }
    if constexpr (sl == 109) {
      waitv<SC.younger(OS, 2, sl)>(fm[0]);
      for (int spin = 0; spin < SPINS; ++spin) {
        if (__builtin_expect(__all((fm[0][1] == tlo) & (fm[0][3] == tlo)), 1)) break;
        __builtin_amdgcn_s_sleep(1);
        gld16<0, true>(fm[0], gu_v, ws + WS_GU);
        waitv<0>(fm[0]);
      }
      *(LAS f32x2*)(lds + RED1 + (wid * 32 + 2 * c) * 4) = (f32x2){xg_sum(__uint_as_float(fm[0][0])), xg_sum(__uint_as_float(fm[0][2]))};
    }
    if constexpr (sl == 111) {
#pragma unroll
      for (int w = 0; w < 2; ++w) { fa0[w] = *(LAS const float*)(lds + RED1 + ((2 * g + w) * 32 + c) * 4); fa0[2 + w] = *(LAS const float*)(lds + RED1 + ((2 * g + w) * 32 + 16 + c) * 4); }
    }
    if constexpr (sl == 112) { fa0[0] = xg_sum(fa0[0] + fa0[1]); fa0[2] = xg_sum(fa0[2] + fa0[3]); PIN(fa0); }
    if constexpr (sl == 113) {
      const float hh = rbf(silu_f(rbf(fa0[0]))) * rbf(fa0[2]);
      *(LAS u16*)(lds + HSC + 32 + wid * 32 + c * 2) = (u16)pk2(hh, hh);
    }
    if constexpr (sl == 114) fxa = *(LAS const s16x4*)(lds + HSC + 32 + wid * 32 + g * 8);
    constexpr int pr = fpiece(sl), pm = fpiece(sl - 1), ps = fpiece(sl - 2);
    if constexpr (ps >= 0) gst4<ps * 128>(sel2(g & 1, fa1[0], fa0[0]), (unsigned)((wid * 128 + (l & 31)) * 4), ws + WS_YP + (size_t)yrow * 4096);
    if constexpr (pm >= 0) { fa0 = MFMA16(fxa, fb0, (f32x4){}); fa1 = MFMA16(fxa, fb1, (f32x4){}); PIN(fa0); PIN(fa1); }
    if constexpr (pr >= 0) {
      waitv<SC.younger(OF, 8 + pr, sl)>();
      const unsigned fp = ldsf + (4 * g + (c >> 2)) * 64 + (g & 1) * 32 + (c & 3) * 8;
      fb0 = trd((LAS const char*)(uintptr_t)fp + (pr % 3) * 8192); fb1 = trd((LAS const char*)(uintptr_t)(fp ^ 32) + (pr % 3) * 8192);
    }
    if constexpr (sl == 111 || sl == 119 || sl == 127) {
      waitv<SC.younger(OW, (sl - 111) / 4 + 1, sl)>();
      if (l == 0) gst8((u32x2){tlo, thi}, (unsigned)(((rb * 8 + wid) * 32 + head) * 8), ws + WS_FO + ((sl - 111) / 8) * 16384);
    }
    if constexpr (sl == 123) {
      waitv<SC.younger(OM, 1, sl)>(fg);
      if (__builtin_expect(!__all((fg[0] == tlo) & (fg[1] == thi)), 0)) poll8(fo_v, ws + WS_FO, tlo, thi, 0xffffffffu);
    }
    SBAR();
    if constexpr (i == 6) {
      if constexpr (u == 5 || u == 13) waitbar_l<SC.younger(OG, 4 * u + 7, sl)>();
      else waitbar<SC.younger(OG, 4 * u + 7, sl)>();
      if constexpr (u == 0 || u == 1 || u == 5 || u == 7 || u == 8 || u == 10 || u == 12 || u == 14)
        STAMP(u == 0 ? 4 : u == 1 ? 5 : u == 5 ? 6 : u == 7 ? 7 : u == 8 ? 8 : u == 10 ? 9 : u == 12 ? 10 : 11);
    }
    if constexpr (PRIO && !(i & 1)) __builtin_amdgcn_s_setprio(i == 6 ? 3 : 2 - i / 2);
  };

  issue(IC(-2), IC(-1));
  if (wid == 5) poll8((unsigned)((rb * 32 + (l & 31)) * 8), ws + WS_G0, glo, ghi, 0xf8000000u);
  waitbar<SC.younger(OG, 3, -2)>();
  issue(IC(-1), IC(-1));
  STAMP(3);

  LBase trb[2], frb;
  trb[0].init(lds0 + g * 2048 + (c >> 2) * 64 + (g & 1) * 32 + (c & 3) * 8);
  trb[1].init(lds0 + g * 2048 + (c >> 2) * 64 + ((g & 1) ^ 1) * 32 + (c & 3) * 8);
  frb.init(lds0 + l * 16);
  f32x4 acc[8][2];
  bf16x8 wf[2][4];
  auto WRD1 = [&](auto Si, auto Base, auto Dd) AI {
    constexpr int s = Si, d = Dd, o = Base + (d >> 1) * 512;
    wf[s & 1][d] = cat8(trd(trb[d & 1].template at<o>()), trd(trb[d & 1].template at<o + 256>()));
  };
  auto WRD = [&](auto Si, auto Base) AI { sfor<4>([&](auto Dd) AI { WRD1(Si, Base, Dd); }); };
#define WA(s) IC((((s) >> 3) & 3) * 32768 + (((s) >> 1) & 3) * 8192 + ((s) & 1) * 1024)
#define WB(s) IC((((s) >> 2) & 3) * 32768 + (((s) >> 1) & 1) * 8192 + ((s) & 1) * 1024)
  bf16x8 fr[4];
  auto FRD = [&](auto Ni) AI { constexpr int n = Ni; if constexpr (n < 256) fr[n & 3] = *(LAS const bf16x8*)(frb.at<((n >> 5) & 3) * 32768 + (n & 31) * 1024>()); };

  WRD(IC(0), WA(0));
  SBAR();
  bf16x8 qf[4][2];
  auto QPACK = [&](auto Ds) AI {
    constexpr int ds = Ds;
#pragma unroll
    for (int qb = 0; qb < 2; ++qb) {
      const f32x4 a = acc[2 * ds][qb] * QSCALE, b = acc[2 * ds + 1][qb] * QSCALE;
      qf[ds][qb] = __builtin_bit_cast(bf16x8, (u32x4){pk2(a[0], a[1]), pk2(a[2], a[3]), pk2(b[0], b[1]), pk2(b[2], b[3])});
      PIN(qf[ds][qb]);
    }
  };
  sfor<64>([&](auto Si) AI {
    constexpr int s = Si, x0 = (s & ~1) % XD, x1 = (s | 1) % XD;
    if constexpr (!(s & 1)) waitv<SC.younger(OX, s + 1, s - 1)>(xf[x0], xf[x1]);
    sfor<4>([&](auto Dd) AI {
      constexpr int d = Dd, dt = (s & 1) * 4 + d;
      if constexpr (s + 1 < 64) WRD1(IC(s + 1), WA(s + 1), Dd);
      else if constexpr (d < 3) FRD(Dd);
      SBAR();
      if constexpr (s < 2) { MFZ(acc[dt][0], wf[s & 1][d], xf[x0]); MFZ(acc[dt][1], wf[s & 1][d], xf[x1]); }
      else { MF(acc[dt][0], wf[s & 1][d], xf[x0]); MF(acc[dt][1], wf[s & 1][d], xf[x1]); }
      issue(Si, Dd);
      if constexpr (s == 63 && (d & 1)) { QPACK(IC(d >> 1)); SBAR(); }
    });
    SBAR();
    endslot(Si);
  });

  f32x4 sc[2][2][2], negm[2];
  u32x4 pw[16][2];
  f32x4 lsum[2];
  bf16x8 ones = {0x3f80, 0x3f80, 0x3f80, 0x3f80, 0x3f80, 0x3f80, 0x3f80, 0x3f80};
  PIN(ones);
#define MX3(a, b, c) __builtin_fmaxf(__builtin_fmaxf((a), (b)), (c))
#define EXP2(x) __builtin_amdgcn_exp2f(x)
  auto SM = [&](auto Hi, auto Pi) AI {
    constexpr int H = Hi, p = Pi, kt = p >> 2, qb = (p >> 1) & 1, r = 2 * (p & 1);
    const float e0 = EXP2(sc[H & 1][kt][qb][r]), e1 = EXP2(sc[H & 1][kt][qb][r + 1]);
    unsigned w = pk2(e0, e1);
    PIN(w);
    pw[H][qb][kt * 2 + (p & 1)] = w;
  };
  sfor<128>([&](auto Ni) AI {
    constexpr int n = Ni, H = n >> 3, i = n & 7, kt = i >> 2, ds = i & 3, sn = H & 1;
    FRD(IC(n + 3));
    if constexpr (n < 2) QPACK(IC(2 + n));
    sfor<2>([&](auto Qb) AI {
      constexpr int qb = Qb;
      if constexpr (ds == 0 && H == 0) MFZ(sc[sn][kt][qb], fr[n & 3], qf[0][qb]);
      else if constexpr (ds == 0) MFC(sc[sn][kt][qb], fr[n & 3], qf[0][qb], negm[qb]);
      else MF(sc[sn][kt][qb], fr[n & 3], qf[ds][qb]);
    });
    if constexpr (n == 5 || n == 6) {
      constexpr int qb = n - 5;
      NOPS(3); PIN(sc[0][0][qb]);
      const float m = -xg_max(__builtin_fmaxf(MX3(sc[0][0][qb][0], sc[0][0][qb][1], sc[0][0][qb][2]), sc[0][0][qb][3]));
      negm[qb] = (f32x4){m, m, m, m};
      sc[0][0][qb] += negm[qb];
      PIN(negm[qb]); PIN(sc[0][0][qb]);
    }
    if constexpr (n == 9 || n == 10) { NOPS(3); PIN(sc[0][1][n - 9]); sc[0][1][n - 9] += negm[n - 9]; PIN(sc[0][1][n - 9]); }
    issue(IC(64 + (n >> 2)), IC(n & 3));
    if constexpr (H == 2 && (i == 1 || i == 5)) MFZ(lsum[i >> 2], ones, pw[0][i >> 2]);
    if constexpr (H > 2 && (i == 1 || i == 5)) MF(lsum[i >> 2], ones, pw[H - 2][i >> 2]);
    if constexpr (H >= 1 && H < 15) SM(IC(H - 1), IC(i));
    if constexpr (n == 125) { PIN(negm[0]); PIN(negm[1]); }
    SBAR();
    if constexpr ((n & 3) == 3) endslot(IC(64 + (n >> 2)));
  });
  constexpr int CHK = 32;
  sfor<128>([&](auto Mi) AI {
    constexpr int m = Mi, n = 128 + m, os = m >> 5, H = (m >> 1) & 15, par = m & 1;
    FRD(IC(n + 3));
    if constexpr (H == 0) { MFZ(o[os & 1][par][0], fr[n & 3], pw[H][0]); MFZ(o[os & 1][par][1], fr[n & 3], pw[H][1]); }
    else { MF(o[os & 1][par][0], fr[n & 3], pw[H][0]); MF(o[os & 1][par][1], fr[n & 3], pw[H][1]); }
    if constexpr (m < 26 && (m % 13) % 5 != 1 && (m % 13) % 5 != 3) {
      constexpr int r = m % 13, p = r / 5 * 3 + (r % 5) / 2;
      SM(IC(14 + m / 13), IC(p));
    }
    if constexpr (m == 14 || m == 16) MF(lsum[(m - 14) / 2], ones, pw[14][(m - 14) / 2]);
    if constexpr (m == 27 || m == 28) MF(lsum[m - 27], ones, pw[15][m - 27]);
    if constexpr (m == CHK + 1 || m == CHK + 2) { inv[m - CHK - 1] = __builtin_amdgcn_rcpf(lsum[m - CHK - 1][0]); PIN(inv[m - CHK - 1]); }
    issue(IC(64 + (n >> 2)), IC(n & 3));
    if constexpr (m == 123) {
      trb[0].init(lds0 + kg * 16384 + g * 2048 + (c >> 2) * 64 + (g & 1) * 32 + (c & 3) * 8);
      trb[1].init(lds0 + kg * 16384 + g * 2048 + (c >> 2) * 64 + ((g & 1) ^ 1) * 32 + (c & 3) * 8);
    }
    if constexpr (m >= 124) WRD1(IC(0), WB(0), IC(m - 124));
    SBAR();
    if constexpr ((n & 3) == 3) endslot(IC(64 + (n >> 2)));
    if constexpr (m == CHK - 1) {
      PIN(lsum[0]); PIN(lsum[1]);
      if (__builtin_expect(__any(!(lsum[0][0] + lsum[1][0] <= LBOUND)), 0)) {
        NOPS(7); SBAR();
        auto tile = [&](unsigned off, f32x4& s0, f32x4& s1) AI {
          bf16x8 kf[4];
          SBAR(); NOPS(7);
          sfor<4>([&](auto Ds) AI { constexpr int ds = Ds; gld16<ds * 1024, true>(kf[ds], off, kv_b); });
          waitv<0>(kf[0], kf[1]);
          PIN(kf[2]); PIN(kf[3]);
#pragma unroll
          for (int ds = 0; ds < 4; ++ds) {
            s0 = __builtin_amdgcn_mfma_f32_16x16x32_bf16(kf[ds], qf[ds][0], s0, 0, 0, 0);
            s1 = __builtin_amdgcn_mfma_f32_16x16x32_bf16(kf[ds], qf[ds][1], s1, 0, 0, 0);
          }
        };
        float m0 = -__builtin_inff(), m1 = m0;
#pragma unroll 1
        for (int kt = 0; kt < 32; ++kt) {
          f32x4 s0 = {}, s1 = {};
          tile((unsigned)(kt * 4096 + l * 16), s0, s1);
          m0 = MX3(MX3(m0, s0[0], s0[1]), s0[2], s0[3]); m1 = MX3(MX3(m1, s1[0], s1[1]), s1[2], s1[3]);
        }
        m0 = -xg_max(m0); m1 = -xg_max(m1);
        lsum[0] = (f32x4){}; lsum[1] = (f32x4){};
        sfor<16>([&](auto Hi) AI {
          constexpr int H = Hi;
#pragma unroll
          for (int kt = 0; kt < 2; ++kt) {
            sc[H & 1][kt][0] = (f32x4){m0, m0, m0, m0}; sc[H & 1][kt][1] = (f32x4){m1, m1, m1, m1};
            tile((unsigned)((2 * H + kt) * 4096 + l * 16), sc[H & 1][kt][0], sc[H & 1][kt][1]);
          }
          sfor<8>([&](auto Pi) AI { SM(Hi, Pi); });
          PIN(pw[H][0]); PIN(pw[H][1]);
          lsum[0] = __builtin_amdgcn_mfma_f32_16x16x32_bf16(ones, __builtin_bit_cast(bf16x8, pw[H][0]), lsum[0], 0, 0, 0);
          lsum[1] = __builtin_amdgcn_mfma_f32_16x16x32_bf16(ones, __builtin_bit_cast(bf16x8, pw[H][1]), lsum[1], 0, 0, 0);
        });
#pragma unroll
        for (int par = 0; par < 2; ++par) { o[0][par][0] = (f32x4){}; o[0][par][1] = (f32x4){}; }
        sfor<CHK / 4>([&](auto Qi) AI {
          constexpr int q = Qi;
          bf16x8 vf[4];
          SBAR(); NOPS(7);
          sfor<4>([&](auto J) AI { constexpr int j = J; gld16<j * 1024, true>(vf[j], (unsigned)((128 + 4 * q) * 1024 + l * 16), kv_b); });
          waitv<0>(vf[0], vf[1]);
          PIN(vf[2]); PIN(vf[3]);
#pragma unroll
          for (int j = 0; j < 4; ++j)
#pragma unroll
            for (int qb = 0; qb < 2; ++qb)
              o[0][j & 1][qb] = __builtin_amdgcn_mfma_f32_16x16x32_bf16(vf[j], __builtin_bit_cast(bf16x8, pw[2 * q + (j >> 1)][qb]), o[0][j & 1][qb], 0, 0, 0);
        });
#pragma unroll
        for (int par = 0; par < 2; ++par) { PIN(o[0][par][0]); PIN(o[0][par][1]); }
        PIN(lsum[0]); PIN(lsum[1]);
        SBAR(); NOPS(7);
      }
      SBAR();
    }
  });
  STAMP(12); STAMP(13); STAMP(14); STAMP(15);

  const int idx = SEQ - 1 - (int)(key & 2047u);
  const bool owner = (idx >> 7) == mb, ownw = (idx >> 4) == mb * 8 + rbw * 2 + kg;
  f32x4 yp[7], ya[4], oth[4];
  unsigned gate[8];
  float glow;
  const unsigned ex_a = ldsf + (c >> 3) * 8192 + (c & 7) * 128 + (((g >> 1) ^ (c >> 1)) << 4) + (g & 1) * 8;
  auto item = [](int s) { return s < 48 ? s : s < 56 ? 48 + 2 * (s - 48) : 49 + 2 * (s - 56); };
  u32x2 pf;
  u32x4 otp[2];
  bool rdy = false;
  sfor<64>([&](auto Si) AI {
    constexpr int s = Si, u = s >> 2, it = item(s), x0 = (it & ~1) % XD2, x1 = (it | 1) % XD2;
    if constexpr (!(it & 1)) waitv<SC.younger(OX, 64 + it + 1, B0 + s - 1)>(xf[x0], xf[x1]);
    sfor<4>([&](auto Dd) AI {
      constexpr int d = Dd, dt = (it & 1) * 4 + d;
      if constexpr (s + 1 < 64) WRD1(IC(s + 1), WB(item(s + 1)), Dd);
      SBAR();
      if constexpr (s < 2) { MFZ(acc[dt][0], wf[s & 1][d], xf[x0]); MFZ(acc[dt][1], wf[s & 1][d], xf[x1]); }
      else { MF(acc[dt][0], wf[s & 1][d], xf[x0]); MF(acc[dt][1], wf[s & 1][d], xf[x1]); }
      issue(IC(B0 + s), Dd);
      if constexpr (s == 56) *(LAS f32x4*)(lds + (wid ^ 4) * 4096 + l * 16 + (d & 1) * 2048 + (d >> 1) * 32768) = acc[d][1];
      if constexpr (s == 57 && d == 0) {
#pragma unroll
        for (int e = 0; e < 4; ++e) oth[e] = *(LAS const f32x4*)(lds + wid * 4096 + l * 16 + (e & 1) * 2048 + (e >> 1) * 32768);
      }
      if constexpr (s == 57 && d >= 2) {
        const f32x4 a = acc[2 * d - 4][0] + oth[2 * d - 4], b = acc[2 * d - 3][0] + oth[2 * d - 3];
        gst16<(d - 2) * 1024>((u32x4){pk2(a[0], a[1]), pk2(a[2], a[3]), pk2(b[0], b[1]), pk2(b[2], b[3])}, (unsigned)(wid * 2048 + l * 16), ws + WS_XC + (size_t)(bid ^ 4) * 16384);
      }
      if constexpr (s == 34) xe[d] = *(LAS const u32x2*)(uintptr_t)(ex_a ^ (d << 5));
      if constexpr (s >= 36 && s < 44 && d == 1) {
        constexpr int j = s - 36;
        PIN(st);
        const unsigned xw = xe[j >> 1][j & 1];
        const unsigned dd = pk2(bflo(xw) - st[0], bfhi(xw) - st[0]);
        gate[j] = pk2(bflo(dd) * st[1], bfhi(dd) * st[1]);
        PIN(gate[j]);
      }
      if constexpr (s >= 47 && s < 63 && d == 1) {
        constexpr int j = (s - 47) >> 1;
        if constexpr ((s - 47) & 1) { gate[j] = pk2(glow, silu_f(bfhi(gate[j]))); PIN(gate[j]); }
        else { glow = silu_f(bflo(gate[j])); PIN(glow); }
      }
    });
    SBAR();
    if constexpr (s == 7) {
      waitv<SC.younger(OW, 7, B0 + s)>();
      if (l == 0) {
        gst8((u32x2){tlo, thi}, (unsigned)(((rb * 8 + wid) * 32 + head) * 8), ws + WS_FO + 3 * 16384);
        gst8((u32x2){tlo, thi}, (unsigned)((wid * 256 + yrow) * 8), ws + WS_FY);
      }
    }
    if constexpr (s == 4 || s == 17 || s == 33) {
      constexpr int j = s == 4 ? 1 : s == 17 ? 2 : 3;
      waitv<SC.younger(OM, j + 1, B0 + s)>(fg);
      if (__builtin_expect(!__all((fg[0] == tlo) & (fg[1] == thi)), 0)) poll8(fo_v, ws + WS_FO + j * 16384, tlo, thi, 0xffffffffu);
    }
    if constexpr (s == 59) gld8<0, true>(pf, (unsigned)((bid * 8 + wid) * 64), ws + WS_FX);
    if constexpr (s == 60) {
      waitv<1>();
      if (l == 0) gst8((u32x2){tlo, thi}, (unsigned)(((bid ^ 4) * 8 + wid) * 64), ws + WS_FX);
    }
    if constexpr (s == 62) {
      waitv<1>(pf);
      rdy = __all((pf[0] == tlo) & (pf[1] == thi));
      if (rdy) sfor<2>([&](auto Dd) AI { constexpr int d = Dd; gld16<d * 1024, true>(otp[d], (unsigned)(wid * 2048 + l * 16), ws + WS_XC + (size_t)bid * 16384); });
    }
    if constexpr (s == 13) {
      if (owner) { gld16<0, true>(fm[0], (unsigned)(nb * 2048 + (l < 56 ? l : 55) * 32), ws + WS_FY); gld16<16, true>(fm[1], (unsigned)(nb * 2048 + (l < 56 ? l : 55) * 32), ws + WS_FY); }
    }
    if constexpr (s == 28) {
      if (owner) {
        PIN(fm[0]); PIN(fm[1]);
        for (int spin = 0; spin < SPINS; ++spin) {
          const bool ok = (fm[0][0] == tlo) & (fm[0][1] == thi) & (fm[0][2] == tlo) & (fm[0][3] == thi) & (fm[1][0] == tlo) & (fm[1][1] == thi) & (fm[1][2] == tlo) & (fm[1][3] == thi);
          if (__builtin_expect(__all(ok), 1)) break;
          __builtin_amdgcn_s_sleep(1);
          gld16<0, true>(fm[0], (unsigned)(nb * 2048 + (l < 56 ? l : 55) * 32), ws + WS_FY); gld16<16, true>(fm[1], (unsigned)(nb * 2048 + (l < 56 ? l : 55) * 32), ws + WS_FY);
          waitv<0>(fm[0], fm[1]);
        }
        const unsigned yv = (unsigned)(((t >> 4) * 1024 + nb * 128 + kh * 64 + (t & 15) * 4) * 4);
        sfor<7>([&](auto I) AI { constexpr int q = I; gld16<0, true>(yp[q], yv, ws + WS_YP + (size_t)q * 32 * 4096); });
      }
    }
    if constexpr (s == 43) {
      if (owner) {
        f32x4 a = {};
#pragma unroll
        for (int q = 0; q < 7; ++q) { PIN(yp[q]); a += yp[q]; }
#pragma unroll
        for (int j = 0; j < 4; ++j) a[j] = xg_sum(a[j]);
        *(LAS f32x4*)(lds + YS + (wid * 64 + c * 4) * 4) = a;
      }
    }
    if constexpr (s == 45) {
      if (ownw) {
        float a = 0.f;
#pragma unroll
        for (int p = 0; p < 8; ++p) a += *(LAS const float*)(lds + YS + (p * 64 + l) * 4);
        const unsigned ord = key >> 11, bits = (ord & 0x8000u) ? (ord & 0x7fffu) : (~ord & 0xffffu);
        *(LAS float*)(lds + RED1 + l * 4) = rbf(rbf(a) * rbf(sigmoid_f(bflo(bits))));
#pragma unroll
        for (int q = 0; q < 4; ++q) ya[q] = *(LAS const f32x4*)(lds + RED1 + (q * 16 + 4 * g) * 4);
      }
    }
    if constexpr (s == 46) {
      if (ownw) {
#pragma unroll
        for (int j = 0; j < 8; ++j) {
          const unsigned w = pk2(bflo(gate[j]) + ya[j >> 1][2 * (j & 1)], bfhi(gate[j]) + ya[j >> 1][2 * (j & 1) + 1]);
          gate[j] = erow == idx ? w : gate[j];
          PIN(gate[j]);
        }
      }
    }
    SBAR();
    if constexpr (((s & 3) == 2 && u < 11) || s == 44) {
      if constexpr (s == 44) waitbar_l<SC.younger(OG, 64 + 4 * u + 7, B0 + s)>();
      else waitbar<SC.younger(OG, 64 + 4 * u + 7, B0 + s)>();
      if constexpr (u % 4 == 1) STAMP(16 + u / 4);
    }
    if constexpr (s == 48) waitbar<SC.younger(OG, 64 + 55, B0 + s)>();
    if constexpr (s == 50) waitbar<SC.younger(OG, 64 + 59, B0 + s)>();
    if constexpr (s == 52) { waitbar<SC.younger(OG, 64 + 63, B0 + s)>(); STAMP(19); }
    if constexpr (s == 56) waitbar_l<63>();
    if constexpr (PRIO) __builtin_amdgcn_s_setprio(3 - ((s + 2) & 3));
  });
  STAMP(20);

  f32x4 kep[4];
  {
    NOPS(7);
#pragma unroll
    for (int d = 0; d < 4; ++d) { PIN(acc[4 + d][1]); *(LAS f32x4*)(lds + 65536 + (wid ^ 4) * 4096 + l * 16 + (d & 1) * 2048 + (d >> 1) * 32768) = acc[4 + d][1]; }
    if (!rdy) {
      poll8((unsigned)((bid * 8 + wid) * 64), ws + WS_FX, tlo, thi, 0xffffffffu);
      sfor<2>([&](auto Dd) AI { constexpr int d = Dd; gld16<d * 1024, true>(otp[d], (unsigned)(wid * 2048 + l * 16), ws + WS_XC + (size_t)bid * 16384); });
    }
    STAMP(21);
    waitbar_l<63>();
#pragma unroll
    for (int d = 0; d < 4; ++d) kep[d] = acc[4 + d][0] + *(LAS const f32x4*)(lds + 65536 + wid * 4096 + l * 16 + (d & 1) * 2048 + (d >> 1) * 32768);
    STAMP(22);
    STAMP(23);
    waitv<0>(otp[0], otp[1]);
    STAMP(24);
  }
#pragma unroll
  for (int d = 0; d < 4; ++d) {
    const unsigned o0 = otp[d >> 1][2 * (d & 1)], o1 = otp[d >> 1][2 * (d & 1) + 1];
    const f32x4 a = kep[d] + (f32x4){bflo(o0), bfhi(o0), bflo(o1), bfhi(o1)};
    u32x2 res;
#pragma unroll
    for (int j = 0; j < 2; ++j) {
      const unsigned sp = pk2(a[2 * j], a[2 * j + 1]), gp = gate[2 * d + j], xw = xe[d][j];
      const unsigned pr = pk2(bflo(sp) * bflo(gp), bfhi(sp) * bfhi(gp));
      res[j] = pk2(bflo(xw) + bflo(pr), bfhi(xw) + bfhi(pr));
    }
    *(LAS u32x2*)(uintptr_t)(ex_a ^ (d << 5)) = res;
  }
#pragma unroll
  for (int i = 0; i < 2; ++i)
    gst16<0>(*(LAS const u32x4*)(uintptr_t)(ldsf + i * 8192 + l * 16), (unsigned)(((erow - c + i * 8 + (l >> 3)) * DM + nb * 128 + kh * 64 + ((l & 7) ^ (i * 4 + (l >> 4))) * 8) * 2), out);
  STAMP(25);
  STAMP_OUT(0, 26);
}

extern "C" void kernel_launch(void* const* d_in, const int* in_sizes, int n_in, void* d_out, int out_size, void* d_ws, size_t ws_size,
                              hipStream_t stream) {
  k_fused<<<256, 512, 0, stream>>>((const u16*)d_in[0], (const u16*)d_in[1], (const u16*)d_in[2], (const u16*)d_in[3], (const u16*)d_in[4],
                                   (const u16*)d_in[6], (const u16*)d_in[7], (const u16*)d_in[8], (const u16*)d_in[9], (char*)d_ws, (u16*)d_out);
}
```

```cpp
#include <hip/hip_runtime.h>
#include <stdint.h>
#include <type_traits>

typedef unsigned short u16;
typedef short bf16x8 __attribute__((ext_vector_type(8)));
typedef short s16x4 __attribute__((ext_vector_type(4)));
typedef float f32x4 __attribute__((ext_vector_type(4)));
typedef float f32x2 __attribute__((ext_vector_type(2)));
typedef unsigned u32x4 __attribute__((ext_vector_type(4)));
typedef unsigned u32x2 __attribute__((ext_vector_type(2)));
typedef __bf16 bf2_t __attribute__((ext_vector_type(2)));
#define LAS __attribute__((address_space(3)))
#define DI __device__ __forceinline__
#define AI __attribute__((always_inline))
#define SBAR() __builtin_amdgcn_sched_barrier(0)
template <class T> __device__ __forceinline__ void PIN(T& x) { asm volatile("" : "+v"(x)); }
template <class B> DI void MF(f32x4& acc, const bf16x8& a, const B& b) { asm volatile("v_mfma_f32_16x16x32_bf16 %0, %1, %2, %0" : "+v"(acc) : "v"(a), "v"(b)); }
template <class B> DI void MFC(f32x4& d, const bf16x8& a, const B& b, const f32x4& c) { asm volatile("v_mfma_f32_16x16x32_bf16 %0, %1, %2, %3" : "=&v"(d) : "v"(a), "v"(b), "v"(c)); }
template <class B> DI void MFZ(f32x4& d, const bf16x8& a, const B& b) { asm volatile("v_mfma_f32_16x16x32_bf16 %0, %1, %2, 0" : "=&v"(d) : "v"(a), "v"(b)); }
#define NOPS(n) asm volatile("s_nop " #n)
#define MFMA16(a, b, c) __builtin_amdgcn_mfma_f32_16x16x16bf16_1k((a), (b), (c), 0, 0, 0)
#define IC(n) std::integral_constant<int, (n)>{}

constexpr int SEQ = 2048, DM = 1024, DH = 128, SK = 2048, WIN = 512, FF = 3584;
constexpr size_t WS_XN = 0;
constexpr size_t WS_KV = 4u << 20;
constexpr size_t WS_ST = 5u << 20;
constexpr size_t WS_EP = WS_ST + 16384;
constexpr size_t WS_G0 = WS_ST + 32768;
constexpr size_t WS_FK = WS_ST + 49152;
constexpr size_t WS_YP = WS_ST + 65536;
constexpr size_t WS_FO = 7u << 20;
constexpr size_t WS_FY = WS_FO + 65536;
constexpr size_t WS_FX = WS_FO + 131072;
constexpr size_t WS_O = 8u << 20;
constexpr size_t WS_GU = 32u << 20;
constexpr size_t WS_XC = 24u << 20;

template <int I, int N, class F> DI void sfor_impl(F&& f) {
  if constexpr (I < N) { f(std::integral_constant<int, I>{}); sfor_impl<I + 1, N>(f); }
}
template <int N, class F> DI void sfor(F&& f) { sfor_impl<0, N>(f); }

DI unsigned pk2(float a, float b) { f32x2 v = {a, b}; return __builtin_bit_cast(unsigned, __builtin_convertvector(v, bf2_t)); }
DI float rbf(float a) { return (float)(__bf16)a; }
DI float bflo(unsigned u) { return __uint_as_float(u << 16); }
DI float bfhi(unsigned u) { return __uint_as_float(u & 0xffff0000u); }
DI float sigmoid_f(float v) { return __builtin_amdgcn_rcpf(1.f + __builtin_amdgcn_exp2f(v * -1.4426950408889634f)); }
DI float sel2(bool c, float a, float b) { PIN(a); PIN(b); return c ? a : b; }
DI float silu_f(float v) { return v * sigmoid_f(v); }
template <int CTRL> DI float dppf(float x) { return __builtin_bit_cast(float, __builtin_amdgcn_mov_dpp(__builtin_bit_cast(int, x), CTRL, 0xf, 0xf, true)); }
template <int CTRL> DI unsigned dppu(unsigned x) { return (unsigned)__builtin_amdgcn_mov_dpp((int)x, CTRL, 0xf, 0xf, true); }
constexpr int XOR1 = 0xB1, XOR2 = 0x4E, XOR7 = 0x141, XOR8 = 0x128;
DI float xg_sum(float x) {
  auto s = __builtin_amdgcn_permlane16_swap(__float_as_uint(x), __float_as_uint(x), false, false);
  x = __uint_as_float(s[0]) + __uint_as_float(s[1]);
  auto t = __builtin_amdgcn_permlane32_swap(__float_as_uint(x), __float_as_uint(x), false, false);
  return __uint_as_float(t[0]) + __uint_as_float(t[1]);
}
DI float xg_max(float x) {
  auto s = __builtin_amdgcn_permlane16_swap(__float_as_uint(x), __float_as_uint(x), false, false);
  x = __builtin_fmaxf(__uint_as_float(s[0]), __uint_as_float(s[1]));
  auto t = __builtin_amdgcn_permlane32_swap(__float_as_uint(x), __float_as_uint(x), false, false);
  return __builtin_fmaxf(__uint_as_float(t[0]), __uint_as_float(t[1]));
}
DI float wave_sum(float v) { v += dppf<XOR1>(v); v += dppf<XOR2>(v); v += dppf<XOR7>(v); v += dppf<XOR8>(v); return xg_sum(v); }
DI unsigned umax(unsigned a, unsigned b) { return a > b ? a : b; }
DI unsigned wave_max_u32(unsigned v) {
  v = umax(v, dppu<XOR1>(v)); v = umax(v, dppu<XOR2>(v)); v = umax(v, dppu<XOR7>(v)); v = umax(v, dppu<XOR8>(v));
  auto s = __builtin_amdgcn_permlane16_swap(v, v, false, false); v = umax(s[0], s[1]);
  auto t = __builtin_amdgcn_permlane32_swap(v, v, false, false); v = umax(t[0], t[1]);
  return __builtin_amdgcn_readfirstlane(v);
}

template <int OFF, bool DEV = false> DI void glds(unsigned ldsaddr, unsigned voff, const void* sbase) {
  if constexpr (DEV) asm volatile("s_mov_b32 m0, %0\n\ts_nop 0\n\tglobal_load_lds_dwordx4 %1, %2 offset:%3 sc1" ::"s"(ldsaddr), "v"(voff), "s"(sbase), "i"(OFF) : "memory");
  else asm volatile("s_mov_b32 m0, %0\n\ts_nop 0\n\tglobal_load_lds_dwordx4 %1, %2 offset:%3" ::"s"(ldsaddr), "v"(voff), "s"(sbase), "i"(OFF) : "memory");
}
DI void touch(unsigned voff, const void* sbase) {
  asm volatile("global_load_dword v255, %0, %1" ::"v"(voff), "s"(sbase) : "memory", "v255");
}
template <int OFF, bool DEV = false, class T> DI void gld16(T& dst, unsigned voff, const void* sbase) {
  if constexpr (DEV) asm volatile("global_load_dwordx4 %0, %1, %2 offset:%3 sc1" : "=v"(dst) : "v"(voff), "s"(sbase), "i"(OFF) : "memory");
  else asm volatile("global_load_dwordx4 %0, %1, %2 offset:%3" : "=v"(dst) : "v"(voff), "s"(sbase), "i"(OFF) : "memory");
}
template <int OFF, bool DEV = false, class T> DI void gld8(T& dst, unsigned voff, const void* sbase) {
  if constexpr (DEV) asm volatile("global_load_dwordx2 %0, %1, %2 offset:%3 sc1" : "=v"(dst) : "v"(voff), "s"(sbase), "i"(OFF) : "memory");
  else asm volatile("global_load_dwordx2 %0, %1, %2 offset:%3" : "=v"(dst) : "v"(voff), "s"(sbase), "i"(OFF) : "memory");
}
template <int OFF> DI void gld2(unsigned& dst, unsigned voff, const void* sbase) {
  asm volatile("global_load_ushort %0, %1, %2 offset:%3" : "=v"(dst) : "v"(voff), "s"(sbase), "i"(OFF) : "memory");
}
template <int OFF, class T> DI void gst16(const T& v, unsigned voff, const void* sbase) {
  asm volatile("global_store_dwordx4 %0, %1, %2 offset:%3 sc1\n\ts_nop 1" ::"v"(voff), "v"(v), "s"(sbase), "i"(OFF) : "memory");
}
template <class T> DI void gst8(const T& v, unsigned voff, const void* sbase) {
  asm volatile("global_store_dwordx2 %0, %1, %2 sc1" ::"v"(voff), "v"(v), "s"(sbase) : "memory");
}
template <int OFF> DI void gst4(float v, unsigned voff, const void* sbase) {
  asm volatile("global_store_dword %0, %1, %2 offset:%3 sc1" ::"v"(voff), "v"(v), "s"(sbase), "i"(OFF) : "memory");
}
constexpr int SPINS = 1 << 16;
DI void poll8(unsigned voff, const void* sbase, unsigned lo, unsigned hi, unsigned lomask) {
  for (int spin = 0; spin < SPINS; ++spin) {
    u32x2 v;
    asm volatile("global_load_dwordx2 %0, %1, %2 sc1\n\ts_waitcnt vmcnt(0)" : "=&v"(v) : "v"(voff), "s"(sbase) : "memory");
    if (__all(((v[0] & lomask) == lo) & (v[1] == hi))) break;
    __builtin_amdgcn_s_sleep(1);
  }
}
template <int N> DI void waitv() { static_assert(N >= 0, "count"); asm volatile("s_waitcnt vmcnt(%0)" ::"n"(N > 63 ? 63 : N) : "memory"); }
template <int N, class T> DI void waitv(T& x) { static_assert(N >= 0, "count"); asm volatile("s_waitcnt vmcnt(%1)" : "+v"(x) : "n"(N > 63 ? 63 : N) : "memory"); }
template <int N, class T> DI void waitv(T& x, T& y) { static_assert(N >= 0, "count"); asm volatile("s_waitcnt vmcnt(%2)" : "+v"(x), "+v"(y) : "n"(N > 63 ? 63 : N) : "memory"); }
template <int N> DI void waitbar() { static_assert(N >= 0, "count"); asm volatile("s_waitcnt vmcnt(%0)\n\ts_barrier" ::"n"(N > 63 ? 63 : N) : "memory"); }
template <int N> DI void waitbar_l() { static_assert(N >= 0, "count"); asm volatile("s_waitcnt vmcnt(%0) lgkmcnt(0)\n\ts_barrier" ::"n"(N > 63 ? 63 : N) : "memory"); }
typedef short v4i16_t __attribute__((ext_vector_type(4)));
DI s16x4 trd(LAS const char* p) { return __builtin_amdgcn_ds_read_tr16_b64_v4i16((LAS v4i16_t*)p); }
struct LBase {
  unsigned lo, hi;
  DI void init(unsigned a) { lo = a; hi = a + 65536; PIN(lo); PIN(hi); }
  template <int OFF> DI LAS const char* at() const { return (LAS const char*)(uintptr_t)(OFF < 65536 ? lo : hi) + (OFF & 65535); }
};
DI bf16x8 cat8(s16x4 lo, s16x4 hi) { return __builtin_shufflevector(lo, hi, 0, 1, 2, 3, 4, 5, 6, 7); }

enum { OX, OG, OF, OM, OS, OP, OE, OW };
struct Sched {
  int n; short kind[400], idx[400], slot[400];
  constexpr Sched() : n(0), kind{}, idx{}, slot{} {}
  constexpr void add(int s, int k, int i) { kind[n] = (short)k; idx[n] = (short)i; slot[n] = (short)s; ++n; }
  constexpr int younger(int k, int i, int s) const {
    int p = -1;
    for (int q = 0; q < n; ++q) if (kind[q] == k && idx[q] == i) p = q;
    if (p < 0 || slot[p] > s) return -1;
    int c = 0;
    for (int q = p + 1; q < n; ++q) if (slot[q] <= s) ++c;
    return c;
  }
  constexpr int first(int s) const { int q = 0; while (q < n && slot[q] < s) ++q; return q; }
  constexpr int count(int s) const { int c = 0; for (int q = 0; q < n; ++q) if (slot[q] == s) ++c; return c; }
};


constexpr int RING = 4 * 32768, FB = RING, RED1 = FB + 24576, HSC = RED1 + 1024, YS = HSC + 1024, LDS_BYTES = FB + 32768;
constexpr int STAGE = 3 * 32768;
constexpr int XD = 16, XD2 = 16;
constexpr float LBOUND = 65536.f;
constexpr float QSCALE = 0.08838834764831845f * 1.4426950408889634f;
constexpr int B0 = 128;
constexpr int FISS[4] = {96, 97, 98, 117}, FRDS[4] = {115, 118, 121, 124};
constexpr int fpiece(int sl) { for (int p = 0; p < 4; ++p) if (FRDS[p] == sl) return p; return -1; }
constexpr Sched make_sched() {
  Sched s;
  constexpr int XP = 8;
  for (int i = 0; i < 12; ++i) s.add(-2, OG, i);
  for (int i = 0; i < XP; ++i) s.add(-1, OX, i);
  for (int t = 0; t < 128; ++t) {
    if (t < 2) for (int i = XP + t * (16 - XP) / 2; i < XP + (t + 1) * (16 - XP) / 2; ++i) s.add(t, OX, i);
    if (t >= 2 && t < 50) s.add(t, OX, t + 14);
    for (int v = 3; v < 18; ++v) for (int p = 0; p < 4; ++p) if (8 * (v - 3) + 7 + p == t) s.add(t, OG, 4 * v + p);
    if (t == 105 || t == 113 || t == 121) { s.add(t, OW, (t - 105) / 4); s.add(t, OW, (t - 105) / 4 + 1); }
    if (t >= 124) for (int i = 0; i < 3; ++i) s.add(t, OX, 64 + 3 * (t - 124) + i);
    if (t == 127) { s.add(t, OG, 72); s.add(t, OG, 73); }
    if (t == 20) s.add(t, OM, 0);
    if (t == 120) s.add(t, OM, 1);
    if (t >= 49 && t < 57 && (t & 1)) s.add(t, OF, (t - 49) / 2);
    if (t >= 71 && t < 79 && (t & 1)) s.add(t, OF, 4 + (t - 71) / 2);
    if (t == 48) s.add(t, OS, 0);
    if (t == 104) s.add(t, OS, 2);
    for (int p = 0; p < 4; ++p) if (FISS[p] == t) s.add(t, OF, 8 + p);
  }
  for (int t = 0; t < 64; ++t) {
    if (t == 0) { s.add(B0, OM, 2); s.add(B0, OG, 74); s.add(B0, OG, 75); s.add(B0, OW, 6); s.add(B0, OW, 7); }
    if (t == 1 || t == 2) { s.add(B0 + t, OX, 74 + 2 * t); s.add(B0 + t, OX, 75 + 2 * t); }
    if (t == 5 || t == 6 || t == 9) s.add(B0 + t, OX, 64 + (t == 5 ? 16 : t == 6 ? 18 : 22));
    if (t >= 5 && t < 50) s.add(B0 + t, OX, 64 + (t == 5 ? 17 : t < 9 ? t + 13 : t + 14));
    for (int v = 3; v < 16; ++v) for (int p = 0; p < 4; ++p) if ((v < 14 ? 4 * (v - 3) + 3 + (p >> 1) : v == 14 ? 45 + (p >> 1) : 49) == t) s.add(B0 + t, OG, 64 + 4 * v + p);
    if (t == 10 || t == 26) s.add(B0 + t, OM, t == 10 ? 3 : 4);
    if (t == 13 || t == 14 || t == 18) s.add(B0 + t, OE, t == 18 ? 2 : t - 13);
  }
  return s;
}
constexpr Sched SC = make_sched();

__global__ __launch_bounds__(512, 2) void k_fused(const u16* __restrict__ x, const u16* __restrict__ kc, const u16* __restrict__ vc,
                                                  const u16* __restrict__ wq, const u16* __restrict__ wo, const u16* __restrict__ wr,
                                                  const u16* __restrict__ wgate, const u16* __restrict__ wup, const u16* __restrict__ wdown,
                                                  char* __restrict__ ws, u16* __restrict__ out) {
  __shared__ __attribute__((aligned(16))) char lds_raw[LDS_BYTES];
  LAS char* const lds = (LAS char*)lds_raw;
  const int t = threadIdx.x, l = t & 63, bid = blockIdx.x, c = l & 15, g = l >> 4;
  const int wid = __builtin_amdgcn_readfirstlane(t >> 6);
  const int xcd = bid & 7, within = bid >> 3, head = xcd * 4 + (within & 3), rb = within >> 2;
  const int fj = xcd * 28 + (within < 28 ? within : within - 28);
  const int fsl = fj / 7, fgr = fj - fsl * 7;
  const int kh = xcd >> 2, mb = 2 * rb + (xcd & 1), nb = ((xcd >> 1) & 1) * 4 + (within & 3);
  const int rbw = wid & 3, kg = wid >> 2;
  const unsigned lds0 = (unsigned)(uintptr_t)lds;
  unsigned tlo, thi;
  {
    const int row = 8 * bid + wid;
    u32x4 a0, a1, w0, w1, kvo;
    u32x2 ep, klo, khi;
    unsigned e[8];
    gld16<0>(a0, (unsigned)(row * DM * 2 + l * 16), x); gld16<1024>(a1, (unsigned)(row * DM * 2 + l * 16), x);
    gld16<0>(w0, (unsigned)(l * 16), wr); gld16<1024>(w1, (unsigned)(l * 16), wr);
    gld8<0, true>(ep, 0u, ws + WS_EP);
    if (wid == 1)
      asm volatile("global_load_dword v255, %0, %1 sc1" ::"v"(l == 0 ? (unsigned)(WS_G0 - WS_ST) + bid * 8 : l == 1 ? (unsigned)(WS_FK - WS_ST) + bid * 32 : (unsigned)(bid * 64)), "s"(ws + WS_ST) : "memory", "v255");
    if (wid == 3) {
      const char* pc;
      asm volatile("s_getpc_b64 %0" : "=s"(pc));
      touch((unsigned)((within * 12 + (l < 12 ? l : 11)) * 128), pc);
    }
    __builtin_amdgcn_s_barrier();
    if (wid == 2) {
      asm volatile("global_load_dword v255, %0, %1 sc1" ::"v"((unsigned)((l < 40 ? l : 39) << 20) + 64 * 1024 - 128), "s"(ws) : "memory", "v255");
      const char* pb = l < 8 ? (const char*)wo : l < 16 ? (const char*)wgate : l < 24 ? (const char*)wup : l < 32 ? (const char*)wdown : (const char*)out;
      pb += (size_t)(l & 7) * (l < 8 ? 1048576 : l < 32 ? 917504 : 524288);
      asm volatile("global_load_dword v255, %0, off" ::"v"(pb) : "memory", "v255");
    }
    const int fn = (bid * 4 + wid) & 255, kvh = bid >> 6;
    if (wid < 4) {
      if (fn < 128) {
        const unsigned ko = (unsigned)(((kvh * SK + (SK - WIN) + (fn >> 2) * 16 + c) * DH + (fn & 3) * 32 + 4 * g) * 2);
        gld8<0>(klo, ko, kc); gld8<32>(khi, ko, kc);
      } else {
        const int d = 32 * ((fn - 128) >> 5) + 8 * (c >> 2) + 4 * (fn & 1) + (c & 3);
        const unsigned vo = (unsigned)(((kvh * SK + (SK - WIN) + ((fn >> 1) & 15) * 32 + 4 * g + 8) * DH + d) * 2);
        sfor<8>([&](auto J) AI { constexpr int j = J; gld2<(16 * (j >> 2) + (j & 3) - 8) * DH * 2>(e[j], vo, vc); });
      }
      if (wid < 2) touch((unsigned)((wid * 256 + within * 8 + (l >> 3)) * 8192 + xcd * 1024 + (l & 7) * 128), wq);
      if (fn < 128) waitv<5>(); else waitv<11>();
    } else {
      waitv<3>();
    }
    PIN(a0); PIN(a1);
    float v[16];
#pragma unroll
    for (int j = 0; j < 4; ++j) { v[2 * j] = bflo(a0[j]); v[2 * j + 1] = bfhi(a0[j]); v[8 + 2 * j] = bflo(a1[j]); v[9 + 2 * j] = bfhi(a1[j]); }
    float s = 0.f;
#pragma unroll
    for (int j = 0; j < 16; ++j) s += v[j];
    const float mean = wave_sum(s) * (1.f / DM);
    float q = 0.f;
#pragma unroll
    for (int j = 0; j < 16; ++j) { const float d = v[j] - mean; q += d * d; }
    const float var = wave_sum(q) * (1.f / DM);
    const float mbf = rbf(mean), rden = __builtin_amdgcn_rcpf(rbf(__builtin_amdgcn_sqrtf(rbf(rbf(var) + 1e-5f))));
    u32x4 o0, o1;
#pragma unroll
    for (int j = 0; j < 8; ++j) {
      const unsigned dd = pk2(v[2 * j] - mbf, v[2 * j + 1] - mbf);
      const unsigned nn = pk2(bflo(dd) * rden, bfhi(dd) * rden);
      if (j < 4) o0[j] = nn; else o1[j - 4] = nn;
    }
    *(LAS u32x4*)(lds + STAGE + (l * 8 + (wid ^ (l & 7))) * 16) = o0;
    *(LAS u32x4*)(lds + STAGE + 8192 + (l * 8 + (wid ^ (l & 7))) * 16) = o1;
    *(LAS f32x2*)(lds + STAGE + 16384 + 32 + wid * 8) = (f32x2){mbf, rden};
    waitbar_l<63>();
    if (wid >= 4) {
#pragma unroll
      for (int hh = 0; hh < 4; ++hh) {
        const int p = hh * 256 + (t - 256), ch = p >> 3, r = 8 * bid + ((p & 7) ^ (ch & 7));
        const u32x4 o = *(LAS const u32x4*)(lds + STAGE + p * 16);
        gst16<0>(o, (unsigned)(((((r >> 5) * 32 + (ch >> 2)) * 2 + ((r >> 4) & 1)) * 64 + (ch & 3) * 16 + (r & 15)) * 16), ws + WS_XN);
      }
      if (t < 256 + 8) gst8(*(LAS const f32x2*)(lds + STAGE + 16384 + 32 + (t - 256) * 8), (unsigned)((8 * bid + t - 256) * 8), ws + WS_ST);
      waitv<4>();
    } else if (fn < 128) waitv<2>(); else waitv<8>();
    PIN(w0); PIN(w1); PIN(ep);
    {
      const unsigned long long tg = (((unsigned long long)(unsigned)__builtin_amdgcn_readfirstlane(ep[1]) << 32) | (unsigned)__builtin_amdgcn_readfirstlane(ep[0])) + 1ull;
      tlo = (unsigned)tg; thi = (unsigned)(tg >> 32);
    }
    float rr = 0.f;
#pragma unroll
    for (int j = 0; j < 8; ++j) {
      const unsigned nn = j < 4 ? o0[j] : o1[j - 4], ww = j < 4 ? w0[j] : w1[j - 4];
      rr += bflo(nn) * bflo(ww); rr += bfhi(nn) * bfhi(ww);
    }
    rr = wave_sum(rr);
    {
      const unsigned bits = pk2(rr, 0.f) & 0xffffu;
      const unsigned ord = (bits & 0x8000u) ? (~bits & 0xffffu) : (bits | 0x8000u);
      *(LAS unsigned*)(lds + STAGE + 16384 + wid * 4) = (ord << 11) | (unsigned)(SEQ - 1 - row);
    }
    if (wid >= 4) waitv<0>();
    waitbar_l<63>();
    if (t == 256) {
      const u32x4 ka = *(LAS const u32x4*)(lds + STAGE + 16384), kb = *(LAS const u32x4*)(lds + STAGE + 16400);
      const unsigned key = umax(umax(umax(ka[0], ka[1]), umax(ka[2], ka[3])), umax(umax(kb[0], kb[1]), umax(kb[2], kb[3])));
      gst8((u32x2){(tlo << 27) | key, (tlo >> 5) | (thi << 27)}, (unsigned)(bid * 8), ws + WS_G0);
    }
    if (wid < 4) {
      if (wid < 2) waitv<1>(); else waitv<0>();
      if (fn < 128) {
        PIN(klo); PIN(khi);
        kvo = (u32x4){klo[0], klo[1], khi[0], khi[1]};
      } else {
#pragma unroll
        for (int j = 0; j < 8; ++j) PIN(e[j]);
        kvo = (u32x4){e[0] | (e[1] << 16), e[2] | (e[3] << 16), e[4] | (e[5] << 16), e[6] | (e[7] << 16)};
      }
      gst16<0>(kvo, (unsigned)((bid * 4 + wid) * 1024 + l * 16), ws + WS_KV);
    }
  }
  const unsigned glo = tlo << 27, ghi = (tlo >> 5) | (thi << 27);

  const char* const wq_b = (const char*)wq + head * 256;
  const unsigned wq_v = (unsigned)(((8 * wid + ((l & 31) >> 2)) * 4096 + (l >> 5) * 32 + ((l & 3) ^ (2 * (wid & 1))) * 8) * 2);
  const char* const kv_b = ws + WS_KV + (size_t)(head >> 3) * 262144;
  const unsigned kv_v = (unsigned)(wid * 4096 + l * 16);
  const char* const xn_b = ws + WS_XN + (size_t)rb * 8 * 65536 + 4096;
  const unsigned xn_v = (unsigned)(wid * 65536 + l * 16);
  const char* const wo_b = (const char*)wo + (size_t)kh * 2048 * (DM * 2) + nb * 256;
  const char* const o_b = ws + WS_O + (size_t)mb * 4 * 262144 + kh * 131072;
  const unsigned ldsw = lds0 + wid * 2048, ldsk = lds0 + wid * 4096, ldsf = lds0 + FB + wid * 1024;
  const int erow = mb * 128 + rbw * 32 + kg * 16 + c;
  bf16x8 xf[XD];
  u32x4 fm[2];
  bf16x8 fxs;
  const unsigned gu_v = (unsigned)((((4 * wid + g) * 2 + ((l >> 3) & 1)) * FF + fj * 16 + (l & 7) * 2) * 8);
  u32x2 fg;
  u32x2 xe[4];
  f32x2 st;
  unsigned key = 0;

  const unsigned fo_v = (unsigned)(((mb * 4 + rbw) * 32 + kh * 16 + c) * 8);
  const int yrow = within < 28 ? fj : 224 + xcd * 4 + within - 28;
  f32x4 o[2][2][2];
  float inv[2];
  const char* const od_b = ws + WS_O + ((size_t)(rb * 8) * 128 + head * 4) * 2048 + 4096;
  auto issue = [&](auto Sl, auto Pos) AI {
    constexpr int sl = Sl, pos = Pos, cnt = SC.count(sl), q0 = SC.first(sl) + (pos < 0 ? 0 : pos);
    constexpr int nq = pos < 0 ? cnt : pos >= cnt ? 0 : pos < 3 ? 1 : cnt - 3;
    sfor<nq>([&](auto Qi) AI {
      constexpr int k = SC.kind[q0 + Qi], i = SC.idx[q0 + Qi];
      if constexpr (k == OX && i < 64) gld16<(i & 7) * 1024 - 4096, true>(xf[i % XD], xn_v, xn_b + (size_t)(i >> 3) * 8192);
      if constexpr (k == OX && i >= 64) {
        constexpr int q = i - 64, v = q >> 2;
        gld16<0, true>(xf[q % XD2], (unsigned)(rbw * 262144 + kg * 16384 + ((q & 1) ^ kg) * 1024 + l * 16), o_b + (size_t)(((v & 3) * 4 + ((q >> 1) & 1)) * 8192 + (v >> 2) * 2048));
      }
      if constexpr (k == OG) {
        constexpr int v = i >> 2, p = i & 3;
        if constexpr (v < 8) {
          const char* sb = wq_b + (size_t)(v * 128 + (p >> 1) * 64) * 8192;
          glds<(p & 1) * 128>(ldsw + (v & 3) * 32768 + (p >> 1) * 16384 + (p & 1) * (1024 - 128), wq_v, sb);
        } else if constexpr (v < 16) {
          glds<p * 1024, true>(ldsk + (v & 3) * 32768, kv_v, kv_b + (size_t)(v - 8) * 32768);
        } else {
          const unsigned wo_v = (unsigned)((((wid >> 2) * 128 + (wid & 3) * 8 + ((l & 31) >> 2)) * DM + (l >> 5) * 32 + ((l & 3) ^ (2 * (wid & 1))) * 8) * 2);
          const char* sb = wo_b + (size_t)(((v & 3) * 4 + (p >> 1) * 2) * 128 + ((v - 16) >> 2) * 32) * (DM * 2) + (((p & 1) ^ kh ^ 1) << 7);
          glds<0>(ldsw + (v & 3) * 32768 + (p >> 1) * 16384 + (p & 1) * 1024, wo_v, sb);
        }
      }
      if constexpr (k == OF && i < 8)
        glds<0>(ldsf + (i & 3) * 8192, (unsigned)(((l >> 3) * FF + wid * 64 + ((l & 7) ^ ((i & 1) * 4 + ((l >> 3) & 2))) * 8) * 2),
                (const char*)(i < 4 ? wgate : wup) + ((size_t)(fsl * 32 + 8 * (i & 3)) * FF + fgr * 512) * 2);
      if constexpr (k == OF && i >= 8) {
        const int frho = l >> 2, fch = (l & 3) ^ (frho & 4 ? 2 : 0);
        glds<0>(ldsf + ((i - 8) % 3) * 8192, (unsigned)((frho * DM + wid * 128 + fch * 8) * 2), (const char*)wdown + (size_t)fj * 16 * DM * 2 + (i - 8) * 64);
      }
      if constexpr (k == OS && i == 0) {
        const int idx = SEQ - 1 - (int)(key & 2047u);
        gld16<0, true>(fxs, (unsigned)((((idx >> 5) * 32 + fsl) * 2 + ((idx >> 4) & 1)) * 1024 + (g * 16 + (idx & 15)) * 16), ws + WS_XN);
      }
      if constexpr (k == OS && i == 2) gld16<0, true>(fm[0], gu_v, ws + WS_GU);
      if constexpr (k == OM && i > 0) gld8<0, true>(fg, fo_v, ws + WS_FO + (i - 1) * 16384);
      if constexpr (k == OM && i == 0) gld8<0, true>(fg, (unsigned)((t & 255) * 8), wid < 4 ? ws + WS_G0 : ws + WS_FK + (head >> 3) * 2048);
      if constexpr (k == OW) {
        constexpr int os = i >> 1, qb = i & 1;
        PIN(o[os & 1][0][qb]); PIN(o[os & 1][1][qb]);
        const f32x4 a = o[os & 1][0][qb] * inv[qb], b = o[os & 1][1][qb] * inv[qb];
        gst16<i * 1024 - 4096>((u32x4){pk2(a[0], a[1]), pk2(a[2], a[3]), pk2(b[0], b[1]), pk2(b[2], b[3])}, (unsigned)(wid * 262144 + l * 16), od_b);
      }
      if constexpr (k == OE && i < 2)
        glds<0>(ldsf + i * 8192, (unsigned)(((erow - c + i * 8 + (l >> 3)) * DM + nb * 128 + kh * 64 + ((l & 7) ^ (i * 4 + (l >> 4))) * 8) * 2), x);
      if constexpr (k == OE && i == 2) gld8<0, true>(st, (unsigned)(erow * 8), ws + WS_ST);
    });
  };
  f32x4 fa0, fa1, fq[4];
  s16x4 fb0, fb1, fxa;
  bf16x8 ft[2];
  auto endslot = [&](auto Sl) AI {
    constexpr int sl = Sl, u = sl >> 3, i = sl & 7;
    if constexpr (sl == 3) {
      if (wid == 6 || wid == 7) touch((unsigned)(((wid - 4) * 256 + within * 8 + (l >> 3)) * 8192 + xcd * 1024 + (l & 7) * 128), wq);
    }
    if constexpr (sl == 2) {
      if (wid < 4 && l == 0) gst8((u32x2){tlo, thi}, (unsigned)((bid * 4 + wid) * 8), ws + WS_FK);
    }
    if constexpr (sl == 38) {
      waitv<SC.younger(OM, 0, sl)>(fg);
      const unsigned lo = wid < 4 ? glo : tlo, hi = wid < 4 ? ghi : thi, mk = wid < 4 ? 0xf8000000u : 0xffffffffu;
      for (int spin = 0; spin < SPINS; ++spin) {
        if (__builtin_expect(__all(((fg[0] & mk) == lo) & (fg[1] == hi)), 1)) break;
        __builtin_amdgcn_s_sleep(1);
        gld8<0, true>(fg, (unsigned)((t & 255) * 8), wid < 4 ? ws + WS_G0 : ws + WS_FK + (head >> 3) * 2048);
        waitv<0>(fg);
      }
      const unsigned km = wave_max_u32(fg[0] & 0x07ffffffu);
      *(LAS unsigned*)(lds + HSC + wid * 4) = km;
    }
    if constexpr (sl == 57) {
      if (wid == 3) {
        const char* pc;
        asm volatile("s_getpc_b64 %0" : "=s"(pc));
        touch((unsigned)((within * 8 + (l & 7)) * 128), pc);
      }
    }
    if constexpr (sl == 47) {
      if (bid == 0 && t == 0) gst8((u32x2){tlo, thi}, 0u, ws + WS_EP);
      const u32x4 kk = *(LAS const u32x4*)(lds + HSC);
      key = __builtin_amdgcn_readfirstlane(umax(umax(kk[0], kk[1]), umax(kk[2], kk[3])));
    }
    if constexpr (sl == 58 || sl == 61 || sl == 64 || sl == 67) {
      if (wid == 4 + (sl - 58) / 3) touch((unsigned)(((wid & 3) * 64 + l) * 128), (const char*)wdown + (size_t)fj * 16 * DM * 2);
    }
    if constexpr (sl == 81 || sl == 85 || sl == 89 || sl == 93) {
      if (wid == (sl - 81) / 4)
        touch((unsigned)((kh * 2048 + (within >> 1) * 128 + wid * 32 + ((within & 1) * 2 + (xcd & 1)) * 8 + (l >> 3)) * (DM * 2) + ((xcd >> 1) & 1) * 1024 + (l & 7) * 128), wo);
    }
    if constexpr (sl == 66) {
      const unsigned fo = (unsigned)(WS_FO - WS_ST) + (l & 3) * 16384 + ((mb * 4 + rbw) * 32 + kh * 16) * 8, fx = (unsigned)(WS_FX - WS_ST) + (bid * 8 + wid) * 64;
      const unsigned fy = (unsigned)(WS_FY - WS_ST) + (wid * 256 + yrow) * 8;
      asm volatile("global_load_dword v255, %0, %1 sc1" ::"v"(l < 4 ? fo : l < 8 ? fy : fx), "s"(ws + WS_ST) : "memory", "v255");
    }
    if constexpr (sl == 97 || sl == 99) {
      if (wid == 4 + (sl - 97) / 2) touch((unsigned)(((mb * 128 + (wid - 4) * 64 + l) * DM + nb * 128 + kh * 64) * 2), x);
    }
    if constexpr ((sl >= 68 && sl <= 70) || (sl >= 90 && sl <= 92)) {
      constexpr int r = sl >= 90, q = sl - (r ? 90 : 68);
      if constexpr (q == 0) waitv<SC.younger(OF, 4 * r + 3, sl)>(fxs);
      if constexpr (q >= 1) {
#pragma unroll
        for (int j = 0; j < 2; ++j) { fq[2 * q - 2 + j] = __builtin_amdgcn_mfma_f32_16x16x32_bf16(fxs, ft[j], (f32x4){}, 0, 0, 0); PIN(fq[2 * q - 2 + j]); }
      }
      if constexpr (q < 2) {
        const unsigned rp = ldsf + g * 8192 + (c >> 2) * 128 + ((((g & 1) << 1) | (c >> 3)) << 5) + (c & 3) * 8;
#pragma unroll
        for (int j = 0; j < 2; ++j) { LAS const char* a = (LAS const char*)(uintptr_t)(rp ^ ((2 * q + j) << 5)); ft[j] = cat8(trd(a), trd(a + 512)); }
      }
    }
    if constexpr (sl == 71 || sl == 93) {
      const float v = sel2(g & 2, sel2(g & 1, fq[3][0], fq[2][0]), sel2(g & 1, fq[1][0], fq[0][0]));
      gst8((u32x2){__float_as_uint(v), tlo}, (unsigned)(((fsl * 2 + (sl == 93)) * FF + fgr * 512 + t) * 8), ws + WS_GU);
    }
    if constexpr (sl == 109) {
      waitv<SC.younger(OS, 2, sl)>(fm[0]);
      for (int spin = 0; spin < SPINS; ++spin) {
        if (__builtin_expect(__all((fm[0][1] == tlo) & (fm[0][3] == tlo)), 1)) break;
        __builtin_amdgcn_s_sleep(1);
        gld16<0, true>(fm[0], gu_v, ws + WS_GU);
        waitv<0>(fm[0]);
      }
      *(LAS f32x2*)(lds + RED1 + (wid * 32 + 2 * c) * 4) = (f32x2){xg_sum(__uint_as_float(fm[0][0])), xg_sum(__uint_as_float(fm[0][2]))};
    }
    if constexpr (sl == 111) {
#pragma unroll
      for (int w = 0; w < 2; ++w) { fa0[w] = *(LAS const float*)(lds + RED1 + ((2 * g + w) * 32 + c) * 4); fa0[2 + w] = *(LAS const float*)(lds + RED1 + ((2 * g + w) * 32 + 16 + c) * 4); }
    }
    if constexpr (sl == 112) { fa0[0] = xg_sum(fa0[0] + fa0[1]); fa0[2] = xg_sum(fa0[2] + fa0[3]); PIN(fa0); }
    if constexpr (sl == 113) {
      const float hh = rbf(silu_f(rbf(fa0[0]))) * rbf(fa0[2]);
      *(LAS u16*)(lds + HSC + 32 + wid * 32 + c * 2) = (u16)pk2(hh, hh);
    }
    if constexpr (sl == 114) fxa = *(LAS const s16x4*)(lds + HSC + 32 + wid * 32 + g * 8);
    constexpr int pr = fpiece(sl), pm = fpiece(sl - 1), ps = fpiece(sl - 2);
    if constexpr (ps >= 0) gst4<ps * 128>(sel2(g & 1, fa1[0], fa0[0]), (unsigned)((wid * 128 + (l & 31)) * 4), ws + WS_YP + (size_t)yrow * 4096);
    if constexpr (pm >= 0) { fa0 = MFMA16(fxa, fb0, (f32x4){}); fa1 = MFMA16(fxa, fb1, (f32x4){}); PIN(fa0); PIN(fa1); }
    if constexpr (pr >= 0) {
      waitv<SC.younger(OF, 8 + pr, sl)>();
      const unsigned fp = ldsf + (4 * g + (c >> 2)) * 64 + (g & 1) * 32 + (c & 3) * 8;
      fb0 = trd((LAS const char*)(uintptr_t)fp + (pr % 3) * 8192); fb1 = trd((LAS const char*)(uintptr_t)(fp ^ 32) + (pr % 3) * 8192);
    }
    if constexpr (sl == 111 || sl == 119 || sl == 127) {
      waitv<SC.younger(OW, (sl - 111) / 4 + 1, sl)>();
      if (l == 0) gst8((u32x2){tlo, thi}, (unsigned)(((rb * 8 + wid) * 32 + head) * 8), ws + WS_FO + ((sl - 111) / 8) * 16384);
    }
    if constexpr (sl == 123) {
      waitv<SC.younger(OM, 1, sl)>(fg);
      if (__builtin_expect(!__all((fg[0] == tlo) & (fg[1] == thi)), 0)) poll8(fo_v, ws + WS_FO, tlo, thi, 0xffffffffu);
    }
    SBAR();
    if constexpr (i == 6) {
      if constexpr (u == 5 || u == 13) waitbar_l<SC.younger(OG, 4 * u + 7, sl)>();
      else waitbar<SC.younger(OG, 4 * u + 7, sl)>();
    }
    if constexpr (!(i & 1)) __builtin_amdgcn_s_setprio(i == 6 ? 3 : 2 - i / 2);
  };

  issue(IC(-2), IC(-1));
  if (wid == 5) poll8((unsigned)((rb * 32 + (l & 31)) * 8), ws + WS_G0, glo, ghi, 0xf8000000u);
  waitbar<SC.younger(OG, 3, -2)>();
  issue(IC(-1), IC(-1));
  LBase trb[2], frb;
  trb[0].init(lds0 + g * 2048 + (c >> 2) * 64 + (g & 1) * 32 + (c & 3) * 8);
  trb[1].init(lds0 + g * 2048 + (c >> 2) * 64 + ((g & 1) ^ 1) * 32 + (c & 3) * 8);
  frb.init(lds0 + l * 16);
  f32x4 acc[8][2];
  bf16x8 wf[2][4];
  auto WRD1 = [&](auto Si, auto Base, auto Dd) AI {
    constexpr int s = Si, d = Dd, o = Base + (d >> 1) * 512;
    wf[s & 1][d] = cat8(trd(trb[d & 1].template at<o>()), trd(trb[d & 1].template at<o + 256>()));
  };
  auto WRD = [&](auto Si, auto Base) AI { sfor<4>([&](auto Dd) AI { WRD1(Si, Base, Dd); }); };
#define WA(s) IC((((s) >> 3) & 3) * 32768 + (((s) >> 1) & 3) * 8192 + ((s) & 1) * 1024)
#define WB(s) IC((((s) >> 2) & 3) * 32768 + (((s) >> 1) & 1) * 8192 + ((s) & 1) * 1024)
  bf16x8 fr[4];
  auto FRD = [&](auto Ni) AI { constexpr int n = Ni; if constexpr (n < 256) fr[n & 3] = *(LAS const bf16x8*)(frb.at<((n >> 5) & 3) * 32768 + (n & 31) * 1024>()); };

  WRD(IC(0), WA(0));
  SBAR();
  bf16x8 qf[4][2];
  auto QPACK = [&](auto Ds) AI {
    constexpr int ds = Ds;
#pragma unroll
    for (int qb = 0; qb < 2; ++qb) {
      const f32x4 a = acc[2 * ds][qb] * QSCALE, b = acc[2 * ds + 1][qb] * QSCALE;
      qf[ds][qb] = __builtin_bit_cast(bf16x8, (u32x4){pk2(a[0], a[1]), pk2(a[2], a[3]), pk2(b[0], b[1]), pk2(b[2], b[3])});
      PIN(qf[ds][qb]);
    }
  };
  sfor<64>([&](auto Si) AI {
    constexpr int s = Si, x0 = (s & ~1) % XD, x1 = (s | 1) % XD;
    if constexpr (!(s & 1)) waitv<SC.younger(OX, s + 1, s - 1)>(xf[x0], xf[x1]);
    sfor<4>([&](auto Dd) AI {
      constexpr int d = Dd, dt = (s & 1) * 4 + d;
      if constexpr (s + 1 < 64) WRD1(IC(s + 1), WA(s + 1), Dd);
      else if constexpr (d < 3) FRD(Dd);
      SBAR();
      if constexpr (s < 2) { MFZ(acc[dt][0], wf[s & 1][d], xf[x0]); MFZ(acc[dt][1], wf[s & 1][d], xf[x1]); }
      else { MF(acc[dt][0], wf[s & 1][d], xf[x0]); MF(acc[dt][1], wf[s & 1][d], xf[x1]); }
      issue(Si, Dd);
      if constexpr (s == 63 && (d & 1)) { QPACK(IC(d >> 1)); SBAR(); }
    });
    SBAR();
    endslot(Si);
  });

  f32x4 sc[2][2][2], negm[2];
  u32x4 pw[16][2];
  f32x4 lsum[2];
  bf16x8 ones = {0x3f80, 0x3f80, 0x3f80, 0x3f80, 0x3f80, 0x3f80, 0x3f80, 0x3f80};
  PIN(ones);
#define MX3(a, b, c) __builtin_fmaxf(__builtin_fmaxf((a), (b)), (c))
#define EXP2(x) __builtin_amdgcn_exp2f(x)
  auto SM = [&](auto Hi, auto Pi) AI {
    constexpr int H = Hi, p = Pi, kt = p >> 2, qb = (p >> 1) & 1, r = 2 * (p & 1);
    const float e0 = EXP2(sc[H & 1][kt][qb][r]), e1 = EXP2(sc[H & 1][kt][qb][r + 1]);
    unsigned w = pk2(e0, e1);
    PIN(w);
    pw[H][qb][kt * 2 + (p & 1)] = w;
  };
  sfor<128>([&](auto Ni) AI {
    constexpr int n = Ni, H = n >> 3, i = n & 7, kt = i >> 2, ds = i & 3, sn = H & 1;
    FRD(IC(n + 3));
    if constexpr (n < 2) QPACK(IC(2 + n));
    sfor<2>([&](auto Qb) AI {
      constexpr int qb = Qb;
      if constexpr (ds == 0 && H == 0) MFZ(sc[sn][kt][qb], fr[n & 3], qf[0][qb]);
      else if constexpr (ds == 0) MFC(sc[sn][kt][qb], fr[n & 3], qf[0][qb], negm[qb]);
      else MF(sc[sn][kt][qb], fr[n & 3], qf[ds][qb]);
    });
    if constexpr (n == 5 || n == 6) {
      constexpr int qb = n - 5;
      NOPS(3); PIN(sc[0][0][qb]);
      const float m = -xg_max(__builtin_fmaxf(MX3(sc[0][0][qb][0], sc[0][0][qb][1], sc[0][0][qb][2]), sc[0][0][qb][3]));
      negm[qb] = (f32x4){m, m, m, m};
      sc[0][0][qb] += negm[qb];
      PIN(negm[qb]); PIN(sc[0][0][qb]);
    }
    if constexpr (n == 9 || n == 10) { NOPS(3); PIN(sc[0][1][n - 9]); sc[0][1][n - 9] += negm[n - 9]; PIN(sc[0][1][n - 9]); }
    issue(IC(64 + (n >> 2)), IC(n & 3));
    if constexpr (H == 2 && (i == 1 || i == 5)) MFZ(lsum[i >> 2], ones, pw[0][i >> 2]);
    if constexpr (H > 2 && (i == 1 || i == 5)) MF(lsum[i >> 2], ones, pw[H - 2][i >> 2]);
    if constexpr (H >= 1 && H < 15) SM(IC(H - 1), IC(i));
    if constexpr (n == 125) { PIN(negm[0]); PIN(negm[1]); }
    SBAR();
    if constexpr ((n & 3) == 3) endslot(IC(64 + (n >> 2)));
  });
  constexpr int CHK = 32;
  sfor<128>([&](auto Mi) AI {
    constexpr int m = Mi, n = 128 + m, os = m >> 5, H = (m >> 1) & 15, par = m & 1;
    FRD(IC(n + 3));
    if constexpr (H == 0) { MFZ(o[os & 1][par][0], fr[n & 3], pw[H][0]); MFZ(o[os & 1][par][1], fr[n & 3], pw[H][1]); }
    else { MF(o[os & 1][par][0], fr[n & 3], pw[H][0]); MF(o[os & 1][par][1], fr[n & 3], pw[H][1]); }
    if constexpr (m < 26 && (m % 13) % 5 != 1 && (m % 13) % 5 != 3) {
      constexpr int r = m % 13, p = r / 5 * 3 + (r % 5) / 2;
      SM(IC(14 + m / 13), IC(p));
    }
    if constexpr (m == 14 || m == 16) MF(lsum[(m - 14) / 2], ones, pw[14][(m - 14) / 2]);
    if constexpr (m == 27 || m == 28) MF(lsum[m - 27], ones, pw[15][m - 27]);
    if constexpr (m == CHK + 1 || m == CHK + 2) { inv[m - CHK - 1] = __builtin_amdgcn_rcpf(lsum[m - CHK - 1][0]); PIN(inv[m - CHK - 1]); }
    issue(IC(64 + (n >> 2)), IC(n & 3));
    if constexpr (m == 123) {
      trb[0].init(lds0 + kg * 16384 + g * 2048 + (c >> 2) * 64 + (g & 1) * 32 + (c & 3) * 8);
      trb[1].init(lds0 + kg * 16384 + g * 2048 + (c >> 2) * 64 + ((g & 1) ^ 1) * 32 + (c & 3) * 8);
    }
    if constexpr (m >= 124) WRD1(IC(0), WB(0), IC(m - 124));
    SBAR();
    if constexpr ((n & 3) == 3) endslot(IC(64 + (n >> 2)));
    if constexpr (m == CHK - 1) {
      PIN(lsum[0]); PIN(lsum[1]);
      if (__builtin_expect(__any(!(lsum[0][0] + lsum[1][0] <= LBOUND)), 0)) {
        NOPS(7); SBAR();
        auto tile = [&](unsigned off, f32x4& s0, f32x4& s1) AI {
          bf16x8 kf[4];
          SBAR(); NOPS(7);
          sfor<4>([&](auto Ds) AI { constexpr int ds = Ds; gld16<ds * 1024, true>(kf[ds], off, kv_b); });
          waitv<0>(kf[0], kf[1]);
          PIN(kf[2]); PIN(kf[3]);
#pragma unroll
          for (int ds = 0; ds < 4; ++ds) {
            s0 = __builtin_amdgcn_mfma_f32_16x16x32_bf16(kf[ds], qf[ds][0], s0, 0, 0, 0);
            s1 = __builtin_amdgcn_mfma_f32_16x16x32_bf16(kf[ds], qf[ds][1], s1, 0, 0, 0);
          }
        };
        float m0 = -__builtin_inff(), m1 = m0;
#pragma unroll 1
        for (int kt = 0; kt < 32; ++kt) {
          f32x4 s0 = {}, s1 = {};
          tile((unsigned)(kt * 4096 + l * 16), s0, s1);
          m0 = MX3(MX3(m0, s0[0], s0[1]), s0[2], s0[3]); m1 = MX3(MX3(m1, s1[0], s1[1]), s1[2], s1[3]);
        }
        m0 = -xg_max(m0); m1 = -xg_max(m1);
        lsum[0] = (f32x4){}; lsum[1] = (f32x4){};
        sfor<16>([&](auto Hi) AI {
          constexpr int H = Hi;
#pragma unroll
          for (int kt = 0; kt < 2; ++kt) {
            sc[H & 1][kt][0] = (f32x4){m0, m0, m0, m0}; sc[H & 1][kt][1] = (f32x4){m1, m1, m1, m1};
            tile((unsigned)((2 * H + kt) * 4096 + l * 16), sc[H & 1][kt][0], sc[H & 1][kt][1]);
          }
          sfor<8>([&](auto Pi) AI { SM(Hi, Pi); });
          PIN(pw[H][0]); PIN(pw[H][1]);
          lsum[0] = __builtin_amdgcn_mfma_f32_16x16x32_bf16(ones, __builtin_bit_cast(bf16x8, pw[H][0]), lsum[0], 0, 0, 0);
          lsum[1] = __builtin_amdgcn_mfma_f32_16x16x32_bf16(ones, __builtin_bit_cast(bf16x8, pw[H][1]), lsum[1], 0, 0, 0);
        });
#pragma unroll
        for (int par = 0; par < 2; ++par) { o[0][par][0] = (f32x4){}; o[0][par][1] = (f32x4){}; }
        sfor<CHK / 4>([&](auto Qi) AI {
          constexpr int q = Qi;
          bf16x8 vf[4];
          SBAR(); NOPS(7);
          sfor<4>([&](auto J) AI { constexpr int j = J; gld16<j * 1024, true>(vf[j], (unsigned)((128 + 4 * q) * 1024 + l * 16), kv_b); });
          waitv<0>(vf[0], vf[1]);
          PIN(vf[2]); PIN(vf[3]);
#pragma unroll
          for (int j = 0; j < 4; ++j)
#pragma unroll
            for (int qb = 0; qb < 2; ++qb)
              o[0][j & 1][qb] = __builtin_amdgcn_mfma_f32_16x16x32_bf16(vf[j], __builtin_bit_cast(bf16x8, pw[2 * q + (j >> 1)][qb]), o[0][j & 1][qb], 0, 0, 0);
        });
#pragma unroll
        for (int par = 0; par < 2; ++par) { PIN(o[0][par][0]); PIN(o[0][par][1]); }
        PIN(lsum[0]); PIN(lsum[1]);
        SBAR(); NOPS(7);
      }
      SBAR();
    }
  });
  const int idx = SEQ - 1 - (int)(key & 2047u);
  const bool owner = (idx >> 7) == mb, ownw = (idx >> 4) == mb * 8 + rbw * 2 + kg;
  f32x4 yp[7], ya[4], oth[4];
  unsigned gate[8];
  float glow;
  const unsigned ex_a = ldsf + (c >> 3) * 8192 + (c & 7) * 128 + (((g >> 1) ^ (c >> 1)) << 4) + (g & 1) * 8;
  auto item = [](int s) { return s < 48 ? s : s < 56 ? 48 + 2 * (s - 48) : 49 + 2 * (s - 56); };
  u32x2 pf;
  u32x4 otp[2];
  bool rdy = false;
  sfor<64>([&](auto Si) AI {
    constexpr int s = Si, u = s >> 2, it = item(s), x0 = (it & ~1) % XD2, x1 = (it | 1) % XD2;
    if constexpr (!(it & 1)) waitv<SC.younger(OX, 64 + it + 1, B0 + s - 1)>(xf[x0], xf[x1]);
    sfor<4>([&](auto Dd) AI {
      constexpr int d = Dd, dt = (it & 1) * 4 + d;
      if constexpr (s + 1 < 64) WRD1(IC(s + 1), WB(item(s + 1)), Dd);
      SBAR();
      if constexpr (s < 2) { MFZ(acc[dt][0], wf[s & 1][d], xf[x0]); MFZ(acc[dt][1], wf[s & 1][d], xf[x1]); }
      else { MF(acc[dt][0], wf[s & 1][d], xf[x0]); MF(acc[dt][1], wf[s & 1][d], xf[x1]); }
      issue(IC(B0 + s), Dd);
      if constexpr (s == 56) *(LAS f32x4*)(lds + (wid ^ 4) * 4096 + l * 16 + (d & 1) * 2048 + (d >> 1) * 32768) = acc[d][1];
      if constexpr (s == 57 && d == 0) {
#pragma unroll
        for (int e = 0; e < 4; ++e) oth[e] = *(LAS const f32x4*)(lds + wid * 4096 + l * 16 + (e & 1) * 2048 + (e >> 1) * 32768);
      }
      if constexpr (s == 57 && d >= 2) {
        const f32x4 a = acc[2 * d - 4][0] + oth[2 * d - 4], b = acc[2 * d - 3][0] + oth[2 * d - 3];
        gst16<(d - 2) * 1024>((u32x4){pk2(a[0], a[1]), pk2(a[2], a[3]), pk2(b[0], b[1]), pk2(b[2], b[3])}, (unsigned)(wid * 2048 + l * 16), ws + WS_XC + (size_t)(bid ^ 4) * 16384);
      }
      if constexpr (s == 34) xe[d] = *(LAS const u32x2*)(uintptr_t)(ex_a ^ (d << 5));
      if constexpr (s >= 36 && s < 44 && d == 1) {
        constexpr int j = s - 36;
        PIN(st);
        const unsigned xw = xe[j >> 1][j & 1];
        const unsigned dd = pk2(bflo(xw) - st[0], bfhi(xw) - st[0]);
        gate[j] = pk2(bflo(dd) * st[1], bfhi(dd) * st[1]);
        PIN(gate[j]);
      }
      if constexpr (s >= 47 && s < 63 && d == 1) {
        constexpr int j = (s - 47) >> 1;
        if constexpr ((s - 47) & 1) { gate[j] = pk2(glow, silu_f(bfhi(gate[j]))); PIN(gate[j]); }
        else { glow = silu_f(bflo(gate[j])); PIN(glow); }
      }
    });
    SBAR();
    if constexpr (s == 7) {
      waitv<SC.younger(OW, 7, B0 + s)>();
      if (l == 0) {
        gst8((u32x2){tlo, thi}, (unsigned)(((rb * 8 + wid) * 32 + head) * 8), ws + WS_FO + 3 * 16384);
        gst8((u32x2){tlo, thi}, (unsigned)((wid * 256 + yrow) * 8), ws + WS_FY);
      }
    }
    if constexpr (s == 4 || s == 17 || s == 33) {
      constexpr int j = s == 4 ? 1 : s == 17 ? 2 : 3;
      waitv<SC.younger(OM, j + 1, B0 + s)>(fg);
      if (__builtin_expect(!__all((fg[0] == tlo) & (fg[1] == thi)), 0)) poll8(fo_v, ws + WS_FO + j * 16384, tlo, thi, 0xffffffffu);
    }
    if constexpr (s == 59) gld8<0, true>(pf, (unsigned)((bid * 8 + wid) * 64), ws + WS_FX);
    if constexpr (s == 60) {
      waitv<1>();
      if (l == 0) gst8((u32x2){tlo, thi}, (unsigned)(((bid ^ 4) * 8 + wid) * 64), ws + WS_FX);
    }
    if constexpr (s == 62) {
      waitv<1>(pf);
      rdy = __all((pf[0] == tlo) & (pf[1] == thi));
      if (rdy) sfor<2>([&](auto Dd) AI { constexpr int d = Dd; gld16<d * 1024, true>(otp[d], (unsigned)(wid * 2048 + l * 16), ws + WS_XC + (size_t)bid * 16384); });
    }
    if constexpr (s == 13) {
      if (owner) { gld16<0, true>(fm[0], (unsigned)(nb * 2048 + (l < 56 ? l : 55) * 32), ws + WS_FY); gld16<16, true>(fm[1], (unsigned)(nb * 2048 + (l < 56 ? l : 55) * 32), ws + WS_FY); }
    }
    if constexpr (s == 28) {
      if (owner) {
        PIN(fm[0]); PIN(fm[1]);
        for (int spin = 0; spin < SPINS; ++spin) {
          const bool ok = (fm[0][0] == tlo) & (fm[0][1] == thi) & (fm[0][2] == tlo) & (fm[0][3] == thi) & (fm[1][0] == tlo) & (fm[1][1] == thi) & (fm[1][2] == tlo) & (fm[1][3] == thi);
          if (__builtin_expect(__all(ok), 1)) break;
          __builtin_amdgcn_s_sleep(1);
          gld16<0, true>(fm[0], (unsigned)(nb * 2048 + (l < 56 ? l : 55) * 32), ws + WS_FY); gld16<16, true>(fm[1], (unsigned)(nb * 2048 + (l < 56 ? l : 55) * 32), ws + WS_FY);
          waitv<0>(fm[0], fm[1]);
        }
        const unsigned yv = (unsigned)(((t >> 4) * 1024 + nb * 128 + kh * 64 + (t & 15) * 4) * 4);
        sfor<7>([&](auto I) AI { constexpr int q = I; gld16<0, true>(yp[q], yv, ws + WS_YP + (size_t)q * 32 * 4096); });
      }
    }
    if constexpr (s == 43) {
      if (owner) {
        f32x4 a = {};
#pragma unroll
        for (int q = 0; q < 7; ++q) { PIN(yp[q]); a += yp[q]; }
#pragma unroll
        for (int j = 0; j < 4; ++j) a[j] = xg_sum(a[j]);
        *(LAS f32x4*)(lds + YS + (wid * 64 + c * 4) * 4) = a;
      }
    }
    if constexpr (s == 45) {
      if (ownw) {
        float a = 0.f;
#pragma unroll
        for (int p = 0; p < 8; ++p) a += *(LAS const float*)(lds + YS + (p * 64 + l) * 4);
        const unsigned ord = key >> 11, bits = (ord & 0x8000u) ? (ord & 0x7fffu) : (~ord & 0xffffu);
        *(LAS float*)(lds + RED1 + l * 4) = rbf(rbf(a) * rbf(sigmoid_f(bflo(bits))));
#pragma unroll
        for (int q = 0; q < 4; ++q) ya[q] = *(LAS const f32x4*)(lds + RED1 + (q * 16 + 4 * g) * 4);
      }
    }
    if constexpr (s == 46) {
      if (ownw) {
#pragma unroll
        for (int j = 0; j < 8; ++j) {
          const unsigned w = pk2(bflo(gate[j]) + ya[j >> 1][2 * (j & 1)], bfhi(gate[j]) + ya[j >> 1][2 * (j & 1) + 1]);
          gate[j] = erow == idx ? w : gate[j];
          PIN(gate[j]);
        }
      }
    }
    SBAR();
    if constexpr (((s & 3) == 2 && u < 11) || s == 44) {
      if constexpr (s == 44) waitbar_l<SC.younger(OG, 64 + 4 * u + 7, B0 + s)>();
      else waitbar<SC.younger(OG, 64 + 4 * u + 7, B0 + s)>();
    }
    if constexpr (s == 48) waitbar<SC.younger(OG, 64 + 55, B0 + s)>();
    if constexpr (s == 50) waitbar<SC.younger(OG, 64 + 59, B0 + s)>();
    if constexpr (s == 52) { waitbar<SC.younger(OG, 64 + 63, B0 + s)>(); }
    if constexpr (s == 56) waitbar_l<63>();
    __builtin_amdgcn_s_setprio(3 - ((s + 2) & 3));
  });
  f32x4 kep[4];
  {
    NOPS(7);
#pragma unroll
    for (int d = 0; d < 4; ++d) { PIN(acc[4 + d][1]); *(LAS f32x4*)(lds + 65536 + (wid ^ 4) * 4096 + l * 16 + (d & 1) * 2048 + (d >> 1) * 32768) = acc[4 + d][1]; }
    if (!rdy) {
      poll8((unsigned)((bid * 8 + wid) * 64), ws + WS_FX, tlo, thi, 0xffffffffu);
      sfor<2>([&](auto Dd) AI { constexpr int d = Dd; gld16<d * 1024, true>(otp[d], (unsigned)(wid * 2048 + l * 16), ws + WS_XC + (size_t)bid * 16384); });
    }
    waitbar_l<63>();
#pragma unroll
    for (int d = 0; d < 4; ++d) kep[d] = acc[4 + d][0] + *(LAS const f32x4*)(lds + 65536 + wid * 4096 + l * 16 + (d & 1) * 2048 + (d >> 1) * 32768);
    waitv<0>(otp[0], otp[1]);
  }
#pragma unroll
  for (int d = 0; d < 4; ++d) {
    const unsigned o0 = otp[d >> 1][2 * (d & 1)], o1 = otp[d >> 1][2 * (d & 1) + 1];
    const f32x4 a = kep[d] + (f32x4){bflo(o0), bfhi(o0), bflo(o1), bfhi(o1)};
    u32x2 res;
#pragma unroll
    for (int j = 0; j < 2; ++j) {
      const unsigned sp = pk2(a[2 * j], a[2 * j + 1]), gp = gate[2 * d + j], xw = xe[d][j];
      const unsigned pr = pk2(bflo(sp) * bflo(gp), bfhi(sp) * bfhi(gp));
      res[j] = pk2(bflo(xw) + bflo(pr), bfhi(xw) + bfhi(pr));
    }
    *(LAS u32x2*)(uintptr_t)(ex_a ^ (d << 5)) = res;
  }
#pragma unroll
  for (int i = 0; i < 2; ++i)
    gst16<0>(*(LAS const u32x4*)(uintptr_t)(ldsf + i * 8192 + l * 16), (unsigned)(((erow - c + i * 8 + (l >> 3)) * DM + nb * 128 + kh * 64 + ((l & 7) ^ (i * 4 + (l >> 4))) * 8) * 2), out);
}

extern "C" void kernel_launch(void* const* d_in, const int* in_sizes, int n_in, void* d_out, int out_size, void* d_ws, size_t ws_size,
                              hipStream_t stream) {
  k_fused<<<256, 512, 0, stream>>>((const u16*)d_in[0], (const u16*)d_in[1], (const u16*)d_in[2], (const u16*)d_in[3], (const u16*)d_in[4],
                                   (const u16*)d_in[6], (const u16*)d_in[7], (const u16*)d_in[8], (const u16*)d_in[9], (char*)d_ws, (u16*)d_out);
}
```

```cpp
#include <hip/hip_runtime.h>
#include <stdint.h>
#include <type_traits>
#pragma clang diagnostic ignored "-Winline-asm"

typedef unsigned short u16;
typedef short bf16x8 __attribute__((ext_vector_type(8)));
typedef short s16x4 __attribute__((ext_vector_type(4)));
typedef float f32x4 __attribute__((ext_vector_type(4)));
typedef float f32x2 __attribute__((ext_vector_type(2)));
typedef unsigned u32x4 __attribute__((ext_vector_type(4)));
typedef unsigned u32x2 __attribute__((ext_vector_type(2)));
typedef __bf16 bf2_t __attribute__((ext_vector_type(2)));
#define LAS __attribute__((address_space(3)))
#define DI __device__ __forceinline__
#define AI __attribute__((always_inline))
#define SBAR() __builtin_amdgcn_sched_barrier(0)
template <class T> __device__ __forceinline__ void PIN(T& x) { asm volatile("" : "+v"(x)); }
template <class B> DI void MF(f32x4& acc, const bf16x8& a, const B& b) { asm volatile("v_mfma_f32_16x16x32_bf16 %0, %1, %2, %0" : "+v"(acc) : "v"(a), "v"(b)); }
template <class B> DI void MFC(f32x4& d, const bf16x8& a, const B& b, const f32x4& c) { asm volatile("v_mfma_f32_16x16x32_bf16 %0, %1, %2, %3" : "=&v"(d) : "v"(a), "v"(b), "v"(c)); }
template <class B> DI void MFZ(f32x4& d, const bf16x8& a, const B& b) { asm volatile("v_mfma_f32_16x16x32_bf16 %0, %1, %2, 0" : "=&v"(d) : "v"(a), "v"(b)); }
#define NOPS(n) asm volatile("s_nop " #n)
#define MFMA16(a, b, c) __builtin_amdgcn_mfma_f32_16x16x16bf16_1k((a), (b), (c), 0, 0, 0)
#define IC(n) std::integral_constant<int, (n)>{}

constexpr int SEQ = 2048, DM = 1024, DH = 128, SK = 2048, WIN = 512, FF = 3584;
constexpr size_t WS_XN = 0;
constexpr size_t WS_KV = 4u << 20;
constexpr size_t WS_ST = 5u << 20;
constexpr size_t WS_EP = WS_ST + 16384;
constexpr size_t WS_G0 = WS_ST + 32768;
constexpr size_t WS_FK = WS_ST + 49152;
constexpr size_t WS_YP = WS_ST + 65536;
constexpr size_t WS_FO = 7u << 20;
constexpr size_t WS_FY = WS_FO + 65536;
constexpr size_t WS_FX = WS_FO + 131072;
constexpr size_t WS_O = 8u << 20;
constexpr size_t WS_GU = 32u << 20;
constexpr size_t WS_XC = 24u << 20;

template <int I, int N, class F> DI void sfor_impl(F&& f) {
  if constexpr (I < N) { f(std::integral_constant<int, I>{}); sfor_impl<I + 1, N>(f); }
}
template <int N, class F> DI void sfor(F&& f) { sfor_impl<0, N>(f); }

DI unsigned pk2(float a, float b) { f32x2 v = {a, b}; return __builtin_bit_cast(unsigned, __builtin_convertvector(v, bf2_t)); }
DI float rbf(float a) { return (float)(__bf16)a; }
DI float bflo(unsigned u) { return __uint_as_float(u << 16); }
DI float bfhi(unsigned u) { return __uint_as_float(u & 0xffff0000u); }
DI float sigmoid_f(float v) { return __builtin_amdgcn_rcpf(1.f + __builtin_amdgcn_exp2f(v * -1.4426950408889634f)); }
DI float sel2(bool c, float a, float b) { PIN(a); PIN(b); return c ? a : b; }
DI float silu_f(float v) { return v * sigmoid_f(v); }
template <int CTRL> DI float dppf(float x) { return __builtin_bit_cast(float, __builtin_amdgcn_mov_dpp(__builtin_bit_cast(int, x), CTRL, 0xf, 0xf, true)); }
template <int CTRL> DI unsigned dppu(unsigned x) { return (unsigned)__builtin_amdgcn_mov_dpp((int)x, CTRL, 0xf, 0xf, true); }
constexpr int XOR1 = 0xB1, XOR2 = 0x4E, XOR7 = 0x141, XOR8 = 0x128;
DI float xg_sum(float x) {
  auto s = __builtin_amdgcn_permlane16_swap(__float_as_uint(x), __float_as_uint(x), false, false);
  x = __uint_as_float(s[0]) + __uint_as_float(s[1]);
  auto t = __builtin_amdgcn_permlane32_swap(__float_as_uint(x), __float_as_uint(x), false, false);
  return __uint_as_float(t[0]) + __uint_as_float(t[1]);
}
DI float xg_max(float x) {
  auto s = __builtin_amdgcn_permlane16_swap(__float_as_uint(x), __float_as_uint(x), false, false);
  x = __builtin_fmaxf(__uint_as_float(s[0]), __uint_as_float(s[1]));
  auto t = __builtin_amdgcn_permlane32_swap(__float_as_uint(x), __float_as_uint(x), false, false);
  return __builtin_fmaxf(__uint_as_float(t[0]), __uint_as_float(t[1]));
}
DI float wave_sum(float v) { v += dppf<XOR1>(v); v += dppf<XOR2>(v); v += dppf<XOR7>(v); v += dppf<XOR8>(v); return xg_sum(v); }
DI unsigned umax(unsigned a, unsigned b) { return a > b ? a : b; }
DI unsigned wave_max_u32(unsigned v) {
  v = umax(v, dppu<XOR1>(v)); v = umax(v, dppu<XOR2>(v)); v = umax(v, dppu<XOR7>(v)); v = umax(v, dppu<XOR8>(v));
  auto s = __builtin_amdgcn_permlane16_swap(v, v, false, false); v = umax(s[0], s[1]);
  auto t = __builtin_amdgcn_permlane32_swap(v, v, false, false); v = umax(t[0], t[1]);
  return __builtin_amdgcn_readfirstlane(v);
}

template <int OFF, bool DEV = false> DI void glds(unsigned ldsaddr, unsigned voff, const void* sbase) {
  if constexpr (DEV) asm volatile("s_mov_b32 m0, %0\n\ts_nop 0\n\tglobal_load_lds_dwordx4 %1, %2 offset:%3 sc1" ::"s"(ldsaddr), "v"(voff), "s"(sbase), "i"(OFF) : "memory");
  else asm volatile("s_mov_b32 m0, %0\n\ts_nop 0\n\tglobal_load_lds_dwordx4 %1, %2 offset:%3" ::"s"(ldsaddr), "v"(voff), "s"(sbase), "i"(OFF) : "memory");
}
DI void touch(unsigned voff, const void* sbase) {
  asm volatile("global_load_dword v255, %0, %1" ::"v"(voff), "s"(sbase) : "memory", "v255");
}
template <int OFF, bool DEV = false, class T> DI void gld16(T& dst, unsigned voff, const void* sbase) {
  if constexpr (DEV) asm volatile("global_load_dwordx4 %0, %1, %2 offset:%3 sc1" : "=v"(dst) : "v"(voff), "s"(sbase), "i"(OFF) : "memory");
  else asm volatile("global_load_dwordx4 %0, %1, %2 offset:%3" : "=v"(dst) : "v"(voff), "s"(sbase), "i"(OFF) : "memory");
}
template <int OFF, bool DEV = false, class T> DI void gld8(T& dst, unsigned voff, const void* sbase) {
  if constexpr (DEV) asm volatile("global_load_dwordx2 %0, %1, %2 offset:%3 sc1" : "=v"(dst) : "v"(voff), "s"(sbase), "i"(OFF) : "memory");
  else asm volatile("global_load_dwordx2 %0, %1, %2 offset:%3" : "=v"(dst) : "v"(voff), "s"(sbase), "i"(OFF) : "memory");
}
template <int OFF> DI void gld2(unsigned& dst, unsigned voff, const void* sbase) {
  asm volatile("global_load_ushort %0, %1, %2 offset:%3" : "=v"(dst) : "v"(voff), "s"(sbase), "i"(OFF) : "memory");
}
template <int OFF, class T> DI void gst16(const T& v, unsigned voff, const void* sbase) {
  asm volatile("global_store_dwordx4 %0, %1, %2 offset:%3 sc1\n\ts_nop 1" ::"v"(voff), "v"(v), "s"(sbase), "i"(OFF) : "memory");
}
template <class T> DI void gst8(const T& v, unsigned voff, const void* sbase) {
  asm volatile("global_store_dwordx2 %0, %1, %2 sc1" ::"v"(voff), "v"(v), "s"(sbase) : "memory");
}
template <int OFF> DI void gst4(float v, unsigned voff, const void* sbase) {
  asm volatile("global_store_dword %0, %1, %2 offset:%3 sc1" ::"v"(voff), "v"(v), "s"(sbase), "i"(OFF) : "memory");
}
constexpr int SPINS = 1 << 16;
DI void poll8(unsigned voff, const void* sbase, unsigned lo, unsigned hi, unsigned lomask) {
  for (int spin = 0; spin < SPINS; ++spin) {
    u32x2 v;
    asm volatile("global_load_dwordx2 %0, %1, %2 sc1\n\ts_waitcnt vmcnt(0)" : "=&v"(v) : "v"(voff), "s"(sbase) : "memory");
    if (__all(((v[0] & lomask) == lo) & (v[1] == hi))) break;
    __builtin_amdgcn_s_sleep(1);
  }
}
template <int N> DI void waitv() { static_assert(N >= 0, "count"); asm volatile("s_waitcnt vmcnt(%0)" ::"n"(N > 63 ? 63 : N) : "memory"); }
template <int N, class T> DI void waitv(T& x) { static_assert(N >= 0, "count"); asm volatile("s_waitcnt vmcnt(%1)" : "+v"(x) : "n"(N > 63 ? 63 : N) : "memory"); }
template <int N, class T> DI void waitv(T& x, T& y) { static_assert(N >= 0, "count"); asm volatile("s_waitcnt vmcnt(%2)" : "+v"(x), "+v"(y) : "n"(N > 63 ? 63 : N) : "memory"); }
template <int N> DI void waitbar() { static_assert(N >= 0, "count"); asm volatile("s_waitcnt vmcnt(%0)\n\ts_barrier" ::"n"(N > 63 ? 63 : N) : "memory"); }
template <int N> DI void waitbar_l() { static_assert(N >= 0, "count"); asm volatile("s_waitcnt vmcnt(%0) lgkmcnt(0)\n\ts_barrier" ::"n"(N > 63 ? 63 : N) : "memory"); }
typedef short v4i16_t __attribute__((ext_vector_type(4)));
DI s16x4 trd(LAS const char* p) { return __builtin_amdgcn_ds_read_tr16_b64_v4i16((LAS v4i16_t*)p); }
struct LBase {
  unsigned lo, hi;
  DI void init(unsigned a) { lo = a; hi = a + 65536; PIN(lo); PIN(hi); }
  template <int OFF> DI LAS const char* at() const { return (LAS const char*)(uintptr_t)(OFF < 65536 ? lo : hi) + (OFF & 65535); }
};
DI bf16x8 cat8(s16x4 lo, s16x4 hi) { return __builtin_shufflevector(lo, hi, 0, 1, 2, 3, 4, 5, 6, 7); }

enum { OX, OG, OF, OM, OS, OP, OE, OW };
struct Sched {
  int n; short kind[400], idx[400], slot[400];
  constexpr Sched() : n(0), kind{}, idx{}, slot{} {}
  constexpr void add(int s, int k, int i) { kind[n] = (short)k; idx[n] = (short)i; slot[n] = (short)s; ++n; }
  constexpr int younger(int k, int i, int s) const {
    int p = -1;
    for (int q = 0; q < n; ++q) if (kind[q] == k && idx[q] == i) p = q;
    if (p < 0 || slot[p] > s) return -1;
    int c = 0;
    for (int q = p + 1; q < n; ++q) if (slot[q] <= s) ++c;
    return c;
  }
  constexpr int first(int s) const { int q = 0; while (q < n && slot[q] < s) ++q; return q; }
  constexpr int count(int s) const { int c = 0; for (int q = 0; q < n; ++q) if (slot[q] == s) ++c; return c; }
};


constexpr int RING = 4 * 32768, FB = RING, RED1 = FB + 24576, HSC = RED1 + 1024, YS = HSC + 1024, LDS_BYTES = FB + 32768;
constexpr int STAGE = 3 * 32768;
constexpr int XD = 16, XD2 = 16;
constexpr float LBOUND = 65536.f;
constexpr float QSCALE = 0.08838834764831845f * 1.4426950408889634f;
constexpr int B0 = 128;
constexpr int FISS[4] = {96, 97, 98, 117}, FRDS[4] = {115, 118, 121, 124};
constexpr int fpiece(int sl) { for (int p = 0; p < 4; ++p) if (FRDS[p] == sl) return p; return -1; }
constexpr Sched make_sched() {
  Sched s;
  constexpr int XP = 8;
  for (int i = 0; i < 12; ++i) s.add(-2, OG, i);
  for (int i = 0; i < XP; ++i) s.add(-1, OX, i);
  for (int t = 0; t < 128; ++t) {
    if (t < 2) for (int i = XP + t * (16 - XP) / 2; i < XP + (t + 1) * (16 - XP) / 2; ++i) s.add(t, OX, i);
    if (t >= 2 && t < 50) s.add(t, OX, t + 14);
    for (int v = 3; v < 18; ++v) for (int p = 0; p < 4; ++p) if (8 * (v - 3) + 7 + p == t) s.add(t, OG, 4 * v + p);
    if (t == 105 || t == 113 || t == 121) { s.add(t, OW, (t - 105) / 4); s.add(t, OW, (t - 105) / 4 + 1); }
    if (t >= 124) for (int i = 0; i < 3; ++i) s.add(t, OX, 64 + 3 * (t - 124) + i);
    if (t == 127) { s.add(t, OG, 72); s.add(t, OG, 73); }
    if (t == 20) s.add(t, OM, 0);
    if (t == 120) s.add(t, OM, 1);
    if (t >= 49 && t < 57 && (t & 1)) s.add(t, OF, (t - 49) / 2);
    if (t >= 71 && t < 79 && (t & 1)) s.add(t, OF, 4 + (t - 71) / 2);
    if (t == 48) s.add(t, OS, 0);
    if (t == 104) s.add(t, OS, 2);
    for (int p = 0; p < 4; ++p) if (FISS[p] == t) s.add(t, OF, 8 + p);
  }
  for (int t = 0; t < 64; ++t) {
    if (t == 0) { s.add(B0, OM, 2); s.add(B0, OG, 74); s.add(B0, OG, 75); s.add(B0, OW, 6); s.add(B0, OW, 7); }
    if (t == 1 || t == 2) { s.add(B0 + t, OX, 74 + 2 * t); s.add(B0 + t, OX, 75 + 2 * t); }
    if (t == 5 || t == 6 || t == 9) s.add(B0 + t, OX, 64 + (t == 5 ? 16 : t == 6 ? 18 : 22));
    if (t >= 5 && t < 50) s.add(B0 + t, OX, 64 + (t == 5 ? 17 : t < 9 ? t + 13 : t + 14));
    for (int v = 3; v < 16; ++v) for (int p = 0; p < 4; ++p) if ((v < 14 ? 4 * (v - 3) + 3 + (p >> 1) : v == 14 ? 45 + (p >> 1) : 49) == t) s.add(B0 + t, OG, 64 + 4 * v + p);
    if (t == 10 || t == 26) s.add(B0 + t, OM, t == 10 ? 3 : 4);
    if (t == 13 || t == 14 || t == 18) s.add(B0 + t, OE, t == 18 ? 2 : t - 13);
  }
  return s;
}
constexpr Sched SC = make_sched();

__global__ __launch_bounds__(512, 2) __attribute__((amdgpu_num_vgpr(126))) void k_fused(const u16* __restrict__ x, const u16* __restrict__ kc, const u16* __restrict__ vc,
                                                  const u16* __restrict__ wq, const u16* __restrict__ wo, const u16* __restrict__ wr,
                                                  const u16* __restrict__ wgate, const u16* __restrict__ wup, const u16* __restrict__ wdown,
                                                  char* __restrict__ ws, u16* __restrict__ out) {
  __shared__ __attribute__((aligned(16))) char lds_raw[LDS_BYTES];
  LAS char* const lds = (LAS char*)lds_raw;
  const int t = threadIdx.x, l = t & 63, bid = blockIdx.x, c = l & 15, g = l >> 4;
  const int wid = __builtin_amdgcn_readfirstlane(t >> 6);
  const int xcd = bid & 7, within = bid >> 3, head = xcd * 4 + (within & 3), rb = within >> 2;
  const int fj = xcd * 28 + (within < 28 ? within : within - 28);
  const int fsl = fj / 7, fgr = fj - fsl * 7;
  const int kh = xcd >> 2, mb = 2 * rb + (xcd & 1), nb = ((xcd >> 1) & 1) * 4 + (within & 3);
  const int rbw = wid & 3, kg = wid >> 2;
  const unsigned lds0 = (unsigned)(uintptr_t)lds;
  unsigned tlo, thi;
  {
    const int row = 8 * bid + wid;
    u32x4 a0, a1, w0, w1, kvo;
    u32x2 ep, klo, khi;
    unsigned e[8];
    gld16<0>(a0, (unsigned)(row * DM * 2 + l * 16), x); gld16<1024>(a1, (unsigned)(row * DM * 2 + l * 16), x);
    gld16<0>(w0, (unsigned)(l * 16), wr); gld16<1024>(w1, (unsigned)(l * 16), wr);
    gld8<0, true>(ep, 0u, ws + WS_EP);
    if (wid == 1)
      asm volatile("global_load_dword v255, %0, %1 sc1" ::"v"(l == 0 ? (unsigned)(WS_G0 - WS_ST) + bid * 8 : l == 1 ? (unsigned)(WS_FK - WS_ST) + bid * 32 : (unsigned)(bid * 64)), "s"(ws + WS_ST) : "memory", "v255");
    if (wid == 3) {
      const char* pc;
      asm volatile("s_getpc_b64 %0" : "=s"(pc));
      touch((unsigned)((within * 12 + (l < 12 ? l : 11)) * 128), pc);
    }
    __builtin_amdgcn_s_barrier();
    if (wid == 2) {
      asm volatile("global_load_dword v255, %0, %1 sc1" ::"v"((unsigned)((l < 40 ? l : 39) << 20) + 64 * 1024 - 128), "s"(ws) : "memory", "v255");
      const char* pb = l < 8 ? (const char*)wo : l < 16 ? (const char*)wgate : l < 24 ? (const char*)wup : l < 32 ? (const char*)wdown : (const char*)out;
      pb += (size_t)(l & 7) * (l < 8 ? 1048576 : l < 32 ? 917504 : 524288);
      asm volatile("global_load_dword v255, %0, off" ::"v"(pb) : "memory", "v255");
    }
    const int fn = (bid * 4 + wid) & 255, kvh = bid >> 6;
    if (wid < 4) {
      if (fn < 128) {
        const unsigned ko = (unsigned)(((kvh * SK + (SK - WIN) + (fn >> 2) * 16 + c) * DH + (fn & 3) * 32 + 4 * g) * 2);
        gld8<0>(klo, ko, kc); gld8<32>(khi, ko, kc);
      } else {
        const int d = 32 * ((fn - 128) >> 5) + 8 * (c >> 2) + 4 * (fn & 1) + (c & 3);
        const unsigned vo = (unsigned)(((kvh * SK + (SK - WIN) + ((fn >> 1) & 15) * 32 + 4 * g + 8) * DH + d) * 2);
        sfor<8>([&](auto J) AI { constexpr int j = J; gld2<(16 * (j >> 2) + (j & 3) - 8) * DH * 2>(e[j], vo, vc); });
      }
      if (wid < 2) touch((unsigned)((wid * 256 + within * 8 + (l >> 3)) * 8192 + xcd * 1024 + (l & 7) * 128), wq);
      if (fn < 128) waitv<6>(); else waitv<12>();
    } else {
      waitv<3>();
    }
    PIN(a0); PIN(a1);
    float v[16];
#pragma unroll
    for (int j = 0; j < 4; ++j) { v[2 * j] = bflo(a0[j]); v[2 * j + 1] = bfhi(a0[j]); v[8 + 2 * j] = bflo(a1[j]); v[9 + 2 * j] = bfhi(a1[j]); }
    float s = 0.f;
#pragma unroll
    for (int j = 0; j < 16; ++j) s += v[j];
    const float mean = wave_sum(s) * (1.f / DM);
    float q = 0.f;
#pragma unroll
    for (int j = 0; j < 16; ++j) { const float d = v[j] - mean; q += d * d; }
    const float var = wave_sum(q) * (1.f / DM);
    const float mbf = rbf(mean), rden = __builtin_amdgcn_rcpf(rbf(__builtin_amdgcn_sqrtf(rbf(rbf(var) + 1e-5f))));
    u32x4 o0, o1;
#pragma unroll
    for (int j = 0; j < 8; ++j) {
      const unsigned dd = pk2(v[2 * j] - mbf, v[2 * j + 1] - mbf);
      const unsigned nn = pk2(bflo(dd) * rden, bfhi(dd) * rden);
      if (j < 4) o0[j] = nn; else o1[j - 4] = nn;
    }
    *(LAS u32x4*)(lds + STAGE + (l * 8 + (wid ^ (l & 7))) * 16) = o0;
    *(LAS u32x4*)(lds + STAGE + 8192 + (l * 8 + (wid ^ (l & 7))) * 16) = o1;
    *(LAS f32x2*)(lds + STAGE + 16384 + 32 + wid * 8) = (f32x2){mbf, rden};
    waitbar_l<63>();
    if (wid >= 4) {
#pragma unroll
      for (int hh = 0; hh < 4; ++hh) {
        const int p = hh * 256 + (t - 256), ch = p >> 3, r = 8 * bid + ((p & 7) ^ (ch & 7));
        const u32x4 o = *(LAS const u32x4*)(lds + STAGE + p * 16);
        gst16<0>(o, (unsigned)(((((r >> 5) * 32 + (ch >> 2)) * 2 + ((r >> 4) & 1)) * 64 + (ch & 3) * 16 + (r & 15)) * 16), ws + WS_XN);
      }
      if (t < 256 + 8) gst8(*(LAS const f32x2*)(lds + STAGE + 16384 + 32 + (t - 256) * 8), (unsigned)((8 * bid + t - 256) * 8), ws + WS_ST);
      waitv<4>();
    } else if (wid == 0 || wid == 3) { if (fn < 128) waitv<3>(); else waitv<9>(); }
    else if (fn < 128) waitv<4>(); else waitv<10>();
    PIN(w0); PIN(w1); PIN(ep);
    {
      const unsigned long long tg = (((unsigned long long)(unsigned)__builtin_amdgcn_readfirstlane(ep[1]) << 32) | (unsigned)__builtin_amdgcn_readfirstlane(ep[0])) + 1ull;
      tlo = (unsigned)tg; thi = (unsigned)(tg >> 32);
    }
    float rr = 0.f;
#pragma unroll
    for (int j = 0; j < 8; ++j) {
      const unsigned nn = j < 4 ? o0[j] : o1[j - 4], ww = j < 4 ? w0[j] : w1[j - 4];
      rr += bflo(nn) * bflo(ww); rr += bfhi(nn) * bfhi(ww);
    }
    rr = wave_sum(rr);
    {
      const unsigned bits = pk2(rr, 0.f) & 0xffffu;
      const unsigned ord = (bits & 0x8000u) ? (~bits & 0xffffu) : (bits | 0x8000u);
      *(LAS unsigned*)(lds + STAGE + 16384 + wid * 4) = (ord << 11) | (unsigned)(SEQ - 1 - row);
    }
    if (wid >= 4) waitv<0>();
    waitbar_l<63>();
    if (t == 256) {
      const u32x4 ka = *(LAS const u32x4*)(lds + STAGE + 16384), kb = *(LAS const u32x4*)(lds + STAGE + 16400);
      const unsigned key = umax(umax(umax(ka[0], ka[1]), umax(ka[2], ka[3])), umax(umax(kb[0], kb[1]), umax(kb[2], kb[3])));
      gst8((u32x2){(tlo << 27) | key, (tlo >> 5) | (thi << 27)}, (unsigned)(bid * 8), ws + WS_G0);
    }
    if (wid < 4) {
      if (wid < 2) waitv<1>(); else waitv<0>();
      if (fn < 128) {
        PIN(klo); PIN(khi);
        kvo = (u32x4){klo[0], klo[1], khi[0], khi[1]};
      } else {
#pragma unroll
        for (int j = 0; j < 8; ++j) PIN(e[j]);
        kvo = (u32x4){e[0] | (e[1] << 16), e[2] | (e[3] << 16), e[4] | (e[5] << 16), e[6] | (e[7] << 16)};
      }
      gst16<0>(kvo, (unsigned)((bid * 4 + wid) * 1024 + l * 16), ws + WS_KV);
    }
  }
  const unsigned glo = tlo << 27, ghi = (tlo >> 5) | (thi << 27);

  const char* const wq_b = (const char*)wq + head * 256;
  const unsigned wq_v = (unsigned)(((8 * wid + ((l & 31) >> 2)) * 4096 + (l >> 5) * 32 + ((l & 3) ^ (2 * (wid & 1))) * 8) * 2);
  const char* const kv_b = ws + WS_KV + (size_t)(head >> 3) * 262144;
  const unsigned kv_v = (unsigned)(wid * 4096 + l * 16);
  const char* const xn_b = ws + WS_XN + (size_t)rb * 8 * 65536 + 4096;
  const unsigned xn_v = (unsigned)(wid * 65536 + l * 16);
  const char* const wo_b = (const char*)wo + (size_t)kh * 2048 * (DM * 2) + nb * 256;
  const char* const o_b = ws + WS_O + (size_t)mb * 4 * 262144 + kh * 131072;
  const unsigned ldsw = lds0 + wid * 2048, ldsk = lds0 + wid * 4096, ldsf = lds0 + FB + wid * 1024;
  const int erow = mb * 128 + rbw * 32 + kg * 16 + c;
  bf16x8 xf[XD];
  u32x4 fm[2];
  bf16x8 fxs;
  const unsigned gu_v = (unsigned)((((4 * wid + g) * 2 + ((l >> 3) & 1)) * FF + fj * 16 + (l & 7) * 2) * 8);
  u32x2 fg;
  u32x2 xe[4];
  f32x2 st;
  unsigned key = 0;

  const unsigned fo_v = (unsigned)(((mb * 4 + rbw) * 32 + kh * 16 + c) * 8);
  const int yrow = within < 28 ? fj : 224 + xcd * 4 + within - 28;
  f32x4 o[2][2][2];
  float inv[2];
  const char* const od_b = ws + WS_O + ((size_t)(rb * 8) * 128 + head * 4) * 2048 + 4096;
  auto issue = [&](auto Sl, auto Pos) AI {
    constexpr int sl = Sl, pos = Pos, cnt = SC.count(sl), q0 = SC.first(sl) + (pos < 0 ? 0 : pos);
    constexpr int nq = pos < 0 ? cnt : pos >= cnt ? 0 : pos < 3 ? 1 : cnt - 3;
    sfor<nq>([&](auto Qi) AI {
      constexpr int k = SC.kind[q0 + Qi], i = SC.idx[q0 + Qi];
      if constexpr (k == OX && i < 64) gld16<(i & 7) * 1024 - 4096, true>(xf[i % XD], xn_v, xn_b + (size_t)(i >> 3) * 8192);
      if constexpr (k == OX && i >= 64) {
        constexpr int q = i - 64, v = q >> 2;
        gld16<0, true>(xf[q % XD2], (unsigned)(rbw * 262144 + kg * 16384 + ((q & 1) ^ kg) * 1024 + l * 16), o_b + (size_t)(((v & 3) * 4 + ((q >> 1) & 1)) * 8192 + (v >> 2) * 2048));
      }
      if constexpr (k == OG) {
        constexpr int v = i >> 2, p = i & 3;
        if constexpr (v < 8) {
          const char* sb = wq_b + (size_t)(v * 128 + (p >> 1) * 64) * 8192;
          glds<(p & 1) * 128>(ldsw + (v & 3) * 32768 + (p >> 1) * 16384 + (p & 1) * (1024 - 128), wq_v, sb);
        } else if constexpr (v < 16) {
          glds<p * 1024, true>(ldsk + (v & 3) * 32768, kv_v, kv_b + (size_t)(v - 8) * 32768);
        } else {
          const unsigned wo_v = (unsigned)((((wid >> 2) * 128 + (wid & 3) * 8 + ((l & 31) >> 2)) * DM + (l >> 5) * 32 + ((l & 3) ^ (2 * (wid & 1))) * 8) * 2);
          const char* sb = wo_b + (size_t)(((v & 3) * 4 + (p >> 1) * 2) * 128 + ((v - 16) >> 2) * 32) * (DM * 2) + (((p & 1) ^ kh ^ 1) << 7);
          glds<0>(ldsw + (v & 3) * 32768 + (p >> 1) * 16384 + (p & 1) * 1024, wo_v, sb);
        }
      }
      if constexpr (k == OF && i < 8)
        glds<0>(ldsf + (i & 3) * 8192, (unsigned)(((l >> 3) * FF + wid * 64 + ((l & 7) ^ ((i & 1) * 4 + ((l >> 3) & 2))) * 8) * 2),
                (const char*)(i < 4 ? wgate : wup) + ((size_t)(fsl * 32 + 8 * (i & 3)) * FF + fgr * 512) * 2);
      if constexpr (k == OF && i >= 8) {
        const int frho = l >> 2, fch = (l & 3) ^ (frho & 4 ? 2 : 0);
        glds<0>(ldsf + ((i - 8) % 3) * 8192, (unsigned)((frho * DM + wid * 128 + fch * 8) * 2), (const char*)wdown + (size_t)fj * 16 * DM * 2 + (i - 8) * 64);
      }
      if constexpr (k == OS && i == 0) {
        const int idx = SEQ - 1 - (int)(key & 2047u);
        gld16<0, true>(fxs, (unsigned)((((idx >> 5) * 32 + fsl) * 2 + ((idx >> 4) & 1)) * 1024 + (g * 16 + (idx & 15)) * 16), ws + WS_XN);
      }
      if constexpr (k == OS && i == 2) gld16<0, true>(fm[0], gu_v, ws + WS_GU);
      if constexpr (k == OM && i > 0) gld8<0, true>(fg, fo_v, ws + WS_FO + (i - 1) * 16384);
      if constexpr (k == OM && i == 0) gld8<0, true>(fg, (unsigned)((t & 255) * 8), wid < 4 ? ws + WS_G0 : ws + WS_FK + (head >> 3) * 2048);
      if constexpr (k == OW) {
        constexpr int os = i >> 1, qb = i & 1;
        PIN(o[os & 1][0][qb]); PIN(o[os & 1][1][qb]);
        const f32x4 a = o[os & 1][0][qb] * inv[qb], b = o[os & 1][1][qb] * inv[qb];
        gst16<i * 1024 - 4096>((u32x4){pk2(a[0], a[1]), pk2(a[2], a[3]), pk2(b[0], b[1]), pk2(b[2], b[3])}, (unsigned)(wid * 262144 + l * 16), od_b);
      }
      if constexpr (k == OE && i < 2)
        glds<0>(ldsf + i * 8192, (unsigned)(((erow - c + i * 8 + (l >> 3)) * DM + nb * 128 + kh * 64 + ((l & 7) ^ (i * 4 + (l >> 4))) * 8) * 2), x);
      if constexpr (k == OE && i == 2) gld8<0, true>(st, (unsigned)(erow * 8), ws + WS_ST);
    });
  };
  f32x4 fa0, fa1, fq[4];
  s16x4 fb0, fb1, fxa;
  bf16x8 ft[2];
  auto endslot = [&](auto Sl) AI {
    constexpr int sl = Sl, u = sl >> 3, i = sl & 7;
    if constexpr (sl == 3) {
      if (wid == 6 || wid == 7) touch((unsigned)(((wid - 4) * 256 + within * 8 + (l >> 3)) * 8192 + xcd * 1024 + (l & 7) * 128), wq);
    }
    if constexpr (sl == 2) {
      if (wid < 4 && l == 0) gst8((u32x2){tlo, thi}, (unsigned)((bid * 4 + wid) * 8), ws + WS_FK);
    }
    if constexpr (sl == 38) {
      waitv<SC.younger(OM, 0, sl)>(fg);
      const unsigned lo = wid < 4 ? glo : tlo, hi = wid < 4 ? ghi : thi, mk = wid < 4 ? 0xf8000000u : 0xffffffffu;
      for (int spin = 0; spin < SPINS; ++spin) {
        if (__builtin_expect(__all(((fg[0] & mk) == lo) & (fg[1] == hi)), 1)) break;
        __builtin_amdgcn_s_sleep(1);
        gld8<0, true>(fg, (unsigned)((t & 255) * 8), wid < 4 ? ws + WS_G0 : ws + WS_FK + (head >> 3) * 2048);
        waitv<0>(fg);
      }
      const unsigned km = wave_max_u32(fg[0] & 0x07ffffffu);
      *(LAS unsigned*)(lds + HSC + wid * 4) = km;
    }
    if constexpr (sl == 57) {
      if (wid == 3) {
        const char* pc;
        asm volatile("s_getpc_b64 %0" : "=s"(pc));
        touch((unsigned)((within * 8 + (l & 7)) * 128), pc);
      }
    }
    if constexpr (sl == 47) {
      if (bid == 0 && t == 0) gst8((u32x2){tlo, thi}, 0u, ws + WS_EP);
      const u32x4 kk = *(LAS const u32x4*)(lds + HSC);
      key = __builtin_amdgcn_readfirstlane(umax(umax(kk[0], kk[1]), umax(kk[2], kk[3])));
    }
    if constexpr (sl == 58 || sl == 61 || sl == 64 || sl == 67) {
      if (wid == 4 + (sl - 58) / 3) touch((unsigned)(((wid & 3) * 64 + l) * 128), (const char*)wdown + (size_t)fj * 16 * DM * 2);
    }
    if constexpr (sl == 81 || sl == 85 || sl == 89 || sl == 93) {
      if (wid == (sl - 81) / 4)
        touch((unsigned)((kh * 2048 + (within >> 1) * 128 + wid * 32 + ((within & 1) * 2 + (xcd & 1)) * 8 + (l >> 3)) * (DM * 2) + ((xcd >> 1) & 1) * 1024 + (l & 7) * 128), wo);
    }
    if constexpr (sl == 66) {
      const unsigned fo = (unsigned)(WS_FO - WS_ST) + (l & 3) * 16384 + ((mb * 4 + rbw) * 32 + kh * 16) * 8, fx = (unsigned)(WS_FX - WS_ST) + (bid * 8 + wid) * 64;
      const unsigned fy = (unsigned)(WS_FY - WS_ST) + (wid * 256 + yrow) * 8;
      asm volatile("global_load_dword v255, %0, %1 sc1" ::"v"(l < 4 ? fo : l < 8 ? fy : fx), "s"(ws + WS_ST) : "memory", "v255");
    }
    if constexpr (sl == 97 || sl == 99) {
      if (wid == 4 + (sl - 97) / 2) touch((unsigned)(((mb * 128 + (wid - 4) * 64 + l) * DM + nb * 128 + kh * 64) * 2), x);
    }
    if constexpr ((sl >= 68 && sl <= 70) || (sl >= 90 && sl <= 92)) {
      constexpr int r = sl >= 90, q = sl - (r ? 90 : 68);
      if constexpr (q == 0) waitv<SC.younger(OF, 4 * r + 3, sl)>(fxs);
      if constexpr (q >= 1) {
#pragma unroll
        for (int j = 0; j < 2; ++j) { fq[2 * q - 2 + j] = __builtin_amdgcn_mfma_f32_16x16x32_bf16(fxs, ft[j], (f32x4){}, 0, 0, 0); PIN(fq[2 * q - 2 + j]); }
      }
      if constexpr (q < 2) {
        const unsigned rp = ldsf + g * 8192 + (c >> 2) * 128 + ((((g & 1) << 1) | (c >> 3)) << 5) + (c & 3) * 8;
#pragma unroll
        for (int j = 0; j < 2; ++j) { LAS const char* a = (LAS const char*)(uintptr_t)(rp ^ ((2 * q + j) << 5)); ft[j] = cat8(trd(a), trd(a + 512)); }
      }
    }
    if constexpr (sl == 71 || sl == 93) {
      const float v = sel2(g & 2, sel2(g & 1, fq[3][0], fq[2][0]), sel2(g & 1, fq[1][0], fq[0][0]));
      gst8((u32x2){__float_as_uint(v), tlo}, (unsigned)(((fsl * 2 + (sl == 93)) * FF + fgr * 512 + t) * 8), ws + WS_GU);
    }
    if constexpr (sl == 109) {
      waitv<SC.younger(OS, 2, sl)>(fm[0]);
      for (int spin = 0; spin < SPINS; ++spin) {
        if (__builtin_expect(__all((fm[0][1] == tlo) & (fm[0][3] == tlo)), 1)) break;
        __builtin_amdgcn_s_sleep(1);
        gld16<0, true>(fm[0], gu_v, ws + WS_GU);
        waitv<0>(fm[0]);
      }
      *(LAS f32x2*)(lds + RED1 + (wid * 32 + 2 * c) * 4) = (f32x2){xg_sum(__uint_as_float(fm[0][0])), xg_sum(__uint_as_float(fm[0][2]))};
    }
    if constexpr (sl == 111) {
#pragma unroll
      for (int w = 0; w < 2; ++w) { fa0[w] = *(LAS const float*)(lds + RED1 + ((2 * g + w) * 32 + c) * 4); fa0[2 + w] = *(LAS const float*)(lds + RED1 + ((2 * g + w) * 32 + 16 + c) * 4); }
    }
    if constexpr (sl == 112) { fa0[0] = xg_sum(fa0[0] + fa0[1]); fa0[2] = xg_sum(fa0[2] + fa0[3]); PIN(fa0); }
    if constexpr (sl == 113) {
      const float hh = rbf(silu_f(rbf(fa0[0]))) * rbf(fa0[2]);
      *(LAS u16*)(lds + HSC + 32 + wid * 32 + c * 2) = (u16)pk2(hh, hh);
    }
    if constexpr (sl == 114) fxa = *(LAS const s16x4*)(lds + HSC + 32 + wid * 32 + g * 8);
    constexpr int pr = fpiece(sl), pm = fpiece(sl - 1), ps = fpiece(sl - 2);
    if constexpr (ps >= 0) gst4<ps * 128>(sel2(g & 1, fa1[0], fa0[0]), (unsigned)((wid * 128 + (l & 31)) * 4), ws + WS_YP + (size_t)yrow * 4096);
    if constexpr (pm >= 0) { fa0 = MFMA16(fxa, fb0, (f32x4){}); fa1 = MFMA16(fxa, fb1, (f32x4){}); PIN(fa0); PIN(fa1); }
    if constexpr (pr >= 0) {
      waitv<SC.younger(OF, 8 + pr, sl)>();
      const unsigned fp = ldsf + (4 * g + (c >> 2)) * 64 + (g & 1) * 32 + (c & 3) * 8;
      fb0 = trd((LAS const char*)(uintptr_t)fp + (pr % 3) * 8192); fb1 = trd((LAS const char*)(uintptr_t)(fp ^ 32) + (pr % 3) * 8192);
    }
    if constexpr (sl == 111 || sl == 119 || sl == 127) {
      waitv<SC.younger(OW, (sl - 111) / 4 + 1, sl)>();
      if (l == 0) gst8((u32x2){tlo, thi}, (unsigned)(((rb * 8 + wid) * 32 + head) * 8), ws + WS_FO + ((sl - 111) / 8) * 16384);
    }
    if constexpr (sl == 123) {
      waitv<SC.younger(OM, 1, sl)>(fg);
      if (__builtin_expect(!__all((fg[0] == tlo) & (fg[1] == thi)), 0)) poll8(fo_v, ws + WS_FO, tlo, thi, 0xffffffffu);
    }
    SBAR();
    if constexpr (i == 6) {
      if constexpr (u == 5 || u == 13) waitbar_l<SC.younger(OG, 4 * u + 7, sl)>();
      else waitbar<SC.younger(OG, 4 * u + 7, sl)>();
    }
    if constexpr (!(i & 1)) __builtin_amdgcn_s_setprio(i == 6 ? 3 : 2 - i / 2);
  };

  issue(IC(-2), IC(-1));
  if (wid == 5) poll8((unsigned)((rb * 32 + (l & 31)) * 8), ws + WS_G0, glo, ghi, 0xf8000000u);
  waitbar<SC.younger(OG, 3, -2)>();
  issue(IC(-1), IC(-1));
  LBase trb[2], frb;
  trb[0].init(lds0 + g * 2048 + (c >> 2) * 64 + (g & 1) * 32 + (c & 3) * 8);
  trb[1].init(lds0 + g * 2048 + (c >> 2) * 64 + ((g & 1) ^ 1) * 32 + (c & 3) * 8);
  frb.init(lds0 + l * 16);
  f32x4 acc[8][2];
  bf16x8 wf[2][4];
  auto WRD1 = [&](auto Si, auto Base, auto Dd) AI {
    constexpr int s = Si, d = Dd, o = Base + (d >> 1) * 512;
    wf[s & 1][d] = cat8(trd(trb[d & 1].template at<o>()), trd(trb[d & 1].template at<o + 256>()));
  };
  auto WRD = [&](auto Si, auto Base) AI { sfor<4>([&](auto Dd) AI { WRD1(Si, Base, Dd); }); };
#define WA(s) IC((((s) >> 3) & 3) * 32768 + (((s) >> 1) & 3) * 8192 + ((s) & 1) * 1024)
#define WB(s) IC((((s) >> 2) & 3) * 32768 + (((s) >> 1) & 1) * 8192 + ((s) & 1) * 1024)
  bf16x8 fr[4];
  auto FRD = [&](auto Ni) AI { constexpr int n = Ni; if constexpr (n < 256) fr[n & 3] = *(LAS const bf16x8*)(frb.at<((n >> 5) & 3) * 32768 + (n & 31) * 1024>()); };

  WRD(IC(0), WA(0));
  SBAR();
  bf16x8 qf[4][2];
  auto QPACK = [&](auto Ds) AI {
    constexpr int ds = Ds;
#pragma unroll
    for (int qb = 0; qb < 2; ++qb) {
      const f32x4 a = acc[2 * ds][qb] * QSCALE, b = acc[2 * ds + 1][qb] * QSCALE;
      qf[ds][qb] = __builtin_bit_cast(bf16x8, (u32x4){pk2(a[0], a[1]), pk2(a[2], a[3]), pk2(b[0], b[1]), pk2(b[2], b[3])});
      PIN(qf[ds][qb]);
    }
  };
  sfor<64>([&](auto Si) AI {
    constexpr int s = Si, x0 = (s & ~1) % XD, x1 = (s | 1) % XD;
    if constexpr (!(s & 1)) waitv<SC.younger(OX, s + 1, s - 1)>(xf[x0], xf[x1]);
    sfor<4>([&](auto Dd) AI {
      constexpr int d = Dd, dt = (s & 1) * 4 + d;
      if constexpr (s + 1 < 64) WRD1(IC(s + 1), WA(s + 1), Dd);
      else if constexpr (d < 3) FRD(Dd);
      SBAR();
      if constexpr (s < 2) { MFZ(acc[dt][0], wf[s & 1][d], xf[x0]); MFZ(acc[dt][1], wf[s & 1][d], xf[x1]); }
      else { MF(acc[dt][0], wf[s & 1][d], xf[x0]); MF(acc[dt][1], wf[s & 1][d], xf[x1]); }
      issue(Si, Dd);
      if constexpr (s == 63 && (d & 1)) { QPACK(IC(d >> 1)); SBAR(); }
    });
    SBAR();
    endslot(Si);
  });

  f32x4 sc[2][2][2], negm[2];
  u32x4 pw[16][2];
  f32x4 lsum[2];
  bf16x8 ones = {0x3f80, 0x3f80, 0x3f80, 0x3f80, 0x3f80, 0x3f80, 0x3f80, 0x3f80};
  PIN(ones);
#define MX3(a, b, c) __builtin_fmaxf(__builtin_fmaxf((a), (b)), (c))
#define EXP2(x) __builtin_amdgcn_exp2f(x)
  auto SM = [&](auto Hi, auto Pi) AI {
    constexpr int H = Hi, p = Pi, kt = p >> 2, qb = (p >> 1) & 1, r = 2 * (p & 1);
    const float e0 = EXP2(sc[H & 1][kt][qb][r]), e1 = EXP2(sc[H & 1][kt][qb][r + 1]);
    unsigned w = pk2(e0, e1);
    PIN(w);
    pw[H][qb][kt * 2 + (p & 1)] = w;
  };
  sfor<128>([&](auto Ni) AI {
    constexpr int n = Ni, H = n >> 3, i = n & 7, kt = i >> 2, ds = i & 3, sn = H & 1;
    FRD(IC(n + 3));
    if constexpr (n < 2) QPACK(IC(2 + n));
    sfor<2>([&](auto Qb) AI {
      constexpr int qb = Qb;
      if constexpr (ds == 0 && H == 0) MFZ(sc[sn][kt][qb], fr[n & 3], qf[0][qb]);
      else if constexpr (ds == 0) MFC(sc[sn][kt][qb], fr[n & 3], qf[0][qb], negm[qb]);
      else MF(sc[sn][kt][qb], fr[n & 3], qf[ds][qb]);
    });
    if constexpr (n == 5 || n == 6) {
      constexpr int qb = n - 5;
      NOPS(3); PIN(sc[0][0][qb]);
      const float m = -xg_max(__builtin_fmaxf(MX3(sc[0][0][qb][0], sc[0][0][qb][1], sc[0][0][qb][2]), sc[0][0][qb][3]));
      negm[qb] = (f32x4){m, m, m, m};
      sc[0][0][qb] += negm[qb];
      PIN(negm[qb]); PIN(sc[0][0][qb]);
    }
    if constexpr (n == 9 || n == 10) { NOPS(3); PIN(sc[0][1][n - 9]); sc[0][1][n - 9] += negm[n - 9]; PIN(sc[0][1][n - 9]); }
    issue(IC(64 + (n >> 2)), IC(n & 3));
    if constexpr (H == 2 && (i == 1 || i == 5)) MFZ(lsum[i >> 2], ones, pw[0][i >> 2]);
    if constexpr (H > 2 && (i == 1 || i == 5)) MF(lsum[i >> 2], ones, pw[H - 2][i >> 2]);
    if constexpr (H >= 1 && H < 15) SM(IC(H - 1), IC(i));
    if constexpr (n == 125) { PIN(negm[0]); PIN(negm[1]); }
    SBAR();
    if constexpr ((n & 3) == 3) endslot(IC(64 + (n >> 2)));
  });
  constexpr int CHK = 32;
  sfor<128>([&](auto Mi) AI {
    constexpr int m = Mi, n = 128 + m, os = m >> 5, H = (m >> 1) & 15, par = m & 1;
    FRD(IC(n + 3));
    if constexpr (H == 0) { MFZ(o[os & 1][par][0], fr[n & 3], pw[H][0]); MFZ(o[os & 1][par][1], fr[n & 3], pw[H][1]); }
    else { MF(o[os & 1][par][0], fr[n & 3], pw[H][0]); MF(o[os & 1][par][1], fr[n & 3], pw[H][1]); }
    if constexpr (m < 26 && (m % 13) % 5 != 1 && (m % 13) % 5 != 3) {
      constexpr int r = m % 13, p = r / 5 * 3 + (r % 5) / 2;
      SM(IC(14 + m / 13), IC(p));
    }
    if constexpr (m == 14 || m == 16) MF(lsum[(m - 14) / 2], ones, pw[14][(m - 14) / 2]);
    if constexpr (m == 27 || m == 28) MF(lsum[m - 27], ones, pw[15][m - 27]);
    if constexpr (m == CHK + 1 || m == CHK + 2) { inv[m - CHK - 1] = __builtin_amdgcn_rcpf(lsum[m - CHK - 1][0]); PIN(inv[m - CHK - 1]); }
    issue(IC(64 + (n >> 2)), IC(n & 3));
    if constexpr (m == 123) {
      trb[0].init(lds0 + kg * 16384 + g * 2048 + (c >> 2) * 64 + (g & 1) * 32 + (c & 3) * 8);
      trb[1].init(lds0 + kg * 16384 + g * 2048 + (c >> 2) * 64 + ((g & 1) ^ 1) * 32 + (c & 3) * 8);
    }
    if constexpr (m >= 124) WRD1(IC(0), WB(0), IC(m - 124));
    SBAR();
    if constexpr ((n & 3) == 3) endslot(IC(64 + (n >> 2)));
    if constexpr (m == CHK - 1) {
      PIN(lsum[0]); PIN(lsum[1]);
      if (__builtin_expect(__any(!(lsum[0][0] + lsum[1][0] <= LBOUND)), 0)) {
        NOPS(7); SBAR();
        auto tile = [&](unsigned off, f32x4& s0, f32x4& s1) AI {
          bf16x8 kf[4];
          SBAR(); NOPS(7);
          sfor<4>([&](auto Ds) AI { constexpr int ds = Ds; gld16<ds * 1024, true>(kf[ds], off, kv_b); });
          waitv<0>(kf[0], kf[1]);
          PIN(kf[2]); PIN(kf[3]);
#pragma unroll
          for (int ds = 0; ds < 4; ++ds) {
            s0 = __builtin_amdgcn_mfma_f32_16x16x32_bf16(kf[ds], qf[ds][0], s0, 0, 0, 0);
            s1 = __builtin_amdgcn_mfma_f32_16x16x32_bf16(kf[ds], qf[ds][1], s1, 0, 0, 0);
          }
        };
        float m0 = -__builtin_inff(), m1 = m0;
#pragma unroll 1
        for (int kt = 0; kt < 32; ++kt) {
          f32x4 s0 = {}, s1 = {};
          tile((unsigned)(kt * 4096 + l * 16), s0, s1);
          m0 = MX3(MX3(m0, s0[0], s0[1]), s0[2], s0[3]); m1 = MX3(MX3(m1, s1[0], s1[1]), s1[2], s1[3]);
        }
        m0 = -xg_max(m0); m1 = -xg_max(m1);
        lsum[0] = (f32x4){}; lsum[1] = (f32x4){};
        sfor<16>([&](auto Hi) AI {
          constexpr int H = Hi;
#pragma unroll
          for (int kt = 0; kt < 2; ++kt) {
            sc[H & 1][kt][0] = (f32x4){m0, m0, m0, m0}; sc[H & 1][kt][1] = (f32x4){m1, m1, m1, m1};
            tile((unsigned)((2 * H + kt) * 4096 + l * 16), sc[H & 1][kt][0], sc[H & 1][kt][1]);
          }
          sfor<8>([&](auto Pi) AI { SM(Hi, Pi); });
          PIN(pw[H][0]); PIN(pw[H][1]);
          lsum[0] = __builtin_amdgcn_mfma_f32_16x16x32_bf16(ones, __builtin_bit_cast(bf16x8, pw[H][0]), lsum[0], 0, 0, 0);
          lsum[1] = __builtin_amdgcn_mfma_f32_16x16x32_bf16(ones, __builtin_bit_cast(bf16x8, pw[H][1]), lsum[1], 0, 0, 0);
        });
#pragma unroll
        for (int par = 0; par < 2; ++par) { o[0][par][0] = (f32x4){}; o[0][par][1] = (f32x4){}; }
        sfor<CHK / 4>([&](auto Qi) AI {
          constexpr int q = Qi;
          bf16x8 vf[4];
          SBAR(); NOPS(7);
          sfor<4>([&](auto J) AI { constexpr int j = J; gld16<j * 1024, true>(vf[j], (unsigned)((128 + 4 * q) * 1024 + l * 16), kv_b); });
          waitv<0>(vf[0], vf[1]);
          PIN(vf[2]); PIN(vf[3]);
#pragma unroll
          for (int j = 0; j < 4; ++j)
#pragma unroll
            for (int qb = 0; qb < 2; ++qb)
              o[0][j & 1][qb] = __builtin_amdgcn_mfma_f32_16x16x32_bf16(vf[j], __builtin_bit_cast(bf16x8, pw[2 * q + (j >> 1)][qb]), o[0][j & 1][qb], 0, 0, 0);
        });
#pragma unroll
        for (int par = 0; par < 2; ++par) { PIN(o[0][par][0]); PIN(o[0][par][1]); }
        PIN(lsum[0]); PIN(lsum[1]);
        SBAR(); NOPS(7);
      }
      SBAR();
    }
  });
  const int idx = SEQ - 1 - (int)(key & 2047u);
  const bool owner = (idx >> 7) == mb, ownw = (idx >> 4) == mb * 8 + rbw * 2 + kg;
  f32x4 yp[7], ya[4], oth[4];
  unsigned gate[8];
  float glow;
  const unsigned ex_a = ldsf + (c >> 3) * 8192 + (c & 7) * 128 + (((g >> 1) ^ (c >> 1)) << 4) + (g & 1) * 8;
  auto item = [](int s) { return s < 48 ? s : s < 56 ? 48 + 2 * (s - 48) : 49 + 2 * (s - 56); };
  u32x2 pf;
  u32x4 otp[2];
  bool rdy = false;
  sfor<64>([&](auto Si) AI {
    constexpr int s = Si, u = s >> 2, it = item(s), x0 = (it & ~1) % XD2, x1 = (it | 1) % XD2;
    if constexpr (!(it & 1)) waitv<SC.younger(OX, 64 + it + 1, B0 + s - 1)>(xf[x0], xf[x1]);
    sfor<4>([&](auto Dd) AI {
      constexpr int d = Dd, dt = (it & 1) * 4 + d;
      if constexpr (s + 1 < 64) WRD1(IC(s + 1), WB(item(s + 1)), Dd);
      SBAR();
      if constexpr (s < 2) { MFZ(acc[dt][0], wf[s & 1][d], xf[x0]); MFZ(acc[dt][1], wf[s & 1][d], xf[x1]); }
      else { MF(acc[dt][0], wf[s & 1][d], xf[x0]); MF(acc[dt][1], wf[s & 1][d], xf[x1]); }
      issue(IC(B0 + s), Dd);
      if constexpr (s == 56) *(LAS f32x4*)(lds + (wid ^ 4) * 4096 + l * 16 + (d & 1) * 2048 + (d >> 1) * 32768) = acc[d][1];
      if constexpr (s == 57 && d == 0) {
#pragma unroll
        for (int e = 0; e < 4; ++e) oth[e] = *(LAS const f32x4*)(lds + wid * 4096 + l * 16 + (e & 1) * 2048 + (e >> 1) * 32768);
      }
      if constexpr (s == 57 && d >= 2) {
        const f32x4 a = acc[2 * d - 4][0] + oth[2 * d - 4], b = acc[2 * d - 3][0] + oth[2 * d - 3];
        gst16<(d - 2) * 1024>((u32x4){pk2(a[0], a[1]), pk2(a[2], a[3]), pk2(b[0], b[1]), pk2(b[2], b[3])}, (unsigned)(wid * 2048 + l * 16), ws + WS_XC + (size_t)(bid ^ 4) * 16384);
      }
      if constexpr (s == 34) xe[d] = *(LAS const u32x2*)(uintptr_t)(ex_a ^ (d << 5));
      if constexpr (s >= 36 && s < 44 && d == 1) {
        constexpr int j = s - 36;
        PIN(st);
        const unsigned xw = xe[j >> 1][j & 1];
        const unsigned dd = pk2(bflo(xw) - st[0], bfhi(xw) - st[0]);
        gate[j] = pk2(bflo(dd) * st[1], bfhi(dd) * st[1]);
        PIN(gate[j]);
      }
      if constexpr (s >= 47 && s < 63 && d == 1) {
        constexpr int j = (s - 47) >> 1;
        if constexpr ((s - 47) & 1) { gate[j] = pk2(glow, silu_f(bfhi(gate[j]))); PIN(gate[j]); }
        else { glow = silu_f(bflo(gate[j])); PIN(glow); }
      }
    });
    SBAR();
    if constexpr (s == 7) {
      waitv<SC.younger(OW, 7, B0 + s)>();
      if (l == 0) {
        gst8((u32x2){tlo, thi}, (unsigned)(((rb * 8 + wid) * 32 + head) * 8), ws + WS_FO + 3 * 16384);
        gst8((u32x2){tlo, thi}, (unsigned)((wid * 256 + yrow) * 8), ws + WS_FY);
      }
    }
    if constexpr (s == 4 || s == 17 || s == 33) {
      constexpr int j = s == 4 ? 1 : s == 17 ? 2 : 3;
      waitv<SC.younger(OM, j + 1, B0 + s)>(fg);
      if (__builtin_expect(!__all((fg[0] == tlo) & (fg[1] == thi)), 0)) poll8(fo_v, ws + WS_FO + j * 16384, tlo, thi, 0xffffffffu);
    }
    if constexpr (s == 59) gld8<0, true>(pf, (unsigned)((bid * 8 + wid) * 64), ws + WS_FX);
    if constexpr (s == 60) {
      waitv<1>();
      if (l == 0) gst8((u32x2){tlo, thi}, (unsigned)(((bid ^ 4) * 8 + wid) * 64), ws + WS_FX);
    }
    if constexpr (s == 62) {
      waitv<1>(pf);
      rdy = __all((pf[0] == tlo) & (pf[1] == thi));
      if (rdy) sfor<2>([&](auto Dd) AI { constexpr int d = Dd; gld16<d * 1024, true>(otp[d], (unsigned)(wid * 2048 + l * 16), ws + WS_XC + (size_t)bid * 16384); });
    }
    if constexpr (s == 13) {
      if (owner) { gld16<0, true>(fm[0], (unsigned)(nb * 2048 + (l < 56 ? l : 55) * 32), ws + WS_FY); gld16<16, true>(fm[1], (unsigned)(nb * 2048 + (l < 56 ? l : 55) * 32), ws + WS_FY); }
    }
    if constexpr (s == 28) {
      if (owner) {
        PIN(fm[0]); PIN(fm[1]);
        for (int spin = 0; spin < SPINS; ++spin) {
          const bool ok = (fm[0][0] == tlo) & (fm[0][1] == thi) & (fm[0][2] == tlo) & (fm[0][3] == thi) & (fm[1][0] == tlo) & (fm[1][1] == thi) & (fm[1][2] == tlo) & (fm[1][3] == thi);
          if (__builtin_expect(__all(ok), 1)) break;
          __builtin_amdgcn_s_sleep(1);
          gld16<0, true>(fm[0], (unsigned)(nb * 2048 + (l < 56 ? l : 55) * 32), ws + WS_FY); gld16<16, true>(fm[1], (unsigned)(nb * 2048 + (l < 56 ? l : 55) * 32), ws + WS_FY);
          waitv<0>(fm[0], fm[1]);
        }
        const unsigned yv = (unsigned)(((t >> 4) * 1024 + nb * 128 + kh * 64 + (t & 15) * 4) * 4);
        sfor<7>([&](auto I) AI { constexpr int q = I; gld16<0, true>(yp[q], yv, ws + WS_YP + (size_t)q * 32 * 4096); });
      }
    }
    if constexpr (s == 43) {
      if (owner) {
        f32x4 a = {};
#pragma unroll
        for (int q = 0; q < 7; ++q) { PIN(yp[q]); a += yp[q]; }
#pragma unroll
        for (int j = 0; j < 4; ++j) a[j] = xg_sum(a[j]);
        *(LAS f32x4*)(lds + YS + (wid * 64 + c * 4) * 4) = a;
      }
    }
    if constexpr (s == 45) {
      if (ownw) {
        float a = 0.f;
#pragma unroll
        for (int p = 0; p < 8; ++p) a += *(LAS const float*)(lds + YS + (p * 64 + l) * 4);
        const unsigned ord = key >> 11, bits = (ord & 0x8000u) ? (ord & 0x7fffu) : (~ord & 0xffffu);
        *(LAS float*)(lds + RED1 + l * 4) = rbf(rbf(a) * rbf(sigmoid_f(bflo(bits))));
#pragma unroll
        for (int q = 0; q < 4; ++q) ya[q] = *(LAS const f32x4*)(lds + RED1 + (q * 16 + 4 * g) * 4);
      }
    }
    if constexpr (s == 46) {
      if (ownw) {
#pragma unroll
        for (int j = 0; j < 8; ++j) {
          const unsigned w = pk2(bflo(gate[j]) + ya[j >> 1][2 * (j & 1)], bfhi(gate[j]) + ya[j >> 1][2 * (j & 1) + 1]);
          gate[j] = erow == idx ? w : gate[j];
          PIN(gate[j]);
        }
      }
    }
    SBAR();
    if constexpr (((s & 3) == 2 && u < 11) || s == 44) {
      if constexpr (s == 44) waitbar_l<SC.younger(OG, 64 + 4 * u + 7, B0 + s)>();
      else waitbar<SC.younger(OG, 64 + 4 * u + 7, B0 + s)>();
    }
    if constexpr (s == 48) waitbar<SC.younger(OG, 64 + 55, B0 + s)>();
    if constexpr (s == 50) waitbar<SC.younger(OG, 64 + 59, B0 + s)>();
    if constexpr (s == 52) { waitbar<SC.younger(OG, 64 + 63, B0 + s)>(); }
    if constexpr (s == 56) waitbar_l<63>();
    __builtin_amdgcn_s_setprio(3 - ((s + 2) & 3));
  });
  f32x4 kep[4];
  {
    NOPS(7);
#pragma unroll
    for (int d = 0; d < 4; ++d) { PIN(acc[4 + d][1]); *(LAS f32x4*)(lds + 65536 + (wid ^ 4) * 4096 + l * 16 + (d & 1) * 2048 + (d >> 1) * 32768) = acc[4 + d][1]; }
    if (!rdy) {
      poll8((unsigned)((bid * 8 + wid) * 64), ws + WS_FX, tlo, thi, 0xffffffffu);
      sfor<2>([&](auto Dd) AI { constexpr int d = Dd; gld16<d * 1024, true>(otp[d], (unsigned)(wid * 2048 + l * 16), ws + WS_XC + (size_t)bid * 16384); });
    }
    waitbar_l<63>();
#pragma unroll
    for (int d = 0; d < 4; ++d) kep[d] = acc[4 + d][0] + *(LAS const f32x4*)(lds + 65536 + wid * 4096 + l * 16 + (d & 1) * 2048 + (d >> 1) * 32768);
    waitv<0>(otp[0], otp[1]);
  }
#pragma unroll
  for (int d = 0; d < 4; ++d) {
    const unsigned o0 = otp[d >> 1][2 * (d & 1)], o1 = otp[d >> 1][2 * (d & 1) + 1];
    const f32x4 a = kep[d] + (f32x4){bflo(o0), bfhi(o0), bflo(o1), bfhi(o1)};
    u32x2 res;
#pragma unroll
    for (int j = 0; j < 2; ++j) {
      const unsigned sp = pk2(a[2 * j], a[2 * j + 1]), gp = gate[2 * d + j], xw = xe[d][j];
      const unsigned pr = pk2(bflo(sp) * bflo(gp), bfhi(sp) * bfhi(gp));
      res[j] = pk2(bflo(xw) + bflo(pr), bfhi(xw) + bfhi(pr));
    }
    *(LAS u32x2*)(uintptr_t)(ex_a ^ (d << 5)) = res;
  }
#pragma unroll
  for (int i = 0; i < 2; ++i)
    gst16<0>(*(LAS const u32x4*)(uintptr_t)(ldsf + i * 8192 + l * 16), (unsigned)(((erow - c + i * 8 + (l >> 3)) * DM + nb * 128 + kh * 64 + ((l & 7) ^ (i * 4 + (l >> 4))) * 8) * 2), out);
}

extern "C" void kernel_launch(void* const* d_in, const int* in_sizes, int n_in, void* d_out, int out_size, void* d_ws, size_t ws_size,
                              hipStream_t stream) {
  k_fused<<<256, 512, 0, stream>>>((const u16*)d_in[0], (const u16*)d_in[1], (const u16*)d_in[2], (const u16*)d_in[3], (const u16*)d_in[4],
                                   (const u16*)d_in[6], (const u16*)d_in[7], (const u16*)d_in[8], (const u16*)d_in[9], (char*)d_ws, (u16*)d_out);
}
```

```cpp
#include <hip/hip_runtime.h>
#include <stdint.h>
#include <type_traits>
#pragma clang diagnostic ignored "-Winline-asm"

typedef unsigned short u16;
typedef short bf16x8 __attribute__((ext_vector_type(8)));
typedef short s16x4 __attribute__((ext_vector_type(4)));
typedef float f32x4 __attribute__((ext_vector_type(4)));
typedef float f32x2 __attribute__((ext_vector_type(2)));
typedef unsigned u32x4 __attribute__((ext_vector_type(4)));
typedef unsigned u32x2 __attribute__((ext_vector_type(2)));
typedef __bf16 bf2_t __attribute__((ext_vector_type(2)));
#define LAS __attribute__((address_space(3)))
#define DI __device__ __forceinline__
#define AI __attribute__((always_inline))
#define SBAR() __builtin_amdgcn_sched_barrier(0)
template <class T> __device__ __forceinline__ void PIN(T& x) { asm volatile("" : "+v"(x)); }
template <class B> DI void MF(f32x4& acc, const bf16x8& a, const B& b) { asm volatile("v_mfma_f32_16x16x32_bf16 %0, %1, %2, %0" : "+v"(acc) : "v"(a), "v"(b)); }
template <class B> DI void MFC(f32x4& d, const bf16x8& a, const B& b, const f32x4& c) { asm volatile("v_mfma_f32_16x16x32_bf16 %0, %1, %2, %3" : "=&v"(d) : "v"(a), "v"(b), "v"(c)); }
template <class B> DI void MFZ(f32x4& d, const bf16x8& a, const B& b) { asm volatile("v_mfma_f32_16x16x32_bf16 %0, %1, %2, 0" : "=&v"(d) : "v"(a), "v"(b)); }
#define NOPS(n) asm volatile("s_nop " #n)
#define MFMA16(a, b, c) __builtin_amdgcn_mfma_f32_16x16x16bf16_1k((a), (b), (c), 0, 0, 0)
#define IC(n) std::integral_constant<int, (n)>{}

constexpr int SEQ = 2048, DM = 1024, DH = 128, SK = 2048, WIN = 512, FF = 3584;
constexpr size_t WS_XN = 0;
constexpr size_t WS_KV = 4u << 20;
constexpr size_t WS_ST = 5u << 20;
constexpr size_t WS_EP = WS_ST + 16384;
constexpr size_t WS_G0 = WS_ST + 32768;
constexpr size_t WS_FK = WS_ST + 49152;
constexpr size_t WS_YP = WS_ST + 65536;
constexpr size_t WS_FO = 7u << 20;
constexpr size_t WS_FY = WS_FO + 65536;
constexpr size_t WS_FX = WS_FO + 131072;
constexpr size_t WS_O = 8u << 20;
constexpr size_t WS_GU = 32u << 20;
constexpr size_t WS_XC = 24u << 20;

template <int I, int N, class F> DI void sfor_impl(F&& f) {
  if constexpr (I < N) { f(std::integral_constant<int, I>{}); sfor_impl<I + 1, N>(f); }
}
template <int N, class F> DI void sfor(F&& f) { sfor_impl<0, N>(f); }

DI unsigned pk2(float a, float b) { f32x2 v = {a, b}; return __builtin_bit_cast(unsigned, __builtin_convertvector(v, bf2_t)); }
DI float rbf(float a) { return (float)(__bf16)a; }
DI float bflo(unsigned u) { return __uint_as_float(u << 16); }
DI float bfhi(unsigned u) { return __uint_as_float(u & 0xffff0000u); }
DI float sigmoid_f(float v) { return __builtin_amdgcn_rcpf(1.f + __builtin_amdgcn_exp2f(v * -1.4426950408889634f)); }
DI float sel2(bool c, float a, float b) { PIN(a); PIN(b); return c ? a : b; }
DI float silu_f(float v) { return v * sigmoid_f(v); }
template <int CTRL> DI float dppf(float x) { return __builtin_bit_cast(float, __builtin_amdgcn_mov_dpp(__builtin_bit_cast(int, x), CTRL, 0xf, 0xf, true)); }
template <int CTRL> DI unsigned dppu(unsigned x) { return (unsigned)__builtin_amdgcn_mov_dpp((int)x, CTRL, 0xf, 0xf, true); }
constexpr int XOR1 = 0xB1, XOR2 = 0x4E, XOR7 = 0x141, XOR8 = 0x128;
DI float xg_sum(float x) {
  auto s = __builtin_amdgcn_permlane16_swap(__float_as_uint(x), __float_as_uint(x), false, false);
  x = __uint_as_float(s[0]) + __uint_as_float(s[1]);
  auto t = __builtin_amdgcn_permlane32_swap(__float_as_uint(x), __float_as_uint(x), false, false);
  return __uint_as_float(t[0]) + __uint_as_float(t[1]);
}
DI float xg_max(float x) {
  auto s = __builtin_amdgcn_permlane16_swap(__float_as_uint(x), __float_as_uint(x), false, false);
  x = __builtin_fmaxf(__uint_as_float(s[0]), __uint_as_float(s[1]));
  auto t = __builtin_amdgcn_permlane32_swap(__float_as_uint(x), __float_as_uint(x), false, false);
  return __builtin_fmaxf(__uint_as_float(t[0]), __uint_as_float(t[1]));
}
DI float wave_sum(float v) { v += dppf<XOR1>(v); v += dppf<XOR2>(v); v += dppf<XOR7>(v); v += dppf<XOR8>(v); return xg_sum(v); }
DI unsigned umax(unsigned a, unsigned b) { return a > b ? a : b; }
DI unsigned wave_max_u32(unsigned v) {
  v = umax(v, dppu<XOR1>(v)); v = umax(v, dppu<XOR2>(v)); v = umax(v, dppu<XOR7>(v)); v = umax(v, dppu<XOR8>(v));
  auto s = __builtin_amdgcn_permlane16_swap(v, v, false, false); v = umax(s[0], s[1]);
  auto t = __builtin_amdgcn_permlane32_swap(v, v, false, false); v = umax(t[0], t[1]);
  return __builtin_amdgcn_readfirstlane(v);
}

template <int OFF, bool DEV = false> DI void glds(unsigned ldsaddr, unsigned voff, const void* sbase) {
  if constexpr (DEV) asm volatile("s_mov_b32 m0, %0\n\ts_nop 0\n\tglobal_load_lds_dwordx4 %1, %2 offset:%3 sc1" ::"s"(ldsaddr), "v"(voff), "s"(sbase), "i"(OFF) : "memory");
  else asm volatile("s_mov_b32 m0, %0\n\ts_nop 0\n\tglobal_load_lds_dwordx4 %1, %2 offset:%3" ::"s"(ldsaddr), "v"(voff), "s"(sbase), "i"(OFF) : "memory");
}
DI void touch(unsigned voff, const void* sbase) {
  asm volatile("global_load_dword v255, %0, %1" ::"v"(voff), "s"(sbase) : "memory", "v255");
}
template <int OFF, bool DEV = false, class T> DI void gld16(T& dst, unsigned voff, const void* sbase) {
  if constexpr (DEV) asm volatile("global_load_dwordx4 %0, %1, %2 offset:%3 sc1" : "=v"(dst) : "v"(voff), "s"(sbase), "i"(OFF) : "memory");
  else asm volatile("global_load_dwordx4 %0, %1, %2 offset:%3" : "=v"(dst) : "v"(voff), "s"(sbase), "i"(OFF) : "memory");
}
template <int OFF, bool DEV = false, class T> DI void gld8(T& dst, unsigned voff, const void* sbase) {
  if constexpr (DEV) asm volatile("global_load_dwordx2 %0, %1, %2 offset:%3 sc1" : "=v"(dst) : "v"(voff), "s"(sbase), "i"(OFF) : "memory");
  else asm volatile("global_load_dwordx2 %0, %1, %2 offset:%3" : "=v"(dst) : "v"(voff), "s"(sbase), "i"(OFF) : "memory");
}
template <int OFF> DI void gld2(unsigned& dst, unsigned voff, const void* sbase) {
  asm volatile("global_load_ushort %0, %1, %2 offset:%3" : "=v"(dst) : "v"(voff), "s"(sbase), "i"(OFF) : "memory");
}
template <int OFF, class T> DI void gst16(const T& v, unsigned voff, const void* sbase) {
  asm volatile("global_store_dwordx4 %0, %1, %2 offset:%3 sc1\n\ts_nop 1" ::"v"(voff), "v"(v), "s"(sbase), "i"(OFF) : "memory");
}
template <class T> DI void gst8(const T& v, unsigned voff, const void* sbase) {
  asm volatile("global_store_dwordx2 %0, %1, %2 sc1" ::"v"(voff), "v"(v), "s"(sbase) : "memory");
}
template <int OFF> DI void gst4(float v, unsigned voff, const void* sbase) {
  asm volatile("global_store_dword %0, %1, %2 offset:%3 sc1" ::"v"(voff), "v"(v), "s"(sbase), "i"(OFF) : "memory");
}
constexpr int SPINS = 1 << 16;
DI void poll8(unsigned voff, const void* sbase, unsigned lo, unsigned hi, unsigned lomask) {
  for (int spin = 0; spin < SPINS; ++spin) {
    u32x2 v;
    asm volatile("global_load_dwordx2 %0, %1, %2 sc1\n\ts_waitcnt vmcnt(0)" : "=&v"(v) : "v"(voff), "s"(sbase) : "memory");
    if (__all(((v[0] & lomask) == lo) & (v[1] == hi))) break;
    __builtin_amdgcn_s_sleep(1);
  }
}
template <int N> DI void waitv() { static_assert(N >= 0, "count"); asm volatile("s_waitcnt vmcnt(%0)" ::"n"(N > 63 ? 63 : N) : "memory"); }
template <int N, class T> DI void waitv(T& x) { static_assert(N >= 0, "count"); asm volatile("s_waitcnt vmcnt(%1)" : "+v"(x) : "n"(N > 63 ? 63 : N) : "memory"); }
template <int N, class T> DI void waitv(T& x, T& y) { static_assert(N >= 0, "count"); asm volatile("s_waitcnt vmcnt(%2)" : "+v"(x), "+v"(y) : "n"(N > 63 ? 63 : N) : "memory"); }
template <int N> DI void waitbar() { static_assert(N >= 0, "count"); asm volatile("s_waitcnt vmcnt(%0)\n\ts_barrier" ::"n"(N > 63 ? 63 : N) : "memory"); }
template <int N> DI void waitbar_l() { static_assert(N >= 0, "count"); asm volatile("s_waitcnt vmcnt(%0) lgkmcnt(0)\n\ts_barrier" ::"n"(N > 63 ? 63 : N) : "memory"); }
typedef short v4i16_t __attribute__((ext_vector_type(4)));
DI s16x4 trd(LAS const char* p) { return __builtin_amdgcn_ds_read_tr16_b64_v4i16((LAS v4i16_t*)p); }
struct LBase {
  unsigned lo, hi;
  DI void init(unsigned a) { lo = a; hi = a + 65536; PIN(lo); PIN(hi); }
  template <int OFF> DI LAS const char* at() const { return (LAS const char*)(uintptr_t)(OFF < 65536 ? lo : hi) + (OFF & 65535); }
};
DI bf16x8 cat8(s16x4 lo, s16x4 hi) { return __builtin_shufflevector(lo, hi, 0, 1, 2, 3, 4, 5, 6, 7); }

enum { OX, OG, OF, OM, OS, OP, OE, OW };
struct Sched {
  int n; short kind[400], idx[400], slot[400];
  constexpr Sched() : n(0), kind{}, idx{}, slot{} {}
  constexpr void add(int s, int k, int i) { kind[n] = (short)k; idx[n] = (short)i; slot[n] = (short)s; ++n; }
  constexpr int younger(int k, int i, int s) const {
    int p = -1;
    for (int q = 0; q < n; ++q) if (kind[q] == k && idx[q] == i) p = q;
    if (p < 0 || slot[p] > s) return -1;
    int c = 0;
    for (int q = p + 1; q < n; ++q) if (slot[q] <= s) ++c;
    return c;
  }
  constexpr int first(int s) const { int q = 0; while (q < n && slot[q] < s) ++q; return q; }
  constexpr int count(int s) const { int c = 0; for (int q = 0; q < n; ++q) if (slot[q] == s) ++c; return c; }
};


constexpr int RING = 4 * 32768, FB = RING, RED1 = FB + 24576, HSC = RED1 + 1024, YS = HSC + 1024, LDS_BYTES = FB + 32768;
constexpr int STAGE = 3 * 32768;
constexpr int XD = 16, XD2 = 16;
constexpr float LBOUND = 65536.f;
constexpr float QSCALE = 0.08838834764831845f * 1.4426950408889634f;
constexpr int B0 = 128;
constexpr int FISS[4] = {96, 97, 98, 117}, FRDS[4] = {115, 118, 121, 124};
constexpr int fpiece(int sl) { for (int p = 0; p < 4; ++p) if (FRDS[p] == sl) return p; return -1; }
constexpr Sched make_sched() {
  Sched s;
  constexpr int XP = 8;
  for (int i = 0; i < 12; ++i) s.add(-2, OG, i);
  for (int i = 0; i < XP; ++i) s.add(-1, OX, i);
  for (int t = 0; t < 128; ++t) {
    if (t < 2) for (int i = XP + t * (16 - XP) / 2; i < XP + (t + 1) * (16 - XP) / 2; ++i) s.add(t, OX, i);
    if (t >= 2 && t < 50) s.add(t, OX, t + 14);
    for (int v = 3; v < 18; ++v) for (int p = 0; p < 4; ++p) if (8 * (v - 3) + 7 + p == t) s.add(t, OG, 4 * v + p);
    if (t == 105 || t == 113 || t == 121) { s.add(t, OW, (t - 105) / 4); s.add(t, OW, (t - 105) / 4 + 1); }
    if (t >= 124) for (int i = 0; i < 3; ++i) s.add(t, OX, 64 + 3 * (t - 124) + i);
    if (t == 127) { s.add(t, OG, 72); s.add(t, OG, 73); }
    if (t == 20) s.add(t, OM, 0);
    if (t == 120) s.add(t, OM, 1);
    if (t >= 49 && t < 57 && (t & 1)) s.add(t, OF, (t - 49) / 2);
    if (t >= 71 && t < 79 && (t & 1)) s.add(t, OF, 4 + (t - 71) / 2);
    if (t == 48) s.add(t, OS, 0);
    if (t == 104) s.add(t, OS, 2);
    for (int p = 0; p < 4; ++p) if (FISS[p] == t) s.add(t, OF, 8 + p);
  }
  for (int t = 0; t < 64; ++t) {
    if (t == 0) { s.add(B0, OM, 2); s.add(B0, OG, 74); s.add(B0, OG, 75); s.add(B0, OW, 6); s.add(B0, OW, 7); }
    if (t == 1 || t == 2) { s.add(B0 + t, OX, 74 + 2 * t); s.add(B0 + t, OX, 75 + 2 * t); }
    if (t == 5 || t == 6 || t == 9) s.add(B0 + t, OX, 64 + (t == 5 ? 16 : t == 6 ? 18 : 22));
    if (t >= 5 && t < 50) s.add(B0 + t, OX, 64 + (t == 5 ? 17 : t < 9 ? t + 13 : t + 14));
    for (int v = 3; v < 16; ++v) for (int p = 0; p < 4; ++p) if ((v < 14 ? 4 * (v - 3) + 3 + (p >> 1) : v == 14 ? 45 + (p >> 1) : 49) == t) s.add(B0 + t, OG, 64 + 4 * v + p);
    if (t == 10 || t == 26) s.add(B0 + t, OM, t == 10 ? 3 : 4);
    if (t == 13 || t == 14) s.add(B0 + t, OE, t - 13);
    if (t == 14) s.add(B0 + t, OE, 2);
  }
  return s;
}
constexpr Sched SC = make_sched();

__global__ __launch_bounds__(512, 2) __attribute__((amdgpu_num_vgpr(126))) void k_fused(const u16* __restrict__ x, const u16* __restrict__ kc, const u16* __restrict__ vc,
                                                  const u16* __restrict__ wq, const u16* __restrict__ wo, const u16* __restrict__ wr,
                                                  const u16* __restrict__ wgate, const u16* __restrict__ wup, const u16* __restrict__ wdown,
                                                  char* __restrict__ ws, u16* __restrict__ out) {
  __shared__ __attribute__((aligned(16))) char lds_raw[LDS_BYTES];
  LAS char* const lds = (LAS char*)lds_raw;
  const int t = threadIdx.x, l = t & 63, bid = blockIdx.x, c = l & 15, g = l >> 4;
  const int wid = __builtin_amdgcn_readfirstlane(t >> 6);
  const int xcd = bid & 7, within = bid >> 3, head = xcd * 4 + (within & 3), rb = within >> 2;
  const int fj = xcd * 28 + (within < 28 ? within : within - 28);
  const int fsl = fj / 7, fgr = fj - fsl * 7;
  const int kh = xcd >> 2, mb = 2 * rb + (xcd & 1), nb = ((xcd >> 1) & 1) * 4 + (within & 3);
  const int rbw = wid & 3, kg = wid >> 2;
  const unsigned lds0 = (unsigned)(uintptr_t)lds;
  unsigned tlo, thi;
  {
    const int row = 8 * bid + wid;
    u32x4 a0, a1, w0, w1, kvo;
    u32x2 ep, klo, khi;
    unsigned e[8];
    gld16<0>(a0, (unsigned)(row * DM * 2 + l * 16), x); gld16<1024>(a1, (unsigned)(row * DM * 2 + l * 16), x);
    gld16<0>(w0, (unsigned)(l * 16), wr); gld16<1024>(w1, (unsigned)(l * 16), wr);
    gld8<0, true>(ep, 0u, ws + WS_EP);
    if (wid == 1)
      asm volatile("global_load_dword v255, %0, %1 sc1" ::"v"(l == 0 ? (unsigned)(WS_G0 - WS_ST) + bid * 8 : l == 1 ? (unsigned)(WS_FK - WS_ST) + bid * 32 : (unsigned)(bid * 64)), "s"(ws + WS_ST) : "memory", "v255");
    if (wid == 3) {
      const char* pc;
      asm volatile("s_getpc_b64 %0" : "=s"(pc));
      touch((unsigned)((within * 12 + (l < 12 ? l : 11)) * 128), pc);
    }
    __builtin_amdgcn_s_barrier();
    if (wid == 2) {
      asm volatile("global_load_dword v255, %0, %1 sc1" ::"v"((unsigned)((l < 40 ? l : 39) << 20) + 64 * 1024 - 128), "s"(ws) : "memory", "v255");
      const char* pb = l < 8 ? (const char*)wo : l < 16 ? (const char*)wgate : l < 24 ? (const char*)wup : l < 32 ? (const char*)wdown : (const char*)out;
      pb += (size_t)(l & 7) * (l < 8 ? 1048576 : l < 32 ? 917504 : 524288);
      asm volatile("global_load_dword v255, %0, off" ::"v"(pb) : "memory", "v255");
    }
    const int fn = (bid * 4 + wid) & 255, kvh = bid >> 6;
    if (wid < 4) {
      if (fn < 128) {
        const unsigned ko = (unsigned)(((kvh * SK + (SK - WIN) + (fn >> 2) * 16 + c) * DH + (fn & 3) * 32 + 4 * g) * 2);
        gld8<0>(klo, ko, kc); gld8<32>(khi, ko, kc);
      } else {
        const int d = 32 * ((fn - 128) >> 5) + 8 * (c >> 2) + 4 * (fn & 1) + (c & 3);
        const unsigned vo = (unsigned)(((kvh * SK + (SK - WIN) + ((fn >> 1) & 15) * 32 + 4 * g + 8) * DH + d) * 2);
        sfor<8>([&](auto J) AI { constexpr int j = J; gld2<(16 * (j >> 2) + (j & 3) - 8) * DH * 2>(e[j], vo, vc); });
      }
      if (wid < 2) touch((unsigned)((wid * 256 + within * 8 + (l >> 3)) * 8192 + xcd * 1024 + (l & 7) * 128), wq);
      if (fn < 128) waitv<6>(); else waitv<12>();
    } else {
      waitv<3>();
    }
    PIN(a0); PIN(a1);
    float v[16];
#pragma unroll
    for (int j = 0; j < 4; ++j) { v[2 * j] = bflo(a0[j]); v[2 * j + 1] = bfhi(a0[j]); v[8 + 2 * j] = bflo(a1[j]); v[9 + 2 * j] = bfhi(a1[j]); }
    float s = 0.f;
#pragma unroll
    for (int j = 0; j < 16; ++j) s += v[j];
    const float mean = wave_sum(s) * (1.f / DM);
    float q = 0.f;
#pragma unroll
    for (int j = 0; j < 16; ++j) { const float d = v[j] - mean; q += d * d; }
    const float var = wave_sum(q) * (1.f / DM);
    const float mbf = rbf(mean), rden = __builtin_amdgcn_rcpf(rbf(__builtin_amdgcn_sqrtf(rbf(rbf(var) + 1e-5f))));
    u32x4 o0, o1;
#pragma unroll
    for (int j = 0; j < 8; ++j) {
      const unsigned dd = pk2(v[2 * j] - mbf, v[2 * j + 1] - mbf);
      const unsigned nn = pk2(bflo(dd) * rden, bfhi(dd) * rden);
      if (j < 4) o0[j] = nn; else o1[j - 4] = nn;
    }
    *(LAS u32x4*)(lds + STAGE + (l * 8 + (wid ^ (l & 7))) * 16) = o0;
    *(LAS u32x4*)(lds + STAGE + 8192 + (l * 8 + (wid ^ (l & 7))) * 16) = o1;
    *(LAS f32x2*)(lds + STAGE + 16384 + 32 + wid * 8) = (f32x2){mbf, rden};
    waitbar_l<63>();
    if (wid >= 4) {
#pragma unroll
      for (int hh = 0; hh < 4; ++hh) {
        const int p = hh * 256 + (t - 256), ch = p >> 3, r = 8 * bid + ((p & 7) ^ (ch & 7));
        const u32x4 o = *(LAS const u32x4*)(lds + STAGE + p * 16);
        gst16<0>(o, (unsigned)(((((r >> 5) * 32 + (ch >> 2)) * 2 + ((r >> 4) & 1)) * 64 + (ch & 3) * 16 + (r & 15)) * 16), ws + WS_XN);
      }
      if (t < 256 + 8) gst8(*(LAS const f32x2*)(lds + STAGE + 16384 + 32 + (t - 256) * 8), (unsigned)((8 * bid + t - 256) * 8), ws + WS_ST);
      waitv<4>();
    } else if (wid == 0 || wid == 3) { if (fn < 128) waitv<3>(); else waitv<9>(); }
    else if (fn < 128) waitv<4>(); else waitv<10>();
    PIN(w0); PIN(w1); PIN(ep);
    {
      const unsigned long long tg = (((unsigned long long)(unsigned)__builtin_amdgcn_readfirstlane(ep[1]) << 32) | (unsigned)__builtin_amdgcn_readfirstlane(ep[0])) + 1ull;
      tlo = (unsigned)tg; thi = (unsigned)(tg >> 32);
    }
    float rr = 0.f;
#pragma unroll
    for (int j = 0; j < 8; ++j) {
      const unsigned nn = j < 4 ? o0[j] : o1[j - 4], ww = j < 4 ? w0[j] : w1[j - 4];
      rr += bflo(nn) * bflo(ww); rr += bfhi(nn) * bfhi(ww);
    }
    rr = wave_sum(rr);
    {
      const unsigned bits = pk2(rr, 0.f) & 0xffffu;
      const unsigned ord = (bits & 0x8000u) ? (~bits & 0xffffu) : (bits | 0x8000u);
      *(LAS unsigned*)(lds + STAGE + 16384 + wid * 4) = (ord << 11) | (unsigned)(SEQ - 1 - row);
    }
    if (wid >= 4) waitv<0>();
    waitbar_l<63>();
    if (t == 256) {
      const u32x4 ka = *(LAS const u32x4*)(lds + STAGE + 16384), kb = *(LAS const u32x4*)(lds + STAGE + 16400);
      const unsigned key = umax(umax(umax(ka[0], ka[1]), umax(ka[2], ka[3])), umax(umax(kb[0], kb[1]), umax(kb[2], kb[3])));
      gst8((u32x2){(tlo << 27) | key, (tlo >> 5) | (thi << 27)}, (unsigned)(bid * 8), ws + WS_G0);
    }
    if (wid < 4) {
      if (wid < 2) waitv<1>(); else waitv<0>();
      if (fn < 128) {
        PIN(klo); PIN(khi);
        kvo = (u32x4){klo[0], klo[1], khi[0], khi[1]};
      } else {
#pragma unroll
        for (int j = 0; j < 8; ++j) PIN(e[j]);
        kvo = (u32x4){e[0] | (e[1] << 16), e[2] | (e[3] << 16), e[4] | (e[5] << 16), e[6] | (e[7] << 16)};
      }
      gst16<0>(kvo, (unsigned)((bid * 4 + wid) * 1024 + l * 16), ws + WS_KV);
    }
  }
  const unsigned glo = tlo << 27, ghi = (tlo >> 5) | (thi << 27);

  const char* const wq_b = (const char*)wq + head * 256;
  const unsigned wq_v = (unsigned)(((8 * wid + (l >> 3)) * 4096 + ((l & 7) ^ ((wid & 1) * 4 + ((l >> 3) & 2))) * 8) * 2);
  const char* const kv_b = ws + WS_KV + (size_t)(head >> 3) * 262144;
  const unsigned kv_v = (unsigned)(wid * 4096 + l * 16);
  const char* const xn_b = ws + WS_XN + (size_t)rb * 8 * 65536 + 4096;
  const unsigned xn_v = (unsigned)(wid * 65536 + l * 16);
  const char* const wo_b = (const char*)wo + (size_t)kh * 2048 * (DM * 2) + nb * 256;
  const char* const o_b = ws + WS_O + (size_t)mb * 4 * 262144 + kh * 131072;
  const unsigned ldsw = lds0 + wid * 2048, ldsk = lds0 + wid * 4096, ldsf = lds0 + FB + wid * 1024;
  const int erow = mb * 128 + rbw * 32 + kg * 16 + c;
  bf16x8 xf[XD];
  u32x4 fm[2];
  bf16x8 fxs;
  const unsigned gu_v = (unsigned)((((4 * wid + g) * 2 + ((l >> 3) & 1)) * FF + fj * 16 + (l & 7) * 2) * 8);
  u32x2 fg;
  u32x2 xe[4];
  f32x2 st;
  unsigned key = 0;

  const unsigned fo_v = (unsigned)(((mb * 4 + rbw) * 32 + kh * 16 + c) * 8);
  const int yrow = within < 28 ? fj : 224 + xcd * 4 + within - 28;
  f32x4 o[2][2][2];
  float inv[2];
  const char* const od_b = ws + WS_O + ((size_t)(rb * 8) * 128 + head * 4) * 2048 + 4096;
  auto issue = [&](auto Sl, auto Pos) AI {
    constexpr int sl = Sl, pos = Pos, cnt = SC.count(sl), q0 = SC.first(sl) + (pos < 0 ? 0 : pos);
    constexpr int nq = pos < 0 ? cnt : pos >= cnt ? 0 : pos < 3 ? 1 : cnt - 3;
    sfor<nq>([&](auto Qi) AI {
      constexpr int k = SC.kind[q0 + Qi], i = SC.idx[q0 + Qi];
      if constexpr (k == OX && i < 64) gld16<(i & 7) * 1024 - 4096, true>(xf[i % XD], xn_v, xn_b + (size_t)(i >> 3) * 8192);
      if constexpr (k == OX && i >= 64) {
        constexpr int q = i - 64, v = q >> 2;
        gld16<0, true>(xf[q % XD2], (unsigned)(rbw * 262144 + kg * 16384 + ((q & 1) ^ kg) * 1024 + l * 16), o_b + (size_t)(((v & 3) * 4 + ((q >> 1) & 1)) * 8192 + (v >> 2) * 2048));
      }
      if constexpr (k == OG) {
        constexpr int v = i >> 2, p = i & 3;
        if constexpr (v < 8) {
          const char* sb = wq_b + (size_t)(v * 128 + (p >> 1) * 64) * 8192;
          glds<(p & 1) * 128>(ldsw + (v & 3) * 32768 + (p >> 1) * 16384 + (p & 1) * (1024 - 128), wq_v, sb);
        } else if constexpr (v < 16) {
          glds<p * 1024, true>(ldsk + (v & 3) * 32768, kv_v, kv_b + (size_t)(v - 8) * 32768);
        } else {
          const unsigned wo_v = (unsigned)((((wid >> 2) * 128 + (wid & 3) * 8 + (l >> 3)) * DM + ((l & 7) ^ ((wid & 1) * 4 + ((l >> 3) & 2))) * 8) * 2);
          const char* sb = wo_b + (size_t)(((v & 3) * 4 + (p >> 1) * 2) * 128 + ((v - 16) >> 2) * 32) * (DM * 2) + (((p & 1) ^ kh ^ 1) << 7);
          glds<0>(ldsw + (v & 3) * 32768 + (p >> 1) * 16384 + (p & 1) * 1024, wo_v, sb);
        }
      }
      if constexpr (k == OF && i < 8)
        glds<0>(ldsf + (i & 3) * 8192, (unsigned)(((l >> 3) * FF + wid * 64 + ((l & 7) ^ ((i & 1) * 4 + ((l >> 3) & 2))) * 8) * 2),
                (const char*)(i < 4 ? wgate : wup) + ((size_t)(fsl * 32 + 8 * (i & 3)) * FF + fgr * 512) * 2);
      if constexpr (k == OF && i >= 8) {
        const int frho = l >> 2, fch = (l & 3) ^ (frho & 4 ? 2 : 0);
        glds<0>(ldsf + ((i - 8) % 3) * 8192, (unsigned)((frho * DM + wid * 128 + fch * 8) * 2), (const char*)wdown + (size_t)fj * 16 * DM * 2 + (i - 8) * 64);
      }
      if constexpr (k == OS && i == 0) {
        const int idx = SEQ - 1 - (int)(key & 2047u);
        gld16<0, true>(fxs, (unsigned)((((idx >> 5) * 32 + fsl) * 2 + ((idx >> 4) & 1)) * 1024 + (g * 16 + (idx & 15)) * 16), ws + WS_XN);
      }
      if constexpr (k == OS && i == 2) gld16<0, true>(fm[0], gu_v, ws + WS_GU);
      if constexpr (k == OM && i > 0) gld8<0, true>(fg, fo_v, ws + WS_FO + (i - 1) * 16384);
      if constexpr (k == OM && i == 0) gld8<0, true>(fg, (unsigned)((t & 255) * 8), wid < 4 ? ws + WS_G0 : ws + WS_FK + (head >> 3) * 2048);
      if constexpr (k == OW) {
        constexpr int os = i >> 1, qb = i & 1;
        PIN(o[os & 1][0][qb]); PIN(o[os & 1][1][qb]);
        const f32x4 a = o[os & 1][0][qb] * inv[qb], b = o[os & 1][1][qb] * inv[qb];
        gst16<i * 1024 - 4096>((u32x4){pk2(a[0], a[1]), pk2(a[2], a[3]), pk2(b[0], b[1]), pk2(b[2], b[3])}, (unsigned)(wid * 262144 + l * 16), od_b);
      }
      if constexpr (k == OE && i < 2)
        glds<0>(ldsf + i * 8192, (unsigned)(((erow - c + i * 8 + (l >> 3)) * DM + nb * 128 + kh * 64 + ((l & 7) ^ (i * 4 + (l >> 4))) * 8) * 2), x);
      if constexpr (k == OE && i == 2) gld8<0, true>(st, (unsigned)(erow * 8), ws + WS_ST);
    });
  };
  f32x4 fa0, fa1, fq[4];
  s16x4 fb0, fb1, fxa;
  bf16x8 ft[2];
  auto endslot = [&](auto Sl) AI {
    constexpr int sl = Sl, u = sl >> 3, i = sl & 7;
    if constexpr (sl == 3) {
      if (wid == 6 || wid == 7) touch((unsigned)(((wid - 4) * 256 + within * 8 + (l >> 3)) * 8192 + xcd * 1024 + (l & 7) * 128), wq);
    }
    if constexpr (sl == 2) {
      if (wid < 4 && l == 0) gst8((u32x2){tlo, thi}, (unsigned)((bid * 4 + wid) * 8), ws + WS_FK);
    }
    if constexpr (sl == 38) {
      waitv<SC.younger(OM, 0, sl)>(fg);
      const unsigned lo = wid < 4 ? glo : tlo, hi = wid < 4 ? ghi : thi, mk = wid < 4 ? 0xf8000000u : 0xffffffffu;
      for (int spin = 0; spin < SPINS; ++spin) {
        if (__builtin_expect(__all(((fg[0] & mk) == lo) & (fg[1] == hi)), 1)) break;
        __builtin_amdgcn_s_sleep(1);
        gld8<0, true>(fg, (unsigned)((t & 255) * 8), wid < 4 ? ws + WS_G0 : ws + WS_FK + (head >> 3) * 2048);
        waitv<0>(fg);
      }
      const unsigned km = wave_max_u32(fg[0] & 0x07ffffffu);
      *(LAS unsigned*)(lds + HSC + wid * 4) = km;
    }
    if constexpr (sl == 57) {
      if (wid == 3) {
        const char* pc;
        asm volatile("s_getpc_b64 %0" : "=s"(pc));
        touch((unsigned)((within * 8 + (l & 7)) * 128), pc);
      }
    }
    if constexpr (sl == 47) {
      if (bid == 0 && t == 0) gst8((u32x2){tlo, thi}, 0u, ws + WS_EP);
      const u32x4 kk = *(LAS const u32x4*)(lds + HSC);
      key = __builtin_amdgcn_readfirstlane(umax(umax(kk[0], kk[1]), umax(kk[2], kk[3])));
    }
    if constexpr (sl == 58 || sl == 61 || sl == 64 || sl == 67) {
      if (wid == 4 + (sl - 58) / 3) touch((unsigned)(((wid & 3) * 64 + l) * 128), (const char*)wdown + (size_t)fj * 16 * DM * 2);
    }
    if constexpr (sl == 81 || sl == 85 || sl == 89 || sl == 93) {
      if (wid == (sl - 81) / 4)
        touch((unsigned)((kh * 2048 + (within >> 1) * 128 + wid * 32 + ((within & 1) * 2 + (xcd & 1)) * 8 + (l >> 3)) * (DM * 2) + ((xcd >> 1) & 1) * 1024 + (l & 7) * 128), wo);
    }
    if constexpr (sl == 66) {
      const unsigned fo = (unsigned)(WS_FO - WS_ST) + (l & 3) * 16384 + ((mb * 4 + rbw) * 32 + kh * 16) * 8, fx = (unsigned)(WS_FX - WS_ST) + (bid * 8 + wid) * 64;
      const unsigned fy = (unsigned)(WS_FY - WS_ST) + (wid * 256 + yrow) * 8;
      asm volatile("global_load_dword v255, %0, %1 sc1" ::"v"(l < 4 ? fo : l < 8 ? fy : fx), "s"(ws + WS_ST) : "memory", "v255");
    }
    if constexpr (sl == 97 || sl == 99) {
      if (wid == 4 + (sl - 97) / 2) touch((unsigned)(((mb * 128 + (wid - 4) * 64 + l) * DM + nb * 128 + kh * 64) * 2), x);
    }
    if constexpr ((sl >= 68 && sl <= 70) || (sl >= 90 && sl <= 92)) {
      constexpr int r = sl >= 90, q = sl - (r ? 90 : 68);
      if constexpr (q == 0) waitv<SC.younger(OF, 4 * r + 3, sl)>(fxs);
      if constexpr (q >= 1) {
#pragma unroll
        for (int j = 0; j < 2; ++j) { fq[2 * q - 2 + j] = __builtin_amdgcn_mfma_f32_16x16x32_bf16(fxs, ft[j], (f32x4){}, 0, 0, 0); PIN(fq[2 * q - 2 + j]); }
      }
      if constexpr (q < 2) {
        const unsigned rp = ldsf + g * 8192 + (c >> 2) * 128 + ((((g & 1) << 1) | (c >> 3)) << 5) + (c & 3) * 8;
#pragma unroll
        for (int j = 0; j < 2; ++j) { LAS const char* a = (LAS const char*)(uintptr_t)(rp ^ ((2 * q + j) << 5)); ft[j] = cat8(trd(a), trd(a + 512)); }
      }
    }
    if constexpr (sl == 71 || sl == 93) {
      const float v = sel2(g & 2, sel2(g & 1, fq[3][0], fq[2][0]), sel2(g & 1, fq[1][0], fq[0][0]));
      gst8((u32x2){__float_as_uint(v), tlo}, (unsigned)(((fsl * 2 + (sl == 93)) * FF + fgr * 512 + t) * 8), ws + WS_GU);
    }
    if constexpr (sl == 109) {
      waitv<SC.younger(OS, 2, sl)>(fm[0]);
      for (int spin = 0; spin < SPINS; ++spin) {
        if (__builtin_expect(__all((fm[0][1] == tlo) & (fm[0][3] == tlo)), 1)) break;
        __builtin_amdgcn_s_sleep(1);
        gld16<0, true>(fm[0], gu_v, ws + WS_GU);
        waitv<0>(fm[0]);
      }
      *(LAS f32x2*)(lds + RED1 + (wid * 32 + 2 * c) * 4) = (f32x2){xg_sum(__uint_as_float(fm[0][0])), xg_sum(__uint_as_float(fm[0][2]))};
    }
    if constexpr (sl == 111) {
#pragma unroll
      for (int w = 0; w < 2; ++w) { fa0[w] = *(LAS const float*)(lds + RED1 + ((2 * g + w) * 32 + c) * 4); fa0[2 + w] = *(LAS const float*)(lds + RED1 + ((2 * g + w) * 32 + 16 + c) * 4); }
    }
    if constexpr (sl == 112) { fa0[0] = xg_sum(fa0[0] + fa0[1]); fa0[2] = xg_sum(fa0[2] + fa0[3]); PIN(fa0); }
    if constexpr (sl == 113) {
      const float hh = rbf(silu_f(rbf(fa0[0]))) * rbf(fa0[2]);
      *(LAS u16*)(lds + HSC + 32 + wid * 32 + c * 2) = (u16)pk2(hh, hh);
    }
    if constexpr (sl == 114) fxa = *(LAS const s16x4*)(lds + HSC + 32 + wid * 32 + g * 8);
    constexpr int pr = fpiece(sl), pm = fpiece(sl - 1), ps = fpiece(sl - 2);
    if constexpr (ps >= 0) gst4<ps * 128>(sel2(g & 1, fa1[0], fa0[0]), (unsigned)((wid * 128 + (l & 31)) * 4), ws + WS_YP + (size_t)yrow * 4096);
    if constexpr (pm >= 0) { fa0 = MFMA16(fxa, fb0, (f32x4){}); fa1 = MFMA16(fxa, fb1, (f32x4){}); PIN(fa0); PIN(fa1); }
    if constexpr (pr >= 0) {
      waitv<SC.younger(OF, 8 + pr, sl)>();
      const unsigned fp = ldsf + (4 * g + (c >> 2)) * 64 + (g & 1) * 32 + (c & 3) * 8;
      fb0 = trd((LAS const char*)(uintptr_t)fp + (pr % 3) * 8192); fb1 = trd((LAS const char*)(uintptr_t)(fp ^ 32) + (pr % 3) * 8192);
    }
    if constexpr (sl == 111 || sl == 119 || sl == 127) {
      waitv<SC.younger(OW, (sl - 111) / 4 + 1, sl)>();
      if (l == 0) gst8((u32x2){tlo, thi}, (unsigned)(((rb * 8 + wid) * 32 + head) * 8), ws + WS_FO + ((sl - 111) / 8) * 16384);
    }
    if constexpr (sl == 123) {
      waitv<SC.younger(OM, 1, sl)>(fg);
      if (__builtin_expect(!__all((fg[0] == tlo) & (fg[1] == thi)), 0)) poll8(fo_v, ws + WS_FO, tlo, thi, 0xffffffffu);
    }
    SBAR();
    if constexpr (i == 6) {
      if constexpr (u == 5 || u == 13) waitbar_l<SC.younger(OG, 4 * u + 7, sl)>();
      else waitbar<SC.younger(OG, 4 * u + 7, sl)>();
    }
    if constexpr (!(i & 1)) __builtin_amdgcn_s_setprio(i == 6 ? 3 : 2 - i / 2);
  };

  issue(IC(-2), IC(-1));
  if (wid == 5) poll8((unsigned)((rb * 32 + (l & 31)) * 8), ws + WS_G0, glo, ghi, 0xf8000000u);
  waitbar<SC.younger(OG, 3, -2)>();
  issue(IC(-1), IC(-1));
  LBase trb[4], frb;
#pragma unroll
  for (int j = 0; j < 4; ++j) trb[j].init(lds0 + g * 2048 + (c >> 2) * 128 + ((j ^ ((g & 1) << 1) ^ (c >> 3)) << 5) + (c & 3) * 8);
  frb.init(lds0 + l * 16);
  f32x4 acc[8][2];
  bf16x8 wf[2][4];
  auto WRD1 = [&](auto Si, auto Base, auto Dd) AI {
    constexpr int s = Si, d = Dd, o = Base;
    wf[s & 1][d] = cat8(trd(trb[d].template at<o>()), trd(trb[d].template at<o + 512>()));
  };
  auto WRD = [&](auto Si, auto Base) AI { sfor<4>([&](auto Dd) AI { WRD1(Si, Base, Dd); }); };
#define WA(s) IC((((s) >> 3) & 3) * 32768 + (((s) >> 1) & 3) * 8192 + ((s) & 1) * 1024)
#define WB(s) IC((((s) >> 2) & 3) * 32768 + (((s) >> 1) & 1) * 8192 + ((s) & 1) * 1024)
  bf16x8 fr[4];
  auto FRD = [&](auto Ni) AI { constexpr int n = Ni; if constexpr (n < 256) fr[n & 3] = *(LAS const bf16x8*)(frb.at<((n >> 5) & 3) * 32768 + (n & 31) * 1024>()); };

  WRD(IC(0), WA(0));
  SBAR();
  bf16x8 qf[4][2];
  auto QPACK = [&](auto Ds) AI {
    constexpr int ds = Ds;
#pragma unroll
    for (int qb = 0; qb < 2; ++qb) {
      const f32x4 a = acc[2 * ds][qb] * QSCALE, b = acc[2 * ds + 1][qb] * QSCALE;
      qf[ds][qb] = __builtin_bit_cast(bf16x8, (u32x4){pk2(a[0], a[1]), pk2(a[2], a[3]), pk2(b[0], b[1]), pk2(b[2], b[3])});
      PIN(qf[ds][qb]);
    }
  };
  sfor<64>([&](auto Si) AI {
    constexpr int s = Si, x0 = (s & ~1) % XD, x1 = (s | 1) % XD;
    if constexpr (!(s & 1)) waitv<SC.younger(OX, s + 1, s - 1)>(xf[x0], xf[x1]);
    sfor<4>([&](auto Dd) AI {
      constexpr int d = Dd, dt = (s & 1) * 4 + d;
      if constexpr (s + 1 < 64) WRD1(IC(s + 1), WA(s + 1), Dd);
      else if constexpr (d < 3) FRD(Dd);
      SBAR();
      if constexpr (s < 2) { MFZ(acc[dt][0], wf[s & 1][d], xf[x0]); MFZ(acc[dt][1], wf[s & 1][d], xf[x1]); }
      else { MF(acc[dt][0], wf[s & 1][d], xf[x0]); MF(acc[dt][1], wf[s & 1][d], xf[x1]); }
      issue(Si, Dd);
      if constexpr (s == 63 && (d & 1)) { QPACK(IC(d >> 1)); SBAR(); }
    });
    SBAR();
    endslot(Si);
  });

  f32x4 sc[2][2][2], negm[2];
  u32x4 pw[16][2];
  f32x4 lsum[2];
  bf16x8 ones = {0x3f80, 0x3f80, 0x3f80, 0x3f80, 0x3f80, 0x3f80, 0x3f80, 0x3f80};
  PIN(ones);
#define MX3(a, b, c) __builtin_fmaxf(__builtin_fmaxf((a), (b)), (c))
#define EXP2(x) __builtin_amdgcn_exp2f(x)
  auto SM = [&](auto Hi, auto Pi) AI {
    constexpr int H = Hi, p = Pi, kt = p >> 2, qb = (p >> 1) & 1, r = 2 * (p & 1);
    const float e0 = EXP2(sc[H & 1][kt][qb][r]), e1 = EXP2(sc[H & 1][kt][qb][r + 1]);
    unsigned w = pk2(e0, e1);
    PIN(w);
    pw[H][qb][kt * 2 + (p & 1)] = w;
  };
  sfor<128>([&](auto Ni) AI {
    constexpr int n = Ni, H = n >> 3, i = n & 7, kt = i >> 2, ds = i & 3, sn = H & 1;
    FRD(IC(n + 3));
    if constexpr (n < 2) QPACK(IC(2 + n));
    sfor<2>([&](auto Qb) AI {
      constexpr int qb = Qb;
      if constexpr (ds == 0 && H == 0) MFZ(sc[sn][kt][qb], fr[n & 3], qf[0][qb]);
      else if constexpr (ds == 0) MFC(sc[sn][kt][qb], fr[n & 3], qf[0][qb], negm[qb]);
      else MF(sc[sn][kt][qb], fr[n & 3], qf[ds][qb]);
    });
    if constexpr (n == 5 || n == 6) {
      constexpr int qb = n - 5;
      NOPS(3); PIN(sc[0][0][qb]);
      const float m = -xg_max(__builtin_fmaxf(MX3(sc[0][0][qb][0], sc[0][0][qb][1], sc[0][0][qb][2]), sc[0][0][qb][3]));
      negm[qb] = (f32x4){m, m, m, m};
      sc[0][0][qb] += negm[qb];
      PIN(negm[qb]); PIN(sc[0][0][qb]);
    }
    if constexpr (n == 9 || n == 10) { NOPS(3); PIN(sc[0][1][n - 9]); sc[0][1][n - 9] += negm[n - 9]; PIN(sc[0][1][n - 9]); }
    issue(IC(64 + (n >> 2)), IC(n & 3));
    if constexpr (H == 2 && (i == 1 || i == 5)) MFZ(lsum[i >> 2], ones, pw[0][i >> 2]);
    if constexpr (H > 2 && (i == 1 || i == 5)) MF(lsum[i >> 2], ones, pw[H - 2][i >> 2]);
    if constexpr (H >= 1 && H < 15) SM(IC(H - 1), IC(i));
    if constexpr (n == 125) { PIN(negm[0]); PIN(negm[1]); }
    SBAR();
    if constexpr ((n & 3) == 3) endslot(IC(64 + (n >> 2)));
  });
  constexpr int CHK = 32;
  sfor<128>([&](auto Mi) AI {
    constexpr int m = Mi, n = 128 + m, os = m >> 5, H = (m >> 1) & 15, par = m & 1;
    FRD(IC(n + 3));
    if constexpr (H == 0) { MFZ(o[os & 1][par][0], fr[n & 3], pw[H][0]); MFZ(o[os & 1][par][1], fr[n & 3], pw[H][1]); }
    else { MF(o[os & 1][par][0], fr[n & 3], pw[H][0]); MF(o[os & 1][par][1], fr[n & 3], pw[H][1]); }
    if constexpr (m < 26 && (m % 13) % 5 != 1 && (m % 13) % 5 != 3) {
      constexpr int r = m % 13, p = r / 5 * 3 + (r % 5) / 2;
      SM(IC(14 + m / 13), IC(p));
    }
    if constexpr (m == 14 || m == 16) MF(lsum[(m - 14) / 2], ones, pw[14][(m - 14) / 2]);
    if constexpr (m == 27 || m == 28) MF(lsum[m - 27], ones, pw[15][m - 27]);
    if constexpr (m == CHK + 1 || m == CHK + 2) { inv[m - CHK - 1] = __builtin_amdgcn_rcpf(lsum[m - CHK - 1][0]); PIN(inv[m - CHK - 1]); }
    issue(IC(64 + (n >> 2)), IC(n & 3));
    if constexpr (m == 123) {
#pragma unroll
      for (int j = 0; j < 4; ++j) trb[j].init(lds0 + kg * 16384 + g * 2048 + (c >> 2) * 128 + ((j ^ ((g & 1) << 1) ^ (c >> 3)) << 5) + (c & 3) * 8);
    }
    if constexpr (m >= 124) WRD1(IC(0), WB(0), IC(m - 124));
    SBAR();
    if constexpr ((n & 3) == 3) endslot(IC(64 + (n >> 2)));
    if constexpr (m == CHK - 1) {
      PIN(lsum[0]); PIN(lsum[1]);
      if (__builtin_expect(__any(!(lsum[0][0] + lsum[1][0] <= LBOUND)), 0)) {
        NOPS(7); SBAR();
        auto tile = [&](unsigned off, f32x4& s0, f32x4& s1) AI {
          bf16x8 kf[4];
          SBAR(); NOPS(7);
          sfor<4>([&](auto Ds) AI { constexpr int ds = Ds; gld16<ds * 1024, true>(kf[ds], off, kv_b); });
          waitv<0>(kf[0], kf[1]);
          PIN(kf[2]); PIN(kf[3]);
#pragma unroll
          for (int ds = 0; ds < 4; ++ds) {
            s0 = __builtin_amdgcn_mfma_f32_16x16x32_bf16(kf[ds], qf[ds][0], s0, 0, 0, 0);
            s1 = __builtin_amdgcn_mfma_f32_16x16x32_bf16(kf[ds], qf[ds][1], s1, 0, 0, 0);
          }
        };
        float m0 = -__builtin_inff(), m1 = m0;
#pragma unroll 1
        for (int kt = 0; kt < 32; ++kt) {
          f32x4 s0 = {}, s1 = {};
          tile((unsigned)(kt * 4096 + l * 16), s0, s1);
          m0 = MX3(MX3(m0, s0[0], s0[1]), s0[2], s0[3]); m1 = MX3(MX3(m1, s1[0], s1[1]), s1[2], s1[3]);
        }
        m0 = -xg_max(m0); m1 = -xg_max(m1);
        lsum[0] = (f32x4){}; lsum[1] = (f32x4){};
        sfor<16>([&](auto Hi) AI {
          constexpr int H = Hi;
#pragma unroll
          for (int kt = 0; kt < 2; ++kt) {
            sc[H & 1][kt][0] = (f32x4){m0, m0, m0, m0}; sc[H & 1][kt][1] = (f32x4){m1, m1, m1, m1};
            tile((unsigned)((2 * H + kt) * 4096 + l * 16), sc[H & 1][kt][0], sc[H & 1][kt][1]);
          }
          sfor<8>([&](auto Pi) AI { SM(Hi, Pi); });
          PIN(pw[H][0]); PIN(pw[H][1]);
          lsum[0] = __builtin_amdgcn_mfma_f32_16x16x32_bf16(ones, __builtin_bit_cast(bf16x8, pw[H][0]), lsum[0], 0, 0, 0);
          lsum[1] = __builtin_amdgcn_mfma_f32_16x16x32_bf16(ones, __builtin_bit_cast(bf16x8, pw[H][1]), lsum[1], 0, 0, 0);
        });
#pragma unroll
        for (int par = 0; par < 2; ++par) { o[0][par][0] = (f32x4){}; o[0][par][1] = (f32x4){}; }
        sfor<CHK / 4>([&](auto Qi) AI {
          constexpr int q = Qi;
          bf16x8 vf[4];
          SBAR(); NOPS(7);
          sfor<4>([&](auto J) AI { constexpr int j = J; gld16<j * 1024, true>(vf[j], (unsigned)((128 + 4 * q) * 1024 + l * 16), kv_b); });
          waitv<0>(vf[0], vf[1]);
          PIN(vf[2]); PIN(vf[3]);
#pragma unroll
          for (int j = 0; j < 4; ++j)
#pragma unroll
            for (int qb = 0; qb < 2; ++qb)
              o[0][j & 1][qb] = __builtin_amdgcn_mfma_f32_16x16x32_bf16(vf[j], __builtin_bit_cast(bf16x8, pw[2 * q + (j >> 1)][qb]), o[0][j & 1][qb], 0, 0, 0);
        });
#pragma unroll
        for (int par = 0; par < 2; ++par) { PIN(o[0][par][0]); PIN(o[0][par][1]); }
        PIN(lsum[0]); PIN(lsum[1]);
        SBAR(); NOPS(7);
      }
      SBAR();
    }
  });
  const int idx = SEQ - 1 - (int)(key & 2047u);
  const bool owner = (idx >> 7) == mb, ownw = (idx >> 4) == mb * 8 + rbw * 2 + kg;
  f32x4 yp[7], oth[4];
  unsigned gate[8];
  float glow;
  auto xnorm = [&](auto Ji) AI {
    constexpr int j = Ji;
    const unsigned xw = xe[j >> 1][j & 1], dd = pk2(bflo(xw) - st[0], bfhi(xw) - st[0]);
    return pk2(bflo(dd) * st[1], bfhi(dd) * st[1]);
  };
  const unsigned ex_a = ldsf + (c >> 3) * 8192 + (c & 7) * 128 + (((g >> 1) ^ (c >> 1)) << 4) + (g & 1) * 8;
  auto item = [](int s) { return s < 48 ? s : s < 56 ? 48 + 2 * (s - 48) : 49 + 2 * (s - 56); };
  u32x2 pf;
  u32x4 otp[2];
  bool rdy = false;
  sfor<64>([&](auto Si) AI {
    constexpr int s = Si, u = s >> 2, it = item(s), x0 = (it & ~1) % XD2, x1 = (it | 1) % XD2;
    if constexpr (!(it & 1) && s <= 52) waitv<SC.younger(OX, 64 + it + 1, B0 + s - 1)>(xf[x0], xf[x1]);
    if constexpr (!(it & 1) && s > 52) { PIN(xf[x0]); PIN(xf[x1]); }
    sfor<4>([&](auto Dd) AI {
      constexpr int d = Dd, dt = (it & 1) * 4 + d;
      if constexpr (s + 1 < 64) WRD1(IC(s + 1), WB(item(s + 1)), Dd);
      SBAR();
      if constexpr (s < 2) { MFZ(acc[dt][0], wf[s & 1][d], xf[x0]); MFZ(acc[dt][1], wf[s & 1][d], xf[x1]); }
      else { MF(acc[dt][0], wf[s & 1][d], xf[x0]); MF(acc[dt][1], wf[s & 1][d], xf[x1]); }
      issue(IC(B0 + s), Dd);
      if constexpr (s == 56) *(LAS f32x4*)(lds + (wid ^ 4) * 4096 + l * 16 + (d & 1) * 2048 + (d >> 1) * 32768) = acc[d][1];
      if constexpr (s == 57 && d == 0) {
#pragma unroll
        for (int e = 0; e < 4; ++e) oth[e] = *(LAS const f32x4*)(lds + wid * 4096 + l * 16 + (e & 1) * 2048 + (e >> 1) * 32768);
      }
      if constexpr (s == 57 && d >= 2) {
        const f32x4 a = acc[2 * d - 4][0] + oth[2 * d - 4], b = acc[2 * d - 3][0] + oth[2 * d - 3];
        gst16<(d - 2) * 1024>((u32x4){pk2(a[0], a[1]), pk2(a[2], a[3]), pk2(b[0], b[1]), pk2(b[2], b[3])}, (unsigned)(wid * 2048 + l * 16), ws + WS_XC + (size_t)(bid ^ 4) * 16384);
      }
      if constexpr (s == 28) xe[d] = *(LAS const u32x2*)(uintptr_t)(ex_a ^ (d << 5));
      if constexpr (s >= 29 && s < 37 && d == 1) { PIN(st); gate[s - 29] = xnorm(IC(s - 29)); PIN(gate[s - 29]); }
      if constexpr (s >= 37 && s < 53 && d == 1) {
        constexpr int j = (s - 37) >> 1;
        if constexpr ((s - 37) & 1) { gate[j] = pk2(glow, silu_f(bfhi(gate[j]))); PIN(gate[j]); }
        else { glow = silu_f(bflo(gate[j])); PIN(glow); }
      }
    });
    SBAR();
    if constexpr (s == 7) {
      waitv<SC.younger(OW, 7, B0 + s)>();
      if (l == 0) {
        gst8((u32x2){tlo, thi}, (unsigned)(((rb * 8 + wid) * 32 + head) * 8), ws + WS_FO + 3 * 16384);
        gst8((u32x2){tlo, thi}, (unsigned)((wid * 256 + yrow) * 8), ws + WS_FY);
      }
    }
    if constexpr (s == 4 || s == 17 || s == 33) {
      constexpr int j = s == 4 ? 1 : s == 17 ? 2 : 3;
      waitv<SC.younger(OM, j + 1, B0 + s)>(fg);
      if (__builtin_expect(!__all((fg[0] == tlo) & (fg[1] == thi)), 0)) poll8(fo_v, ws + WS_FO + j * 16384, tlo, thi, 0xffffffffu);
    }
    if constexpr (s == 59) gld8<0, true>(pf, (unsigned)((bid * 8 + wid) * 64), ws + WS_FX);
    if constexpr (s == 60) {
      waitv<1>();
      if (l == 0) gst8((u32x2){tlo, thi}, (unsigned)(((bid ^ 4) * 8 + wid) * 64), ws + WS_FX);
    }
    if constexpr (s == 62) {
      waitv<1>(pf);
      rdy = __all((pf[0] == tlo) & (pf[1] == thi));
      if (rdy) sfor<2>([&](auto Dd) AI { constexpr int d = Dd; gld16<d * 1024, true>(otp[d], (unsigned)(wid * 2048 + l * 16), ws + WS_XC + (size_t)bid * 16384); });
    }
    if constexpr (s == 37) {
      if (owner) { gld16<0, true>(fm[0], (unsigned)(nb * 2048 + (l < 56 ? l : 55) * 32), ws + WS_FY); gld16<16, true>(fm[1], (unsigned)(nb * 2048 + (l < 56 ? l : 55) * 32), ws + WS_FY); }
    }
    if constexpr (s == 53) {
      if (owner) {
        PIN(fm[0]); PIN(fm[1]);
        for (int spin = 0; spin < SPINS; ++spin) {
          const bool ok = (fm[0][0] == tlo) & (fm[0][1] == thi) & (fm[0][2] == tlo) & (fm[0][3] == thi) & (fm[1][0] == tlo) & (fm[1][1] == thi) & (fm[1][2] == tlo) & (fm[1][3] == thi);
          if (__builtin_expect(__all(ok), 1)) break;
          __builtin_amdgcn_s_sleep(1);
          gld16<0, true>(fm[0], (unsigned)(nb * 2048 + (l < 56 ? l : 55) * 32), ws + WS_FY); gld16<16, true>(fm[1], (unsigned)(nb * 2048 + (l < 56 ? l : 55) * 32), ws + WS_FY);
          waitv<0>(fm[0], fm[1]);
        }
        const unsigned yv = (unsigned)(((t >> 4) * 1024 + nb * 128 + kh * 64 + (t & 15) * 4) * 4);
        sfor<7>([&](auto I) AI { constexpr int q = I; gld16<0, true>(yp[q], yv, ws + WS_YP + (size_t)q * 32 * 4096); });
      }
    }
    if constexpr (s == 61) {
      if (owner) {
        f32x4 a = {};
#pragma unroll
        for (int q = 0; q < 7; ++q) { PIN(yp[q]); a += yp[q]; }
#pragma unroll
        for (int j = 0; j < 4; ++j) a[j] = xg_sum(a[j]);
        *(LAS f32x4*)(lds + YS + (wid * 64 + c * 4) * 4) = a;
      }
    }
    SBAR();
    if constexpr (((s & 3) == 2 && u < 11) || s == 44) {
      waitbar<SC.younger(OG, 64 + 4 * u + 7, B0 + s)>();
    }
    if constexpr (s == 48) waitbar<SC.younger(OG, 64 + 55, B0 + s)>();
    if constexpr (s == 50) waitbar<SC.younger(OG, 64 + 59, B0 + s)>();
    if constexpr (s == 52) { waitbar<SC.younger(OG, 64 + 63, B0 + s)>(); }
    if constexpr (s == 56) waitbar_l<63>();
    __builtin_amdgcn_s_setprio(3 - ((s + 2) & 3));
  });
  f32x4 kep[4];
  {
    NOPS(7);
#pragma unroll
    for (int d = 0; d < 4; ++d) { PIN(acc[4 + d][1]); *(LAS f32x4*)(lds + 65536 + (wid ^ 4) * 4096 + l * 16 + (d & 1) * 2048 + (d >> 1) * 32768) = acc[4 + d][1]; }
    if (!rdy) {
      poll8((unsigned)((bid * 8 + wid) * 64), ws + WS_FX, tlo, thi, 0xffffffffu);
      sfor<2>([&](auto Dd) AI { constexpr int d = Dd; gld16<d * 1024, true>(otp[d], (unsigned)(wid * 2048 + l * 16), ws + WS_XC + (size_t)bid * 16384); });
    }
    waitbar_l<63>();
#pragma unroll
    for (int d = 0; d < 4; ++d) kep[d] = acc[4 + d][0] + *(LAS const f32x4*)(lds + 65536 + wid * 4096 + l * 16 + (d & 1) * 2048 + (d >> 1) * 32768);
    {
      if (ownw) {
        const int rc = idx & 15;
        float a = 0.f;
#pragma unroll
        for (int p = 0; p < 8; ++p) a += *(LAS const float*)(lds + YS + (p * 64 + l) * 4);
        const float xv = bflo(*(LAS const u16*)(uintptr_t)(ldsf + (rc >> 3) * 8192 + (rc & 7) * 128 + (((l >> 3) ^ (rc >> 1)) << 4) + (l & 7) * 2));
        const float s0 = st[0], s1 = st[1];
        const float m = __int_as_float(__builtin_amdgcn_readlane(__float_as_int(s0), rc)), rd = __int_as_float(__builtin_amdgcn_readlane(__float_as_int(s1), rc));
        const unsigned ord = key >> 11, bits = (ord & 0x8000u) ? (ord & 0x7fffu) : (~ord & 0xffffu);
        const float gv = silu_f(rbf(rbf(rbf(xv - m) * rd) + rbf(rbf(a) * rbf(sigmoid_f(bflo(bits))))));
        *(LAS u16*)(lds + RED1 + l * 2) = (u16)pk2(gv, gv);
        asm volatile("" ::: "memory");
        u32x2 w[4];
#pragma unroll
        for (int q = 0; q < 4; ++q) w[q] = *(LAS const u32x2*)(lds + RED1 + (q * 16 + 4 * g) * 2);
#pragma unroll
        for (int j = 0; j < 8; ++j) { gate[j] = c == rc ? w[j >> 1][j & 1] : gate[j]; PIN(gate[j]); }
      }
    }
    waitv<0>(otp[0], otp[1]);
  }
#pragma unroll
  for (int d = 0; d < 4; ++d) {
    const unsigned o0 = otp[d >> 1][2 * (d & 1)], o1 = otp[d >> 1][2 * (d & 1) + 1];
    const f32x4 a = kep[d] + (f32x4){bflo(o0), bfhi(o0), bflo(o1), bfhi(o1)};
    u32x2 res;
#pragma unroll
    for (int j = 0; j < 2; ++j) {
      const unsigned sp = pk2(a[2 * j], a[2 * j + 1]), gp = gate[2 * d + j], xw = xe[d][j];
      const unsigned pr = pk2(bflo(sp) * bflo(gp), bfhi(sp) * bfhi(gp));
      res[j] = pk2(bflo(xw) + bflo(pr), bfhi(xw) + bfhi(pr));
    }
    *(LAS u32x2*)(uintptr_t)(ex_a ^ (d << 5)) = res;
  }
#pragma unroll
  for (int i = 0; i < 2; ++i)
    gst16<0>(*(LAS const u32x4*)(uintptr_t)(ldsf + i * 8192 + l * 16), (unsigned)(((erow - c + i * 8 + (l >> 3)) * DM + nb * 128 + kh * 64 + ((l & 7) ^ (i * 4 + (l >> 4))) * 8) * 2), out);
}

extern "C" void kernel_launch(void* const* d_in, const int* in_sizes, int n_in, void* d_out, int out_size, void* d_ws, size_t ws_size,
                              hipStream_t stream) {
  k_fused<<<256, 512, 0, stream>>>((const u16*)d_in[0], (const u16*)d_in[1], (const u16*)d_in[2], (const u16*)d_in[3], (const u16*)d_in[4],
                                   (const u16*)d_in[6], (const u16*)d_in[7], (const u16*)d_in[8], (const u16*)d_in[9], (char*)d_ws, (u16*)d_out);
}
```

```cpp
#include <hip/hip_runtime.h>
#include <stdint.h>
#include <type_traits>
#pragma clang diagnostic ignored "-Winline-asm"

typedef unsigned short u16;
typedef short bf16x8 __attribute__((ext_vector_type(8)));
typedef short s16x4 __attribute__((ext_vector_type(4)));
typedef float f32x4 __attribute__((ext_vector_type(4)));
typedef float f32x2 __attribute__((ext_vector_type(2)));
typedef unsigned u32x4 __attribute__((ext_vector_type(4)));
typedef unsigned u32x2 __attribute__((ext_vector_type(2)));
typedef __bf16 bf2_t __attribute__((ext_vector_type(2)));
#define LAS __attribute__((address_space(3)))
#define DI __device__ __forceinline__
#define AI __attribute__((always_inline))
#define SBAR() __builtin_amdgcn_sched_barrier(0)
template <class T> __device__ __forceinline__ void PIN(T& x) { asm volatile("" : "+v"(x)); }
template <class B> DI void MF(f32x4& acc, const bf16x8& a, const B& b) { asm volatile("v_mfma_f32_16x16x32_bf16 %0, %1, %2, %0" : "+v"(acc) : "v"(a), "v"(b)); }
template <class B> DI void MFC(f32x4& d, const bf16x8& a, const B& b, const f32x4& c) { asm volatile("v_mfma_f32_16x16x32_bf16 %0, %1, %2, %3" : "=&v"(d) : "v"(a), "v"(b), "v"(c)); }
template <class B> DI void MFZ(f32x4& d, const bf16x8& a, const B& b) { asm volatile("v_mfma_f32_16x16x32_bf16 %0, %1, %2, 0" : "=&v"(d) : "v"(a), "v"(b)); }
#define NOPS(n) asm volatile("s_nop " #n)
#define MFMA16(a, b, c) __builtin_amdgcn_mfma_f32_16x16x16bf16_1k((a), (b), (c), 0, 0, 0)
#define IC(n) std::integral_constant<int, (n)>{}

constexpr int SEQ = 2048, DM = 1024, DH = 128, SK = 2048, WIN = 512, FF = 3584;
constexpr size_t WS_XN = 0;
constexpr size_t WS_KV = 4u << 20;
constexpr size_t WS_ST = 5u << 20;
constexpr size_t WS_EP = WS_ST + 16384;
constexpr size_t WS_G0 = WS_ST + 32768;
constexpr size_t WS_FK = WS_ST + 49152;
constexpr size_t WS_YP = WS_ST + 65536;
constexpr size_t WS_FO = 7u << 20;
constexpr size_t WS_FY = WS_FO + 65536;
constexpr size_t WS_FX = WS_FO + 131072;
constexpr size_t WS_O = 8u << 20;
constexpr size_t WS_GU = 32u << 20;
constexpr size_t WS_XC = 24u << 20;

template <int I, int N, class F> DI void sfor_impl(F&& f) {
  if constexpr (I < N) { f(std::integral_constant<int, I>{}); sfor_impl<I + 1, N>(f); }
}
template <int N, class F> DI void sfor(F&& f) { sfor_impl<0, N>(f); }

DI unsigned pk2(float a, float b) { f32x2 v = {a, b}; return __builtin_bit_cast(unsigned, __builtin_convertvector(v, bf2_t)); }
DI float rbf(float a) { return (float)(__bf16)a; }
DI float bflo(unsigned u) { return __uint_as_float(u << 16); }
DI float bfhi(unsigned u) { return __uint_as_float(u & 0xffff0000u); }
DI float sigmoid_f(float v) { return __builtin_amdgcn_rcpf(1.f + __builtin_amdgcn_exp2f(v * -1.4426950408889634f)); }
DI float sel2(bool c, float a, float b) { PIN(a); PIN(b); return c ? a : b; }
DI float silu_f(float v) { return v * sigmoid_f(v); }
template <int CTRL> DI float dppf(float x) { return __builtin_bit_cast(float, __builtin_amdgcn_mov_dpp(__builtin_bit_cast(int, x), CTRL, 0xf, 0xf, true)); }
template <int CTRL> DI unsigned dppu(unsigned x) { return (unsigned)__builtin_amdgcn_mov_dpp((int)x, CTRL, 0xf, 0xf, true); }
constexpr int XOR1 = 0xB1, XOR2 = 0x4E, XOR7 = 0x141, XOR8 = 0x128;
DI float xg_sum(float x) {
  auto s = __builtin_amdgcn_permlane16_swap(__float_as_uint(x), __float_as_uint(x), false, false);
  x = __uint_as_float(s[0]) + __uint_as_float(s[1]);
  auto t = __builtin_amdgcn_permlane32_swap(__float_as_uint(x), __float_as_uint(x), false, false);
  return __uint_as_float(t[0]) + __uint_as_float(t[1]);
}
DI float xg_max(float x) {
  auto s = __builtin_amdgcn_permlane16_swap(__float_as_uint(x), __float_as_uint(x), false, false);
  x = __builtin_fmaxf(__uint_as_float(s[0]), __uint_as_float(s[1]));
  auto t = __builtin_amdgcn_permlane32_swap(__float_as_uint(x), __float_as_uint(x), false, false);
  return __builtin_fmaxf(__uint_as_float(t[0]), __uint_as_float(t[1]));
}
DI float wave_sum(float v) { v += dppf<XOR1>(v); v += dppf<XOR2>(v); v += dppf<XOR7>(v); v += dppf<XOR8>(v); return xg_sum(v); }
DI unsigned umax(unsigned a, unsigned b) { return a > b ? a : b; }
DI unsigned wave_max_u32(unsigned v) {
  v = umax(v, dppu<XOR1>(v)); v = umax(v, dppu<XOR2>(v)); v = umax(v, dppu<XOR7>(v)); v = umax(v, dppu<XOR8>(v));
  auto s = __builtin_amdgcn_permlane16_swap(v, v, false, false); v = umax(s[0], s[1]);
  auto t = __builtin_amdgcn_permlane32_swap(v, v, false, false); v = umax(t[0], t[1]);
  return __builtin_amdgcn_readfirstlane(v);
}

template <int OFF, bool DEV = false> DI void glds(unsigned ldsaddr, unsigned voff, const void* sbase) {
  if constexpr (DEV) asm volatile("s_mov_b32 m0, %0\n\ts_nop 0\n\tglobal_load_lds_dwordx4 %1, %2 offset:%3 sc1" ::"s"(ldsaddr), "v"(voff), "s"(sbase), "i"(OFF) : "memory");
  else asm volatile("s_mov_b32 m0, %0\n\ts_nop 0\n\tglobal_load_lds_dwordx4 %1, %2 offset:%3" ::"s"(ldsaddr), "v"(voff), "s"(sbase), "i"(OFF) : "memory");
}
DI void touch(unsigned voff, const void* sbase) {
  asm volatile("global_load_dword v255, %0, %1" ::"v"(voff), "s"(sbase) : "memory", "v255");
}
template <int OFF, bool DEV = false, class T> DI void gld16(T& dst, unsigned voff, const void* sbase) {
  if constexpr (DEV) asm volatile("global_load_dwordx4 %0, %1, %2 offset:%3 sc1" : "=v"(dst) : "v"(voff), "s"(sbase), "i"(OFF) : "memory");
  else asm volatile("global_load_dwordx4 %0, %1, %2 offset:%3" : "=v"(dst) : "v"(voff), "s"(sbase), "i"(OFF) : "memory");
}
template <int OFF, bool DEV = false, class T> DI void gld8(T& dst, unsigned voff, const void* sbase) {
  if constexpr (DEV) asm volatile("global_load_dwordx2 %0, %1, %2 offset:%3 sc1" : "=v"(dst) : "v"(voff), "s"(sbase), "i"(OFF) : "memory");
  else asm volatile("global_load_dwordx2 %0, %1, %2 offset:%3" : "=v"(dst) : "v"(voff), "s"(sbase), "i"(OFF) : "memory");
}
template <int OFF> DI void gld2(unsigned& dst, unsigned voff, const void* sbase) {
  asm volatile("global_load_ushort %0, %1, %2 offset:%3" : "=v"(dst) : "v"(voff), "s"(sbase), "i"(OFF) : "memory");
}
template <int OFF, class T> DI void gst16(const T& v, unsigned voff, const void* sbase) {
  asm volatile("global_store_dwordx4 %0, %1, %2 offset:%3 sc1\n\ts_nop 1" ::"v"(voff), "v"(v), "s"(sbase), "i"(OFF) : "memory");
}
template <class T> DI void gst8(const T& v, unsigned voff, const void* sbase) {
  asm volatile("global_store_dwordx2 %0, %1, %2 sc1" ::"v"(voff), "v"(v), "s"(sbase) : "memory");
}
template <int OFF> DI void gst4(float v, unsigned voff, const void* sbase) {
  asm volatile("global_store_dword %0, %1, %2 offset:%3 sc1" ::"v"(voff), "v"(v), "s"(sbase), "i"(OFF) : "memory");
}
constexpr int SPINS = 1 << 16;
DI void poll8(unsigned voff, const void* sbase, unsigned lo, unsigned hi, unsigned lomask) {
  for (int spin = 0; spin < SPINS; ++spin) {
    u32x2 v;
    asm volatile("global_load_dwordx2 %0, %1, %2 sc1\n\ts_waitcnt vmcnt(0)" : "=&v"(v) : "v"(voff), "s"(sbase) : "memory");
    if (__all(((v[0] & lomask) == lo) & (v[1] == hi))) break;
    __builtin_amdgcn_s_sleep(1);
  }
}
template <int N> DI void waitv() { static_assert(N >= 0, "count"); asm volatile("s_waitcnt vmcnt(%0)" ::"n"(N > 63 ? 63 : N) : "memory"); }
template <int N, class T> DI void waitv(T& x) { static_assert(N >= 0, "count"); asm volatile("s_waitcnt vmcnt(%1)" : "+v"(x) : "n"(N > 63 ? 63 : N) : "memory"); }
template <int N, class T> DI void waitv(T& x, T& y) { static_assert(N >= 0, "count"); asm volatile("s_waitcnt vmcnt(%2)" : "+v"(x), "+v"(y) : "n"(N > 63 ? 63 : N) : "memory"); }
template <int N> DI void waitbar() { static_assert(N >= 0, "count"); asm volatile("s_waitcnt vmcnt(%0)\n\ts_barrier" ::"n"(N > 63 ? 63 : N) : "memory"); }
template <int N> DI void waitbar_l() { static_assert(N >= 0, "count"); asm volatile("s_waitcnt vmcnt(%0) lgkmcnt(0)\n\ts_barrier" ::"n"(N > 63 ? 63 : N) : "memory"); }
typedef short v4i16_t __attribute__((ext_vector_type(4)));
DI s16x4 trd(LAS const char* p) { return __builtin_amdgcn_ds_read_tr16_b64_v4i16((LAS v4i16_t*)p); }
struct LBase {
  unsigned lo, hi;
  DI void init(unsigned a) { lo = a; hi = a + 65536; PIN(lo); PIN(hi); }
  template <int OFF> DI LAS const char* at() const { return (LAS const char*)(uintptr_t)(OFF < 65536 ? lo : hi) + (OFF & 65535); }
};
DI bf16x8 cat8(s16x4 lo, s16x4 hi) { return __builtin_shufflevector(lo, hi, 0, 1, 2, 3, 4, 5, 6, 7); }

enum { OX, OG, OF, OM, OS, OP, OE, OW };
struct Sched {
  int n; short kind[400], idx[400], slot[400];
  constexpr Sched() : n(0), kind{}, idx{}, slot{} {}
  constexpr void add(int s, int k, int i) { kind[n] = (short)k; idx[n] = (short)i; slot[n] = (short)s; ++n; }
  constexpr int younger(int k, int i, int s) const {
    int p = -1;
    for (int q = 0; q < n; ++q) if (kind[q] == k && idx[q] == i) p = q;
    if (p < 0 || slot[p] > s) return -1;
    int c = 0;
    for (int q = p + 1; q < n; ++q) if (slot[q] <= s) ++c;
    return c;
  }
  constexpr int first(int s) const { int q = 0; while (q < n && slot[q] < s) ++q; return q; }
  constexpr int count(int s) const { int c = 0; for (int q = 0; q < n; ++q) if (slot[q] == s) ++c; return c; }
};


constexpr int RING = 4 * 32768, FB = RING, RED1 = FB + 24576, HSC = RED1 + 1024, YS = HSC + 1024, LDS_BYTES = FB + 32768;
constexpr int STAGE = 3 * 32768;
constexpr int XD = 16, XD2 = 16;
constexpr float LBOUND = 65536.f;
constexpr float QSCALE = 0.08838834764831845f * 1.4426950408889634f;
constexpr int B0 = 128;
constexpr int FISS[4] = {96, 97, 98, 117}, FRDS[4] = {115, 118, 121, 124};
constexpr int fpiece(int sl) { for (int p = 0; p < 4; ++p) if (FRDS[p] == sl) return p; return -1; }
constexpr int XPAUSE = 6;
constexpr int xslot(int q) {
  if (q < 20) return q - 14;
  int lag = XPAUSE - (q < 28 ? 0 : (q - 28) / 5 + 1);
  return q - 14 + (lag > 0 ? lag : 0);
}
constexpr Sched make_sched() {
  Sched s;
  constexpr int XP = 8;
  for (int i = 0; i < 12; ++i) s.add(-2, OG, i);
  for (int i = 0; i < XP; ++i) s.add(-1, OX, i);
  for (int t = 0; t < 128; ++t) {
    if (t < 2) for (int i = XP + t * (16 - XP) / 2; i < XP + (t + 1) * (16 - XP) / 2; ++i) s.add(t, OX, i);
    for (int q = 16; q < 64; ++q) if (xslot(q) == t) s.add(t, OX, q);
    for (int v = 3; v < 18; ++v) for (int p = 0; p < 4; ++p) if (8 * (v - 3) + 7 + p == t) s.add(t, OG, 4 * v + p);
    if (t == 105 || t == 113 || t == 121) { s.add(t, OW, (t - 105) / 4); s.add(t, OW, (t - 105) / 4 + 1); }
    if (t >= 124) for (int i = 0; i < 3; ++i) s.add(t, OX, 64 + 3 * (t - 124) + i);
    if (t == 127) { s.add(t, OG, 72); s.add(t, OG, 73); }
    if (t == 20) s.add(t, OM, 0);
    if (t == 120) s.add(t, OM, 1);
    if (t >= 49 && t < 57 && (t & 1)) s.add(t, OF, (t - 49) / 2);
    if (t >= 71 && t < 79 && (t & 1)) s.add(t, OF, 4 + (t - 71) / 2);
    if (t == 48) s.add(t, OS, 0);
    if (t == 104) s.add(t, OS, 2);
    for (int p = 0; p < 4; ++p) if (FISS[p] == t) s.add(t, OF, 8 + p);
  }
  for (int t = 0; t < 64; ++t) {
    if (t == 0) { s.add(B0, OM, 2); s.add(B0, OG, 74); s.add(B0, OG, 75); s.add(B0, OW, 6); s.add(B0, OW, 7); }
    if (t == 1 || t == 2) { s.add(B0 + t, OX, 74 + 2 * t); s.add(B0 + t, OX, 75 + 2 * t); }
    if (t == 5 || t == 6 || t == 9) s.add(B0 + t, OX, 64 + (t == 5 ? 16 : t == 6 ? 18 : 22));
    if (t >= 5 && t < 50) s.add(B0 + t, OX, 64 + (t == 5 ? 17 : t < 9 ? t + 13 : t + 14));
    for (int v = 3; v < 16; ++v) for (int p = 0; p < 4; ++p) if ((v < 14 ? 4 * (v - 3) + 3 + (p >> 1) : v == 14 ? 45 + (p >> 1) : 49) == t) s.add(B0 + t, OG, 64 + 4 * v + p);
    if (t == 10 || t == 26) s.add(B0 + t, OM, t == 10 ? 3 : 4);
    if (t == 13 || t == 14) s.add(B0 + t, OE, t - 13);
    if (t == 14) s.add(B0 + t, OE, 2);
  }
  return s;
}
constexpr Sched SC = make_sched();

__global__ __launch_bounds__(512, 2) __attribute__((amdgpu_num_vgpr(126))) void k_fused(const u16* __restrict__ x, const u16* __restrict__ kc, const u16* __restrict__ vc,
                                                  const u16* __restrict__ wq, const u16* __restrict__ wo, const u16* __restrict__ wr,
                                                  const u16* __restrict__ wgate, const u16* __restrict__ wup, const u16* __restrict__ wdown,
                                                  char* __restrict__ ws, u16* __restrict__ out) {
  __shared__ __attribute__((aligned(16))) char lds_raw[LDS_BYTES];
  LAS char* const lds = (LAS char*)lds_raw;
  const int t = threadIdx.x, l = t & 63, bid = blockIdx.x, c = l & 15, g = l >> 4;
  const int wid = __builtin_amdgcn_readfirstlane(t >> 6);
  const int xcd = bid & 7, within = bid >> 3, head = xcd * 4 + (within & 3), rb = within >> 2;
  const int fj = xcd * 28 + (within < 28 ? within : within - 28);
  const int fsl = fj / 7, fgr = fj - fsl * 7;
  const int kh = xcd >> 2, mb = 2 * rb + (xcd & 1), nb = ((xcd >> 1) & 1) * 4 + (within & 3);
  const int rbw = wid & 3, kg = wid >> 2;
  const unsigned lds0 = (unsigned)(uintptr_t)lds;
  unsigned tlo, thi;
  {
    const int row = 8 * bid + wid;
    u32x4 a0, a1, w0, w1, kvo;
    u32x2 ep, klo, khi;
    unsigned e[8];
    gld16<0>(a0, (unsigned)(row * DM * 2 + l * 16), x); gld16<1024>(a1, (unsigned)(row * DM * 2 + l * 16), x);
    gld16<0>(w0, (unsigned)(l * 16), wr); gld16<1024>(w1, (unsigned)(l * 16), wr);
    gld8<0, true>(ep, 0u, ws + WS_EP);
    if (wid == 1)
      asm volatile("global_load_dword v255, %0, %1 sc1" ::"v"(l == 0 ? (unsigned)(WS_G0 - WS_ST) + bid * 8 : l == 1 ? (unsigned)(WS_FK - WS_ST) + bid * 32 : (unsigned)(bid * 64)), "s"(ws + WS_ST) : "memory", "v255");
    if (wid == 3) {
      const char* pc;
      asm volatile("s_getpc_b64 %0" : "=s"(pc));
      touch((unsigned)((within * 12 + (l < 12 ? l : 11)) * 128), pc);
    }
    __builtin_amdgcn_s_barrier();
    if (wid == 2) {
      asm volatile("global_load_dword v255, %0, %1 sc1" ::"v"((unsigned)((l < 40 ? l : 39) << 20) + 64 * 1024 - 128), "s"(ws) : "memory", "v255");
      const char* pb = l < 8 ? (const char*)wo : l < 16 ? (const char*)wgate : l < 24 ? (const char*)wup : l < 32 ? (const char*)wdown : (const char*)out;
      pb += (size_t)(l & 7) * (l < 8 ? 1048576 : l < 32 ? 917504 : 524288);
      asm volatile("global_load_dword v255, %0, off" ::"v"(pb) : "memory", "v255");
    }
    const int fn = (bid * 4 + wid) & 255, kvh = bid >> 6;
    if (wid < 4) {
      if (fn < 128) {
        const unsigned ko = (unsigned)(((kvh * SK + (SK - WIN) + (fn >> 2) * 16 + c) * DH + (fn & 3) * 32 + 4 * g) * 2);
        gld8<0>(klo, ko, kc); gld8<32>(khi, ko, kc);
      } else {
        const int d = 32 * ((fn - 128) >> 5) + 8 * (c >> 2) + 4 * (fn & 1) + (c & 3);
        const unsigned vo = (unsigned)(((kvh * SK + (SK - WIN) + ((fn >> 1) & 15) * 32 + 4 * g + 8) * DH + d) * 2);
        sfor<8>([&](auto J) AI { constexpr int j = J; gld2<(16 * (j >> 2) + (j & 3) - 8) * DH * 2>(e[j], vo, vc); });
      }
      if (wid < 2) touch((unsigned)((wid * 256 + within * 8 + (l >> 3)) * 8192 + xcd * 1024 + (l & 7) * 128), wq);
      if (fn < 128) waitv<6>(); else waitv<12>();
    } else {
      waitv<3>();
    }
    PIN(a0); PIN(a1);
    float v[16];
#pragma unroll
    for (int j = 0; j < 4; ++j) { v[2 * j] = bflo(a0[j]); v[2 * j + 1] = bfhi(a0[j]); v[8 + 2 * j] = bflo(a1[j]); v[9 + 2 * j] = bfhi(a1[j]); }
    float s = 0.f;
#pragma unroll
    for (int j = 0; j < 16; ++j) s += v[j];
    const float mean = wave_sum(s) * (1.f / DM);
    float q = 0.f;
#pragma unroll
    for (int j = 0; j < 16; ++j) { const float d = v[j] - mean; q += d * d; }
    const float var = wave_sum(q) * (1.f / DM);
    const float mbf = rbf(mean), rden = __builtin_amdgcn_rcpf(rbf(__builtin_amdgcn_sqrtf(rbf(rbf(var) + 1e-5f))));
    u32x4 o0, o1;
#pragma unroll
    for (int j = 0; j < 8; ++j) {
      const unsigned dd = pk2(v[2 * j] - mbf, v[2 * j + 1] - mbf);
      const unsigned nn = pk2(bflo(dd) * rden, bfhi(dd) * rden);
      if (j < 4) o0[j] = nn; else o1[j - 4] = nn;
    }
    *(LAS u32x4*)(lds + STAGE + (l * 8 + (wid ^ (l & 7))) * 16) = o0;
    *(LAS u32x4*)(lds + STAGE + 8192 + (l * 8 + (wid ^ (l & 7))) * 16) = o1;
    *(LAS f32x2*)(lds + STAGE + 16384 + 32 + wid * 8) = (f32x2){mbf, rden};
    waitbar_l<63>();
    if (wid >= 4) {
#pragma unroll
      for (int hh = 0; hh < 4; ++hh) {
        const int p = hh * 256 + (t - 256), ch = p >> 3, r = 8 * bid + ((p & 7) ^ (ch & 7));
        const u32x4 o = *(LAS const u32x4*)(lds + STAGE + p * 16);
        gst16<0>(o, (unsigned)(((((r >> 5) * 32 + (ch >> 2)) * 2 + ((r >> 4) & 1)) * 64 + (ch & 3) * 16 + (r & 15)) * 16), ws + WS_XN);
      }
      if (t < 256 + 8) gst8(*(LAS const f32x2*)(lds + STAGE + 16384 + 32 + (t - 256) * 8), (unsigned)((8 * bid + t - 256) * 8), ws + WS_ST);
      waitv<4>();
    } else if (wid == 0 || wid == 3) { if (fn < 128) waitv<3>(); else waitv<9>(); }
    else if (fn < 128) waitv<4>(); else waitv<10>();
    PIN(w0); PIN(w1); PIN(ep);
    {
      const unsigned long long tg = (((unsigned long long)(unsigned)__builtin_amdgcn_readfirstlane(ep[1]) << 32) | (unsigned)__builtin_amdgcn_readfirstlane(ep[0])) + 1ull;
      tlo = (unsigned)tg; thi = (unsigned)(tg >> 32);
    }
    float rr = 0.f;
#pragma unroll
    for (int j = 0; j < 8; ++j) {
      const unsigned nn = j < 4 ? o0[j] : o1[j - 4], ww = j < 4 ? w0[j] : w1[j - 4];
      rr += bflo(nn) * bflo(ww); rr += bfhi(nn) * bfhi(ww);
    }
    rr = wave_sum(rr);
    {
      const unsigned bits = pk2(rr, 0.f) & 0xffffu;
      const unsigned ord = (bits & 0x8000u) ? (~bits & 0xffffu) : (bits | 0x8000u);
      *(LAS unsigned*)(lds + STAGE + 16384 + wid * 4) = (ord << 11) | (unsigned)(SEQ - 1 - row);
    }
    if (wid >= 4) waitv<0>();
    waitbar_l<63>();
    if (t == 256) {
      const u32x4 ka = *(LAS const u32x4*)(lds + STAGE + 16384), kb = *(LAS const u32x4*)(lds + STAGE + 16400);
      const unsigned key = umax(umax(umax(ka[0], ka[1]), umax(ka[2], ka[3])), umax(umax(kb[0], kb[1]), umax(kb[2], kb[3])));
      gst8((u32x2){(tlo << 27) | key, (tlo >> 5) | (thi << 27)}, (unsigned)(bid * 8), ws + WS_G0);
    }
    if (wid < 4) {
      if (wid < 2) waitv<1>(); else waitv<0>();
      if (fn < 128) {
        PIN(klo); PIN(khi);
        kvo = (u32x4){klo[0], klo[1], khi[0], khi[1]};
      } else {
#pragma unroll
        for (int j = 0; j < 8; ++j) PIN(e[j]);
        kvo = (u32x4){e[0] | (e[1] << 16), e[2] | (e[3] << 16), e[4] | (e[5] << 16), e[6] | (e[7] << 16)};
      }
      gst16<0>(kvo, (unsigned)((bid * 4 + wid) * 1024 + l * 16), ws + WS_KV);
    }
  }
  const unsigned glo = tlo << 27, ghi = (tlo >> 5) | (thi << 27);

  const char* const wq_b = (const char*)wq + head * 256;
  const unsigned wq_v = (unsigned)(((8 * wid + (l >> 3)) * 4096 + ((l & 7) ^ ((wid & 1) * 4 + ((l >> 3) & 2))) * 8) * 2);
  const char* const kv_b = ws + WS_KV + (size_t)(head >> 3) * 262144;
  const unsigned kv_v = (unsigned)(wid * 4096 + l * 16);
  const char* const xn_b = ws + WS_XN + (size_t)rb * 8 * 65536 + 4096;
  const unsigned xn_v = (unsigned)(wid * 65536 + l * 16);
  const char* const wo_b = (const char*)wo + (size_t)kh * 2048 * (DM * 2) + nb * 256;
  const char* const o_b = ws + WS_O + (size_t)mb * 4 * 262144 + kh * 131072;
  const unsigned ldsw = lds0 + wid * 2048, ldsk = lds0 + wid * 4096, ldsf = lds0 + FB + wid * 1024;
  const int erow = mb * 128 + rbw * 32 + kg * 16 + c;
  bf16x8 xf[XD];
  u32x4 fm[2];
  bf16x8 fxs;
  const unsigned gu_v = (unsigned)((((4 * wid + g) * 2 + ((l >> 3) & 1)) * FF + fj * 16 + (l & 7) * 2) * 8);
  u32x2 fg;
  u32x2 xe[4];
  f32x2 st;
  unsigned key = 0;

  const unsigned fo_v = (unsigned)(((mb * 4 + rbw) * 32 + kh * 16 + c) * 8);
  const int yrow = within < 28 ? fj : 224 + xcd * 4 + within - 28;
  f32x4 o[2][2][2];
  float inv[2];
  const char* const od_b = ws + WS_O + ((size_t)(rb * 8) * 128 + head * 4) * 2048 + 4096;
  auto issue = [&](auto Sl, auto Pos) AI {
    constexpr int sl = Sl, pos = Pos, cnt = SC.count(sl), q0 = SC.first(sl) + (pos < 0 ? 0 : pos);
    constexpr int nq = pos < 0 ? cnt : pos >= cnt ? 0 : pos < 3 ? 1 : cnt - 3;
    sfor<nq>([&](auto Qi) AI {
      constexpr int k = SC.kind[q0 + Qi], i = SC.idx[q0 + Qi];
      if constexpr (k == OX && i < 64) gld16<(i & 7) * 1024 - 4096, true>(xf[i % XD], xn_v, xn_b + (size_t)(i >> 3) * 8192);
      if constexpr (k == OX && i >= 64) {
        constexpr int q = i - 64, v = q >> 2;
        gld16<0, true>(xf[q % XD2], (unsigned)(rbw * 262144 + kg * 16384 + ((q & 1) ^ kg) * 1024 + l * 16), o_b + (size_t)(((v & 3) * 4 + ((q >> 1) & 1)) * 8192 + (v >> 2) * 2048));
      }
      if constexpr (k == OG) {
        constexpr int v = i >> 2, p = i & 3;
        if constexpr (v < 8) {
          const char* sb = wq_b + (size_t)(v * 128 + (p >> 1) * 64) * 8192;
          glds<(p & 1) * 128>(ldsw + (v & 3) * 32768 + (p >> 1) * 16384 + (p & 1) * (1024 - 128), wq_v, sb);
        } else if constexpr (v < 16) {
          glds<p * 1024, true>(ldsk + (v & 3) * 32768, kv_v, kv_b + (size_t)(v - 8) * 32768);
        } else {
          const unsigned wo_v = (unsigned)((((wid >> 2) * 128 + (wid & 3) * 8 + (l >> 3)) * DM + ((l & 7) ^ ((wid & 1) * 4 + ((l >> 3) & 2))) * 8) * 2);
          const char* sb = wo_b + (size_t)(((v & 3) * 4 + (p >> 1) * 2) * 128 + ((v - 16) >> 2) * 32) * (DM * 2) + (((p & 1) ^ kh ^ 1) << 7);
          glds<0>(ldsw + (v & 3) * 32768 + (p >> 1) * 16384 + (p & 1) * 1024, wo_v, sb);
        }
      }
      if constexpr (k == OF && i < 8)
        glds<0>(ldsf + (i & 3) * 8192, (unsigned)(((l >> 3) * FF + wid * 64 + ((l & 7) ^ ((i & 1) * 4 + ((l >> 3) & 2))) * 8) * 2),
                (const char*)(i < 4 ? wgate : wup) + ((size_t)(fsl * 32 + 8 * (i & 3)) * FF + fgr * 512) * 2);
      if constexpr (k == OF && i >= 8) {
        const int frho = l >> 2, fch = (l & 3) ^ (frho & 4 ? 2 : 0);
        glds<0>(ldsf + ((i - 8) % 3) * 8192, (unsigned)((frho * DM + wid * 128 + fch * 8) * 2), (const char*)wdown + (size_t)fj * 16 * DM * 2 + (i - 8) * 64);
      }
      if constexpr (k == OS && i == 0) {
        const int idx = SEQ - 1 - (int)(key & 2047u);
        gld16<0, true>(fxs, (unsigned)((((idx >> 5) * 32 + fsl) * 2 + ((idx >> 4) & 1)) * 1024 + (g * 16 + (idx & 15)) * 16), ws + WS_XN);
      }
      if constexpr (k == OS && i == 2) gld16<0, true>(fm[0], gu_v, ws + WS_GU);
      if constexpr (k == OM && i > 0) gld8<0, true>(fg, fo_v, ws + WS_FO + (i - 1) * 16384);
      if constexpr (k == OM && i == 0) gld8<0, true>(fg, (unsigned)((t & 255) * 8), wid < 4 ? ws + WS_G0 : ws + WS_FK + (head >> 3) * 2048);
      if constexpr (k == OW) {
        constexpr int os = i >> 1, qb = i & 1;
        PIN(o[os & 1][0][qb]); PIN(o[os & 1][1][qb]);
        const f32x4 a = o[os & 1][0][qb] * inv[qb], b = o[os & 1][1][qb] * inv[qb];
        gst16<i * 1024 - 4096>((u32x4){pk2(a[0], a[1]), pk2(a[2], a[3]), pk2(b[0], b[1]), pk2(b[2], b[3])}, (unsigned)(wid * 262144 + l * 16), od_b);
      }
      if constexpr (k == OE && i < 2)
        glds<0>(ldsf + i * 8192, (unsigned)(((erow - c + i * 8 + (l >> 3)) * DM + nb * 128 + kh * 64 + ((l & 7) ^ (i * 4 + (l >> 4))) * 8) * 2), x);
      if constexpr (k == OE && i == 2) gld8<0, true>(st, (unsigned)(erow * 8), ws + WS_ST);
    });
  };
  f32x4 fa0, fa1, fq[4];
  s16x4 fb0, fb1, fxa;
  bf16x8 ft[2];
  auto endslot = [&](auto Sl) AI {
    constexpr int sl = Sl, u = sl >> 3, i = sl & 7;
    if constexpr (sl == 3) {
      if (wid == 6 || wid == 7) touch((unsigned)(((wid - 4) * 256 + within * 8 + (l >> 3)) * 8192 + xcd * 1024 + (l & 7) * 128), wq);
    }
    if constexpr (sl == 2) {
      if (wid < 4 && l == 0) gst8((u32x2){tlo, thi}, (unsigned)((bid * 4 + wid) * 8), ws + WS_FK);
    }
    if constexpr (sl == 38) {
      waitv<SC.younger(OM, 0, sl)>(fg);
      const unsigned lo = wid < 4 ? glo : tlo, hi = wid < 4 ? ghi : thi, mk = wid < 4 ? 0xf8000000u : 0xffffffffu;
      for (int spin = 0; spin < SPINS; ++spin) {
        if (__builtin_expect(__all(((fg[0] & mk) == lo) & (fg[1] == hi)), 1)) break;
        __builtin_amdgcn_s_sleep(1);
        gld8<0, true>(fg, (unsigned)((t & 255) * 8), wid < 4 ? ws + WS_G0 : ws + WS_FK + (head >> 3) * 2048);
        waitv<0>(fg);
      }
      const unsigned km = wave_max_u32(fg[0] & 0x07ffffffu);
      *(LAS unsigned*)(lds + HSC + wid * 4) = km;
    }
    if constexpr (sl == 57) {
      if (wid == 3) {
        const char* pc;
        asm volatile("s_getpc_b64 %0" : "=s"(pc));
        touch((unsigned)((within * 8 + (l & 7)) * 128), pc);
      }
    }
    if constexpr (sl == 47) {
      if (bid == 0 && t == 0) gst8((u32x2){tlo, thi}, 0u, ws + WS_EP);
      const u32x4 kk = *(LAS const u32x4*)(lds + HSC);
      key = __builtin_amdgcn_readfirstlane(umax(umax(kk[0], kk[1]), umax(kk[2], kk[3])));
    }
    if constexpr (sl == 58 || sl == 61 || sl == 64 || sl == 67) {
      if (wid == 4 + (sl - 58) / 3) touch((unsigned)(((wid & 3) * 64 + l) * 128), (const char*)wdown + (size_t)fj * 16 * DM * 2);
    }
    if constexpr (sl == 81 || sl == 85 || sl == 89 || sl == 93) {
      if (wid == (sl - 81) / 4)
        touch((unsigned)((kh * 2048 + (within >> 1) * 128 + wid * 32 + ((within & 1) * 2 + (xcd & 1)) * 8 + (l >> 3)) * (DM * 2) + ((xcd >> 1) & 1) * 1024 + (l & 7) * 128), wo);
    }
    if constexpr (sl == 66) {
      const unsigned fo = (unsigned)(WS_FO - WS_ST) + (l & 3) * 16384 + ((mb * 4 + rbw) * 32 + kh * 16) * 8, fx = (unsigned)(WS_FX - WS_ST) + (bid * 8 + wid) * 64;
      const unsigned fy = (unsigned)(WS_FY - WS_ST) + (wid * 256 + yrow) * 8;
      asm volatile("global_load_dword v255, %0, %1 sc1" ::"v"(l < 4 ? fo : l < 8 ? fy : fx), "s"(ws + WS_ST) : "memory", "v255");
    }
    if constexpr (sl == 97 || sl == 99) {
      if (wid == 4 + (sl - 97) / 2) touch((unsigned)(((mb * 128 + (wid - 4) * 64 + l) * DM + nb * 128 + kh * 64) * 2), x);
    }
    if constexpr ((sl >= 68 && sl <= 70) || (sl >= 90 && sl <= 92)) {
      constexpr int r = sl >= 90, q = sl - (r ? 90 : 68);
      if constexpr (q == 0) waitv<SC.younger(OF, 4 * r + 3, sl)>(fxs);
      if constexpr (q >= 1) {
#pragma unroll
        for (int j = 0; j < 2; ++j) { fq[2 * q - 2 + j] = __builtin_amdgcn_mfma_f32_16x16x32_bf16(fxs, ft[j], (f32x4){}, 0, 0, 0); PIN(fq[2 * q - 2 + j]); }
      }
      if constexpr (q < 2) {
        const unsigned rp = ldsf + g * 8192 + (c >> 2) * 128 + ((((g & 1) << 1) | (c >> 3)) << 5) + (c & 3) * 8;
#pragma unroll
        for (int j = 0; j < 2; ++j) { LAS const char* a = (LAS const char*)(uintptr_t)(rp ^ ((2 * q + j) << 5)); ft[j] = cat8(trd(a), trd(a + 512)); }
      }
    }
    if constexpr (sl == 71 || sl == 93) {
      const float v = sel2(g & 2, sel2(g & 1, fq[3][0], fq[2][0]), sel2(g & 1, fq[1][0], fq[0][0]));
      gst8((u32x2){__float_as_uint(v), tlo}, (unsigned)(((fsl * 2 + (sl == 93)) * FF + fgr * 512 + t) * 8), ws + WS_GU);
    }
    if constexpr (sl == 109) {
      waitv<SC.younger(OS, 2, sl)>(fm[0]);
      for (int spin = 0; spin < SPINS; ++spin) {
        if (__builtin_expect(__all((fm[0][1] == tlo) & (fm[0][3] == tlo)), 1)) break;
        __builtin_amdgcn_s_sleep(1);
        gld16<0, true>(fm[0], gu_v, ws + WS_GU);
        waitv<0>(fm[0]);
      }
      *(LAS f32x2*)(lds + RED1 + (wid * 32 + 2 * c) * 4) = (f32x2){xg_sum(__uint_as_float(fm[0][0])), xg_sum(__uint_as_float(fm[0][2]))};
    }
    if constexpr (sl == 111) {
#pragma unroll
      for (int w = 0; w < 2; ++w) { fa0[w] = *(LAS const float*)(lds + RED1 + ((2 * g + w) * 32 + c) * 4); fa0[2 + w] = *(LAS const float*)(lds + RED1 + ((2 * g + w) * 32 + 16 + c) * 4); }
    }
    if constexpr (sl == 112) { fa0[0] = xg_sum(fa0[0] + fa0[1]); fa0[2] = xg_sum(fa0[2] + fa0[3]); PIN(fa0); }
    if constexpr (sl == 113) {
      const float hh = rbf(silu_f(rbf(fa0[0]))) * rbf(fa0[2]);
      *(LAS u16*)(lds + HSC + 32 + wid * 32 + c * 2) = (u16)pk2(hh, hh);
    }
    if constexpr (sl == 114) fxa = *(LAS const s16x4*)(lds + HSC + 32 + wid * 32 + g * 8);
    constexpr int pr = fpiece(sl), pm = fpiece(sl - 1), ps = fpiece(sl - 2);
    if constexpr (ps >= 0) gst4<ps * 128>(sel2(g & 1, fa1[0], fa0[0]), (unsigned)((wid * 128 + (l & 31)) * 4), ws + WS_YP + (size_t)yrow * 4096);
    if constexpr (pm >= 0) { fa0 = MFMA16(fxa, fb0, (f32x4){}); fa1 = MFMA16(fxa, fb1, (f32x4){}); PIN(fa0); PIN(fa1); }
    if constexpr (pr >= 0) {
      waitv<SC.younger(OF, 8 + pr, sl)>();
      const unsigned fp = ldsf + (4 * g + (c >> 2)) * 64 + (g & 1) * 32 + (c & 3) * 8;
      fb0 = trd((LAS const char*)(uintptr_t)fp + (pr % 3) * 8192); fb1 = trd((LAS const char*)(uintptr_t)(fp ^ 32) + (pr % 3) * 8192);
    }
    if constexpr (sl == 111 || sl == 119 || sl == 127) {
      waitv<SC.younger(OW, (sl - 111) / 4 + 1, sl)>();
      if (l == 0) gst8((u32x2){tlo, thi}, (unsigned)(((rb * 8 + wid) * 32 + head) * 8), ws + WS_FO + ((sl - 111) / 8) * 16384);
    }
    if constexpr (sl == 123) {
      waitv<SC.younger(OM, 1, sl)>(fg);
      if (__builtin_expect(!__all((fg[0] == tlo) & (fg[1] == thi)), 0)) poll8(fo_v, ws + WS_FO, tlo, thi, 0xffffffffu);
    }
    SBAR();
    if constexpr (i == 6) {
      if constexpr (u == 5 || u == 13) waitbar_l<SC.younger(OG, 4 * u + 7, sl)>();
      else waitbar<SC.younger(OG, 4 * u + 7, sl)>();
    }
    if constexpr (!(i & 1)) __builtin_amdgcn_s_setprio(i == 6 ? 3 : 2 - i / 2);
  };

  issue(IC(-2), IC(-1));
  if (wid == 5) poll8((unsigned)((rb * 32 + (l & 31)) * 8), ws + WS_G0, glo, ghi, 0xf8000000u);
  waitbar<SC.younger(OG, 3, -2)>();
  issue(IC(-1), IC(-1));
  LBase trb[4], frb;
#pragma unroll
  for (int j = 0; j < 4; ++j) trb[j].init(lds0 + g * 2048 + (c >> 2) * 128 + ((j ^ ((g & 1) << 1) ^ (c >> 3)) << 5) + (c & 3) * 8);
  frb.init(lds0 + l * 16);
  f32x4 acc[8][2];
  bf16x8 wf[2][4];
  auto WRD1 = [&](auto Si, auto Base, auto Dd) AI {
    constexpr int s = Si, d = Dd, o = Base;
    wf[s & 1][d] = cat8(trd(trb[d].template at<o>()), trd(trb[d].template at<o + 512>()));
  };
  auto WRD = [&](auto Si, auto Base) AI { sfor<4>([&](auto Dd) AI { WRD1(Si, Base, Dd); }); };
#define WA(s) IC((((s) >> 3) & 3) * 32768 + (((s) >> 1) & 3) * 8192 + ((s) & 1) * 1024)
#define WB(s) IC((((s) >> 2) & 3) * 32768 + (((s) >> 1) & 1) * 8192 + ((s) & 1) * 1024)
  bf16x8 fr[4];
  auto FRD = [&](auto Ni) AI { constexpr int n = Ni; if constexpr (n < 256) fr[n & 3] = *(LAS const bf16x8*)(frb.at<((n >> 5) & 3) * 32768 + (n & 31) * 1024>()); };

  WRD(IC(0), WA(0));
  SBAR();
  bf16x8 qf[4][2];
  auto QPACK = [&](auto Ds) AI {
    constexpr int ds = Ds;
#pragma unroll
    for (int qb = 0; qb < 2; ++qb) {
      const f32x4 a = acc[2 * ds][qb] * QSCALE, b = acc[2 * ds + 1][qb] * QSCALE;
      qf[ds][qb] = __builtin_bit_cast(bf16x8, (u32x4){pk2(a[0], a[1]), pk2(a[2], a[3]), pk2(b[0], b[1]), pk2(b[2], b[3])});
      PIN(qf[ds][qb]);
    }
  };
  sfor<64>([&](auto Si) AI {
    constexpr int s = Si, x0 = (s & ~1) % XD, x1 = (s | 1) % XD;
    if constexpr (!(s & 1)) waitv<SC.younger(OX, s + 1, s - 1)>(xf[x0], xf[x1]);
    sfor<4>([&](auto Dd) AI {
      constexpr int d = Dd, dt = (s & 1) * 4 + d;
      if constexpr (s + 1 < 64) WRD1(IC(s + 1), WA(s + 1), Dd);
      else if constexpr (d < 3) FRD(Dd);
      SBAR();
      if constexpr (s < 2) { MFZ(acc[dt][0], wf[s & 1][d], xf[x0]); MFZ(acc[dt][1], wf[s & 1][d], xf[x1]); }
      else { MF(acc[dt][0], wf[s & 1][d], xf[x0]); MF(acc[dt][1], wf[s & 1][d], xf[x1]); }
      issue(Si, Dd);
      if constexpr (s == 63 && (d & 1)) { QPACK(IC(d >> 1)); SBAR(); }
    });
    SBAR();
    endslot(Si);
  });

  f32x4 sc[2][2][2], negm[2];
  u32x4 pw[16][2];
  f32x4 lsum[2];
  bf16x8 ones = {0x3f80, 0x3f80, 0x3f80, 0x3f80, 0x3f80, 0x3f80, 0x3f80, 0x3f80};
  PIN(ones);
#define MX3(a, b, c) __builtin_fmaxf(__builtin_fmaxf((a), (b)), (c))
#define EXP2(x) __builtin_amdgcn_exp2f(x)
  auto SM = [&](auto Hi, auto Pi) AI {
    constexpr int H = Hi, p = Pi, kt = p >> 2, qb = (p >> 1) & 1, r = 2 * (p & 1);
    const float e0 = EXP2(sc[H & 1][kt][qb][r]), e1 = EXP2(sc[H & 1][kt][qb][r + 1]);
    unsigned w = pk2(e0, e1);
    PIN(w);
    pw[H][qb][kt * 2 + (p & 1)] = w;
  };
  sfor<128>([&](auto Ni) AI {
    constexpr int n = Ni, H = n >> 3, i = n & 7, kt = i >> 2, ds = i & 3, sn = H & 1;
    FRD(IC(n + 3));
    if constexpr (n < 2) QPACK(IC(2 + n));
    sfor<2>([&](auto Qb) AI {
      constexpr int qb = Qb;
      if constexpr (ds == 0 && H == 0) MFZ(sc[sn][kt][qb], fr[n & 3], qf[0][qb]);
      else if constexpr (ds == 0) MFC(sc[sn][kt][qb], fr[n & 3], qf[0][qb], negm[qb]);
      else MF(sc[sn][kt][qb], fr[n & 3], qf[ds][qb]);
    });
    if constexpr (n == 5 || n == 6) {
      constexpr int qb = n - 5;
      NOPS(3); PIN(sc[0][0][qb]);
      const float m = -xg_max(__builtin_fmaxf(MX3(sc[0][0][qb][0], sc[0][0][qb][1], sc[0][0][qb][2]), sc[0][0][qb][3]));
      negm[qb] = (f32x4){m, m, m, m};
      sc[0][0][qb] += negm[qb];
      PIN(negm[qb]); PIN(sc[0][0][qb]);
    }
    if constexpr (n == 9 || n == 10) { NOPS(3); PIN(sc[0][1][n - 9]); sc[0][1][n - 9] += negm[n - 9]; PIN(sc[0][1][n - 9]); }
    issue(IC(64 + (n >> 2)), IC(n & 3));
    if constexpr (H == 2 && (i == 1 || i == 5)) MFZ(lsum[i >> 2], ones, pw[0][i >> 2]);
    if constexpr (H > 2 && (i == 1 || i == 5)) MF(lsum[i >> 2], ones, pw[H - 2][i >> 2]);
    if constexpr (H >= 1 && H < 15) SM(IC(H - 1), IC(i));
    if constexpr (n == 125) { PIN(negm[0]); PIN(negm[1]); }
    SBAR();
    if constexpr ((n & 3) == 3) endslot(IC(64 + (n >> 2)));
  });
  constexpr int CHK = 32;
  sfor<128>([&](auto Mi) AI {
    constexpr int m = Mi, n = 128 + m, os = m >> 5, H = (m >> 1) & 15, par = m & 1;
    FRD(IC(n + 3));
    if constexpr (H == 0) { MFZ(o[os & 1][par][0], fr[n & 3], pw[H][0]); MFZ(o[os & 1][par][1], fr[n & 3], pw[H][1]); }
    else { MF(o[os & 1][par][0], fr[n & 3], pw[H][0]); MF(o[os & 1][par][1], fr[n & 3], pw[H][1]); }
    if constexpr (m < 26 && (m % 13) % 5 != 1 && (m % 13) % 5 != 3) {
      constexpr int r = m % 13, p = r / 5 * 3 + (r % 5) / 2;
      SM(IC(14 + m / 13), IC(p));
    }
    if constexpr (m == 14 || m == 16) MF(lsum[(m - 14) / 2], ones, pw[14][(m - 14) / 2]);
    if constexpr (m == 27 || m == 28) MF(lsum[m - 27], ones, pw[15][m - 27]);
    if constexpr (m == CHK + 1 || m == CHK + 2) { inv[m - CHK - 1] = __builtin_amdgcn_rcpf(lsum[m - CHK - 1][0]); PIN(inv[m - CHK - 1]); }
    issue(IC(64 + (n >> 2)), IC(n & 3));
    if constexpr (m == 123) {
#pragma unroll
      for (int j = 0; j < 4; ++j) trb[j].init(lds0 + kg * 16384 + g * 2048 + (c >> 2) * 128 + ((j ^ ((g & 1) << 1) ^ (c >> 3)) << 5) + (c & 3) * 8);
    }
    if constexpr (m >= 124) WRD1(IC(0), WB(0), IC(m - 124));
    SBAR();
    if constexpr ((n & 3) == 3) endslot(IC(64 + (n >> 2)));
    if constexpr (m == CHK - 1) {
      PIN(lsum[0]); PIN(lsum[1]);
      if (__builtin_expect(__any(!(lsum[0][0] + lsum[1][0] <= LBOUND)), 0)) {
        NOPS(7); SBAR();
        auto tile = [&](unsigned off, f32x4& s0, f32x4& s1) AI {
          bf16x8 kf[4];
          SBAR(); NOPS(7);
          sfor<4>([&](auto Ds) AI { constexpr int ds = Ds; gld16<ds * 1024, true>(kf[ds], off, kv_b); });
          waitv<0>(kf[0], kf[1]);
          PIN(kf[2]); PIN(kf[3]);
#pragma unroll
          for (int ds = 0; ds < 4; ++ds) {
            s0 = __builtin_amdgcn_mfma_f32_16x16x32_bf16(kf[ds], qf[ds][0], s0, 0, 0, 0);
            s1 = __builtin_amdgcn_mfma_f32_16x16x32_bf16(kf[ds], qf[ds][1], s1, 0, 0, 0);
          }
        };
        float m0 = -__builtin_inff(), m1 = m0;
#pragma unroll 1
        for (int kt = 0; kt < 32; ++kt) {
          f32x4 s0 = {}, s1 = {};
          tile((unsigned)(kt * 4096 + l * 16), s0, s1);
          m0 = MX3(MX3(m0, s0[0], s0[1]), s0[2], s0[3]); m1 = MX3(MX3(m1, s1[0], s1[1]), s1[2], s1[3]);
        }
        m0 = -xg_max(m0); m1 = -xg_max(m1);
        lsum[0] = (f32x4){}; lsum[1] = (f32x4){};
        sfor<16>([&](auto Hi) AI {
          constexpr int H = Hi;
#pragma unroll
          for (int kt = 0; kt < 2; ++kt) {
            sc[H & 1][kt][0] = (f32x4){m0, m0, m0, m0}; sc[H & 1][kt][1] = (f32x4){m1, m1, m1, m1};
            tile((unsigned)((2 * H + kt) * 4096 + l * 16), sc[H & 1][kt][0], sc[H & 1][kt][1]);
          }
          sfor<8>([&](auto Pi) AI { SM(Hi, Pi); });
          PIN(pw[H][0]); PIN(pw[H][1]);
          lsum[0] = __builtin_amdgcn_mfma_f32_16x16x32_bf16(ones, __builtin_bit_cast(bf16x8, pw[H][0]), lsum[0], 0, 0, 0);
          lsum[1] = __builtin_amdgcn_mfma_f32_16x16x32_bf16(ones, __builtin_bit_cast(bf16x8, pw[H][1]), lsum[1], 0, 0, 0);
        });
#pragma unroll
        for (int par = 0; par < 2; ++par) { o[0][par][0] = (f32x4){}; o[0][par][1] = (f32x4){}; }
        sfor<CHK / 4>([&](auto Qi) AI {
          constexpr int q = Qi;
          bf16x8 vf[4];
          SBAR(); NOPS(7);
          sfor<4>([&](auto J) AI { constexpr int j = J; gld16<j * 1024, true>(vf[j], (unsigned)((128 + 4 * q) * 1024 + l * 16), kv_b); });
          waitv<0>(vf[0], vf[1]);
          PIN(vf[2]); PIN(vf[3]);
#pragma unroll
          for (int j = 0; j < 4; ++j)
#pragma unroll
            for (int qb = 0; qb < 2; ++qb)
              o[0][j & 1][qb] = __builtin_amdgcn_mfma_f32_16x16x32_bf16(vf[j], __builtin_bit_cast(bf16x8, pw[2 * q + (j >> 1)][qb]), o[0][j & 1][qb], 0, 0, 0);
        });
#pragma unroll
        for (int par = 0; par < 2; ++par) { PIN(o[0][par][0]); PIN(o[0][par][1]); }
        PIN(lsum[0]); PIN(lsum[1]);
        SBAR(); NOPS(7);
      }
      SBAR();
    }
  });
  const int idx = SEQ - 1 - (int)(key & 2047u);
  const bool owner = (idx >> 7) == mb, ownw = (idx >> 4) == mb * 8 + rbw * 2 + kg;
  f32x4 yp[7], oth[4];
  unsigned gate[8];
  float glow;
  auto xnorm = [&](auto Ji) AI {
    constexpr int j = Ji;
    const unsigned xw = xe[j >> 1][j & 1], dd = pk2(bflo(xw) - st[0], bfhi(xw) - st[0]);
    return pk2(bflo(dd) * st[1], bfhi(dd) * st[1]);
  };
  const unsigned ex_a = ldsf + (c >> 3) * 8192 + (c & 7) * 128 + (((g >> 1) ^ (c >> 1)) << 4) + (g & 1) * 8;
  auto item = [](int s) { return s < 48 ? s : s < 56 ? 48 + 2 * (s - 48) : 49 + 2 * (s - 56); };
  u32x2 pf;
  u32x4 otp[2];
  bool rdy = false;
  sfor<64>([&](auto Si) AI {
    constexpr int s = Si, u = s >> 2, it = item(s), x0 = (it & ~1) % XD2, x1 = (it | 1) % XD2;
    if constexpr (!(it & 1) && s <= 52) waitv<SC.younger(OX, 64 + it + 1, B0 + s - 1)>(xf[x0], xf[x1]);
    if constexpr (!(it & 1) && s > 52) { PIN(xf[x0]); PIN(xf[x1]); }
    sfor<4>([&](auto Dd) AI {
      constexpr int d = Dd, dt = (it & 1) * 4 + d;
      if constexpr (s + 1 < 64) WRD1(IC(s + 1), WB(item(s + 1)), Dd);
      SBAR();
      if constexpr (s < 2) { MFZ(acc[dt][0], wf[s & 1][d], xf[x0]); MFZ(acc[dt][1], wf[s & 1][d], xf[x1]); }
      else { MF(acc[dt][0], wf[s & 1][d], xf[x0]); MF(acc[dt][1], wf[s & 1][d], xf[x1]); }
      issue(IC(B0 + s), Dd);
      if constexpr (s == 56) *(LAS f32x4*)(lds + (wid ^ 4) * 4096 + l * 16 + (d & 1) * 2048 + (d >> 1) * 32768) = acc[d][1];
      if constexpr (s == 57 && d == 0) {
#pragma unroll
        for (int e = 0; e < 4; ++e) oth[e] = *(LAS const f32x4*)(lds + wid * 4096 + l * 16 + (e & 1) * 2048 + (e >> 1) * 32768);
      }
      if constexpr (s == 57 && d >= 2) {
        const f32x4 a = acc[2 * d - 4][0] + oth[2 * d - 4], b = acc[2 * d - 3][0] + oth[2 * d - 3];
        gst16<(d - 2) * 1024>((u32x4){pk2(a[0], a[1]), pk2(a[2], a[3]), pk2(b[0], b[1]), pk2(b[2], b[3])}, (unsigned)(wid * 2048 + l * 16), ws + WS_XC + (size_t)(bid ^ 4) * 16384);
      }
      if constexpr (s == 28) xe[d] = *(LAS const u32x2*)(uintptr_t)(ex_a ^ (d << 5));
      if constexpr (s >= 29 && s < 37 && d == 1) { PIN(st); gate[s - 29] = xnorm(IC(s - 29)); PIN(gate[s - 29]); }
      if constexpr (s >= 37 && s < 53 && d == 1) {
        constexpr int j = (s - 37) >> 1;
        if constexpr ((s - 37) & 1) { gate[j] = pk2(glow, silu_f(bfhi(gate[j]))); PIN(gate[j]); }
        else { glow = silu_f(bflo(gate[j])); PIN(glow); }
      }
    });
    SBAR();
    if constexpr (s == 7) {
      waitv<SC.younger(OW, 7, B0 + s)>();
      if (l == 0) {
        gst8((u32x2){tlo, thi}, (unsigned)(((rb * 8 + wid) * 32 + head) * 8), ws + WS_FO + 3 * 16384);
        gst8((u32x2){tlo, thi}, (unsigned)((wid * 256 + yrow) * 8), ws + WS_FY);
      }
    }
    if constexpr (s == 4 || s == 17 || s == 33) {
      constexpr int j = s == 4 ? 1 : s == 17 ? 2 : 3;
      waitv<SC.younger(OM, j + 1, B0 + s)>(fg);
      if (__builtin_expect(!__all((fg[0] == tlo) & (fg[1] == thi)), 0)) poll8(fo_v, ws + WS_FO + j * 16384, tlo, thi, 0xffffffffu);
    }
    if constexpr (s == 59) gld8<0, true>(pf, (unsigned)((bid * 8 + wid) * 64), ws + WS_FX);
    if constexpr (s == 60) {
      waitv<1>();
      if (l == 0) gst8((u32x2){tlo, thi}, (unsigned)(((bid ^ 4) * 8 + wid) * 64), ws + WS_FX);
    }
    if constexpr (s == 62) {
      waitv<1>(pf);
      rdy = __all((pf[0] == tlo) & (pf[1] == thi));
      if (rdy) sfor<2>([&](auto Dd) AI { constexpr int d = Dd; gld16<d * 1024, true>(otp[d], (unsigned)(wid * 2048 + l * 16), ws + WS_XC + (size_t)bid * 16384); });
    }
    if constexpr (s == 37) {
      if (owner) { gld16<0, true>(fm[0], (unsigned)(nb * 2048 + (l < 56 ? l : 55) * 32), ws + WS_FY); gld16<16, true>(fm[1], (unsigned)(nb * 2048 + (l < 56 ? l : 55) * 32), ws + WS_FY); }
    }
    if constexpr (s == 53) {
      if (owner) {
        PIN(fm[0]); PIN(fm[1]);
        for (int spin = 0; spin < SPINS; ++spin) {
          const bool ok = (fm[0][0] == tlo) & (fm[0][1] == thi) & (fm[0][2] == tlo) & (fm[0][3] == thi) & (fm[1][0] == tlo) & (fm[1][1] == thi) & (fm[1][2] == tlo) & (fm[1][3] == thi);
          if (__builtin_expect(__all(ok), 1)) break;
          __builtin_amdgcn_s_sleep(1);
          gld16<0, true>(fm[0], (unsigned)(nb * 2048 + (l < 56 ? l : 55) * 32), ws + WS_FY); gld16<16, true>(fm[1], (unsigned)(nb * 2048 + (l < 56 ? l : 55) * 32), ws + WS_FY);
          waitv<0>(fm[0], fm[1]);
        }
        const unsigned yv = (unsigned)(((t >> 4) * 1024 + nb * 128 + kh * 64 + (t & 15) * 4) * 4);
        sfor<7>([&](auto I) AI { constexpr int q = I; gld16<0, true>(yp[q], yv, ws + WS_YP + (size_t)q * 32 * 4096); });
      }
    }
    if constexpr (s == 61) {
      if (owner) {
        f32x4 a = {};
#pragma unroll
        for (int q = 0; q < 7; ++q) { PIN(yp[q]); a += yp[q]; }
#pragma unroll
        for (int j = 0; j < 4; ++j) a[j] = xg_sum(a[j]);
        *(LAS f32x4*)(lds + YS + (wid * 64 + c * 4) * 4) = a;
      }
    }
    SBAR();
    if constexpr (((s & 3) == 2 && u < 11) || s == 44) {
      waitbar<SC.younger(OG, 64 + 4 * u + 7, B0 + s)>();
    }
    if constexpr (s == 48) waitbar<SC.younger(OG, 64 + 55, B0 + s)>();
    if constexpr (s == 50) waitbar<SC.younger(OG, 64 + 59, B0 + s)>();
    if constexpr (s == 52) { waitbar<SC.younger(OG, 64 + 63, B0 + s)>(); }
    if constexpr (s == 56) waitbar_l<63>();
    __builtin_amdgcn_s_setprio(3 - ((s + 2) & 3));
  });
  f32x4 kep[4];
  {
    NOPS(7);
#pragma unroll
    for (int d = 0; d < 4; ++d) { PIN(acc[4 + d][1]); *(LAS f32x4*)(lds + 65536 + (wid ^ 4) * 4096 + l * 16 + (d & 1) * 2048 + (d >> 1) * 32768) = acc[4 + d][1]; }
    if (!rdy) {
      poll8((unsigned)((bid * 8 + wid) * 64), ws + WS_FX, tlo, thi, 0xffffffffu);
      sfor<2>([&](auto Dd) AI { constexpr int d = Dd; gld16<d * 1024, true>(otp[d], (unsigned)(wid * 2048 + l * 16), ws + WS_XC + (size_t)bid * 16384); });
    }
    waitbar_l<63>();
#pragma unroll
    for (int d = 0; d < 4; ++d) kep[d] = acc[4 + d][0] + *(LAS const f32x4*)(lds + 65536 + wid * 4096 + l * 16 + (d & 1) * 2048 + (d >> 1) * 32768);
    {
      if (ownw) {
        const int rc = idx & 15;
        float a = 0.f;
#pragma unroll
        for (int p = 0; p < 8; ++p) a += *(LAS const float*)(lds + YS + (p * 64 + l) * 4);
        const float xv = bflo(*(LAS const u16*)(uintptr_t)(ldsf + (rc >> 3) * 8192 + (rc & 7) * 128 + (((l >> 3) ^ (rc >> 1)) << 4) + (l & 7) * 2));
        const float s0 = st[0], s1 = st[1];
        const float m = __int_as_float(__builtin_amdgcn_readlane(__float_as_int(s0), rc)), rd = __int_as_float(__builtin_amdgcn_readlane(__float_as_int(s1), rc));
        const unsigned ord = key >> 11, bits = (ord & 0x8000u) ? (ord & 0x7fffu) : (~ord & 0xffffu);
        const float gv = silu_f(rbf(rbf(rbf(xv - m) * rd) + rbf(rbf(a) * rbf(sigmoid_f(bflo(bits))))));
        *(LAS u16*)(lds + RED1 + l * 2) = (u16)pk2(gv, gv);
        asm volatile("" ::: "memory");
        u32x2 w[4];
#pragma unroll
        for (int q = 0; q < 4; ++q) w[q] = *(LAS const u32x2*)(lds + RED1 + (q * 16 + 4 * g) * 2);
#pragma unroll
        for (int j = 0; j < 8; ++j) { gate[j] = c == rc ? w[j >> 1][j & 1] : gate[j]; PIN(gate[j]); }
      }
    }
    waitv<0>(otp[0], otp[1]);
  }
#pragma unroll
  for (int d = 0; d < 4; ++d) {
    const unsigned o0 = otp[d >> 1][2 * (d & 1)], o1 = otp[d >> 1][2 * (d & 1) + 1];
    const f32x4 a = kep[d] + (f32x4){bflo(o0), bfhi(o0), bflo(o1), bfhi(o1)};
    u32x2 res;
#pragma unroll
    for (int j = 0; j < 2; ++j) {
      const unsigned sp = pk2(a[2 * j], a[2 * j + 1]), gp = gate[2 * d + j], xw = xe[d][j];
      const unsigned pr = pk2(bflo(sp) * bflo(gp), bfhi(sp) * bfhi(gp));
      res[j] = pk2(bflo(xw) + bflo(pr), bfhi(xw) + bfhi(pr));
    }
    *(LAS u32x2*)(uintptr_t)(ex_a ^ (d << 5)) = res;
  }
#pragma unroll
  for (int i = 0; i < 2; ++i)
    gst16<0>(*(LAS const u32x4*)(uintptr_t)(ldsf + i * 8192 + l * 16), (unsigned)(((erow - c + i * 8 + (l >> 3)) * DM + nb * 128 + kh * 64 + ((l & 7) ^ (i * 4 + (l >> 4))) * 8) * 2), out);
}

extern "C" void kernel_launch(void* const* d_in, const int* in_sizes, int n_in, void* d_out, int out_size, void* d_ws, size_t ws_size,
                              hipStream_t stream) {
  k_fused<<<256, 512, 0, stream>>>((const u16*)d_in[0], (const u16*)d_in[1], (const u16*)d_in[2], (const u16*)d_in[3], (const u16*)d_in[4],
                                   (const u16*)d_in[6], (const u16*)d_in[7], (const u16*)d_in[8], (const u16*)d_in[9], (char*)d_ws, (u16*)d_out);
}
```
